```python
import torch

def _softmax(x, axis=-1):
    return torch.softmax(x, dim=axis)
def _gelu(x):
    return torch.nn.functional.gelu(x, approximate="tanh")
def _silu(x):
    return torch.nn.functional.silu(x)
def _sigmoid(x):
    return torch.sigmoid(x)
def _layernorm(x, axis=-1, eps=1e-5):
    m = x.mean(dim=axis, keepdim=True)
    v = x.var(dim=axis, keepdim=True, unbiased=False)
    return (x - m) / torch.sqrt(v + eps)
def _scatter_add(tgt, idx, src, axis=0):
    return tgt.clone().index_add_(axis, idx, src)
def _segment_sum(src, seg, n):
    out = torch.zeros((n,) + src.shape[1:], dtype=src.dtype)
    return out.index_add_(0, seg, src)
def _pad(x, pw):
    flat = tuple(v for pair in reversed(pw) for v in pair)
    return torch.nn.functional.pad(x, flat)

kernel_name = 'gated_mix__linear_attn_block__ssm_conv_h3__prompt'

def setup_inputs():

    _w = (lambda a: (lambda t: t.to(torch.bfloat16) if t.is_floating_point() else t)(torch.as_tensor(a)))
    def _A_setup():
        B, S, D = 1, 2048, 640
        return {
            "x": _w((lambda *s: torch.randn(*s, dtype=torch.bfloat16) * 0.05)(B, S, D)),
            "wqkv": _w((lambda *s: torch.randn(*s, dtype=torch.bfloat16) * 1.6)(D, 3 * D)),
            "wo": _w((lambda *s: torch.randn(*s, dtype=torch.bfloat16) * 0.1)(D, D)),
        }
    _A_in = _A_setup()

    def _B_setup():
        B, S, D = 1, 2048, 640
        ks = 4
        return {
            "q": _w((lambda *s: torch.randn(*s, dtype=torch.bfloat16) * 0.05)(B, S, D)),
            "k": _w((lambda *s: torch.randn(*s, dtype=torch.bfloat16) * 0.8)(B, S, D)),
            "v": _w((lambda *s: torch.randn(*s, dtype=torch.bfloat16) * 0.8)(B, S, D)),
            "shift_k": _w((lambda *s: torch.randn(*s, dtype=torch.bfloat16) * 0.8)(D, ks) * 0.5),
            "ssm_k": _w((lambda *s: torch.randn(*s, dtype=torch.bfloat16) * 0.8)(D, S) * (1.0 / S ** 0.5)),
            "ssm_D": _w((lambda *s: torch.randn(*s, dtype=torch.bfloat16) * 0.8)(D)),
        }
    _B_in = _B_setup()

    return {
        "A_x": _A_in["x"],
        "A_wqkv": _A_in["wqkv"],
        "A_wo": _A_in["wo"],
        "B_q": _B_in["q"],
        "B_k": _B_in["k"],
        "B_v": _B_in["v"],
        "B_shift_k": _B_in["shift_k"],
        "B_ssm_k": _B_in["ssm_k"],
        "B_ssm_D": _B_in["ssm_D"],
        "gate_w": _w((lambda *s: torch.randn(*s, dtype=torch.bfloat16) * 0.05)(640) * 16.0),
    }

def reference(A_x, A_wqkv, A_wo, B_q, B_k, B_v, B_shift_k, B_ssm_k, B_ssm_D, gate_w):
    def _A_ref(x, wqkv, wo):
        B, S, D = x.shape
        H = 32; Dh = D // H
        qkv = (lambda x, *d: x.permute(*d))((x @ wqkv).reshape(B, S, 3, H, Dh), 2, 0, 3, 1, 4)
        q, k, v = qkv[0], qkv[1], qkv[2]
        phi = lambda t: torch.where(t > 0, t + 1, torch.exp(t)) + 1e-6
        pq, pk = phi(q), phi(k)
        kv = torch.einsum("bhsd,bhse->bhde", pk, v)
        z = torch.einsum("bhsd->bhd", pk)
        num = torch.einsum("bhqd,bhde->bhqe", pq, kv)
        den = torch.einsum("bhqd,bhd->bhq", pq, z)[..., None] + 1e-6
        o = num / den
        return (lambda x, *d: x.permute(*d))(o, 0, 2, 1, 3).reshape(B, S, D) @ wo

    def _B_ref(q, k, v, shift_k, ssm_k, ssm_D):
        B, S, D = q.shape
        ks = shift_k.shape[-1]
        k_pad = _pad(k, ((0, 0), (ks - 1, 0), (0, 0)))
        k_shift = torch.zeros_like(k)
        for t in range(ks):
            k_shift = k_shift + k_pad[:, t:t + S, :] * shift_k[:, ks - 1 - t][None, None, :]
        kv = k_shift * v
        L = 2 * S
        kv_pad = _pad(kv, ((0, 0), (0, S), (0, 0)))
        kf_pad = _pad(ssm_k, ((0, 0), (0, S)))
        kv_f = (lambda x, axes=(-2,-1): torch.fft.rfft2(x.float(), dim=axes))(kv_pad[:, None, :, :], axes=(1, 2))
        kf_f = (lambda x, axes=(-2,-1): torch.fft.rfft2(x.float(), dim=axes))(kf_pad[:, None, :], axes=(1, 2))
        kf_f = (lambda x, *d: x.permute(*d))(kf_f, 1, 2, 0)
        y_f = kv_f * kf_f[None, :, :, :]
        y = (lambda x, s, axes=(-2,-1): torch.fft.irfft2(x, s=s, dim=axes).to(torch.bfloat16))(y_f, (1, L), axes=(1, 2))[:, 0, :S, :]
        y = y + kv * ssm_D[None, None, :]
        return q * y

    x = A_x
    g = _sigmoid((x * gate_w).sum(-1, keepdims=True))
    a = _A_ref(x, A_wqkv, A_wo)
    b = _B_ref(x, B_k, B_v, B_shift_k, B_ssm_k, B_ssm_D)
    return g * a + (1 - g) * b
```

```cpp
#include <hip/hip_runtime.h>
#include <hip/hip_bf16.h>
typedef unsigned short u16;
__device__ __forceinline__ float bf2f(u16 v){ return __uint_as_float(((unsigned)v)<<16); }
__device__ __forceinline__ u16 f2bf(float f){ unsigned u=__float_as_uint(f); u += 0x7fff + ((u>>16)&1); return (u16)(u>>16); }
__device__ __forceinline__ float rbf(float f){ return bf2f(f2bf(f)); }
constexpr int S=2048, D=640, H=32, DH=20;

__global__ void k_qkv(const u16* x, const u16* w, u16* qkv){
  int n = blockIdx.x*64 + (threadIdx.x&63);
  int s = blockIdx.y*4 + (threadIdx.x>>6);
  float acc=0;
  for(int k=0;k<D;k++) acc += bf2f(x[s*D+k])*bf2f(w[k*1920+n]);
  qkv[s*1920+n]=f2bf(acc);
}
__device__ __forceinline__ float phi(float t){ float r = t>0.f ? rbf(t+1.f) : rbf(__expf(t)); return rbf(r+1e-6f); }
__global__ void k_stats(const u16* qkv, float* kv, float* z){
  int h = blockIdx.x; int t = threadIdx.x;
  if(t<400){ int d=t/20,e=t%20; float acc=0; for(int s=0;s<S;s++){ acc += phi(bf2f(qkv[s*1920+640+h*20+d]))*bf2f(qkv[s*1920+1280+h*20+e]); } kv[h*400+t]=rbf(acc);}
  else if(t<420){ int d=t-400; float acc=0; for(int s=0;s<S;s++) acc+=phi(bf2f(qkv[s*1920+640+h*20+d])); z[h*20+d]=rbf(acc);}
}
__global__ void k_o(const u16* qkv, const float* kv, const float* z, u16* o){
  int s = blockIdx.x; int c = threadIdx.x;
  int h=c/20,e=c%20; float num=0,den=0;
  for(int d=0;d<20;d++){ float pq=phi(bf2f(qkv[s*1920+h*20+d])); num+=pq*kv[h*400+d*20+e]; den+=pq*z[h*20+d]; }
  num=rbf(num); den=rbf(rbf(den)+1e-6f);
  o[s*D+c]=f2bf(num/den);
}
__global__ void k_u(const u16* k, const u16* v, const u16* sk, u16* uT){
  int s = blockIdx.x; int c = threadIdx.x;
  float acc=0;
  for(int t=0;t<4;t++){ int ss=s-3+t; float kk = ss>=0? bf2f(k[ss*D+c]):0.f; acc = rbf(acc + rbf(kk*bf2f(sk[c*4+3-t]))); }
  uT[c*S+s]=f2bf(acc*bf2f(v[s*D+c]));
}
__global__ void k_conv(const u16* uT, const u16* h, const u16* Dv, u16* yT){
  int c = blockIdx.y; int t = blockIdx.x*256+threadIdx.x;
  __shared__ float su[S]; __shared__ float sh[S];
  for(int i=threadIdx.x;i<S;i+=256){ su[i]=bf2f(uT[c*S+i]); sh[i]=bf2f(h[c*S+i]); }
  __syncthreads();
  float acc=0; for(int j=0;j<=t;j++) acc += sh[t-j]*su[j];
  float y = rbf(acc); y = rbf(y + rbf(su[t]*bf2f(Dv[c])));
  yT[c*S+t]=f2bf(y);
}
__global__ void k_out(const u16* x, const u16* gw, const u16* o, const u16* wo, const u16* yT, u16* out){
  int s = blockIdx.x; int c = threadIdx.x;
  __shared__ float red[640]; __shared__ float so[640];
  red[c]=rbf(bf2f(x[s*D+c])*bf2f(gw[c])); so[c]=bf2f(o[s*D+c]);
  __syncthreads();
  float gs=0; for(int i=0;i<640;i++) gs+=red[i];
  gs=rbf(gs); float g = rbf(1.f/(1.f+__expf(-gs)));
  float a=0; for(int k=0;k<D;k++) a+= so[k]*bf2f(wo[k*D+c]);
  a=rbf(a);
  float b=rbf(bf2f(x[s*D+c])*bf2f(yT[c*S+s]));
  float r = rbf(g*a) + rbf(rbf(1.f-g)*b);
  out[s*D+c]=f2bf(r);
}
extern "C" void kernel_launch(void* const* d_in, const int* in_sizes, int n_in, void* d_out, int out_size, void* d_ws, size_t ws_size, hipStream_t stream) {
  const u16* x=(const u16*)d_in[0]; const u16* wqkv=(const u16*)d_in[1]; const u16* wo=(const u16*)d_in[2];
  const u16* Bk=(const u16*)d_in[4]; const u16* Bv=(const u16*)d_in[5]; const u16* sk=(const u16*)d_in[6];
  const u16* hk=(const u16*)d_in[7]; const u16* Dv=(const u16*)d_in[8]; const u16* gw=(const u16*)d_in[9];
  char* ws=(char*)d_ws;
  u16* qkv=(u16*)ws; ws+= (size_t)S*1920*2;
  float* kv=(float*)ws; ws+= 32*400*4; float* z=(float*)ws; ws+= 32*20*4;
  u16* o=(u16*)ws; ws+=(size_t)S*D*2; u16* uT=(u16*)ws; ws+=(size_t)S*D*2; u16* yT=(u16*)ws;
  k_qkv<<<dim3(30,512),256,0,stream>>>(x,wqkv,qkv);
  k_stats<<<32,448,0,stream>>>(qkv,kv,z);
  k_o<<<S,640,0,stream>>>(qkv,kv,z,o);
  k_u<<<S,640,0,stream>>>(Bk,Bv,sk,uT);
  k_conv<<<dim3(8,640),256,0,stream>>>(uT,hk,Dv,yT);
  k_out<<<S,640,0,stream>>>(x,gw,o,wo,yT,(u16*)d_out);
}
```

```cpp
#include <hip/hip_runtime.h>
#include <hip/hip_bf16.h>
#include <type_traits>

typedef unsigned short u16;
typedef __bf16 bf16x2 __attribute__((ext_vector_type(2)));
typedef __bf16 bf16x8 __attribute__((ext_vector_type(8)));
typedef float f32x4 __attribute__((ext_vector_type(4)));
typedef float f32x2 __attribute__((ext_vector_type(2)));
typedef unsigned u32x4 __attribute__((ext_vector_type(4)));
typedef unsigned u32x2 __attribute__((ext_vector_type(2)));
typedef short v4i16_t __attribute__((ext_vector_type(4)));
typedef int i32x4 __attribute__((ext_vector_type(4)));
#define LAS __attribute__((address_space(3)))

constexpr int S = 2048, D = 640;

__device__ __forceinline__ unsigned pk2(float a, float b) { bf16x2 r = {(__bf16)a, (__bf16)b}; return __builtin_bit_cast(unsigned, r); }
__device__ __forceinline__ float rbf(float a) { return (float)(__bf16)a; }
__device__ __forceinline__ float bflo(unsigned v) { return __uint_as_float(v << 16); }
__device__ __forceinline__ float bfhi(unsigned v) { return __uint_as_float(v & 0xffff0000u); }
__device__ __forceinline__ float phi(float t) {
  float r = t > 0.f ? t + 1.f : __expf(t);
  return rbf(rbf(r) + 1e-6f);
}
__device__ __forceinline__ i32x4 make_srd(const void* p, unsigned bytes) {
  unsigned long long a = (unsigned long long)p; i32x4 r;
  r.x = __builtin_amdgcn_readfirstlane((int)(unsigned)a); r.y = __builtin_amdgcn_readfirstlane((int)(unsigned)(a >> 32));
  r.z = __builtin_amdgcn_readfirstlane((int)bytes); r.w = 0x00020000; return r;
}
__device__ __forceinline__ void glds16(unsigned ldsaddr, unsigned voff, i32x4 srd, unsigned soff) {
  asm volatile("s_mov_b32 m0, %0\n\ts_nop 0\n\tbuffer_load_dwordx4 %1, %2, %3 offen lds" :: "s"(ldsaddr), "v"(voff), "s"(srd), "s"(soff) : "memory");
}
template <int N> __device__ __forceinline__ void wait_bar() { asm volatile("s_waitcnt vmcnt(%0) lgkmcnt(0)\n\ts_barrier" :: "n"(N) : "memory"); }
__device__ __forceinline__ bf16x8 lds_b128(unsigned addr) { return *(const LAS bf16x8*)(size_t)addr; }
__device__ __forceinline__ u32x2 lds_tr(unsigned addr) { return __builtin_bit_cast(u32x2, __builtin_amdgcn_ds_read_tr16_b64_v4i16((LAS v4i16_t*)(size_t)addr)); }
__device__ __forceinline__ bf16x8 mk8(u32x2 lo, u32x2 hi) { u32x4 t = {lo.x, lo.y, hi.x, hi.y}; return __builtin_bit_cast(bf16x8, t); }
__device__ __forceinline__ f32x4 mfma16(bf16x8 a, bf16x8 b, f32x4 c) { return __builtin_amdgcn_mfma_f32_16x16x32_bf16(a, b, c, 0, 0, 0); }

struct Ws { u16* pq; float* P; u16* uT; float* G; u16* woP; u16* o; u16* yT; };

constexpr int XS = 16384, WSB = 20480, STG = XS + WSB, NST = 4;
constexpr int K1_LDS = STG * NST;

__global__ __launch_bounds__(256) void k1(const u16* __restrict__ x, const u16* __restrict__ wqkv, const u16* __restrict__ wo,
                                          const u16* __restrict__ Bk, const u16* __restrict__ Bv, const u16* __restrict__ shk,
                                          const u16* __restrict__ gw, u16* __restrict__ pq, float* __restrict__ P,
                                          u16* __restrict__ uT, float* __restrict__ G, u16* __restrict__ woP) {
  extern __shared__ __attribute__((aligned(16))) char sm[];
  const int tid = threadIdx.x, lane = tid & 63;
  const int w = __builtin_amdgcn_readfirstlane(tid >> 6);
  const int b = blockIdx.x, xcd = b & 7, r = b >> 3;
  if (r < 24) {
    const bool isKV = r < 16;
    const int rt = (xcd & 3) * 4 + (r & 3);
    const int cg = isKV ? (xcd >> 2) * 4 + (r >> 2) : (xcd >> 2) * 2 + ((r - 16) >> 2);
    const unsigned seg0 = isKV ? 1280u + 160u * cg : 320u * cg;
    const unsigned seg1 = isKV ? 2560u + 160u * cg : 320u * cg + 160u;
    const i32x4 sx = make_srd(x, S * D * 2), sw = make_srd(wqkv, D * 3 * D * 2);
    const unsigned lb = (unsigned)(size_t)(LAS char*)sm;
    unsigned vx[4], vw[5];
#pragma unroll
    for (int i = 0; i < 4; i++) { int row = (w * 4 + i) * 8 + (lane >> 3); vx[i] = (rt * 128 + row) * 1280 + (((lane & 7) ^ ((row >> 1) & 7)) << 4); }
#pragma unroll
    for (int i = 0; i < 5; i++) {
      int ci = (w * 5 + i) * 64 + lane; int k = ci / 20, pos = ci - k * 20; int gc = pos ^ (((k >> 3) & 1) << 1);
      vw[i] = k * 3840 + (gc < 10 ? seg0 + gc * 16 : seg1 + (gc - 10) * 16);
    }
    auto stage = [&](int st, int buf) {
      const unsigned base = lb + buf * STG;
#pragma unroll
      for (int i = 0; i < 4; i++) glds16(base + (w * 4 + i) * 1024, vx[i], sx, st * 128);
#pragma unroll
      for (int i = 0; i < 5; i++) glds16(base + XS + (w * 5 + i) * 1024, vw[i], sw, st * (64 * 3840));
    };
    const int fr = lane & 15, g4 = lane >> 4;
    const unsigned xa0 = lb + (w * 32 + fr) * 128 + ((g4 ^ ((fr >> 1) & 7)) << 4);
    const unsigned wl = lb + XS + (8 * g4 + (fr >> 2)) * 320 + ((lane & 3) >> 1) * 16 + (lane & 1) * 8;
    const unsigned wE = wl + (g4 & 1) * 32, wO = wl - (g4 & 1) * 32;
    f32x4 acc[2][10];
#pragma unroll
    for (int i = 0; i < 2; i++)
#pragma unroll
      for (int t = 0; t < 10; t++) acc[i][t] = (f32x4){0.f, 0.f, 0.f, 0.f};
    auto compute = [&](int buf, auto kvtag) {
      constexpr bool KV = decltype(kvtag)::value;
      const unsigned bo = buf * STG;
#pragma unroll
      for (int kk = 0; kk < 2; kk++) {
        bf16x8 xf[2];
#pragma unroll
        for (int i = 0; i < 2; i++) xf[i] = lds_b128(((xa0 + bo) ^ (kk * 64)) + i * 2048);
#pragma unroll
        for (int t = 0; t < 10; t++) {
          unsigned a = ((t & 1) ? wO : wE) + bo + 32 * t + kk * 10240;
          bf16x8 wf = mk8(lds_tr(a), lds_tr(a + 1280));
#pragma unroll
          for (int i = 0; i < 2; i++) acc[i][t] = KV ? mfma16(xf[i], wf, acc[i][t]) : mfma16(wf, xf[i], acc[i][t]);
        }
      }
    };
    auto mainloop = [&](auto kvtag) {
      stage(0, 0); stage(1, 1); stage(2, 2);
      for (int s = 0; s < 7; s++) { wait_bar<18>(); stage(s + 3, (s + 3) & 3); compute(s & 3, kvtag); }
      wait_bar<18>(); compute(3, kvtag);
      wait_bar<9>(); compute(0, kvtag);
      wait_bar<0>(); compute(1, kvtag);
    };
    if (isKV) {
      mainloop(std::true_type{});
      bf16x8 pk[5], vv[5];
#pragma unroll
      for (int t = 0; t < 5; t++) {
        u32x4 a, c;
        float p0[4], p1[4];
#pragma unroll
        for (int j = 0; j < 4; j++) { p0[j] = phi(rbf(acc[0][t][j])); p1[j] = phi(rbf(acc[1][t][j])); }
        a.x = pk2(p0[0], p0[1]); a.y = pk2(p0[2], p0[3]); a.z = pk2(p1[0], p1[1]); a.w = pk2(p1[2], p1[3]);
        c.x = pk2(acc[0][5 + t][0], acc[0][5 + t][1]); c.y = pk2(acc[0][5 + t][2], acc[0][5 + t][3]);
        c.z = pk2(acc[1][5 + t][0], acc[1][5 + t][1]); c.w = pk2(acc[1][5 + t][2], acc[1][5 + t][3]);
        pk[t] = __builtin_bit_cast(bf16x8, a); vv[t] = __builtin_bit_cast(bf16x8, c);
      }
      u32x4 o1 = {0x3F803F80u, 0x3F803F80u, 0x3F803F80u, 0x3F803F80u};
      const bf16x8 ones = __builtin_bit_cast(bf16x8, o1);
      const f32x4 z4 = {0.f, 0.f, 0.f, 0.f};
      __syncthreads();
      f32x4* red = (f32x4*)sm;
#pragma unroll
      for (int i = 0; i < 5; i++) {
#pragma unroll
        for (int j = 0; j < 5; j++) {
          if (j >= i - 1 && j <= i + 1) {
            const int idx = (i == 0 ? 0 : 3 * i - 1) + (j - (i == 0 ? 0 : i - 1));
            red[(w * 18 + idx) * 64 + lane] = mfma16(pk[i], vv[j], z4);
          }
        }
        red[(w * 18 + 13 + i) * 64 + lane] = mfma16(pk[i], ones, z4);
      }
      __syncthreads();
      const float* redf = (const float*)sm;
      float* Pout = P + (size_t)(rt * 8 + cg) * 1680;
      for (int o = tid; o < 1680; o += 256) {
        int h = o / 420, rem = o - h * 420; int idx, ln, rg;
        if (rem < 400) {
          int d = rem / 20, e = rem - d * 20; int Dd = 20 * h + d, E = 20 * h + e; int i = Dd >> 4, j = E >> 4;
          idx = (i == 0 ? 0 : 3 * i - 1) + (j - (i == 0 ? 0 : i - 1)); ln = (E & 15) + 16 * ((Dd & 15) >> 2); rg = Dd & 3;
        } else {
          int Dd = 20 * h + rem - 400; idx = 13 + (Dd >> 4); ln = 16 * ((Dd & 15) >> 2); rg = Dd & 3;
        }
        float s = 0.f;
#pragma unroll
        for (int ww = 0; ww < 4; ww++) s += redf[((ww * 18 + idx) * 64 + ln) * 4 + rg];
        Pout[o] = s;
      }
    } else {
      mainloop(std::false_type{});
#pragma unroll
      for (int i = 0; i < 2; i++) {
        u16* prow = pq + (size_t)(rt * 128 + w * 32 + i * 16 + fr) * D + cg * 160 + 4 * g4;
#pragma unroll
        for (int t = 0; t < 10; t++) {
          u32x2 v;
          v.x = pk2(phi(rbf(acc[i][t][0])), phi(rbf(acc[i][t][1])));
          v.y = pk2(phi(rbf(acc[i][t][2])), phi(rbf(acc[i][t][3])));
          *(u32x2*)(prow + 16 * t) = v;
        }
      }
    }
  } else {
    const int pw = xcd * 8 + (r - 24);
    const int rbase = xcd * 256 + ((r - 24) >> 1) * 64, cbase = ((r - 24) & 1) * 320;
    for (int it = w; it < 10; it += 4) {
      const int ch = cbase + (it >> 1) * 64 + (lane & 7) * 8;
      const int s0 = rbase + (it & 1) * 32 + (lane >> 3) * 4;
      u32x4 kr[7], vr[4];
#pragma unroll
      for (int j = 0; j < 7; j++) { int s = s0 - 3 + j; kr[j] = s >= 0 ? *(const u32x4*)(Bk + (size_t)s * D + ch) : (u32x4){0, 0, 0, 0}; }
#pragma unroll
      for (int j = 0; j < 4; j++) vr[j] = *(const u32x4*)(Bv + (size_t)(s0 + j) * D + ch);
      u32x2 sw[8];
      { const u32x4* sp = (const u32x4*)(shk + ch * 4);
#pragma unroll
        for (int j = 0; j < 4; j++) { u32x4 t = sp[j]; sw[2 * j] = (u32x2){t.x, t.y}; sw[2 * j + 1] = (u32x2){t.z, t.w}; } }
#pragma unroll
      for (int c = 0; c < 8; c++) {
        const float w0 = bflo(sw[c].x), w1 = bfhi(sw[c].x), w2 = bflo(sw[c].y), w3 = bfhi(sw[c].y);
        float kk[7], u[4];
#pragma unroll
        for (int j = 0; j < 7; j++) { unsigned d = kr[j][c >> 1]; kk[j] = (c & 1) ? bfhi(d) : bflo(d); }
#pragma unroll
        for (int j = 0; j < 4; j++) {
          unsigned d = vr[j][c >> 1]; float v = (c & 1) ? bfhi(d) : bflo(d);
          float ks = kk[j] * w3; ks = fmaf(kk[j + 1], w2, ks); ks = fmaf(kk[j + 2], w1, ks); ks = fmaf(kk[j + 3], w0, ks);
          u[j] = rbf(ks) * v;
        }
        u32x2 ov = {pk2(u[0], u[1]), pk2(u[2], u[3])};
        *(u32x2*)(uT + (size_t)(ch + c) * S + s0) = ov;
      }
    }
    {
      u32x4 g0 = *(const u32x4*)(gw + lane * 8), g1 = lane < 16 ? *(const u32x4*)(gw + 512 + lane * 8) : (u32x4){0, 0, 0, 0};
#pragma unroll
      for (int i = 0; i < 8; i++) {
        const int s = pw * 32 + w * 8 + i;
        u32x4 x0 = *(const u32x4*)(x + (size_t)s * D + lane * 8), x1 = lane < 16 ? *(const u32x4*)(x + (size_t)s * D + 512 + lane * 8) : (u32x4){0, 0, 0, 0};
        float a = 0.f;
#pragma unroll
        for (int j = 0; j < 4; j++) {
          a += rbf(bflo(x0[j]) * bflo(g0[j])) + rbf(bfhi(x0[j]) * bfhi(g0[j]));
          a += rbf(bflo(x1[j]) * bflo(g1[j])) + rbf(bfhi(x1[j]) * bfhi(g1[j]));
        }
#pragma unroll
        for (int m = 32; m >= 1; m >>= 1) a += __shfl_xor(a, m);
        if (lane == 0) { float t = rbf(a); G[s] = rbf(1.f / (1.f + __expf(-t))); }
      }
    }
    for (int q = pw * 256 + tid; q < 51200; q += 64 * 256) {
      int ln = q & 63, f = q >> 6; int t = f % 5, ks = (f / 5) % 20, ct = f / 100;
      const u16* src = wo + (size_t)(32 * ks + 8 * (ln >> 4)) * D + 80 * ct + 16 * t + (ln & 15);
      u32x4 v;
      v.x = src[0] | ((unsigned)src[D] << 16); v.y = src[2 * D] | ((unsigned)src[3 * D] << 16);
      v.z = src[4 * D] | ((unsigned)src[5 * D] << 16); v.w = src[6 * D] | ((unsigned)src[7 * D] << 16);
      *(u32x4*)(woP + (size_t)q * 8) = v;
    }
  }
}

constexpr int NOWG = 64;
constexpr int CPS = 4160;
constexpr int UOFF = 4 * CPS;
constexpr int K2_LDS = 2 * 80 * 208 > 24576 ? 2 * 80 * 208 : 24576;

__global__ __launch_bounds__(256) void k2(const u16* __restrict__ pq, const float* __restrict__ P, const u16* __restrict__ uT,
                                          const u16* __restrict__ hk, const u16* __restrict__ Dv, u16* __restrict__ o, u16* __restrict__ yT) {
  extern __shared__ __attribute__((aligned(16))) char sm[];
  const int tid = threadIdx.x, lane = tid & 63;
  const int w = __builtin_amdgcn_readfirstlane(tid >> 6);
  const int fr = lane & 15, g4 = lane >> 4;
  const unsigned lb = (unsigned)(size_t)(LAS char*)sm;
  if (blockIdx.x < NOWG) {
    const int g = blockIdx.x & 7, tch = blockIdx.x >> 3;
    for (int i = tid; i < 2 * 80 * 208 / 16; i += 256) ((u32x4*)sm)[i] = (u32x4){0, 0, 0, 0};
    float st[7];
#pragma unroll
    for (int i = 0; i < 7; i++) {
      int o_ = tid + i * 256; float s = 0.f;
      if (o_ < 1680) {
#pragma unroll
        for (int rt = 0; rt < 16; rt++) s += P[(size_t)(rt * 8 + g) * 1680 + o_];
      }
      st[i] = s;
    }
    __syncthreads();
    u16* KVT = (u16*)sm; u16* ZT = KVT + 80 * 104;
#pragma unroll
    for (int i = 0; i < 7; i++) {
      int o_ = tid + i * 256;
      if (o_ < 1680) {
        int h = o_ / 420, rem = o_ - h * 420; u16 bv = (u16)(pk2(st[i], 0.f) & 0xffff);
        if (rem < 400) { int d = rem / 20, e = rem - d * 20; KVT[(20 * h + e) * 104 + 20 * h + d] = bv; }
        else { int d = rem - 400; for (int e = 0; e < 20; e++) ZT[(20 * h + e) * 104 + 20 * h + d] = bv; }
      }
    }
    __syncthreads();
    constexpr int PJ[9] = {0, 1, 1, 2, 2, 3, 3, 4, 4}, PK[9] = {0, 0, 1, 0, 1, 1, 2, 1, 2};
    bf16x8 An[9], Ad[9];
#pragma unroll
    for (int p = 0; p < 9; p++) {
      unsigned a = lb + (16 * PJ[p] + fr) * 208 + (32 * PK[p] + 8 * g4) * 2;
      An[p] = lds_b128(a); Ad[p] = lds_b128(a + 80 * 208);
    }
#pragma unroll 2
    for (int i = 0; i < 4; i++) {
      const int s = tch * 256 + w * 64 + i * 16 + fr;
      const u16* prow = pq + (size_t)s * D + 80 * g + 8 * g4;
      bf16x8 B[3];
#pragma unroll
      for (int ks = 0; ks < 3; ks++) B[ks] = *(const bf16x8*)(prow + 32 * ks);
      if (g4 >= 2) B[2] = __builtin_bit_cast(bf16x8, (u32x4){0, 0, 0, 0});
      f32x4 nu[5], de[5];
#pragma unroll
      for (int j = 0; j < 5; j++) { nu[j] = (f32x4){0.f, 0.f, 0.f, 0.f}; de[j] = nu[j]; }
#pragma unroll
      for (int p = 0; p < 9; p++) { nu[PJ[p]] = mfma16(An[p], B[PK[p]], nu[PJ[p]]); de[PJ[p]] = mfma16(Ad[p], B[PK[p]], de[PJ[p]]); }
      u16* orow = o + (size_t)s * D + 80 * g + 4 * g4;
#pragma unroll
      for (int j = 0; j < 5; j++) {
        float r_[4];
#pragma unroll
        for (int q = 0; q < 4; q++) r_[q] = rbf(nu[j][q]) * __builtin_amdgcn_rcpf(rbf(rbf(de[j][q]) + 1e-6f));
        *(u32x2*)(orow + 16 * j) = (u32x2){pk2(r_[0], r_[1]), pk2(r_[2], r_[3])};
      }
    }
  } else {
    const int d = blockIdx.x - NOWG;
    {
      const u16* hrow = hk + (size_t)d * S;
      u32x4 cur = *(const u32x4*)(hrow + tid * 8);
      u32x4 prv = tid > 0 ? *(const u32x4*)(hrow + tid * 8 - 8) : (u32x4){0, 0, 0, 0};
      u32x4 uv = *(const u32x4*)(uT + (size_t)d * S + tid * 8);
      unsigned W_[8] = {prv.x, prv.y, prv.z, prv.w, cur.x, cur.y, cur.z, cur.w};
      const int c = 255 - tid;
#pragma unroll
      for (int e = 0; e < 4; e++) {
        u32x4 ov;
#pragma unroll
        for (int q = 0; q < 4; q++) {
          const int i0 = 15 - e - 2 * q, i1 = i0 - 1;
          const unsigned hiw = W_[i0 >> 1], low = W_[i1 >> 1];
          const unsigned sel = ((i0 & 1) ? 0x0706u : 0x0504u) | (((i1 & 1) ? 0x0302u : 0x0100u) << 16);
          ov[q] = __builtin_amdgcn_perm(hiw, low, sel);
        }
        *(u32x4*)(sm + e * CPS + c * 16) = ov;
      }
      if (tid < 8) *(u32x4*)(sm + (tid >> 1) * CPS + (256 + (tid & 1)) * 16) = (u32x4){0, 0, 0, 0};
      *(u32x4*)(sm + UOFF + 512 + tid * 16) = uv;
      if (tid < 32) *(u32x4*)(sm + UOFF + tid * 16) = (u32x4){0, 0, 0, 0};
    }
    __syncthreads();
    f32x4 acc[8];
#pragma unroll
    for (int c = 0; c < 8; c++) acc[c] = (f32x4){0.f, 0.f, 0.f, 0.f};
    const unsigned abase = lb + (3 - (fr & 3)) * CPS + 2 * (2044 - 16 * 112 - 16 * (g4 >> 1) + 8 * (g4 & 1) - (fr & ~3));
    const unsigned bbase = lb + UOFF + 512 + 2 * (16 * fr - 16 * (g4 >> 1) + 8 * (g4 & 1));
#pragma unroll
    for (int cc = 0; cc < 2; cc++) {
      const int rp = 2 * (2 * w + cc);
      const unsigned aa = abase - 32 * rp, ba = bbase - 32 * rp;
      bf16x8 A[8], B[8];
#pragma unroll
      for (int a = 0; a < 8; a++) {
        u32x2 lo = *(const LAS u32x2*)(size_t)(aa + 512 * (7 - a)), hi = *(const LAS u32x2*)(size_t)(aa + 512 * (7 - a) + 8);
        A[a] = mk8(lo, hi); B[a] = lds_b128(ba + 512 * a);
      }
#pragma unroll
      for (int a = 0; a < 8; a++)
#pragma unroll
        for (int i = 0; i + a < 8; i++) acc[a + i] = mfma16(A[a], B[i], acc[a + i]);
    }
    u32x2 uu[2];
#pragma unroll
    for (int cc = 0; cc < 2; cc++) uu[cc] = *(const LAS u32x2*)(size_t)(lb + UOFF + 512 + 2 * (256 * (2 * w + cc) + 16 * fr + 4 * g4));
    __syncthreads();
    f32x4* red = (f32x4*)sm;
#pragma unroll
    for (int c = 0; c < 8; c++) {
      const int ow = c >> 1;
      if (ow != w) { const int slot = (w - ow - 1) & 3; red[(c * 3 + slot) * 64 + lane] = acc[c]; }
    }
    __syncthreads();
    const float Dd = bflo(Dv[d]);
#pragma unroll
    for (int cc = 0; cc < 2; cc++) {
      const int c = 2 * w + cc;
      f32x4 own = (w == 0) ? acc[cc] : (w == 1) ? acc[2 + cc] : (w == 2) ? acc[4 + cc] : acc[6 + cc];
      f32x4 s0 = red[(c * 3 + 0) * 64 + lane], s1 = red[(c * 3 + 1) * 64 + lane], s2 = red[(c * 3 + 2) * 64 + lane];
      f32x4 y = (own + s0) + (s1 + s2);
      float r_[4];
      r_[0] = rbf(y[0]) + rbf(bflo(uu[cc].x) * Dd); r_[1] = rbf(y[1]) + rbf(bfhi(uu[cc].x) * Dd);
      r_[2] = rbf(y[2]) + rbf(bflo(uu[cc].y) * Dd); r_[3] = rbf(y[3]) + rbf(bfhi(uu[cc].y) * Dd);
      *(u32x2*)(yT + (size_t)d * S + 256 * c + 16 * fr + 4 * g4) = (u32x2){pk2(r_[0], r_[1]), pk2(r_[2], r_[3])};
    }
  }
}

constexpr int K3_OS = 8192, K3_WS = 10240, K3_STG = K3_OS + K3_WS, K3_NST = 8, K3_YOFF = K3_STG * K3_NST, K3_LDS = K3_YOFF + 10240;

__global__ __launch_bounds__(256) void k3(const u16* __restrict__ o, const u16* __restrict__ woP, const u16* __restrict__ yT,
                                          const u16* __restrict__ x, const float* __restrict__ G, u16* __restrict__ out) {
  extern __shared__ __attribute__((aligned(16))) char sm[];
  const int tid = threadIdx.x, lane = tid & 63;
  const int w = __builtin_amdgcn_readfirstlane(tid >> 6);
  const int b = blockIdx.x, xcd = b & 7, r = b >> 3;
  const int rt = xcd * 4 + (r & 3), ct = r >> 2;
  const int fr = lane & 15, g4 = lane >> 4;
  const unsigned lb = (unsigned)(size_t)(LAS char*)sm;
  const i32x4 so = make_srd(o, S * D * 2), sw = make_srd(woP, D * D * 2), sy = make_srd(yT, S * D * 2);
  unsigned vo[2];
#pragma unroll
  for (int i = 0; i < 2; i++) { int row = (w * 2 + i) * 8 + (lane >> 3); vo[i] = (rt * 64 + row) * 1280 + (((lane & 7) ^ ((row >> 1) & 7)) << 4); }
  const unsigned vwv = ct * 102400 + lane * 16;
  auto stage = [&](int st, int buf) {
    const unsigned base = lb + buf * K3_STG;
#pragma unroll
    for (int i = 0; i < 2; i++) glds16(base + (w * 2 + i) * 1024, vo[i], so, st * 128);
#pragma unroll
    for (int i = 0; i < 3; i++) { int pc = w + 4 * i; if (pc >= 10) pc = w + 4; glds16(base + K3_OS + pc * 1024, vwv, sw, st * 10240 + pc * 1024); }
  };
  {
#pragma unroll
    for (int i = 0; i < 3; i++) { int pc = w + 4 * i; if (pc >= 10) pc = w + 4; int ch = pc * 8 + (lane >> 3);
      glds16(lb + K3_YOFF + pc * 1024, (ct * 80 + ch) * 4096 + rt * 128 + (lane & 7) * 16, sy, 0); }
  }
  const int srow = rt * 64 + w * 16 + fr;
  u32x2 xv[5];
#pragma unroll
  for (int t = 0; t < 5; t++) xv[t] = *(const u32x2*)(x + (size_t)srow * D + ct * 80 + 16 * t + 4 * g4);
  const float g = G[srow];
#pragma unroll
  for (int s = 0; s < 7; s++) stage(s, s);
  f32x4 acc[5];
#pragma unroll
  for (int t = 0; t < 5; t++) acc[t] = (f32x4){0.f, 0.f, 0.f, 0.f};
  const unsigned oa0 = lb + (w * 16 + fr) * 128 + ((g4 ^ ((fr >> 1) & 7)) << 4);
  const unsigned wa0 = lb + K3_OS + lane * 16;
  auto compute = [&](int buf) {
    const unsigned bo = buf * K3_STG;
#pragma unroll
    for (int kk = 0; kk < 2; kk++) {
      bf16x8 of = lds_b128((oa0 + bo) ^ (kk * 64));
#pragma unroll
      for (int t = 0; t < 5; t++) acc[t] = mfma16(lds_b128(wa0 + bo + (kk * 5 + t) * 1024), of, acc[t]);
    }
  };
  wait_bar<30>(); stage(7, 7); compute(0);
  wait_bar<30>(); stage(8, 0); compute(1);
  wait_bar<30>(); stage(9, 1); compute(2);
  wait_bar<30>(); compute(3);
  wait_bar<25>(); compute(4);
  wait_bar<20>(); compute(5);
  wait_bar<15>(); compute(6);
  wait_bar<10>(); compute(7);
  wait_bar<5>(); compute(0);
  wait_bar<0>(); compute(1);
  const float omg = rbf(1.f - g);
#pragma unroll
  for (int t = 0; t < 5; t++) {
    u32x2 yv = lds_tr(lb + K3_YOFF + (16 * t + 4 * g4 + (fr >> 2)) * 128 + (w * 16 + 4 * (fr & 3)) * 2);
    float xs[4] = {bflo(xv[t].x), bfhi(xv[t].x), bflo(xv[t].y), bfhi(xv[t].y)};
    float ys[4] = {bflo(yv.x), bfhi(yv.x), bflo(yv.y), bfhi(yv.y)};
    float r_[4];
#pragma unroll
    for (int q = 0; q < 4; q++) r_[q] = rbf(g * rbf(acc[t][q])) + rbf(omg * rbf(xs[q] * ys[q]));
    *(u32x2*)(out + (size_t)srow * D + ct * 80 + 16 * t + 4 * g4) = (u32x2){pk2(r_[0], r_[1]), pk2(r_[2], r_[3])};
  }
}

extern "C" void kernel_launch(void* const* d_in, const int* in_sizes, int n_in, void* d_out, int out_size, void* d_ws, size_t ws_size,
                              hipStream_t stream) {
  const u16* x = (const u16*)d_in[0]; const u16* wqkv = (const u16*)d_in[1]; const u16* wo = (const u16*)d_in[2];
  const u16* Bk = (const u16*)d_in[4]; const u16* Bv = (const u16*)d_in[5]; const u16* shk = (const u16*)d_in[6];
  const u16* hk = (const u16*)d_in[7]; const u16* Dv = (const u16*)d_in[8]; const u16* gw = (const u16*)d_in[9];
  char* ws = (char*)d_ws;
  constexpr size_t SZ = (size_t)S * D * 2;
  u16* pq = (u16*)ws; float* P = (float*)(ws + SZ + 4096); u16* uT = (u16*)(ws + SZ + 4096 + (1 << 20));
  float* G = (float*)((char*)uT + SZ); u16* woP = (u16*)((char*)G + 8192); u16* o = (u16*)((char*)woP + (1 << 20)); u16* yT = (u16*)((char*)o + SZ);
  hipFuncSetAttribute((const void*)k1, hipFuncAttributeMaxDynamicSharedMemorySize, K1_LDS);
  hipFuncSetAttribute((const void*)k3, hipFuncAttributeMaxDynamicSharedMemorySize, K3_LDS);
  k1<<<256, 256, K1_LDS, stream>>>(x, wqkv, wo, Bk, Bv, shk, gw, pq, P, uT, G, woP);
  k2<<<NOWG + 640, 256, K2_LDS, stream>>>(pq, P, uT, hk, Dv, o, yT);
  k3<<<256, 256, K3_LDS, stream>>>(o, woP, yT, x, G, (u16*)d_out);
}
```

```cpp
#include <hip/hip_runtime.h>
#include <hip/hip_bf16.h>
#include <type_traits>

typedef unsigned short u16;
typedef __bf16 bf16x2 __attribute__((ext_vector_type(2)));
typedef __bf16 bf16x8 __attribute__((ext_vector_type(8)));
typedef float f32x4 __attribute__((ext_vector_type(4)));
typedef float f32x2 __attribute__((ext_vector_type(2)));
typedef unsigned u32x4 __attribute__((ext_vector_type(4)));
typedef unsigned u32x2 __attribute__((ext_vector_type(2)));
typedef short v4i16_t __attribute__((ext_vector_type(4)));
typedef int i32x4 __attribute__((ext_vector_type(4)));
#define LAS __attribute__((address_space(3)))
#define SBAR() __builtin_amdgcn_sched_barrier(0)
#define SGB(mask, n) __builtin_amdgcn_sched_group_barrier(mask, n, 0)

#define TS_DECL
#define TSARG
#define TS(i)
#define TS_OUT(kid, n)

constexpr int S = 2048, D = 640;

__device__ __forceinline__ unsigned pk2(float a, float b) { bf16x2 r = {(__bf16)a, (__bf16)b}; return __builtin_bit_cast(unsigned, r); }
__device__ __forceinline__ float rbf(float a) { return (float)(__bf16)a; }
__device__ __forceinline__ float bflo(unsigned v) { return __uint_as_float(v << 16); }
__device__ __forceinline__ float bfhi(unsigned v) { return __uint_as_float(v & 0xffff0000u); }
__device__ __forceinline__ float phi(float t) {
  return __builtin_amdgcn_fmed3f(t, 0.f, 3.0e38f) + (__builtin_amdgcn_exp2f(fminf(t * 1.44269504f, 0.f)) + 1e-6f);
}
__device__ __forceinline__ i32x4 make_srd(const void* p, unsigned bytes) {
  unsigned long long a = (unsigned long long)p; i32x4 r;
  r.x = __builtin_amdgcn_readfirstlane((int)(unsigned)a); r.y = __builtin_amdgcn_readfirstlane((int)(unsigned)(a >> 32));
  r.z = __builtin_amdgcn_readfirstlane((int)bytes); r.w = 0x00020000; return r;
}
__device__ __forceinline__ void glds16(unsigned ldsaddr, unsigned voff, i32x4 srd, unsigned soff) {
  asm volatile("s_mov_b32 m0, %0\n\ts_nop 0\n\tbuffer_load_dwordx4 %1, %2, %3 offen lds" :: "s"(ldsaddr), "v"(voff), "s"(srd), "s"(soff) : "memory");
}
template <int OFF> __device__ __forceinline__ void bload16(bf16x8& dst, unsigned voff, i32x4 srd) {
  asm volatile("buffer_load_dwordx4 %0, %1, %2, 0 offen offset:%3" : "=v"(dst) : "v"(voff), "s"(srd), "n"(OFF) : "memory");
}
template <int N> __device__ __forceinline__ void wait_bar() { asm volatile("s_waitcnt vmcnt(%0) lgkmcnt(0)\n\ts_barrier" :: "n"(N) : "memory"); }
__device__ __forceinline__ bf16x8 lds_b128(unsigned addr) { return *(const LAS bf16x8*)(size_t)addr; }
__device__ __forceinline__ u32x2 lds_b64(unsigned addr) { return *(const LAS u32x2*)(size_t)addr; }
__device__ __forceinline__ u32x2 lds_tr(unsigned addr) { return __builtin_bit_cast(u32x2, __builtin_amdgcn_ds_read_tr16_b64_v4i16((LAS v4i16_t*)(size_t)addr)); }
__device__ __forceinline__ bf16x8 mk8(u32x2 lo, u32x2 hi) { u32x4 t = {lo.x, lo.y, hi.x, hi.y}; return __builtin_bit_cast(bf16x8, t); }
__device__ __forceinline__ f32x4 mfma16(bf16x8 a, bf16x8 b, f32x4 c) { return __builtin_amdgcn_mfma_f32_16x16x32_bf16(a, b, c, 0, 0, 0); }

constexpr int XS = 16384, WSB = 20480, STG = XS + WSB;
constexpr int K1_LDS = STG * 4;

template <bool KV>
__device__ __forceinline__ void gemm_compute(f32x4 (&acc)[2][5], unsigned xa0, unsigned wa0, unsigned wa1
) {
  bf16x8 xa[2], xb[2], wa[5], wb[5];
  auto load = [&](bf16x8 (&xf)[2], bf16x8 (&wf)[5], int buf, int kk) {
    const unsigned bo = buf * STG;
    const unsigned xa = (xa0 + bo) ^ (kk * 64);
    xf[0] = lds_b128(xa); xf[1] = lds_b128(xa + 2048);
    const unsigned a0 = wa0 + bo + kk * 10240, a1 = wa1 + bo + kk * 10240;
#pragma unroll
    for (int t = 0; t < 5; t++) { const unsigned a = ((t & 1) ? a1 : a0) + 32 * t; wf[t] = mk8(lds_tr(a), lds_tr(a + 1280)); }
  };
  auto mma = [&](const bf16x8 (&xf)[2], const bf16x8 (&wf)[5]) {
#pragma unroll
    for (int t = 0; t < 5; t++)
#pragma unroll
      for (int i = 0; i < 2; i++) acc[i][t] = KV ? mfma16(xf[i], wf[t], acc[i][t]) : mfma16(wf[t], xf[i], acc[i][t]);
  };
  auto interleave = [&]() {
    SGB(0x8, 1); SGB(0x100, 2); SGB(0x8, 1); SGB(0x100, 2);
#pragma unroll
    for (int t = 0; t < 8; t++) { SGB(0x8, 1); SGB(0x100, 1); }
  };
  asm volatile("s_barrier" ::: "memory");
  TS(4);
  load(xa, wa, 0, 0);
  SBAR();
  for (int s = 0; s < 9; s++) {
    load(xb, wb, s & 3, 1); mma(xa, wa); interleave();
    SBAR();
    asm volatile("s_waitcnt lgkmcnt(0)\n\ts_barrier" ::: "memory");
    load(xa, wa, (s + 1) & 3, 0); mma(xb, wb); interleave();
    SBAR();
  }
  load(xb, wb, 1, 1); mma(xa, wa); interleave();
  SBAR();
  mma(xb, wb);
}

__device__ __forceinline__ void gemm_loader(int L, int lane, unsigned lb, const u16* x, const u16* wqkv, int rt, int cg, bool isKV) {
  const i32x4 sx = make_srd(x, S * D * 2), sw = make_srd(wqkv, D * 3 * D * 2);
  unsigned vx[2], vw[5];
#pragma unroll
  for (int i = 0; i < 2; i++) vx[i] = (rt * 128 + L * 64 + i * 8 + (lane >> 3)) * 1280 + (((lane & 7) ^ ((4 * i + (lane >> 4)) & 7)) << 4);
#pragma unroll
  for (int i = 0; i < 5; i++) {
    const int ci = i * 64 + lane, k = (ci * 3277) >> 16, pos = ci - k * 20, gc = pos ^ (((k >> 3) & 1) << 1), tl = gc >> 1;
    unsigned col;
    if (isKV) { const int isv = (tl >= 3 && tl < 5) || tl >= 7; const int tt = tl < 3 ? tl : tl < 5 ? tl - 3 : tl < 7 ? tl - 2 : tl - 5;
                col = (isv ? 2560u : 1280u) + 160u * cg + tt * 32; }
    else col = 320u * cg + tl * 32;
    vw[i] = (L * 32 + k) * 3840 + col + (gc & 1) * 16;
  }
  auto stage = [&](int st, int buf) {
    const unsigned base = lb + buf * STG;
#pragma unroll
    for (int p = 0; p < 10; p++) {
      glds16(base + XS + (L * 10 + p) * 1024, vw[p % 5], sw, st * (64 * 3840) + (p / 5) * (16 * 3840));
      if (p < 8) glds16(base + (L * 8 + p) * 1024, vx[p & 1], sx, st * 128 + (p >> 1) * (16 * 1280));
    }
  };
  stage(0, 0); stage(1, 1);
  wait_bar<18>();
  stage(2, 2); stage(3, 3);
  for (int s = 0; s < 9; s++) {
    if (s < 7) wait_bar<36>(); else if (s == 7) wait_bar<18>(); else wait_bar<0>();
    if (s < 6) stage(s + 4, s & 3);
  }
}

__global__ __launch_bounds__(640, 3) void k1(const u16* __restrict__ x, const u16* __restrict__ wqkv, const u16* __restrict__ wo,
                                             const u16* __restrict__ Bk, const u16* __restrict__ Bv, const u16* __restrict__ shk,
                                             const u16* __restrict__ gw, u16* __restrict__ pq, float* __restrict__ P,
                                             u16* __restrict__ uT, float* __restrict__ G, u16* __restrict__ woP) {
  extern __shared__ __attribute__((aligned(16))) char sm[];
  const int tid = threadIdx.x, lane = tid & 63;
  const int w = __builtin_amdgcn_readfirstlane(tid >> 6);
  const int b = blockIdx.x, xcd = b & 7, r = b >> 3;
  const int fr = lane & 15, g4 = lane >> 4;
  TS_DECL TS(0);
  if (r < 24) {
    const bool isKV = r < 16;
    const int rt = (xcd & 3) * 4 + (r & 3);
    const int cg = isKV ? (xcd >> 2) * 4 + (r >> 2) : (xcd >> 2) * 2 + ((r - 16) >> 2);
    const unsigned lb = (unsigned)(size_t)(LAS char*)sm;
    if (w >= 8) { gemm_loader(w - 8, lane, lb, x, wqkv, rt, cg, isKV); return; }
    const int rp = w >> 1, hf = w & 1;
    const unsigned xa0 = lb + (rp * 32 + fr) * 128 + ((g4 ^ ((fr >> 1) & 7)) << 4);
    const unsigned wl = lb + XS + (8 * g4 + (fr >> 2)) * 320 + ((lane & 3) >> 1) * 16 + (lane & 1) * 8 + hf * 160;
    const unsigned sgn = (g4 & 1) * 32;
    const unsigned wa0 = hf ? wl - sgn : wl + sgn, wa1 = hf ? wl + sgn : wl - sgn;
    f32x4 acc[2][5];
#pragma unroll
    for (int i = 0; i < 2; i++)
#pragma unroll
      for (int t = 0; t < 5; t++) acc[i][t] = (f32x4){0.f, 0.f, 0.f, 0.f};
    if (isKV) {
      gemm_compute<true>(acc, xa0, wa0, wa1 TSARG);
      TS(1);
      u32x4 fr_[5];
#pragma unroll
      for (int t = 0; t < 5; t++) {
        const bool isk = hf ? t < 2 : t < 3;
        if (isk) fr_[t] = (u32x4){pk2(phi(acc[0][t][0]), phi(acc[0][t][1])), pk2(phi(acc[0][t][2]), phi(acc[0][t][3])),
                                  pk2(phi(acc[1][t][0]), phi(acc[1][t][1])), pk2(phi(acc[1][t][2]), phi(acc[1][t][3]))};
        else fr_[t] = (u32x4){pk2(acc[0][t][0], acc[0][t][1]), pk2(acc[0][t][2], acc[0][t][3]), pk2(acc[1][t][0], acc[1][t][1]), pk2(acc[1][t][2], acc[1][t][3])};
      }
      __syncthreads();
#pragma unroll
      for (int t = 0; t < 5; t++) {
        const int slot = hf ? (t < 2 ? 3 + t : 5 + t) : (t < 3 ? t : 2 + t);
        *(LAS u32x4*)(size_t)(lb + (rp * 10 + slot) * 1024 + lane * 16) = fr_[t];
      }
      __syncthreads();
      TS(2);
      float* Pout = P + (size_t)(rt * 8 + cg) * 1680;
      const u32x4 o1 = {0x3F803F80u, 0x3F803F80u, 0x3F803F80u, 0x3F803F80u};
#pragma unroll
      for (int q = 0; q < 3; q++) {
        const int T = w + 8 * q;
        if (T < 18) {
          const bool isz = T >= 13;
          const int i = isz ? T - 13 : (T + 1) / 3;
          const int j = isz ? 0 : T - (i == 0 ? 0 : 3 * i - 1) + (i == 0 ? 0 : i - 1);
          f32x4 d4 = {0.f, 0.f, 0.f, 0.f};
          const unsigned aA = lb + i * 1024 + lane * 16, aB = lb + (5 + j) * 1024 + lane * 16;
#pragma unroll
          for (int m = 0; m < 4; m++) {
            bf16x8 A = lds_b128(aA + m * 10240);
            bf16x8 B = isz ? __builtin_bit_cast(bf16x8, o1) : lds_b128(aB + m * 10240);
            d4 = mfma16(A, B, d4);
          }
          const int Dg = 16 * i + 4 * g4, h = (Dg * 205) >> 12, dd = Dg - 20 * h;
          const int ee = isz ? 20 : 16 * j + fr - 20 * h;
          if (isz ? fr == 0 : (ee >= 0 && ee < 20)) *(f32x4*)(Pout + h * 420 + ee * 20 + dd) = d4;
        }
      }
      TS(3); TS_OUT(0, 8);
    } else {
      gemm_compute<false>(acc, xa0, wa0, wa1 TSARG);
      TS(1);
#pragma unroll
      for (int i = 0; i < 2; i++) {
        u16* prow = pq + (size_t)(rt * 128 + rp * 32 + i * 16 + fr) * D + cg * 160 + hf * 80 + 4 * g4;
#pragma unroll
        for (int t = 0; t < 5; t++)
          *(u32x2*)(prow + 16 * t) = (u32x2){pk2(phi(acc[i][t][0]), phi(acc[i][t][1])), pk2(phi(acc[i][t][2]), phi(acc[i][t][3]))};
      }
      TS(2); TS(3); TS_OUT(0, 8);
    }
  } else {
    const int pw = xcd * 8 + (r - 24);
    const int rbase = xcd * 256 + ((r - 24) >> 1) * 64, cbase = ((r - 24) & 1) * 320;
    const int ch = cbase + (w >> 1) * 64 + (lane & 7) * 8;
    const int s0 = rbase + (w & 1) * 32 + (lane >> 3) * 4;
    u32x4 kr[7], vr[4], sp[4];
#pragma unroll
    for (int j = 0; j < 7; j++) { int s = s0 - 3 + j; kr[j] = *(const u32x4*)(Bk + (size_t)(s < 0 ? 0 : s) * D + ch); }
#pragma unroll
    for (int j = 0; j < 4; j++) vr[j] = *(const u32x4*)(Bv + (size_t)(s0 + j) * D + ch);
#pragma unroll
    for (int j = 0; j < 4; j++) sp[j] = ((const u32x4*)(shk + ch * 4))[j];
    u32x4 gx0[4], gx1[4];
    const int l16 = lane & 15, wg = w & 7;
#pragma unroll
    for (int i = 0; i < 4; i++) {
      const u16* xr = x + (size_t)(pw * 32 + wg * 4 + i) * D;
      gx0[i] = *(const u32x4*)(xr + lane * 8); gx1[i] = *(const u32x4*)(xr + 512 + l16 * 8);
    }
    const u32x4 g0 = *(const u32x4*)(gw + lane * 8), g1 = *(const u32x4*)(gw + 512 + l16 * 8);
    SBAR();
    if (s0 == 0) { kr[0] = (u32x4){0, 0, 0, 0}; kr[1] = kr[0]; kr[2] = kr[0]; }
#pragma unroll
    for (int c = 0; c < 8; c++) {
      const unsigned s01 = sp[c >> 1][(c & 1) * 2], s23 = sp[c >> 1][(c & 1) * 2 + 1];
      const float w0 = bflo(s01), w1 = bfhi(s01), w2 = bflo(s23), w3 = bfhi(s23);
      float kk[7], u[4];
#pragma unroll
      for (int j = 0; j < 7; j++) { unsigned d = kr[j][c >> 1]; kk[j] = (c & 1) ? bfhi(d) : bflo(d); }
#pragma unroll
      for (int j = 0; j < 4; j++) {
        unsigned d = vr[j][c >> 1]; float v = (c & 1) ? bfhi(d) : bflo(d);
        float ks = kk[j] * w3; ks = fmaf(kk[j + 1], w2, ks); ks = fmaf(kk[j + 2], w1, ks); ks = fmaf(kk[j + 3], w0, ks);
        u[j] = ks * v;
      }
      *(u32x2*)(uT + (size_t)(ch + c) * S + s0) = (u32x2){pk2(u[0], u[1]), pk2(u[2], u[3])};
    }
    TS(1);
    if (w < 8) {
      float a[4];
      const float m1 = lane < 16 ? 1.f : 0.f;
#pragma unroll
      for (int i = 0; i < 4; i++) {
        float a0 = 0.f, a1 = 0.f;
#pragma unroll
        for (int j = 0; j < 4; j++) {
          a0 = fmaf(bflo(gx0[i][j]), bflo(g0[j]), a0); a0 = fmaf(bfhi(gx0[i][j]), bfhi(g0[j]), a0);
          a1 = fmaf(bflo(gx1[i][j]), bflo(g1[j]), a1); a1 = fmaf(bfhi(gx1[i][j]), bfhi(g1[j]), a1);
        }
        a[i] = fmaf(a1, m1, a0);
      }
#pragma unroll
      for (int m = 32; m >= 1; m >>= 1)
#pragma unroll
        for (int i = 0; i < 4; i++) a[i] += __shfl_xor(a[i], m);
      if (lane < 4) {
        float t = lane == 0 ? a[0] : lane == 1 ? a[1] : lane == 2 ? a[2] : a[3];
        G[pw * 32 + w * 4 + lane] = rbf(1.f / (1.f + __expf(-rbf(t))));
      }
    }
    TS(2);
#pragma unroll
    for (int it = 0; it < 2; it++) {
      const int q = pw * 640 + tid + it * 40960;
      if (q < 51200) {
        int f = q >> 6; int f5 = (f * 3277) >> 14, t = f - 5 * f5; int ct = (f5 * 3277) >> 16, ks = f5 - 20 * ct;
        const u16* src = wo + (size_t)(32 * ks + 8 * g4) * D + 80 * ct + 16 * t + fr;
        u32x4 v;
        v.x = src[0] | ((unsigned)src[D] << 16); v.y = src[2 * D] | ((unsigned)src[3 * D] << 16);
        v.z = src[4 * D] | ((unsigned)src[5 * D] << 16); v.w = src[6 * D] | ((unsigned)src[7 * D] << 16);
        *(u32x4*)(woP + (size_t)q * 8) = v;
      }
    }
    TS(3); TS_OUT(0, 4);
  }
}

constexpr int NOWG = 128;
constexpr int CPS = 4160;
constexpr int UOFF = 4 * CPS;
constexpr int K2_LDS = 24576;

__global__ __launch_bounds__(256, 3) void k2(const u16* __restrict__ pq, const float* __restrict__ P, const u16* __restrict__ uT,
                                             const u16* __restrict__ hk, const u16* __restrict__ Dv, u16* __restrict__ o, u16* __restrict__ yT) {
  extern __shared__ __attribute__((aligned(16))) char sm[];
  const int tid = threadIdx.x, lane = tid & 63;
  const int w = __builtin_amdgcn_readfirstlane(tid >> 6);
  const int fr = lane & 15, g4 = lane >> 4;
  const unsigned lb = (unsigned)(size_t)(LAS char*)sm;
  TS_DECL TS(0);
  if (blockIdx.x < NOWG) {
    const int g = blockIdx.x & 7, tch = blockIdx.x >> 3;
    f32x4 st[2];
    const int i0 = tid < 210 ? tid : 0;
    {
      const f32x4* Pg = (const f32x4*)(P + (size_t)g * 1680) + i0;
      st[0] = (f32x4){0.f, 0.f, 0.f, 0.f}; st[1] = st[0];
#pragma unroll
      for (int hf = 0; hf < 2; hf++) {
        f32x4 v0[8], v1[8];
#pragma unroll
        for (int rt = 0; rt < 8; rt++) { v0[rt] = Pg[(hf * 8 + rt) * 8 * 420]; v1[rt] = Pg[(hf * 8 + rt) * 8 * 420 + 210]; }
#pragma unroll
        for (int rt = 0; rt < 8; rt++) { st[0] += v0[rt]; st[1] += v1[rt]; }
      }
    }
    SBAR();
    bf16x8 B[2][3];
#pragma unroll
    for (int i = 0; i < 2; i++) {
      const u16* prow = pq + (size_t)(tch * 128 + w * 32 + i * 16 + fr) * D + 80 * g + 8 * g4;
#pragma unroll
      for (int ks = 0; ks < 3; ks++) B[i][ks] = *(const bf16x8*)(prow + 32 * ks);
    }
    for (int i = tid; i < 6048 / 16; i += 256) ((u32x4*)sm)[i] = (u32x4){0, 0, 0, 0};
    __syncthreads();
    TS(1);
    if (tid < 210) {
#pragma unroll
      for (int q = 0; q < 2; q++) {
        const int i4 = tid + 210 * q;
        const int row = (i4 * 13108) >> 16, d = 4 * (i4 - row * 5);
        *(LAS u32x2*)(size_t)(lb + row * 72 + (8 + d) * 2) = (u32x2){pk2(st[q][0], st[q][1]), pk2(st[q][2], st[q][3])};
      }
    }
    __syncthreads();
    TS(2);
    constexpr int PJ[9] = {0, 1, 1, 2, 2, 3, 3, 4, 4}, PK[9] = {0, 0, 1, 0, 1, 1, 2, 1, 2};
    bf16x8 An[9], Ad[9];
#pragma unroll
    for (int p = 0; p < 9; p++) {
      const int E = 16 * PJ[p] + fr, h = (E * 205) >> 12, e = E - 20 * h;
      int o_ = 32 * PK[p] + 8 * g4 - 20 * h; o_ = o_ < -8 ? -8 : o_ > 20 ? 20 : o_;
      const unsigned an = lb + (h * 21 + e) * 72 + (8 + o_) * 2, ad = lb + (h * 21 + 20) * 72 + (8 + o_) * 2;
      unsigned an8 = an + 8, ad8 = ad + 8;
      asm volatile("" : "+v"(an8), "+v"(ad8));
      An[p] = mk8(lds_b64(an), lds_b64(an8)); Ad[p] = mk8(lds_b64(ad), lds_b64(ad8));
    }
#pragma unroll
    for (int i = 0; i < 2; i++) {
      if (g4 >= 2) B[i][2] = __builtin_bit_cast(bf16x8, (u32x4){0, 0, 0, 0});
      f32x4 nu[5], de[5];
#pragma unroll
      for (int j = 0; j < 5; j++) { nu[j] = (f32x4){0.f, 0.f, 0.f, 0.f}; de[j] = nu[j]; }
#pragma unroll
      for (int p = 0; p < 9; p++) { nu[PJ[p]] = mfma16(An[p], B[i][PK[p]], nu[PJ[p]]); de[PJ[p]] = mfma16(Ad[p], B[i][PK[p]], de[PJ[p]]); }
      u16* orow = o + (size_t)(tch * 128 + w * 32 + i * 16 + fr) * D + 80 * g + 4 * g4;
#pragma unroll
      for (int j = 0; j < 5; j++) {
        const float rd = __builtin_amdgcn_rcpf(rbf(rbf(de[j][0]) + 1e-6f));
        *(u32x2*)(orow + 16 * j) = (u32x2){pk2(rbf(nu[j][0]) * rd, rbf(nu[j][1]) * rd), pk2(rbf(nu[j][2]) * rd, rbf(nu[j][3]) * rd)};
      }
    }
    TS(3); TS_OUT(1, 4);
  } else {
    const int d = blockIdx.x - NOWG;
    float Dd;
    {
      const u16* hrow = hk + (size_t)d * S;
      u32x4 cur = *(const u32x4*)(hrow + tid * 8);
      u32x4 prv = *(const u32x4*)(hrow + (tid > 0 ? tid * 8 - 8 : 0));
      u32x4 uv = *(const u32x4*)(uT + (size_t)d * S + tid * 8);
      Dd = bflo(Dv[d]);
      SBAR();
      if (tid == 0) prv = (u32x4){0, 0, 0, 0};
      unsigned W_[8] = {prv.x, prv.y, prv.z, prv.w, cur.x, cur.y, cur.z, cur.w};
      const int c = 255 - tid;
#pragma unroll
      for (int e = 0; e < 4; e++) {
        u32x4 ov;
#pragma unroll
        for (int q = 0; q < 4; q++) {
          const int i0 = 15 - e - 2 * q, i1 = i0 - 1;
          const unsigned sel = ((i0 & 1) ? 0x0706u : 0x0504u) | (((i1 & 1) ? 0x0302u : 0x0100u) << 16);
          ov[q] = __builtin_amdgcn_perm(W_[i0 >> 1], W_[i1 >> 1], sel);
        }
        *(u32x4*)(sm + e * CPS + c * 16) = ov;
      }
      if (tid < 8) *(u32x4*)(sm + (tid >> 1) * CPS + (256 + (tid & 1)) * 16) = (u32x4){0, 0, 0, 0};
      *(u32x4*)(sm + UOFF + 512 + tid * 16) = uv;
      if (tid < 32) *(u32x4*)(sm + UOFF + tid * 16) = (u32x4){0, 0, 0, 0};
    }
    __syncthreads();
    TS(1);
    f32x4 acc[8];
#pragma unroll
    for (int c = 0; c < 8; c++) acc[c] = (f32x4){0.f, 0.f, 0.f, 0.f};
    const unsigned abase = lb + (3 - (fr & 3)) * CPS + 2 * (2044 - 16 * 112 - 16 * (g4 >> 1) + 8 * (g4 & 1) - (fr & ~3)) - 128 * w;
    const unsigned bbase = lb + UOFF + 512 + 2 * (16 * fr - 16 * (g4 >> 1) + 8 * (g4 & 1)) - 128 * w;
#pragma unroll
    for (int cc = 0; cc < 2; cc++) {
      const unsigned aa = abase - 64 * cc, ba = bbase - 64 * cc;
      u32x2 al[8], ah[8]; u32x4 B[8];
      asm volatile(
          "ds_read_b128 %16, %25\n\tds_read_b64 %0, %24\n\tds_read_b64 %8, %24 offset:8\n\t"
          "ds_read_b128 %17, %25 offset:512\n\tds_read_b64 %1, %24 offset:512\n\tds_read_b64 %9, %24 offset:520\n\t"
          "ds_read_b128 %18, %25 offset:1024\n\tds_read_b64 %2, %24 offset:1024\n\tds_read_b64 %10, %24 offset:1032\n\t"
          "ds_read_b128 %19, %25 offset:1536\n\tds_read_b64 %3, %24 offset:1536\n\tds_read_b64 %11, %24 offset:1544\n\t"
          "ds_read_b128 %20, %25 offset:2048\n\tds_read_b64 %4, %24 offset:2048\n\tds_read_b64 %12, %24 offset:2056\n\t"
          "ds_read_b128 %21, %25 offset:2560\n\tds_read_b64 %5, %24 offset:2560\n\tds_read_b64 %13, %24 offset:2568\n\t"
          "ds_read_b128 %22, %25 offset:3072\n\tds_read_b64 %6, %24 offset:3072\n\tds_read_b64 %14, %24 offset:3080\n\t"
          "ds_read_b128 %23, %25 offset:3584\n\tds_read_b64 %7, %24 offset:3584\n\tds_read_b64 %15, %24 offset:3592\n\t"
          "s_waitcnt lgkmcnt(0)"
          : "=&v"(al[0]), "=&v"(al[1]), "=&v"(al[2]), "=&v"(al[3]), "=&v"(al[4]), "=&v"(al[5]), "=&v"(al[6]), "=&v"(al[7]),
            "=&v"(ah[0]), "=&v"(ah[1]), "=&v"(ah[2]), "=&v"(ah[3]), "=&v"(ah[4]), "=&v"(ah[5]), "=&v"(ah[6]), "=&v"(ah[7]),
            "=&v"(B[0]), "=&v"(B[1]), "=&v"(B[2]), "=&v"(B[3]), "=&v"(B[4]), "=&v"(B[5]), "=&v"(B[6]), "=&v"(B[7])
          : "v"(aa), "v"(ba) : "memory");
#pragma unroll
      for (int a = 0; a < 8; a++)
#pragma unroll
        for (int i = 0; i + a < 8; i++) acc[a + i] = mfma16(mk8(al[7 - a], ah[7 - a]), __builtin_bit_cast(bf16x8, B[i]), acc[a + i]);
    }
    TS(2);
    u32x2 uu[2];
#pragma unroll
    for (int cc = 0; cc < 2; cc++) uu[cc] = lds_b64(lb + UOFF + 512 + 2 * (256 * (2 * w + cc) + 16 * fr + 4 * g4));
    __syncthreads();
#pragma unroll
    for (int k = 1; k < 4; k++) {
      const int ow = (w + k) & 3;
      f32x4 v0 = ow == 0 ? acc[0] : ow == 1 ? acc[2] : ow == 2 ? acc[4] : acc[6];
      f32x4 v1 = ow == 0 ? acc[1] : ow == 1 ? acc[3] : ow == 2 ? acc[5] : acc[7];
      *(LAS f32x4*)(size_t)(lb + ((2 * ow) * 3 + (3 - k)) * 1024 + lane * 16) = v0;
      *(LAS f32x4*)(size_t)(lb + ((2 * ow + 1) * 3 + (3 - k)) * 1024 + lane * 16) = v1;
    }
    __syncthreads();
#pragma unroll
    for (int cc = 0; cc < 2; cc++) {
      const int c = 2 * w + cc;
      f32x4 own = (w == 0) ? acc[cc] : (w == 1) ? acc[2 + cc] : (w == 2) ? acc[4 + cc] : acc[6 + cc];
      const unsigned ra = lb + c * 3072 + lane * 16;
      f32x4 s0 = *(const LAS f32x4*)(size_t)ra, s1 = *(const LAS f32x4*)(size_t)(ra + 1024), s2 = *(const LAS f32x4*)(size_t)(ra + 2048);
      f32x4 y = (own + s0) + (s1 + s2);
      float r_[4];
      r_[0] = rbf(y[0]) + rbf(bflo(uu[cc].x) * Dd); r_[1] = rbf(y[1]) + rbf(bfhi(uu[cc].x) * Dd);
      r_[2] = rbf(y[2]) + rbf(bflo(uu[cc].y) * Dd); r_[3] = rbf(y[3]) + rbf(bfhi(uu[cc].y) * Dd);
      *(u32x2*)(yT + (size_t)d * S + 256 * c + 16 * fr + 4 * g4) = (u32x2){pk2(r_[0], r_[1]), pk2(r_[2], r_[3])};
    }
    TS(3); TS_OUT(1, 4);
  }
}

constexpr int K3_YOFF = 102400, K3_RED = K3_YOFF + 10240, K3_LDS = K3_RED + 20480;

__global__ __launch_bounds__(512, 2) void k3(const u16* __restrict__ o, const u16* __restrict__ woP, const u16* __restrict__ yT,
                                             const u16* __restrict__ x, const float* __restrict__ G, u16* __restrict__ out) {
  extern __shared__ __attribute__((aligned(16))) char sm[];
  const int tid = threadIdx.x, lane = tid & 63;
  const int w = __builtin_amdgcn_readfirstlane(tid >> 6);
  const int b = blockIdx.x, xcd = b & 7, r = b >> 3;
  const int rt = xcd * 4 + (r & 3), ct = r >> 2;
  const int fr = lane & 15, g4 = lane >> 4;
  const int sw_ = w & 3, kk = w >> 2;
  const unsigned lb = (unsigned)(size_t)(LAS char*)sm;
  TS_DECL TS(0);
  const i32x4 sw = make_srd(woP, D * D * 2), sy = make_srd(yT, S * D * 2);
  const int srow = rt * 64 + sw_ * 16 + fr;
  const unsigned vwv = ct * 102400 + lane * 16;
  bf16x8 of[10];
  const i32x4 so = make_srd(o, S * D * 2);
  const unsigned vo = srow * 1280 + kk * 64 + 16 * g4;
  bload16<0>(of[0], vo, so); bload16<128>(of[1], vo, so); bload16<256>(of[2], vo, so); bload16<384>(of[3], vo, so); bload16<512>(of[4], vo, so);
  SBAR();
#pragma unroll
  for (int i = 0; i < 7; i++) { const int pc = w + 8 * i; if (pc < 50) glds16(lb + pc * 1024, vwv, sw, pc * 1024); }
  SBAR();
  bload16<640>(of[5], vo, so); bload16<768>(of[6], vo, so); bload16<896>(of[7], vo, so); bload16<1024>(of[8], vo, so); bload16<1152>(of[9], vo, so);
  SBAR();
#pragma unroll
  for (int i = 0; i < 7; i++) { const int pc = 50 + w + 8 * i; if (pc < 100) glds16(lb + pc * 1024, vwv, sw, pc * 1024); }
  glds16(lb + K3_YOFF + w * 1024, (ct * 80 + w * 8 + (lane >> 3)) * 4096 + rt * 128 + (lane & 7) * 16, sy, 0);
  if (w < 2) glds16(lb + K3_YOFF + (8 + w) * 1024, (ct * 80 + (8 + w) * 8 + (lane >> 3)) * 4096 + rt * 128 + (lane & 7) * 16, sy, 0);
  SBAR();
  u32x2 xv[3];
#pragma unroll
  for (int t = 0; t < 3; t++) { const int tt = kk ? (t < 2 ? 3 + t : 4) : t; xv[t] = *(const u32x2*)(x + (size_t)srow * D + ct * 80 + 16 * tt + 4 * g4); }
  const float g = G[srow];
  SBAR();
  TS(1);
  f32x4 acc[5];
#pragma unroll
  for (int t = 0; t < 5; t++) acc[t] = (f32x4){0.f, 0.f, 0.f, 0.f};
  const unsigned wa0 = lb + kk * 5120 + lane * 16;
  if (w < 2) wait_bar<18>(); else wait_bar<16>();
  asm volatile("" : "+v"(of[0]), "+v"(of[1]), "+v"(of[2]), "+v"(of[3]), "+v"(of[4]));
  TS(2);
  auto half = [&](int s0) {
    bf16x8 wf[5][5];
#pragma unroll
    for (int s = 0; s < 5; s++)
#pragma unroll
      for (int t = 0; t < 5; t++) wf[s][t] = lds_b128(wa0 + (s0 + s) * 10240 + t * 1024);
#pragma unroll
    for (int s = 0; s < 5; s++)
#pragma unroll
      for (int t = 0; t < 5; t++) acc[t] = mfma16(wf[s][t], of[s0 + s], acc[t]);
    SGB(0x100, 10);
#pragma unroll
    for (int i = 0; i < 15; i++) { SGB(0x8, 1); SGB(0x100, 1); }
    SGB(0x8, 10);
  };
  half(0);
  SBAR();
  wait_bar<0>();
  asm volatile("" : "+v"(of[5]), "+v"(of[6]), "+v"(of[7]), "+v"(of[8]), "+v"(of[9]));
  half(5);
  TS(3);
  {
    const unsigned ra = lb + K3_RED + (sw_ * 5) * 1024 + lane * 16;
    if (kk == 0) { *(LAS f32x4*)(size_t)(ra + 3 * 1024) = acc[3]; *(LAS f32x4*)(size_t)(ra + 4 * 1024) = acc[4]; }
    else { *(LAS f32x4*)(size_t)(ra) = acc[0]; *(LAS f32x4*)(size_t)(ra + 1024) = acc[1]; *(LAS f32x4*)(size_t)(ra + 2048) = acc[2]; }
    __syncthreads();
    const float omg = rbf(1.f - g);
    const int nt = kk ? 2 : 3;
#pragma unroll
    for (int t = 0; t < 3; t++) {
      if (t < nt) {
        const int tt = kk ? 3 + t : t;
        f32x4 a = *(const LAS f32x4*)(size_t)(ra + tt * 1024) + (kk ? acc[3 + (t < 2 ? t : 1)] : acc[t]);
        u32x2 yv = lds_tr(lb + K3_YOFF + (16 * tt + 4 * g4 + (fr >> 2)) * 128 + (sw_ * 16 + 4 * (fr & 3)) * 2);
        float xs[4] = {bflo(xv[t].x), bfhi(xv[t].x), bflo(xv[t].y), bfhi(xv[t].y)};
        float ys[4] = {bflo(yv.x), bfhi(yv.x), bflo(yv.y), bfhi(yv.y)};
        float r_[4];
#pragma unroll
        for (int q = 0; q < 4; q++) r_[q] = rbf(g * rbf(a[q])) + rbf(omg * rbf(xs[q] * ys[q]));
        *(u32x2*)(out + (size_t)srow * D + ct * 80 + 16 * tt + 4 * g4) = (u32x2){pk2(r_[0], r_[1]), pk2(r_[2], r_[3])};
      }
    }
  }
  TS(4); TS_OUT(2, 5);
}

extern "C" void kernel_launch(void* const* d_in, const int* in_sizes, int n_in, void* d_out, int out_size, void* d_ws, size_t ws_size,
                              hipStream_t stream) {
  const u16* x = (const u16*)d_in[0]; const u16* wqkv = (const u16*)d_in[1]; const u16* wo = (const u16*)d_in[2];
  const u16* Bk = (const u16*)d_in[4]; const u16* Bv = (const u16*)d_in[5]; const u16* shk = (const u16*)d_in[6];
  const u16* hk = (const u16*)d_in[7]; const u16* Dv = (const u16*)d_in[8]; const u16* gw = (const u16*)d_in[9];
  char* ws = (char*)d_ws;
  constexpr size_t SZ = (size_t)S * D * 2;
  u16* pq = (u16*)ws; float* P = (float*)(ws + SZ + 4096); u16* uT = (u16*)(ws + SZ + 4096 + (1 << 20));
  float* G = (float*)((char*)uT + SZ); u16* woP = (u16*)((char*)G + 8192); u16* o = (u16*)((char*)woP + (1 << 20)); u16* yT = (u16*)((char*)o + SZ);
  (void)hipFuncSetAttribute((const void*)k1, hipFuncAttributeMaxDynamicSharedMemorySize, K1_LDS);
  (void)hipFuncSetAttribute((const void*)k3, hipFuncAttributeMaxDynamicSharedMemorySize, K3_LDS);
  k1<<<256, 640, K1_LDS, stream>>>(x, wqkv, wo, Bk, Bv, shk, gw, pq, P, uT, G, woP);
  k2<<<NOWG + 640, 256, K2_LDS, stream>>>(pq, P, uT, hk, Dv, o, yT);
  k3<<<256, 512, K3_LDS, stream>>>(o, woP, yT, x, G, (u16*)d_out);
}
```

```cpp
#include <hip/hip_runtime.h>
#include <hip/hip_bf16.h>
#include <type_traits>

typedef unsigned short u16;
typedef __bf16 bf16x2 __attribute__((ext_vector_type(2)));
typedef __bf16 bf16x8 __attribute__((ext_vector_type(8)));
typedef float f32x4 __attribute__((ext_vector_type(4)));
typedef unsigned u32x4 __attribute__((ext_vector_type(4)));
typedef unsigned u32x2 __attribute__((ext_vector_type(2)));
typedef short v4i16_t __attribute__((ext_vector_type(4)));
typedef int i32x4 __attribute__((ext_vector_type(4)));
typedef __attribute__((address_space(1))) unsigned gu32;
#define LAS __attribute__((address_space(3)))
#define SBAR() __builtin_amdgcn_sched_barrier(0)
#define SGB(mask, n) __builtin_amdgcn_sched_group_barrier(mask, n, 0)
#define BAR() asm volatile("s_waitcnt lgkmcnt(0)\n\ts_barrier" ::: "memory")
#define DRAIN() asm volatile("s_waitcnt vmcnt(0)" ::: "memory")
#define SC1 16

#define TS_DECL
#define TSARG
#define TSPAR
#define TS(i)
#define TS_OUT(n)

constexpr int S = 2048, D = 640;
constexpr unsigned SZ = S * D * 2;
constexpr unsigned F_E = 0, F_U = 128, F_PQ = F_U + 64 * 128, F_P = F_PQ + 64 * 128, F_O = F_P + 128 * 128, F_Y = F_O + 128 * 128;
constexpr unsigned OFF_PQ = 0x20000, OFF_P = OFF_PQ + SZ + 4096, OFF_UT = OFF_P + (1u << 20), OFF_G = OFF_UT + SZ, OFF_WOP = OFF_G + 8192,
                   OFF_O = OFF_WOP + (1u << 20), OFF_YT = OFF_O + SZ, WS_USED = OFF_YT + SZ;
constexpr int XS = 16384, WSB = 20480, STG = XS + WSB;
constexpr int L_EXTRA = 4 * STG;
constexpr int L_TAG = L_EXTRA, L_T = L_EXTRA + 64;
constexpr int LDS_TOTAL = L_T + 6144;
constexpr int CPS = 4160, UOFF = 4 * CPS, CHS = UOFF + 4608;
constexpr int L_Y = 102400, L_RED = L_Y + 10240;

__device__ __forceinline__ unsigned pk2(float a, float b) { bf16x2 r = {(__bf16)a, (__bf16)b}; return __builtin_bit_cast(unsigned, r); }
__device__ __forceinline__ float rbf(float a) { return (float)(__bf16)a; }
__device__ __forceinline__ float bflo(unsigned v) { return __uint_as_float(v << 16); }
__device__ __forceinline__ float bfhi(unsigned v) { return __uint_as_float(v & 0xffff0000u); }
__device__ __forceinline__ float phi(float t) {
  return __builtin_amdgcn_fmed3f(t, 0.f, 3.0e38f) + (__builtin_amdgcn_exp2f(fminf(t * 1.44269504f, 0.f)) + 1e-6f);
}
__device__ __forceinline__ i32x4 make_srd(const void* p, unsigned bytes) {
  unsigned long long a = (unsigned long long)p; i32x4 r;
  r.x = __builtin_amdgcn_readfirstlane((int)(unsigned)a); r.y = __builtin_amdgcn_readfirstlane((int)(unsigned)(a >> 32));
  r.z = __builtin_amdgcn_readfirstlane((int)bytes); r.w = 0x00020000; return r;
}
__device__ __forceinline__ void glds16(unsigned ldsaddr, unsigned voff, i32x4 srd, unsigned soff) {
  asm volatile("s_mov_b32 m0, %0\n\ts_nop 0\n\tbuffer_load_dwordx4 %1, %2, %3 offen lds" :: "s"(ldsaddr), "v"(voff), "s"(srd), "s"(soff) : "memory");
}
__device__ __forceinline__ void glds16_sc1(unsigned ldsaddr, unsigned voff, i32x4 srd, unsigned soff) {
  asm volatile("s_mov_b32 m0, %0\n\ts_nop 0\n\tbuffer_load_dwordx4 %1, %2, %3 offen sc1 lds" :: "s"(ldsaddr), "v"(voff), "s"(srd), "s"(soff) : "memory");
}
template <int N> __device__ __forceinline__ void wait_bar() { asm volatile("s_waitcnt vmcnt(%0) lgkmcnt(0)\n\ts_barrier" :: "n"(N) : "memory"); }
__device__ __forceinline__ bf16x8 lds_b128(unsigned addr) { return *(const LAS bf16x8*)(size_t)addr; }
__device__ __forceinline__ u32x2 lds_b64(unsigned addr) { return *(const LAS u32x2*)(size_t)addr; }
__device__ __forceinline__ u32x2 lds_tr(unsigned addr) { return __builtin_bit_cast(u32x2, __builtin_amdgcn_ds_read_tr16_b64_v4i16((LAS v4i16_t*)(size_t)addr)); }
__device__ __forceinline__ bf16x8 mk8(u32x2 lo, u32x2 hi) { u32x4 t = {lo.x, lo.y, hi.x, hi.y}; return __builtin_bit_cast(bf16x8, t); }
__device__ __forceinline__ f32x4 mfma16(bf16x8 a, bf16x8 b, f32x4 c) { return __builtin_amdgcn_mfma_f32_16x16x32_bf16(a, b, c, 0, 0, 0); }

__device__ __forceinline__ unsigned flag_ld(const char* ws, unsigned off) { return __hip_atomic_load((const gu32*)(ws + off), __ATOMIC_RELAXED, __HIP_MEMORY_SCOPE_AGENT); }
__device__ __forceinline__ void flag_st(char* ws, unsigned off, unsigned v) { __hip_atomic_store((gu32*)(ws + off), v, __ATOMIC_RELAXED, __HIP_MEMORY_SCOPE_AGENT); }
__device__ __forceinline__ void poll(const char* ws, unsigned off, bool act, unsigned tag) {
  for (int spins = 0; spins < (1 << 18); spins++) {
    const unsigned v = act ? flag_ld(ws, off) : tag;
    if (__all(v == tag)) break;
    __builtin_amdgcn_s_sleep(2);
  }
  asm volatile("" ::: "memory");
}

template <bool KV>
__device__ __forceinline__ void gemm_compute(f32x4 (&acc)[4][5], unsigned xa0, unsigned wa0, unsigned wa1) {
  bf16x8 xa[4], xb[4], wa[5], wb[5];
  auto load = [&](bf16x8 (&xf)[4], bf16x8 (&wf)[5], int buf, int kk) {
    const unsigned bo = buf * STG;
    const unsigned xa = (xa0 + bo) ^ (kk * 64);
    const unsigned a0 = wa0 + bo + kk * 10240, a1 = wa1 + bo + kk * 10240;
#pragma unroll
    for (int i = 0; i < 4; i++) xf[i] = lds_b128(xa + 2048 * i);
#pragma unroll
    for (int t = 0; t < 5; t++) { const unsigned a = ((t & 1) ? a1 : a0) + 32 * t; wf[t] = mk8(lds_tr(a), lds_tr(a + 1280)); }
  };
  auto mma = [&](const bf16x8 (&xf)[4], const bf16x8 (&wf)[5]) {
#pragma unroll
    for (int t = 0; t < 5; t++)
#pragma unroll
      for (int i = 0; i < 4; i++) acc[i][t] = KV ? mfma16(xf[i], wf[t], acc[i][t]) : mfma16(wf[t], xf[i], acc[i][t]);
  };
  auto interleave = [&]() {
#pragma unroll
    for (int t = 0; t < 14; t++) { SGB(0x8, 1); SGB(0x100, 1); }
    SGB(0x8, 6);
  };
  BAR();
  load(xa, wa, 0, 0);
  SBAR();
  for (int s = 0; s < 9; s++) {
    load(xb, wb, s & 3, 1); mma(xa, wa); interleave();
    SBAR();
    BAR();
    load(xa, wa, (s + 1) & 3, 0); mma(xb, wb); interleave();
    SBAR();
  }
  load(xb, wb, 1, 1); mma(xa, wa); interleave();
  SBAR();
  mma(xb, wb);
}

__device__ __forceinline__ void gemm_loader(int L, int lane, unsigned lb, const u16* x, const u16* wqkv, int rt, int cg, bool isKV) {
  const i32x4 sx = make_srd(x, S * D * 2), sw = make_srd(wqkv, D * 3 * D * 2);
  unsigned vx[2], vw[5];
#pragma unroll
  for (int i = 0; i < 2; i++) vx[i] = (rt * 128 + L * 64 + i * 8 + (lane >> 3)) * 1280 + (((lane & 7) ^ ((4 * i + (lane >> 4)) & 7)) << 4);
#pragma unroll
  for (int i = 0; i < 5; i++) {
    const int ci = i * 64 + lane, k = (ci * 3277) >> 16, pos = ci - k * 20, gc = pos ^ (((k >> 3) & 1) << 1), tl = gc >> 1;
    unsigned col;
    if (isKV) { const int isv = (tl >= 3 && tl < 5) || tl >= 7; const int tt = tl < 3 ? tl : tl < 5 ? tl - 3 : tl < 7 ? tl - 2 : tl - 5;
                col = (isv ? 2560u : 1280u) + 160u * cg + tt * 32; }
    else col = 320u * cg + tl * 32;
    vw[i] = (L * 32 + k) * 3840 + col + (gc & 1) * 16;
  }
  auto stage = [&](int st, int buf) {
    const unsigned base = lb + buf * STG;
#pragma unroll
    for (int p = 0; p < 10; p++) {
      glds16(base + XS + (L * 10 + p) * 1024, vw[p % 5], sw, st * (64 * 3840) + (p / 5) * (16 * 3840));
      if (p < 8) glds16(base + (L * 8 + p) * 1024, vx[p & 1], sx, st * 128 + (p >> 1) * (16 * 1280));
    }
  };
  stage(0, 0); stage(1, 1);
  wait_bar<18>();
  stage(2, 2); stage(3, 3);
  for (int s = 0; s < 9; s++) {
    if (s < 7) wait_bar<36>(); else if (s == 7) wait_bar<18>(); else wait_bar<0>();
    if (s < 6) stage(s + 4, s & 3);
  }
}

__device__ __forceinline__ void conv_prep_h(const u16* __restrict__ hrow, unsigned cb, int lane) {
  u32x4 cur[4], prv[4];
#pragma unroll
  for (int it = 0; it < 4; it++) {
    const int i = it * 64 + lane;
    cur[it] = *(const u32x4*)(hrow + i * 8); prv[it] = *(const u32x4*)(hrow + (i > 0 ? i * 8 - 8 : 0));
  }
  SBAR();
  if (lane == 0) prv[0] = (u32x4){0, 0, 0, 0};
#pragma unroll
  for (int it = 0; it < 4; it++) {
    const unsigned W_[8] = {prv[it].x, prv[it].y, prv[it].z, prv[it].w, cur[it].x, cur[it].y, cur[it].z, cur[it].w};
    const int c = 255 - (it * 64 + lane);
#pragma unroll
    for (int e = 0; e < 4; e++) {
      u32x4 ov;
#pragma unroll
      for (int q = 0; q < 4; q++) {
        const int i0 = 15 - e - 2 * q, i1 = i0 - 1;
        const unsigned sel = ((i0 & 1) ? 0x0706u : 0x0504u) | (((i1 & 1) ? 0x0302u : 0x0100u) << 16);
        ov[q] = __builtin_amdgcn_perm(W_[i0 >> 1], W_[i1 >> 1], sel);
      }
      *(LAS u32x4*)(size_t)(cb + e * CPS + c * 16) = ov;
    }
  }
  if (lane < 8) *(LAS u32x4*)(size_t)(cb + (lane >> 1) * CPS + (256 + (lane & 1)) * 16) = (u32x4){0, 0, 0, 0};
  if (lane < 32) *(LAS u32x4*)(size_t)(cb + UOFF + lane * 16) = (u32x4){0, 0, 0, 0};
}
__device__ __forceinline__ void conv_load_u(__amdgpu_buffer_rsrc_t rw, int d, unsigned cb, int lane) {
  u32x4 uv[4];
#pragma unroll
  for (int it = 0; it < 4; it++) uv[it] = __builtin_amdgcn_raw_buffer_load_b128(rw, OFF_UT + d * 4096 + (it * 64 + lane) * 16, 0, SC1);
#pragma unroll
  for (int it = 0; it < 4; it++) *(LAS u32x4*)(size_t)(cb + UOFF + 512 + (it * 64 + lane) * 16) = uv[it];
}
__device__ __forceinline__ void conv_main(f32x4 (&acc)[8], unsigned cb, int lane, int c0, int c1) {
  const int fr = lane & 15, g4 = lane >> 4;
  const unsigned abase = cb + (3 - (fr & 3)) * CPS + 2 * (2044 - 16 * 112 - 16 * (g4 >> 1) + 8 * (g4 & 1) - (fr & ~3));
  const unsigned bbase = cb + UOFF + 512 + 2 * (16 * fr - 16 * (g4 >> 1) + 8 * (g4 & 1));
#pragma unroll
  for (int c = 0; c < 8; c++) acc[c] = (f32x4){0.f, 0.f, 0.f, 0.f};
  for (int cc = c0; cc < c1; cc++) {
    const unsigned aa = abase - 64 * cc, ba = bbase - 64 * cc;
    u32x2 al[8], ah[8]; u32x4 B[8];
    asm volatile(
        "s_waitcnt lgkmcnt(0)\n\t"
        "ds_read_b128 %16, %25\n\tds_read_b64 %0, %24\n\tds_read_b64 %8, %24 offset:8\n\t"
        "ds_read_b128 %17, %25 offset:512\n\tds_read_b64 %1, %24 offset:512\n\tds_read_b64 %9, %24 offset:520\n\t"
        "ds_read_b128 %18, %25 offset:1024\n\tds_read_b64 %2, %24 offset:1024\n\tds_read_b64 %10, %24 offset:1032\n\t"
        "ds_read_b128 %19, %25 offset:1536\n\tds_read_b64 %3, %24 offset:1536\n\tds_read_b64 %11, %24 offset:1544\n\t"
        "ds_read_b128 %20, %25 offset:2048\n\tds_read_b64 %4, %24 offset:2048\n\tds_read_b64 %12, %24 offset:2056\n\t"
        "ds_read_b128 %21, %25 offset:2560\n\tds_read_b64 %5, %24 offset:2560\n\tds_read_b64 %13, %24 offset:2568\n\t"
        "ds_read_b128 %22, %25 offset:3072\n\tds_read_b64 %6, %24 offset:3072\n\tds_read_b64 %14, %24 offset:3080\n\t"
        "ds_read_b128 %23, %25 offset:3584\n\tds_read_b64 %7, %24 offset:3584\n\tds_read_b64 %15, %24 offset:3592\n\t"
        "s_waitcnt lgkmcnt(0)"
        : "=&v"(al[0]), "=&v"(al[1]), "=&v"(al[2]), "=&v"(al[3]), "=&v"(al[4]), "=&v"(al[5]), "=&v"(al[6]), "=&v"(al[7]),
          "=&v"(ah[0]), "=&v"(ah[1]), "=&v"(ah[2]), "=&v"(ah[3]), "=&v"(ah[4]), "=&v"(ah[5]), "=&v"(ah[6]), "=&v"(ah[7]),
          "=&v"(B[0]), "=&v"(B[1]), "=&v"(B[2]), "=&v"(B[3]), "=&v"(B[4]), "=&v"(B[5]), "=&v"(B[6]), "=&v"(B[7])
        : "v"(aa), "v"(ba) : "memory");
#pragma unroll
    for (int a = 0; a < 8; a++)
#pragma unroll
      for (int i = 0; i + a < 8; i++) acc[a + i] = mfma16(mk8(al[7 - a], ah[7 - a]), __builtin_bit_cast(bf16x8, B[i]), acc[a + i]);
  }
}
__device__ __forceinline__ void conv_store(f32x4 y, unsigned cb, int c, int lane, float Dd, __amdgpu_buffer_rsrc_t rw, int d) {
  const int fr = lane & 15, g4 = lane >> 4;
  const u32x2 uu = lds_b64(cb + UOFF + 512 + 2 * (256 * c + 16 * fr + 4 * g4));
  const float r0 = rbf(y[0]) + rbf(bflo(uu.x) * Dd), r1 = rbf(y[1]) + rbf(bfhi(uu.x) * Dd);
  const float r2 = rbf(y[2]) + rbf(bflo(uu.y) * Dd), r3 = rbf(y[3]) + rbf(bfhi(uu.y) * Dd);
  __builtin_amdgcn_raw_buffer_store_b64((u32x2){pk2(r0, r1), pk2(r2, r3)}, rw, OFF_YT + d * 4096 + (256 * c + 16 * fr + 4 * g4) * 2, 0, SC1);
}

__global__ __launch_bounds__(512, 2) void fused(const u16* __restrict__ x, const u16* __restrict__ wqkv, const u16* __restrict__ wo,
                                                const u16* __restrict__ Bk, const u16* __restrict__ Bv, const u16* __restrict__ shk,
                                                const u16* __restrict__ hk, const u16* __restrict__ Dv, const u16* __restrict__ gw,
                                                u16* __restrict__ out, char* __restrict__ ws) {
  extern __shared__ __attribute__((aligned(16))) char sm[];
  const int tid = threadIdx.x, lane = tid & 63;
  const int w = __builtin_amdgcn_readfirstlane(tid >> 6);
  const int b = blockIdx.x, xcd = b & 7, r = b >> 3;
  const int fr = lane & 15, g4 = lane >> 4;
  const unsigned lb = (unsigned)(size_t)(LAS char*)sm;
  const __amdgpu_buffer_rsrc_t rw = __builtin_amdgcn_make_buffer_rsrc(ws, 0, WS_USED, 0x00020000);
  TS_DECL TS(0);
  if (w == 0) {
    const unsigned e = flag_ld(ws, F_E);
    if (lane == 0) *(LAS unsigned*)(size_t)(lb + L_TAG) = e + 1u;
  }
#define TAG (*(const LAS unsigned*)(size_t)(lb + L_TAG))
  bool wo_issued = false;
  if (r < 24) {
    const bool isKV = r < 16;
    const int rt = (xcd & 3) * 4 + (r & 3);
    const int cg = isKV ? (xcd >> 2) * 4 + (r >> 2) : (xcd >> 2) * 2 + ((r - 16) >> 2);
    f32x4 acc[4][5];
    const int rp = (w >> 1) & 1, hf = w & 1;
    if (w < 4) {
      const unsigned xa0 = lb + (rp * 64 + fr) * 128 + ((g4 ^ ((fr >> 1) & 7)) << 4);
      const unsigned wl = lb + XS + (8 * g4 + (fr >> 2)) * 320 + ((lane & 3) >> 1) * 16 + (lane & 1) * 8 + hf * 160;
      const unsigned sgn = (g4 & 1) * 32;
      const unsigned wa0 = hf ? wl - sgn : wl + sgn, wa1 = hf ? wl + sgn : wl - sgn;
#pragma unroll
      for (int i = 0; i < 4; i++)
#pragma unroll
        for (int t = 0; t < 5; t++) acc[i][t] = (f32x4){0.f, 0.f, 0.f, 0.f};
      if (isKV) gemm_compute<true>(acc, xa0, wa0, wa1); else gemm_compute<false>(acc, xa0, wa0, wa1);
    } else if (w < 6) {
      gemm_loader(w - 4, lane, lb, x, wqkv, rt, cg, isKV);
    } else {
      for (int s = 0; s < 10; s++) BAR();
    }
    TS(1);
    BAR();
    const unsigned tag = TAG;
    if (isKV) {
      if (w < 4) {
#pragma unroll
        for (int t = 0; t < 5; t++) {
          const bool isk = hf ? t < 2 : t < 3;
          const int slot = hf ? (t < 2 ? 3 + t : 5 + t) : (t < 3 ? t : 2 + t);
#pragma unroll
          for (int m = 0; m < 2; m++) {
            u32x4 f;
            if (isk) f = (u32x4){pk2(phi(acc[2 * m][t][0]), phi(acc[2 * m][t][1])), pk2(phi(acc[2 * m][t][2]), phi(acc[2 * m][t][3])),
                                 pk2(phi(acc[2 * m + 1][t][0]), phi(acc[2 * m + 1][t][1])), pk2(phi(acc[2 * m + 1][t][2]), phi(acc[2 * m + 1][t][3]))};
            else f = (u32x4){pk2(acc[2 * m][t][0], acc[2 * m][t][1]), pk2(acc[2 * m][t][2], acc[2 * m][t][3]),
                             pk2(acc[2 * m + 1][t][0], acc[2 * m + 1][t][1]), pk2(acc[2 * m + 1][t][2], acc[2 * m + 1][t][3])};
            *(LAS u32x4*)(size_t)(lb + ((rp * 2 + m) * 10 + slot) * 1024 + lane * 16) = f;
          }
        }
      } else if (w >= 6) {
        poll(ws, F_U + lane * 128, true, tag);
      }
      for (int i = tid; i < 6048 / 16; i += 512) *(LAS u32x4*)(size_t)(lb + L_T + i * 16) = (u32x4){0, 0, 0, 0};
      BAR();
      TS(2);
      {
        const u32x4 o1 = {0x3F803F80u, 0x3F803F80u, 0x3F803F80u, 0x3F803F80u};
        const unsigned pbase = OFF_P + (rt * 8 + cg) * 6720;
#pragma unroll
        for (int q = 0; q < 3; q++) {
          const int T = w + 8 * q;
          if (T < 18) {
            const bool isz = T >= 13;
            const int i = isz ? T - 13 : (T + 1) / 3;
            const int j = isz ? 0 : T - (i == 0 ? 0 : 3 * i - 1) + (i == 0 ? 0 : i - 1);
            f32x4 d4 = {0.f, 0.f, 0.f, 0.f};
            const unsigned aA = lb + i * 1024 + lane * 16, aB = lb + (5 + j) * 1024 + lane * 16;
#pragma unroll
            for (int m = 0; m < 4; m++) {
              bf16x8 A = lds_b128(aA + m * 10240);
              bf16x8 B = isz ? __builtin_bit_cast(bf16x8, o1) : lds_b128(aB + m * 10240);
              d4 = mfma16(A, B, d4);
            }
            const int Dg = 16 * i + 4 * g4, h = (Dg * 205) >> 12, dd = Dg - 20 * h;
            const int ee = isz ? 20 : 16 * j + fr - 20 * h;
            if (isz ? fr == 0 : (ee >= 0 && ee < 20))
              __builtin_amdgcn_raw_buffer_store_b128(__builtin_bit_cast(u32x4, d4), rw, pbase + (h * 420 + ee * 20 + dd) * 4, 0, SC1);
          }
        }
      }
      DRAIN();
      BAR();
      TS(3);
      if (w == 0) {
        if (lane == 0) flag_st(ws, F_P + (rt * 8 + cg) * 128, tag);
        poll(ws, lane < 16 ? F_P + (lane * 8 + cg) * 128 : F_PQ + (rt * 4 + (cg >> 1)) * 128, lane <= 16, tag);
      } else if (w >= 6) {
        const i32x4 sws = make_srd(ws, WS_USED);
        const int ct = (xcd >> 2) * 4 + (r >> 3);
        const unsigned vwv = OFF_WOP + ct * 102400 + lane * 16;
        for (int pc = w - 6; pc < 100; pc += 2) glds16_sc1(lb + pc * 1024, vwv, sws, pc * 1024);
      }
      wo_issued = true;
      BAR();
      TS(4);
      bf16x8 Bq[3];
      {
        const unsigned prow = OFF_PQ + (rt * 128 + w * 16 + fr) * 1280 + (80 * cg + 8 * g4) * 2;
#pragma unroll
        for (int ks = 0; ks < 3; ks++) Bq[ks] = __builtin_bit_cast(bf16x8, __builtin_amdgcn_raw_buffer_load_b128(rw, prow + 64 * ks, 0, SC1));
      }
      if (tid < 210) {
        f32x4 st[2] = {{0.f, 0.f, 0.f, 0.f}, {0.f, 0.f, 0.f, 0.f}};
        const unsigned pb = OFF_P + cg * 6720 + tid * 16;
#pragma unroll
        for (int hh = 0; hh < 2; hh++) {
          u32x4 v0[8], v1[8];
#pragma unroll
          for (int k = 0; k < 8; k++) {
            v0[k] = __builtin_amdgcn_raw_buffer_load_b128(rw, pb + (hh * 8 + k) * (8 * 6720), 0, SC1);
            v1[k] = __builtin_amdgcn_raw_buffer_load_b128(rw, pb + (hh * 8 + k) * (8 * 6720) + 210 * 16, 0, SC1);
          }
#pragma unroll
          for (int k = 0; k < 8; k++) { st[0] += __builtin_bit_cast(f32x4, v0[k]); st[1] += __builtin_bit_cast(f32x4, v1[k]); }
        }
#pragma unroll
        for (int q = 0; q < 2; q++) {
          const int i4 = tid + 210 * q;
          const int row = (i4 * 13108) >> 16, d = 4 * (i4 - row * 5);
          *(LAS u32x2*)(size_t)(lb + L_T + row * 72 + (8 + d) * 2) = (u32x2){pk2(st[q][0], st[q][1]), pk2(st[q][2], st[q][3])};
        }
      }
      BAR();
      TS(5);
      {
        constexpr int PJ[9] = {0, 1, 1, 2, 2, 3, 3, 4, 4}, PK[9] = {0, 0, 1, 0, 1, 1, 2, 1, 2};
        if (g4 >= 2) Bq[2] = __builtin_bit_cast(bf16x8, (u32x4){0, 0, 0, 0});
        f32x4 nu[5], de[5];
#pragma unroll
        for (int j = 0; j < 5; j++) { nu[j] = (f32x4){0.f, 0.f, 0.f, 0.f}; de[j] = nu[j]; }
#pragma unroll
        for (int p = 0; p < 9; p++) {
          const int E = 16 * PJ[p] + fr, h = (E * 205) >> 12, e = E - 20 * h;
          int o_ = 32 * PK[p] + 8 * g4 - 20 * h; o_ = o_ < -8 ? -8 : o_ > 20 ? 20 : o_;
          const unsigned an = lb + L_T + (h * 21 + e) * 72 + (8 + o_) * 2, ad = lb + L_T + (h * 21 + 20) * 72 + (8 + o_) * 2;
          unsigned an8 = an + 8, ad8 = ad + 8;
          asm volatile("" : "+v"(an8), "+v"(ad8));
          nu[PJ[p]] = mfma16(mk8(lds_b64(an), lds_b64(an8)), Bq[PK[p]], nu[PJ[p]]);
          de[PJ[p]] = mfma16(mk8(lds_b64(ad), lds_b64(ad8)), Bq[PK[p]], de[PJ[p]]);
        }
        const unsigned orow = OFF_O + (rt * 128 + w * 16 + fr) * 1280 + (80 * cg + 4 * g4) * 2;
#pragma unroll
        for (int j = 0; j < 5; j++) {
          const float rd = __builtin_amdgcn_rcpf(rbf(rbf(de[j][0]) + 1e-6f));
          __builtin_amdgcn_raw_buffer_store_b64((u32x2){pk2(rbf(nu[j][0]) * rd, rbf(nu[j][1]) * rd), pk2(rbf(nu[j][2]) * rd, rbf(nu[j][3]) * rd)}, rw, orow + 32 * j, 0, SC1);
        }
      }
      DRAIN();
      BAR();
      if (tid == 0) flag_st(ws, F_O + (rt * 8 + cg) * 128, tag);
      TS(6);
    } else {
      if (w < 4) {
#pragma unroll
        for (int i = 0; i < 4; i++) {
          const unsigned prow = OFF_PQ + (rt * 128 + rp * 64 + i * 16 + fr) * 1280 + (cg * 160 + hf * 80 + 4 * g4) * 2;
#pragma unroll
          for (int t = 0; t < 5; t++)
            __builtin_amdgcn_raw_buffer_store_b64((u32x2){pk2(phi(acc[i][t][0]), phi(acc[i][t][1])), pk2(phi(acc[i][t][2]), phi(acc[i][t][3]))}, rw, prow + 32 * t, 0, SC1);
        }
        DRAIN();
      } else if (w == 7) {
        poll(ws, F_U + lane * 128, true, tag);
      }
      BAR();
      TS(2);
      const int vb = xcd * 8 + (r - 16);
      if (tid == 0) flag_st(ws, F_PQ + (rt * 4 + cg) * 128, tag);
      if (w < 4) {
        const int d = 80 * (vb >> 3) + 48 + 4 * (vb & 7) + w;
        const unsigned cb = lb + w * CHS;
        const float Dd = bflo(Dv[d]);
        conv_prep_h(hk + (size_t)d * S, cb, lane);
        conv_load_u(rw, d, cb, lane);
        TS(3);
        f32x4 y[8];
        conv_main(y, cb, lane, 0, 8);
        TS(4);
#pragma unroll
        for (int c = 0; c < 8; c++) conv_store(y[c], cb, c, lane, Dd, rw, d);
        DRAIN();
      }
      BAR();
      if (tid == 0) flag_st(ws, F_Y + (64 + vb) * 128, tag);
      TS(5); TS(6);
    }
  } else {
    const int pw = xcd * 8 + (r - 24);
    {
      u32x4 gx0[4], gx1[4];
      const int l16 = lane & 15;
#pragma unroll
      for (int i = 0; i < 4; i++) {
        const u16* xr = x + (size_t)(pw * 32 + w * 4 + i) * D;
        gx0[i] = *(const u32x4*)(xr + lane * 8); gx1[i] = *(const u32x4*)(xr + 512 + l16 * 8);
      }
      const u32x4 g0 = *(const u32x4*)(gw + lane * 8), g1 = *(const u32x4*)(gw + 512 + l16 * 8);
      const int rbase = xcd * 256 + ((r - 24) >> 1) * 64, cbase = ((r - 24) & 1) * 320;
      for (int it = w; it < 10; it += 8) {
        const int ch = cbase + (it >> 1) * 64 + (lane & 7) * 8;
        const int s0 = rbase + (it & 1) * 32 + (lane >> 3) * 4;
        u32x4 kr[7], vr[4], sp[4];
#pragma unroll
        for (int j = 0; j < 7; j++) { int s = s0 - 3 + j; kr[j] = *(const u32x4*)(Bk + (size_t)(s < 0 ? 0 : s) * D + ch); }
#pragma unroll
        for (int j = 0; j < 4; j++) vr[j] = *(const u32x4*)(Bv + (size_t)(s0 + j) * D + ch);
#pragma unroll
        for (int j = 0; j < 4; j++) sp[j] = ((const u32x4*)(shk + ch * 4))[j];
        SBAR();
        if (s0 == 0) { kr[0] = (u32x4){0, 0, 0, 0}; kr[1] = kr[0]; kr[2] = kr[0]; }
#pragma unroll
        for (int c = 0; c < 8; c++) {
          const unsigned s01 = sp[c >> 1][(c & 1) * 2], s23 = sp[c >> 1][(c & 1) * 2 + 1];
          const float w0 = bflo(s01), w1 = bfhi(s01), w2 = bflo(s23), w3 = bfhi(s23);
          float kk[7], u[4];
#pragma unroll
          for (int j = 0; j < 7; j++) { unsigned d = kr[j][c >> 1]; kk[j] = (c & 1) ? bfhi(d) : bflo(d); }
#pragma unroll
          for (int j = 0; j < 4; j++) {
            unsigned d = vr[j][c >> 1]; float v = (c & 1) ? bfhi(d) : bflo(d);
            float ks = kk[j] * w3; ks = fmaf(kk[j + 1], w2, ks); ks = fmaf(kk[j + 2], w1, ks); ks = fmaf(kk[j + 3], w0, ks);
            u[j] = ks * v;
          }
          __builtin_amdgcn_raw_buffer_store_b64((u32x2){pk2(u[0], u[1]), pk2(u[2], u[3])}, rw, OFF_UT + (ch + c) * 4096 + s0 * 2, 0, SC1);
        }
      }
      TS(1);
      float a[4];
      const float m1 = lane < 16 ? 1.f : 0.f;
#pragma unroll
      for (int i = 0; i < 4; i++) {
        float a0 = 0.f, a1 = 0.f;
#pragma unroll
        for (int j = 0; j < 4; j++) {
          a0 = fmaf(bflo(gx0[i][j]), bflo(g0[j]), a0); a0 = fmaf(bfhi(gx0[i][j]), bfhi(g0[j]), a0);
          a1 = fmaf(bflo(gx1[i][j]), bflo(g1[j]), a1); a1 = fmaf(bfhi(gx1[i][j]), bfhi(g1[j]), a1);
        }
        a[i] = fmaf(a1, m1, a0);
      }
#pragma unroll
      for (int m = 32; m >= 1; m >>= 1)
#pragma unroll
        for (int i = 0; i < 4; i++) a[i] += __shfl_xor(a[i], m);
      if (lane < 4) {
        const float t = lane == 0 ? a[0] : lane == 1 ? a[1] : lane == 2 ? a[2] : a[3];
        __builtin_amdgcn_raw_buffer_store_b32(__float_as_uint(rbf(1.f / (1.f + __expf(-rbf(t))))), rw, OFF_G + (pw * 32 + w * 4 + lane) * 4, 0, SC1);
      }
#pragma unroll
      for (int it = 0; it < 2; it++) {
        const int q = pw * 512 + tid + it * 32768;
        if (q < 51200) {
          int f = q >> 6; int f5 = (f * 3277) >> 14, t = f - 5 * f5; int ct = (f5 * 3277) >> 16, ks = f5 - 20 * ct;
          const u16* src = wo + (size_t)(32 * ks + 8 * g4) * D + 80 * ct + 16 * t + fr;
          u32x4 v;
          v.x = src[0] | ((unsigned)src[D] << 16); v.y = src[2 * D] | ((unsigned)src[3 * D] << 16);
          v.z = src[4 * D] | ((unsigned)src[5 * D] << 16); v.w = src[6 * D] | ((unsigned)src[7 * D] << 16);
          __builtin_amdgcn_raw_buffer_store_b128(v, rw, OFF_WOP + q * 16, 0, SC1);
        }
      }
    }
    DRAIN();
    BAR();
    TS(2);
    const unsigned tag = TAG;
    if (tid == 0) flag_st(ws, F_U + pw * 128, tag);
    const int c6 = w < 4 ? w : 4 + ((w - 4) >> 1);
    const int d = 80 * (pw >> 3) + 6 * (pw & 7) + c6;
    const unsigned cb = lb + c6 * CHS;
    const float Dd = bflo(Dv[d]);
    if (w < 4 || !(w & 1)) conv_prep_h(hk + (size_t)d * S, cb, lane);
    if (w == 7) poll(ws, F_U + lane * 128, true, tag);
    BAR();
    if (w < 4 || !(w & 1)) conv_load_u(rw, d, cb, lane);
    BAR();
    TS(3);
    f32x4 y[8];
    conv_main(y, cb, lane, w < 4 ? 0 : (w & 1) * 4, w < 4 ? 8 : (w & 1) * 4 + 4);
    TS(4);
    if (w < 4) {
#pragma unroll
      for (int c = 0; c < 8; c++) conv_store(y[c], cb, c, lane, Dd, rw, d);
    }
    BAR();
    if (w >= 4) {
#pragma unroll
      for (int c = 0; c < 4; c++) { const f32x4 lo = y[c], hi = y[4 + c]; *(LAS f32x4*)(size_t)(cb + ((w & 1) * 4 + c) * 1024 + lane * 16) = (w & 1) ? lo : hi; }
    }
    BAR();
    if (w >= 4) {
#pragma unroll
      for (int c = 0; c < 4; c++) {
        const f32x4 o_ = *(const LAS f32x4*)(size_t)(cb + (((w & 1) ^ 1) * 4 + c) * 1024 + lane * 16);
        const f32x4 lo = y[c], hi = y[4 + c];
        const f32x4 mine = (w & 1) ? hi : lo;
        conv_store((w & 1) ? o_ + mine : mine + o_, cb, (w & 1) * 4 + c, lane, Dd, rw, d);
      }
    }
    DRAIN();
    BAR();
    if (tid == 0) flag_st(ws, F_Y + pw * 128, tag);
    TS(5); TS(6);
  }

  {
    const unsigned tag = TAG;
    const int rt = (xcd & 3) * 8 + (r & 7), ct = (xcd >> 2) * 4 + (r >> 3);
    const int sw_ = w & 3, kk = w >> 2;
    const i32x4 sws = make_srd(ws, WS_USED);
    if (!wo_issued) {
      const unsigned vwv = OFF_WOP + ct * 102400 + lane * 16;
      for (int pc = w; pc < 100; pc += 8) glds16_sc1(lb + pc * 1024, vwv, sws, pc * 1024);
    }
    const int srow = rt * 64 + sw_ * 16 + fr;
    u32x2 xv[3];
#pragma unroll
    for (int t = 0; t < 3; t++) { const int tt = kk ? (t < 2 ? 3 + t : 4) : t; xv[t] = *(const u32x2*)(x + (size_t)srow * D + ct * 80 + 16 * tt + 4 * g4); }
    const float g = __uint_as_float(__builtin_amdgcn_raw_buffer_load_b32(rw, OFF_G + srow * 4, 0, SC1));
    if (w == 0) {
      poll(ws, F_O + ((rt >> 1) * 8 + (lane & 7)) * 128, lane < 8, tag);
    } else if (w == 7) {
      poll(ws, F_Y + ((lane & 8 ? 64 : 0) + ct * 8 + (lane & 7)) * 128, lane < 16, tag);
      for (int pc = 0; pc < 10; pc++)
        glds16_sc1(lb + L_Y + pc * 1024, OFF_YT + (ct * 80 + pc * 8 + (lane >> 3)) * 4096 + rt * 128 + (lane & 7) * 16, sws, 0);
    } else if (w == 6 && b == 0) {
      poll(ws, F_U + lane * 128, true, tag); poll(ws, F_PQ + lane * 128, true, tag);
      poll(ws, F_P + lane * 128, true, tag); poll(ws, F_P + (64 + lane) * 128, true, tag);
      if (lane == 0) flag_st(ws, F_E, tag);
    }
    BAR();
    TS(7);
    bf16x8 of[10];
    {
      const unsigned vo = OFF_O + srow * 1280 + kk * 64 + 16 * g4;
#pragma unroll
      for (int s = 0; s < 10; s++) of[s] = __builtin_bit_cast(bf16x8, __builtin_amdgcn_raw_buffer_load_b128(rw, vo + 128 * s, 0, SC1));
    }
    DRAIN();
    BAR();
    TS(8);
    f32x4 acc[5];
#pragma unroll
    for (int t = 0; t < 5; t++) acc[t] = (f32x4){0.f, 0.f, 0.f, 0.f};
    const unsigned wa0 = lb + kk * 5120 + lane * 16;
#pragma unroll
    for (int hh = 0; hh < 2; hh++) {
      bf16x8 wf[5][5];
#pragma unroll
      for (int s = 0; s < 5; s++)
#pragma unroll
        for (int t = 0; t < 5; t++) wf[s][t] = lds_b128(wa0 + (hh * 5 + s) * 10240 + t * 1024);
#pragma unroll
      for (int s = 0; s < 5; s++)
#pragma unroll
        for (int t = 0; t < 5; t++) acc[t] = mfma16(wf[s][t], of[hh * 5 + s], acc[t]);
      SGB(0x100, 10);
#pragma unroll
      for (int i = 0; i < 15; i++) { SGB(0x8, 1); SGB(0x100, 1); }
      SGB(0x8, 10);
      SBAR();
    }
    TS(9);
    const unsigned ra = lb + L_RED + (sw_ * 5) * 1024 + lane * 16;
    if (kk == 0) { *(LAS f32x4*)(size_t)(ra + 3 * 1024) = acc[3]; *(LAS f32x4*)(size_t)(ra + 4 * 1024) = acc[4]; }
    else { *(LAS f32x4*)(size_t)(ra) = acc[0]; *(LAS f32x4*)(size_t)(ra + 1024) = acc[1]; *(LAS f32x4*)(size_t)(ra + 2048) = acc[2]; }
    BAR();
    const float omg = rbf(1.f - g);
    const int nt = kk ? 2 : 3;
#pragma unroll
    for (int t = 0; t < 3; t++) {
      if (t < nt) {
        const int tt = kk ? 3 + t : t;
        const f32x4 a0 = acc[t], a1 = acc[3 + (t < 2 ? t : 1)];
        f32x4 a = *(const LAS f32x4*)(size_t)(ra + tt * 1024) + (kk ? a1 : a0);
        u32x2 yv = lds_tr(lb + L_Y + (16 * tt + 4 * g4 + (fr >> 2)) * 128 + (sw_ * 16 + 4 * (fr & 3)) * 2);
        float xs[4] = {bflo(xv[t].x), bfhi(xv[t].x), bflo(xv[t].y), bfhi(xv[t].y)};
        float ys[4] = {bflo(yv.x), bfhi(yv.x), bflo(yv.y), bfhi(yv.y)};
        float r_[4];
#pragma unroll
        for (int q = 0; q < 4; q++) r_[q] = rbf(g * rbf(a[q])) + rbf(omg * rbf(xs[q] * ys[q]));
        *(u32x2*)(out + (size_t)srow * D + ct * 80 + 16 * tt + 4 * g4) = (u32x2){pk2(r_[0], r_[1]), pk2(r_[2], r_[3])};
      }
    }
    TS(10); TS_OUT(11);
  }
}

extern "C" void kernel_launch(void* const* d_in, const int* in_sizes, int n_in, void* d_out, int out_size, void* d_ws, size_t ws_size,
                              hipStream_t stream) {
  const u16* x = (const u16*)d_in[0]; const u16* wqkv = (const u16*)d_in[1]; const u16* wo = (const u16*)d_in[2];
  const u16* Bk = (const u16*)d_in[4]; const u16* Bv = (const u16*)d_in[5]; const u16* shk = (const u16*)d_in[6];
  const u16* hk = (const u16*)d_in[7]; const u16* Dv = (const u16*)d_in[8]; const u16* gw = (const u16*)d_in[9];
  (void)hipFuncSetAttribute((const void*)fused, hipFuncAttributeMaxDynamicSharedMemorySize, LDS_TOTAL);
  fused<<<256, 512, LDS_TOTAL, stream>>>(x, wqkv, wo, Bk, Bv, shk, hk, Dv, gw, (u16*)d_out, (char*)d_ws);
}
```

```cpp
#include <hip/hip_runtime.h>
#include <hip/hip_bf16.h>
#include <type_traits>

typedef unsigned short u16;
typedef __bf16 bf16x2 __attribute__((ext_vector_type(2)));
typedef __bf16 bf16x8 __attribute__((ext_vector_type(8)));
typedef float f32x4 __attribute__((ext_vector_type(4)));
typedef unsigned u32x4 __attribute__((ext_vector_type(4)));
typedef unsigned u32x2 __attribute__((ext_vector_type(2)));
typedef short v4i16_t __attribute__((ext_vector_type(4)));
typedef int i32x4 __attribute__((ext_vector_type(4)));
typedef __attribute__((address_space(1))) unsigned gu32;
#define LAS __attribute__((address_space(3)))
#define SBAR() __builtin_amdgcn_sched_barrier(0)
#define SGB(mask, n) __builtin_amdgcn_sched_group_barrier(mask, n, 0)
#define BAR() asm volatile("s_waitcnt lgkmcnt(0)\n\ts_barrier" ::: "memory")
#define DRAIN() asm volatile("s_waitcnt vmcnt(0)" ::: "memory")
#define SC1 16

#define TS_DECL
#define TSARG
#define TSPAR
#define TS(i)
#define TS_OUT(n)

constexpr int S = 2048, D = 640;
constexpr unsigned SZ = S * D * 2;
constexpr unsigned F_E = 0, F_U = 128, F_PQ = F_U + 64 * 128, F_P = F_PQ + 64 * 128, F_O = F_P + 128 * 128, F_Y = F_O + 128 * 128, F_W = F_Y + 128 * 128;
constexpr unsigned OFF_PQ = 0x20000, OFF_P = OFF_PQ + 128 * 8 * 3 * 1024, OFF_UT = OFF_P + (1u << 19), OFF_G = OFF_UT + SZ, OFF_WOP = OFF_G + 8192,
                   OFF_O = OFF_WOP + (1u << 20), OFF_YT = OFF_O + SZ, WS_USED = OFF_YT + SZ;
constexpr int XS = 16384, WSB = 20480, STG = XS + WSB;
constexpr int L_EXTRA = 4 * STG;
constexpr int L_TAG = L_EXTRA, L_CNT = L_EXTRA + 16, L_T = L_EXTRA + 64;
constexpr int LDS_TOTAL = L_T + 6144;
constexpr int CPS = 4160, UOFF = 4 * CPS, CHS = UOFF + 4608;
constexpr int L_UIMG = 5 * CHS;
constexpr int L_Y = 102400, L_RED = L_Y + 10240;

__device__ __forceinline__ unsigned pk2(float a, float b) { bf16x2 r = {(__bf16)a, (__bf16)b}; return __builtin_bit_cast(unsigned, r); }
__device__ __forceinline__ float rbf(float a) { return (float)(__bf16)a; }
__device__ __forceinline__ float bflo(unsigned v) { return __uint_as_float(v << 16); }
__device__ __forceinline__ float bfhi(unsigned v) { return __uint_as_float(v & 0xffff0000u); }
__device__ __forceinline__ float phi(float t) {
  return __builtin_amdgcn_fmed3f(t, 0.f, 3.0e38f) + (__builtin_amdgcn_exp2f(fminf(t * 1.44269504f, 0.f)) + 1e-6f);
}
__device__ __forceinline__ i32x4 make_srd(const void* p, unsigned bytes) {
  unsigned long long a = (unsigned long long)p; i32x4 r;
  r.x = __builtin_amdgcn_readfirstlane((int)(unsigned)a); r.y = __builtin_amdgcn_readfirstlane((int)(unsigned)(a >> 32));
  r.z = __builtin_amdgcn_readfirstlane((int)bytes); r.w = 0x00020000; return r;
}
__device__ __forceinline__ void glds16(unsigned ldsaddr, unsigned voff, i32x4 srd, unsigned soff) {
  asm volatile("s_mov_b32 m0, %0\n\ts_nop 0\n\tbuffer_load_dwordx4 %1, %2, %3 offen lds" :: "s"(ldsaddr), "v"(voff), "s"(srd), "s"(soff) : "memory");
}
__device__ __forceinline__ void glds16_sc1(unsigned ldsaddr, unsigned voff, i32x4 srd, unsigned soff) {
  asm volatile("s_mov_b32 m0, %0\n\ts_nop 0\n\tbuffer_load_dwordx4 %1, %2, %3 offen sc1 lds" :: "s"(ldsaddr), "v"(voff), "s"(srd), "s"(soff) : "memory");
}
template <int N> __device__ __forceinline__ void wait_bar() { asm volatile("s_waitcnt vmcnt(%0) lgkmcnt(0)\n\ts_barrier" :: "n"(N) : "memory"); }
__device__ __forceinline__ bf16x8 lds_b128(unsigned addr) { return *(const LAS bf16x8*)(size_t)addr; }
__device__ __forceinline__ u32x2 lds_b64(unsigned addr) { return *(const LAS u32x2*)(size_t)addr; }
__device__ __forceinline__ u32x2 lds_tr(unsigned addr) { return __builtin_bit_cast(u32x2, __builtin_amdgcn_ds_read_tr16_b64_v4i16((LAS v4i16_t*)(size_t)addr)); }
__device__ __forceinline__ bf16x8 mk8(u32x2 lo, u32x2 hi) { u32x4 t = {lo.x, lo.y, hi.x, hi.y}; return __builtin_bit_cast(bf16x8, t); }
__device__ __forceinline__ f32x4 mfma16(bf16x8 a, bf16x8 b, f32x4 c) { return __builtin_amdgcn_mfma_f32_16x16x32_bf16(a, b, c, 0, 0, 0); }

__device__ __forceinline__ u32x4 pair16(u32x2 a, u32x2 b) {
  const auto r0 = __builtin_amdgcn_permlane16_swap(a.x, b.x, false, false);
  const auto r1 = __builtin_amdgcn_permlane16_swap(a.y, b.y, false, false);
  return (u32x4){r0[0], r1[0], r0[1], r1[1]};
}
__device__ __forceinline__ void store_frags(const u32x2 (&v)[5], __amdgpu_buffer_rsrc_t rw, unsigned base, int k0, int fr, int g4) {
#pragma unroll
  for (int j = 0; j < 4; j += 2) {
    const int k = k0 + 16 * (j + (g4 & 1)) + 8 * (g4 >> 1);
    __builtin_amdgcn_raw_buffer_store_b128(pair16(v[j], v[j + 1]), rw, base + (k >> 5) * 1024 + (((k >> 3) & 3) * 16 + fr) * 16, 0, SC1);
  }
  const int k = k0 + 64 + 4 * g4;
  __builtin_amdgcn_raw_buffer_store_b64(v[4], rw, base + (k >> 5) * 1024 + (((k >> 3) & 3) * 16 + fr) * 16 + ((k >> 2) & 1) * 8, 0, SC1);
}

__device__ __forceinline__ void lds_sync(unsigned cnt_addr, int lane, unsigned n) {
  asm volatile("s_waitcnt lgkmcnt(0)" ::: "memory");
  if (lane == 0) __hip_atomic_fetch_add((LAS unsigned*)(size_t)cnt_addr, 1u, __ATOMIC_RELAXED, __HIP_MEMORY_SCOPE_WORKGROUP);
  while (__builtin_amdgcn_readfirstlane(__hip_atomic_load((LAS unsigned*)(size_t)cnt_addr, __ATOMIC_RELAXED, __HIP_MEMORY_SCOPE_WORKGROUP)) < n) {}
  asm volatile("" ::: "memory");
}
__device__ __forceinline__ bool lds_last(unsigned cnt_addr, int lane, unsigned n) {
  unsigned old = 0;
  if (lane == 0) old = __hip_atomic_fetch_add((LAS unsigned*)(size_t)cnt_addr, 1u, __ATOMIC_RELAXED, __HIP_MEMORY_SCOPE_WORKGROUP);
  return __builtin_amdgcn_readfirstlane(old) == n - 1;
}

__device__ __forceinline__ unsigned flag_ld(const char* ws, unsigned off) { return __hip_atomic_load((const gu32*)(ws + off), __ATOMIC_RELAXED, __HIP_MEMORY_SCOPE_AGENT); }
__device__ __forceinline__ void flag_st(char* ws, unsigned off, unsigned v) { __hip_atomic_store((gu32*)(ws + off), v, __ATOMIC_RELAXED, __HIP_MEMORY_SCOPE_AGENT); }
__device__ __forceinline__ void poll(const char* ws, unsigned off, bool act, unsigned tag) {
  for (int spins = 0; spins < (1 << 18); spins++) {
    const unsigned v = act ? flag_ld(ws, off) : tag;
    if (__all(v == tag)) break;
    __builtin_amdgcn_s_sleep(2);
  }
  asm volatile("" ::: "memory");
}

template <bool KV>
__device__ __forceinline__ void gemm_compute(f32x4 (&acc)[4][5], unsigned xa0, unsigned wa0, unsigned wa1) {
  bf16x8 xa[4], xb[4], wa[5], wb[5];
  auto load = [&](bf16x8 (&xf)[4], bf16x8 (&wf)[5], int buf, int kk) {
    const unsigned bo = buf * STG;
    const unsigned xa = (xa0 + bo) ^ (kk * 64);
    const unsigned a0 = wa0 + bo + kk * 10240, a1 = wa1 + bo + kk * 10240;
#pragma unroll
    for (int i = 0; i < 4; i++) xf[i] = lds_b128(xa + 2048 * i);
#pragma unroll
    for (int t = 0; t < 5; t++) { const unsigned a = ((t & 1) ? a1 : a0) + 32 * t; wf[t] = mk8(lds_tr(a), lds_tr(a + 1280)); }
  };
  auto mma = [&](const bf16x8 (&xf)[4], const bf16x8 (&wf)[5]) {
#pragma unroll
    for (int t = 0; t < 5; t++)
#pragma unroll
      for (int i = 0; i < 4; i++) acc[i][t] = KV ? mfma16(xf[i], wf[t], acc[i][t]) : mfma16(wf[t], xf[i], acc[i][t]);
  };
  auto interleave = [&]() {
#pragma unroll
    for (int t = 0; t < 14; t++) { SGB(0x8, 1); SGB(0x100, 1); }
    SGB(0x8, 6);
  };
  BAR();
  load(xa, wa, 0, 0);
  SBAR();
  for (int s = 0; s < 9; s++) {
    load(xb, wb, s & 3, 1); mma(xa, wa); interleave();
    SBAR();
    BAR();
    load(xa, wa, (s + 1) & 3, 0); mma(xb, wb); interleave();
    SBAR();
  }
  load(xb, wb, 1, 1); mma(xa, wa); interleave();
  SBAR();
  mma(xb, wb);
}

__device__ __forceinline__ void gemm_loader(int L, int lane, unsigned lb, const u16* x, const u16* wqkv, int rt, int cg, bool isKV) {
  const int s0 = (((rt & 3) * 5 + 1) / 2 + (isKV ? 2 * (cg & 3) : 8 + (cg & 1))) % 10;
  const i32x4 sx = make_srd(x, S * D * 2), sw = make_srd(wqkv, D * 3 * D * 2);
  unsigned vx[2], vw[5];
#pragma unroll
  for (int i = 0; i < 2; i++) vx[i] = (rt * 128 + L * 64 + i * 8 + (lane >> 3)) * 1280 + (((lane & 7) ^ ((4 * i + (lane >> 4)) & 7)) << 4);
#pragma unroll
  for (int i = 0; i < 5; i++) {
    const int ci = i * 64 + lane, k = (ci * 3277) >> 16, pos = ci - k * 20, gc = pos ^ (((k >> 3) & 1) << 1), tl = gc >> 1;
    unsigned col;
    if (isKV) { const int isv = (tl >= 3 && tl < 5) || tl >= 7; const int tt = tl < 3 ? tl : tl < 5 ? tl - 3 : tl < 7 ? tl - 2 : tl - 5;
                col = (isv ? 2560u : 1280u) + 160u * cg + tt * 32; }
    else col = 320u * cg + tl * 32;
    vw[i] = (L * 32 + k) * 3840 + col + (gc & 1) * 16;
  }
  auto stage = [&](int st_, int buf) {
    const unsigned base = lb + buf * STG;
    int st = st_ + s0; st = st >= 10 ? st - 10 : st;
#pragma unroll
    for (int p = 0; p < 10; p++) {
      glds16(base + XS + (L * 10 + p) * 1024, vw[p % 5], sw, st * (64 * 3840) + (p / 5) * (16 * 3840));
      if (p < 8) glds16(base + (L * 8 + p) * 1024, vx[p & 1], sx, st * 128 + (p >> 1) * (16 * 1280));
    }
  };
  stage(0, 0); stage(1, 1);
  wait_bar<18>();
  stage(2, 2); stage(3, 3);
  for (int s = 0; s < 9; s++) {
    if (s < 7) wait_bar<36>(); else if (s == 7) wait_bar<18>(); else wait_bar<0>();
    if (s < 6) stage(s + 4, s & 3);
  }
}

__device__ __forceinline__ void conv_load_h(u32x4 (&cur)[4], u32x4 (&prv)[4], const u16* __restrict__ hrow, int lane) {
#pragma unroll
  for (int it = 0; it < 4; it++) {
    const int i = it * 64 + lane;
    cur[it] = *(const u32x4*)(hrow + i * 8); prv[it] = *(const u32x4*)(hrow + (i > 0 ? i * 8 - 8 : 0));
  }
}
__device__ __forceinline__ void conv_prep_h(const u32x4 (&cur)[4], const u32x4 (&prv_)[4], unsigned cb, int lane) {
#pragma unroll
  for (int it = 0; it < 4; it++) {
    u32x4 prv = prv_[it];
    if (it == 0 && lane == 0) prv = (u32x4){0, 0, 0, 0};
    const unsigned W_[8] = {prv.x, prv.y, prv.z, prv.w, cur[it].x, cur[it].y, cur[it].z, cur[it].w};
    const int c = 255 - (it * 64 + lane);
#pragma unroll
    for (int e = 0; e < 4; e++) {
      u32x4 ov;
#pragma unroll
      for (int q = 0; q < 4; q++) {
        const int i0 = 15 - e - 2 * q, i1 = i0 - 1;
        const unsigned sel = ((i0 & 1) ? 0x0706u : 0x0504u) | (((i1 & 1) ? 0x0302u : 0x0100u) << 16);
        ov[q] = __builtin_amdgcn_perm(W_[i0 >> 1], W_[i1 >> 1], sel);
      }
      *(LAS u32x4*)(size_t)(cb + e * CPS + c * 16) = ov;
    }
  }
  if (lane < 8) *(LAS u32x4*)(size_t)(cb + (lane >> 1) * CPS + (256 + (lane & 1)) * 16) = (u32x4){0, 0, 0, 0};
  if (lane < 32) *(LAS u32x4*)(size_t)(cb + UOFF + lane * 16) = (u32x4){0, 0, 0, 0};
}
template <class F>
__device__ __forceinline__ void conv_load_u(__amdgpu_buffer_rsrc_t rw, int d, unsigned cb, int lane, F&& between) {
  u32x4 uv[4];
#pragma unroll
  for (int it = 0; it < 4; it++) uv[it] = __builtin_amdgcn_raw_buffer_load_b128(rw, OFF_UT + d * 4096 + (it * 64 + lane) * 16, 0, SC1);
  SBAR();
  between();
  SBAR();
#pragma unroll
  for (int it = 0; it < 4; it++) *(LAS u32x4*)(size_t)(cb + UOFF + 512 + (it * 64 + lane) * 16) = uv[it];
}
__device__ __forceinline__ void conv_main(f32x4 (&acc)[8], unsigned cb, int lane, int c0, int c1) {
  const int fr = lane & 15, g4 = lane >> 4;
  const unsigned abase = cb + (3 - (fr & 3)) * CPS + 2 * (2044 - 16 * 112 - 16 * (g4 >> 1) + 8 * (g4 & 1) - (fr & ~3));
  const unsigned bbase = cb + UOFF + 512 + 2 * (16 * fr - 16 * (g4 >> 1) + 8 * (g4 & 1));
#pragma unroll
  for (int c = 0; c < 8; c++) acc[c] = (f32x4){0.f, 0.f, 0.f, 0.f};
  for (int cc = c0; cc < c1; cc++) {
    const unsigned aa = abase - 64 * cc, ba = bbase - 64 * cc;
    u32x2 al[8], ah[8]; u32x4 B[8];
    asm volatile(
        "s_waitcnt lgkmcnt(0)\n\t"
        "ds_read_b128 %16, %25\n\tds_read_b64 %0, %24\n\tds_read_b64 %8, %24 offset:8\n\t"
        "ds_read_b128 %17, %25 offset:512\n\tds_read_b64 %1, %24 offset:512\n\tds_read_b64 %9, %24 offset:520\n\t"
        "ds_read_b128 %18, %25 offset:1024\n\tds_read_b64 %2, %24 offset:1024\n\tds_read_b64 %10, %24 offset:1032\n\t"
        "ds_read_b128 %19, %25 offset:1536\n\tds_read_b64 %3, %24 offset:1536\n\tds_read_b64 %11, %24 offset:1544\n\t"
        "ds_read_b128 %20, %25 offset:2048\n\tds_read_b64 %4, %24 offset:2048\n\tds_read_b64 %12, %24 offset:2056\n\t"
        "ds_read_b128 %21, %25 offset:2560\n\tds_read_b64 %5, %24 offset:2560\n\tds_read_b64 %13, %24 offset:2568\n\t"
        "ds_read_b128 %22, %25 offset:3072\n\tds_read_b64 %6, %24 offset:3072\n\tds_read_b64 %14, %24 offset:3080\n\t"
        "ds_read_b128 %23, %25 offset:3584\n\tds_read_b64 %7, %24 offset:3584\n\tds_read_b64 %15, %24 offset:3592\n\t"
        "s_waitcnt lgkmcnt(0)"
        : "=&v"(al[0]), "=&v"(al[1]), "=&v"(al[2]), "=&v"(al[3]), "=&v"(al[4]), "=&v"(al[5]), "=&v"(al[6]), "=&v"(al[7]),
          "=&v"(ah[0]), "=&v"(ah[1]), "=&v"(ah[2]), "=&v"(ah[3]), "=&v"(ah[4]), "=&v"(ah[5]), "=&v"(ah[6]), "=&v"(ah[7]),
          "=&v"(B[0]), "=&v"(B[1]), "=&v"(B[2]), "=&v"(B[3]), "=&v"(B[4]), "=&v"(B[5]), "=&v"(B[6]), "=&v"(B[7])
        : "v"(aa), "v"(ba) : "memory");
#pragma unroll
    for (int a = 0; a < 8; a++)
#pragma unroll
      for (int i = 0; i + a < 8; i++) acc[a + i] = mfma16(mk8(al[7 - a], ah[7 - a]), __builtin_bit_cast(bf16x8, B[i]), acc[a + i]);
  }
}
__device__ __forceinline__ void conv_store(f32x4 y, unsigned cb, int c, int lane, float Dd, __amdgpu_buffer_rsrc_t rw, int d) {
  const int fr = lane & 15, g4 = lane >> 4;
  const u32x2 uu = lds_b64(cb + UOFF + 512 + 2 * (256 * c + 16 * fr + 4 * g4));
  const float r0 = rbf(y[0]) + rbf(bflo(uu.x) * Dd), r1 = rbf(y[1]) + rbf(bfhi(uu.x) * Dd);
  const float r2 = rbf(y[2]) + rbf(bflo(uu.y) * Dd), r3 = rbf(y[3]) + rbf(bfhi(uu.y) * Dd);
  __builtin_amdgcn_raw_buffer_store_b64((u32x2){pk2(r0, r1), pk2(r2, r3)}, rw, OFF_YT + d * 4096 + (256 * c + 16 * fr + 4 * g4) * 2, 0, SC1);
}

__global__ __launch_bounds__(512, 2) void fused(const u16* __restrict__ x, const u16* __restrict__ wqkv, const u16* __restrict__ wo,
                                                const u16* __restrict__ Bk, const u16* __restrict__ Bv, const u16* __restrict__ shk,
                                                const u16* __restrict__ hk, const u16* __restrict__ Dv, const u16* __restrict__ gw,
                                                u16* __restrict__ out, char* __restrict__ ws) {
  extern __shared__ __attribute__((aligned(16))) char sm[];
  const int tid = threadIdx.x, lane = tid & 63;
  const int w = __builtin_amdgcn_readfirstlane(tid >> 6);
  const int b = blockIdx.x, xcd = b & 7, r = b >> 3;
  const int fr = lane & 15, g4 = lane >> 4;
  const unsigned lb = (unsigned)(size_t)(LAS char*)sm;
  const __amdgpu_buffer_rsrc_t rw = __builtin_amdgcn_make_buffer_rsrc(ws, 0, WS_USED, 0x00020000);
  TS_DECL TS(0);
  unsigned e0 = 0;
  if (w == 7) e0 = flag_ld(ws, F_E);
#define PUT_TAG() do { if (w == 7 && lane == 0) { *(LAS unsigned*)(size_t)(lb + L_TAG) = e0 + 1u; *(LAS u32x4*)(size_t)(lb + L_CNT) = (u32x4){0u, 0u, 0u, 0u}; } } while (0)
#define TAG (*(const LAS unsigned*)(size_t)(lb + L_TAG))
  bool wo_issued = false;
  if (r < 24) {
    const bool isKV = r < 16;
    const int rt = (xcd & 3) * 4 + (r & 3);
    const int cg = isKV ? (xcd >> 2) * 4 + (r >> 2) : (xcd >> 2) * 2 + ((r - 16) >> 2);
    f32x4 acc[4][5];
    const int rp = (w >> 1) & 1, hf = w & 1;
    if (w < 4) {
      const unsigned xa0 = lb + (rp * 64 + fr) * 128 + ((g4 ^ ((fr >> 1) & 7)) << 4);
      const unsigned wl = lb + XS + (8 * g4 + (fr >> 2)) * 320 + ((lane & 3) >> 1) * 16 + (lane & 1) * 8 + hf * 160;
      const unsigned sgn = (g4 & 1) * 32;
      const unsigned wa0 = hf ? wl - sgn : wl + sgn, wa1 = hf ? wl + sgn : wl - sgn;
#pragma unroll
      for (int i = 0; i < 4; i++)
#pragma unroll
        for (int t = 0; t < 5; t++) acc[i][t] = (f32x4){0.f, 0.f, 0.f, 0.f};
      if (isKV) gemm_compute<true>(acc, xa0, wa0, wa1); else gemm_compute<false>(acc, xa0, wa0, wa1);
    } else if (w < 6) {
      gemm_loader(w - 4, lane, lb, x, wqkv, rt, cg, isKV);
    } else {
      for (int s = 0; s < 5; s++) BAR();
      PUT_TAG();
      for (int s = 5; s < 10; s++) BAR();
    }
    TS(1);
    BAR();
    TS(11);
    const unsigned tag = TAG;
    if (isKV) {
      constexpr int L_PS = 40960;
      if (w < 4) {
#pragma unroll
        for (int t = 0; t < 5; t++) {
          const int slot = hf ? (t < 2 ? 3 + t : 5 + t) : (t < 3 ? t : 2 + t);
#pragma unroll
          for (int m = 0; m < 2; m++)
            *(LAS u32x4*)(size_t)(lb + ((rp * 2 + m) * 10 + slot) * 1024 + lane * 16) =
                (u32x4){pk2(acc[2 * m][t][0], acc[2 * m][t][1]), pk2(acc[2 * m][t][2], acc[2 * m][t][3]),
                        pk2(acc[2 * m + 1][t][0], acc[2 * m + 1][t][1]), pk2(acc[2 * m + 1][t][2], acc[2 * m + 1][t][3])};
        }
      } else {
        for (int i = tid - 256; i < 6048 / 16; i += 256) *(LAS u32x4*)(size_t)(lb + L_T + i * 16) = (u32x4){0, 0, 0, 0};
      }
      TS(12);
      BAR();
      {
        const int f0 = w < 4 ? 2 * w : 8 + 3 * (w - 4);
        u32x4 v[3];
#pragma unroll
        for (int q = 0; q < 3; q++) { const int f = f0 + (q < 2 || w >= 4 ? q : 0); v[q] = *(const LAS u32x4*)(size_t)(lb + ((f / 5) * 10 + (f % 5)) * 1024 + lane * 16); }
#pragma unroll
        for (int q = 0; q < 3; q++) {
          if (q < 2 || w >= 4) {
            const int f = f0 + q;
            *(LAS u32x4*)(size_t)(lb + ((f / 5) * 10 + (f % 5)) * 1024 + lane * 16) =
                (u32x4){pk2(phi(bflo(v[q].x)), phi(bfhi(v[q].x))), pk2(phi(bflo(v[q].y)), phi(bfhi(v[q].y))),
                        pk2(phi(bflo(v[q].z)), phi(bfhi(v[q].z))), pk2(phi(bflo(v[q].w)), phi(bfhi(v[q].w)))};
          }
        }
      }
      TS(13);
      BAR();
      TS(2);
      {
        const u32x4 o1 = {0x3F803F80u, 0x3F803F80u, 0x3F803F80u, 0x3F803F80u};
        bf16x8 A[3][4], B[3][4];
        int ti[3], tj[3];
#pragma unroll
        for (int q = 0; q < 3; q++) {
          const int T = (q < 2 || w < 2) ? w + 8 * q : w;
          const bool isz = T >= 13;
          ti[q] = isz ? T - 13 : (T + 1) / 3;
          tj[q] = isz ? -1 : T - (ti[q] == 0 ? 0 : 3 * ti[q] - 1) + (ti[q] == 0 ? 0 : ti[q] - 1);
          const unsigned aA = lb + ti[q] * 1024 + lane * 16, aB = lb + (5 + (isz ? 0 : tj[q])) * 1024 + lane * 16;
#pragma unroll
          for (int m = 0; m < 4; m++) { A[q][m] = lds_b128(aA + m * 10240); B[q][m] = lds_b128(aB + m * 10240); }
        }
        f32x4 d4[3];
#pragma unroll
        for (int q = 0; q < 3; q++) {
          d4[q] = (f32x4){0.f, 0.f, 0.f, 0.f};
          if (tj[q] < 0) {
#pragma unroll
            for (int m = 0; m < 4; m++) B[q][m] = __builtin_bit_cast(bf16x8, o1);
          }
        }
#pragma unroll
        for (int m = 0; m < 4; m++)
#pragma unroll
          for (int q = 0; q < 3; q++) d4[q] = mfma16(A[q][m], B[q][m], d4[q]);
#pragma unroll
        for (int q = 0; q < 3; q++) {
          if (q < 2 || w < 2) {
            const bool isz = tj[q] < 0;
            const int Dg = 16 * ti[q] + 4 * g4, h = (Dg * 205) >> 12, dd = Dg - 20 * h;
            const int ee = isz ? 20 : 16 * tj[q] + fr - 20 * h;
            if (isz ? fr == 0 : (ee >= 0 && ee < 20))
              *(LAS u32x2*)(size_t)(lb + L_PS + (h * 420 + ee * 20 + dd) * 2) = (u32x2){pk2(d4[q][0], d4[q][1]), pk2(d4[q][2], d4[q][3])};
          }
        }
      }
      BAR();
      if (tid < 210) __builtin_amdgcn_raw_buffer_store_b128(*(const LAS u32x4*)(size_t)(lb + L_PS + tid * 16), rw, OFF_P + (rt * 8 + cg) * 3360 + tid * 16, 0, SC1);
      TS(14);
      DRAIN();
      TS(15);
      BAR();
      TS(3);
      if (w == 0) {
        if (lane == 0) flag_st(ws, F_P + (rt * 8 + cg) * 128, tag);
        poll(ws, lane < 16 ? F_P + (lane * 8 + cg) * 128 : F_PQ + (rt * 4 + (cg >> 1)) * 128, lane <= 16, tag);
      }
      constexpr int PJ[9] = {0, 1, 1, 2, 2, 3, 3, 4, 4}, PK[9] = {0, 0, 1, 0, 1, 1, 2, 1, 2};
      unsigned an[9], ad[9];
#pragma unroll
      for (int p = 0; p < 9; p++) {
        const int E = 16 * PJ[p] + fr, h = (E * 205) >> 12, e = E - 20 * h;
        int o_ = 32 * PK[p] + 8 * g4 - 20 * h; o_ = o_ < -8 ? -8 : o_ > 20 ? 20 : o_;
        an[p] = lb + L_T + (h * 21 + e) * 72 + (8 + o_) * 2; ad[p] = lb + L_T + (h * 21 + 20) * 72 + (8 + o_) * 2;
      }
      BAR();
      TS(4);
      if (w < 4) {
        bf16x8 Bq[2][3];
#pragma unroll
        for (int i = 0; i < 2; i++) {
          const unsigned prow = OFF_PQ + ((rt * 8 + 2 * w + i) * 8 + cg) * 3072 + lane * 16;
#pragma unroll
          for (int ks = 0; ks < 3; ks++) Bq[i][ks] = __builtin_bit_cast(bf16x8, __builtin_amdgcn_raw_buffer_load_b128(rw, prow + 1024 * ks, 0, SC1));
        }
        if (tid < 210) {
          const unsigned pb = OFF_P + cg * 3360 + tid * 16;
          u32x4 v[16];
#pragma unroll
          for (int k = 0; k < 16; k++) v[k] = __builtin_amdgcn_raw_buffer_load_b128(rw, pb, (k ^ rt) * (8 * 3360), SC1);
          unsigned o4[4];
#pragma unroll
          for (int c = 0; c < 4; c++) {
            float lo[16], hi[16];
#pragma unroll
            for (int k = 0; k < 16; k++) { lo[k] = bflo(v[k][c]); hi[k] = bfhi(v[k][c]); }
#pragma unroll
            for (int st = 1; st < 16; st <<= 1)
#pragma unroll
              for (int k = 0; k < 16; k += 2 * st) { lo[k] += lo[k + st]; hi[k] += hi[k + st]; }
            o4[c] = pk2(lo[0], hi[0]);
          }
          TS(16);
#pragma unroll
          for (int q = 0; q < 2; q++) {
            const int i4 = 2 * tid + q, row = (i4 * 13108) >> 16, d = 4 * (i4 - row * 5);
            *(LAS u32x2*)(size_t)(lb + L_T + row * 72 + (8 + d) * 2) = (u32x2){o4[2 * q], o4[2 * q + 1]};
          }
        }
        lds_sync(lb + L_CNT, lane, 4);
        TS(5);
        bf16x8 An[9], Ad[9];
#pragma unroll
        for (int p = 0; p < 9; p++) {
          unsigned an8 = an[p] + 8, ad8 = ad[p] + 8;
          asm volatile("" : "+v"(an8), "+v"(ad8));
          An[p] = mk8(lds_b64(an[p]), lds_b64(an8)); Ad[p] = mk8(lds_b64(ad[p]), lds_b64(ad8));
        }
#pragma unroll
        for (int i = 0; i < 2; i++) {
          if (g4 >= 2) Bq[i][2] = __builtin_bit_cast(bf16x8, (u32x4){0, 0, 0, 0});
          f32x4 nu[5], de[5];
#pragma unroll
          for (int j = 0; j < 5; j++) { nu[j] = (f32x4){0.f, 0.f, 0.f, 0.f}; de[j] = nu[j]; }
#pragma unroll
          for (int p = 0; p < 9; p++) { nu[PJ[p]] = mfma16(An[p], Bq[i][PK[p]], nu[PJ[p]]); de[PJ[p]] = mfma16(Ad[p], Bq[i][PK[p]], de[PJ[p]]); }
          u32x2 ov[5];
#pragma unroll
          for (int j = 0; j < 5; j++) {
            const float rd = __builtin_amdgcn_rcpf(rbf(rbf(de[j][0]) + 1e-6f));
            ov[j] = (u32x2){pk2(rbf(nu[j][0]) * rd, rbf(nu[j][1]) * rd), pk2(rbf(nu[j][2]) * rd, rbf(nu[j][3]) * rd)};
          }
          store_frags(ov, rw, OFF_O + (rt * 8 + 2 * w + i) * 20480, 80 * cg, fr, g4);
        }
        TS(17);
        DRAIN();
        if (lds_last(lb + L_CNT + 4, lane, 4) && lane == 0) flag_st(ws, F_O + (rt * 8 + cg) * 128, tag);
        TS(6);
      } else {
        poll(ws, F_W + lane * 128, true, tag);
        const i32x4 sws = make_srd(ws, WS_USED);
        const unsigned vwv = OFF_WOP + ((xcd >> 2) * 4 + (r >> 3)) * 102400 + lane * 16;
        for (int i = w - 4; i < 100; i += 4) { int pc = 12 * (r & 7) + i; pc = pc >= 100 ? pc - 100 : pc; glds16_sc1(lb + pc * 1024, vwv, sws, pc * 1024); }
        DRAIN();
      }
      wo_issued = true;
    } else {
      const int vb = xcd * 8 + (r - 16);
      const int d = 80 * (vb >> 3) + 48 + 4 * (vb & 7) + (w & 3);
      u32x4 hc[4], hp[4];
      if (w < 4) {
        conv_load_h(hc, hp, hk + (size_t)d * S, lane);
#pragma unroll
        for (int i = 0; i < 4; i++) {
          u32x2 pv[5];
#pragma unroll
          for (int t = 0; t < 5; t++) pv[t] = (u32x2){pk2(phi(acc[i][t][0]), phi(acc[i][t][1])), pk2(phi(acc[i][t][2]), phi(acc[i][t][3]))};
          store_frags(pv, rw, OFF_PQ + ((rt * 8 + rp * 4 + i) * 8 + cg * 2 + hf) * 3072, 0, fr, g4);
        }
        DRAIN();
      } else if (w == 7) {
        poll(ws, F_U + lane * 128, true, tag);
      }
      BAR();
      TS(2);
      if (tid == 0) flag_st(ws, F_PQ + (rt * 4 + cg) * 128, tag);
      if (w < 4) {
        const unsigned cb = lb + w * CHS;
        const float Dd = bflo(Dv[d]);
        conv_load_u(rw, d, cb, lane, [&]() { conv_prep_h(hc, hp, cb, lane); });
        TS(3);
        f32x4 y[8];
        conv_main(y, cb, lane, 0, 8);
        TS(4);
#pragma unroll
        for (int c = 0; c < 8; c++) conv_store(y[c], cb, c, lane, Dd, rw, d);
        DRAIN();
      } else {
        const int w4 = w - 4;
        const int l16 = lane & 15;
        const u32x4 g0 = *(const u32x4*)(gw + lane * 8), g1 = *(const u32x4*)(gw + 512 + l16 * 8);
        u32x4 gx0[8], gx1[8];
#pragma unroll
        for (int i = 0; i < 8; i++) {
          const u16* xr = x + (size_t)(vb * 32 + w4 * 8 + i) * D;
          gx0[i] = *(const u32x4*)(xr + lane * 8); gx1[i] = *(const u32x4*)(xr + 512 + l16 * 8);
        }
        u16 we[4][8];
#pragma unroll
        for (int it = 0; it < 4; it++) {
          const int ql = w4 * 64 + lane + it * 256, q = vb * 800 + (ql < 800 ? ql : 0);
          const int f = q >> 6, ln = q & 63, f5 = (f * 3277) >> 14, t = f - 5 * f5, ct = (f5 * 3277) >> 16, ks = f5 - 20 * ct;
          const u16* src = wo + (size_t)(32 * ks + 8 * (ln >> 4)) * D + 80 * ct + 16 * t + (ln & 15);
#pragma unroll
          for (int j = 0; j < 8; j++) we[it][j] = src[j * D];
        }
        SBAR();
        float a[8];
        const float m1 = lane < 16 ? 1.f : 0.f;
#pragma unroll
        for (int i = 0; i < 8; i++) {
          float a0 = 0.f, a1 = 0.f;
#pragma unroll
          for (int j = 0; j < 4; j++) {
            a0 = fmaf(bflo(gx0[i][j]), bflo(g0[j]), a0); a0 = fmaf(bfhi(gx0[i][j]), bfhi(g0[j]), a0);
            a1 = fmaf(bflo(gx1[i][j]), bflo(g1[j]), a1); a1 = fmaf(bfhi(gx1[i][j]), bfhi(g1[j]), a1);
          }
          a[i] = fmaf(a1, m1, a0);
        }
#pragma unroll
        for (int i = 0; i < 4; i++) { const float lo = a[i], hi = a[i + 4]; const bool up = lane & 4; a[i] = (up ? hi : lo) + __shfl_xor(up ? lo : hi, 4); }
#pragma unroll
        for (int i = 0; i < 2; i++) { const float lo = a[i], hi = a[i + 2]; const bool up = lane & 2; a[i] = (up ? hi : lo) + __shfl_xor(up ? lo : hi, 2); }
        { const float lo = a[0], hi = a[1]; const bool up = lane & 1; a[0] = (up ? hi : lo) + __shfl_xor(up ? lo : hi, 1); }
        a[0] += __shfl_xor(a[0], 8); a[0] += __shfl_xor(a[0], 16); a[0] += __shfl_xor(a[0], 32);
        if (lane < 8) {
          const int row = ((lane & 4) ? 4 : 0) + ((lane & 2) ? 2 : 0) + (lane & 1);
          __builtin_amdgcn_raw_buffer_store_b32(__float_as_uint(rbf(1.f / (1.f + __expf(-rbf(a[0]))))), rw, OFF_G + (vb * 32 + w4 * 8 + row) * 4, 0, SC1);
        }
#pragma unroll
        for (int it = 0; it < 4; it++) {
          const int ql = w4 * 64 + lane + it * 256;
          if (ql < 800) {
            const u32x4 v = {we[it][0] | ((unsigned)we[it][1] << 16), we[it][2] | ((unsigned)we[it][3] << 16),
                             we[it][4] | ((unsigned)we[it][5] << 16), we[it][6] | ((unsigned)we[it][7] << 16)};
            __builtin_amdgcn_raw_buffer_store_b128(v, rw, OFF_WOP + (vb * 800 + ql) * 16, 0, SC1);
          }
        }
        DRAIN();
        unsigned old = 0;
        if (lane == 0) old = __hip_atomic_fetch_add((LAS unsigned*)(size_t)(lb + L_CNT), 1u, __ATOMIC_RELAXED, __HIP_MEMORY_SCOPE_WORKGROUP);
        if (lane == 0 && old == 3u) flag_st(ws, F_W + vb * 128, tag);
      }
      BAR();
      if (tid == 0) flag_st(ws, F_Y + (64 + vb) * 128, tag);
      TS(5); TS(6);
    }
  } else {
    const int pw = xcd * 8 + (r - 24);
    const int c6 = w < 4 ? w : 4 + ((w - 4) >> 1);
    const int d = 80 * (pw >> 3) + 6 * (pw & 7) + c6;
    const unsigned cb = lb + c6 * CHS;
    const bool prep = w < 4 || !(w & 1);
    u32x4 hc[4], hp[4];
    {
      const int rbase = xcd * 256 + ((r - 24) >> 1) * 64, cbase = ((r - 24) & 1) * 320;
      u32x4 kr[2][7], vr[2][4], sp[2][4];
#pragma unroll
      for (int q = 0; q < 2; q++) {
        const int it = w + 8 * q;
        if (q == 0 || w < 2) {
          const int ch = cbase + (it >> 1) * 64 + (lane & 7) * 8;
          const int s0 = rbase + (it & 1) * 32 + (lane >> 3) * 4;
#pragma unroll
          for (int j = 0; j < 7; j++) { int s = s0 - 3 + j; kr[q][j] = *(const u32x4*)(Bk + (size_t)(s < 0 ? 0 : s) * D + ch); }
#pragma unroll
          for (int j = 0; j < 4; j++) vr[q][j] = *(const u32x4*)(Bv + (size_t)(s0 + j) * D + ch);
#pragma unroll
          for (int j = 0; j < 4; j++) sp[q][j] = ((const u32x4*)(shk + ch * 4))[j];
        }
      }
      if (prep) conv_load_h(hc, hp, hk + (size_t)d * S, lane);
      SBAR();
#pragma unroll
      for (int q = 0; q < 2; q++) {
        const int it = w + 8 * q;
        if (q == 0 || w < 2) {
          const int s0 = rbase + (it & 1) * 32 + (lane >> 3) * 4;
          if (s0 == 0) { kr[q][0] = (u32x4){0, 0, 0, 0}; kr[q][1] = kr[q][0]; kr[q][2] = kr[q][0]; }
          const int rg = (it & 1) * 8 + (lane >> 3);
          const unsigned la = lb + L_UIMG + ((it >> 1) * 64 + (lane & 7) * 8) * 128 + (((rg >> 1) ^ (lane & 7)) << 4) + (rg & 1) * 8;
#pragma unroll
          for (int c = 0; c < 8; c++) {
            const unsigned s01 = sp[q][c >> 1][(c & 1) * 2], s23 = sp[q][c >> 1][(c & 1) * 2 + 1];
            const float w0 = bflo(s01), w1 = bfhi(s01), w2 = bflo(s23), w3 = bfhi(s23);
            float kk[7], u[4];
#pragma unroll
            for (int j = 0; j < 7; j++) { unsigned dd = kr[q][j][c >> 1]; kk[j] = (c & 1) ? bfhi(dd) : bflo(dd); }
#pragma unroll
            for (int j = 0; j < 4; j++) {
              unsigned dd = vr[q][j][c >> 1]; float v = (c & 1) ? bfhi(dd) : bflo(dd);
              float ks = kk[j] * w3; ks = fmaf(kk[j + 1], w2, ks); ks = fmaf(kk[j + 2], w1, ks); ks = fmaf(kk[j + 3], w0, ks);
              u[j] = ks * v;
            }
            *(LAS u32x2*)(size_t)(la + c * 128) = (u32x2){pk2(u[0], u[1]), pk2(u[2], u[3])};
          }
        }
      }
      PUT_TAG();
      BAR();
#pragma unroll
      for (int i = 0; i < 5; i++) {
        const int chl = (w * 5 + i) * 8 + (lane >> 3), k = lane & 7;
        const u32x4 v = *(const LAS u32x4*)(size_t)(lb + L_UIMG + chl * 128 + ((k ^ ((chl >> 3) & 7)) << 4));
        __builtin_amdgcn_raw_buffer_store_b128(v, rw, OFF_UT + (cbase + chl) * 4096 + rbase * 2 + k * 16, 0, SC1);
      }
      TS(1);
    }
    if (w < 5 && prep) conv_prep_h(hc, hp, cb, lane);
    DRAIN();
    BAR();
    TS(2);
    const unsigned tag = TAG;
    if (tid == 0) flag_st(ws, F_U + pw * 128, tag);
    const float Dd = bflo(Dv[d]);
    if (w == 6) conv_prep_h(hc, hp, cb, lane);
    if (w == 7) poll(ws, F_U + lane * 128, true, tag);
    BAR();
    if (prep) conv_load_u(rw, d, cb, lane, [&]() {});
    BAR();
    TS(3);
    f32x4 y[8];
    conv_main(y, cb, lane, w < 4 ? 0 : (w & 1) * 4, w < 4 ? 8 : (w & 1) * 4 + 4);
    TS(4);
    if (w < 4) {
#pragma unroll
      for (int c = 0; c < 8; c++) conv_store(y[c], cb, c, lane, Dd, rw, d);
    }
    BAR();
    if (w >= 4) {
#pragma unroll
      for (int c = 0; c < 4; c++) { const f32x4 lo = y[c], hi = y[4 + c]; *(LAS f32x4*)(size_t)(cb + ((w & 1) * 4 + c) * 1024 + lane * 16) = (w & 1) ? lo : hi; }
    }
    BAR();
    if (w >= 4) {
#pragma unroll
      for (int c = 0; c < 4; c++) {
        const f32x4 o_ = *(const LAS f32x4*)(size_t)(cb + (((w & 1) ^ 1) * 4 + c) * 1024 + lane * 16);
        const f32x4 lo = y[c], hi = y[4 + c];
        const f32x4 mine = (w & 1) ? hi : lo;
        conv_store((w & 1) ? o_ + mine : mine + o_, cb, (w & 1) * 4 + c, lane, Dd, rw, d);
      }
    }
    DRAIN();
    BAR();
    if (tid == 0) flag_st(ws, F_Y + pw * 128, tag);
    TS(5); TS(6);
  }

  {
    const unsigned tag = TAG;
    const int rt = (xcd & 3) * 8 + (r & 7), ct = (xcd >> 2) * 4 + (r >> 3);
    const int sw_ = w & 3, kk = w >> 2;
    const i32x4 sws = make_srd(ws, WS_USED);
    if (!wo_issued) {
      if (w == 0) poll(ws, F_W + lane * 128, true, tag);
      BAR();
      const unsigned vwv = OFF_WOP + ct * 102400 + lane * 16;
      for (int i = w; i < 100; i += 8) { int pc = 12 * (r & 7) + i; pc = pc >= 100 ? pc - 100 : pc; glds16_sc1(lb + pc * 1024, vwv, sws, pc * 1024); }
    }
    TS(18);
    TS(19);
    const int srow = rt * 64 + sw_ * 16 + fr;
    u32x2 xv[3];
#pragma unroll
    for (int t = 0; t < 3; t++) { const int tt = kk ? (t < 2 ? 3 + t : 4) : t; xv[t] = *(const u32x2*)(x + (size_t)srow * D + ct * 80 + 16 * tt + 4 * g4); }
    if (w == 0) {
      poll(ws, F_O + ((rt >> 1) * 8 + (lane & 7)) * 128, lane < 8, tag);
    } else if (w == 7) {
      poll(ws, F_Y + ((lane & 8 ? 64 : 0) + ct * 8 + (lane & 7)) * 128, lane < 16, tag);
      for (int pc = 0; pc < 10; pc++)
        glds16_sc1(lb + L_Y + pc * 1024, OFF_YT + (ct * 80 + pc * 8 + (lane >> 3)) * 4096 + rt * 128 + (lane & 7) * 16, sws, 0);
    } else if (w == 6 && b == 0) {
      poll(ws, F_U + lane * 128, true, tag); poll(ws, F_PQ + lane * 128, true, tag);
      poll(ws, F_P + lane * 128, true, tag); poll(ws, F_P + (64 + lane) * 128, true, tag);
      if (lane == 0) flag_st(ws, F_E, tag);
    }
    DRAIN();
    BAR();
    TS(7);
    const float g = __uint_as_float(__builtin_amdgcn_raw_buffer_load_b32(rw, OFF_G + srow * 4, 0, SC1));
    bf16x8 of[10];
    const int rot = ((ct & 3) * 5) >> 1;
    {
      const unsigned vo = OFF_O + (rt * 4 + sw_) * 20480 + kk * 1024 + lane * 16;
#pragma unroll
      for (int s = 0; s < 10; s++) { int q = s + rot; q = q >= 10 ? q - 10 : q; of[s] = __builtin_bit_cast(bf16x8, __builtin_amdgcn_raw_buffer_load_b128(rw, vo, q * 2048, SC1)); }
    }
    TS(8);
    f32x4 acc[5];
#pragma unroll
    for (int t = 0; t < 5; t++) acc[t] = (f32x4){0.f, 0.f, 0.f, 0.f};
    const unsigned wa0 = lb + kk * 5120 + lane * 16;
#pragma unroll
    for (int hh = 0; hh < 2; hh++) {
      bf16x8 wf[5][5];
#pragma unroll
      for (int s = 0; s < 5; s++) {
        int q = hh * 5 + s + rot; q = q >= 10 ? q - 10 : q;
        const unsigned wa = wa0 + q * 10240;
#pragma unroll
        for (int t = 0; t < 5; t++) wf[s][t] = lds_b128(wa + t * 1024);
      }
#pragma unroll
      for (int s = 0; s < 5; s++)
#pragma unroll
        for (int t = 0; t < 5; t++) acc[t] = mfma16(wf[s][t], of[hh * 5 + s], acc[t]);
      SGB(0x100, 10);
#pragma unroll
      for (int i = 0; i < 15; i++) { SGB(0x8, 1); SGB(0x100, 1); }
      SGB(0x8, 10);
      SBAR();
    }
    TS(9);
    const unsigned ra = lb + L_RED + (sw_ * 5) * 1024 + lane * 16;
    if (kk == 0) { *(LAS f32x4*)(size_t)(ra + 3 * 1024) = acc[3]; *(LAS f32x4*)(size_t)(ra + 4 * 1024) = acc[4]; }
    else { *(LAS f32x4*)(size_t)(ra) = acc[0]; *(LAS f32x4*)(size_t)(ra + 1024) = acc[1]; *(LAS f32x4*)(size_t)(ra + 2048) = acc[2]; }
    BAR();
    TS(20);
    const float omg = rbf(1.f - g);
    const int nt = kk ? 2 : 3;
#pragma unroll
    for (int t = 0; t < 3; t++) {
      if (t < nt) {
        const int tt = kk ? 3 + t : t;
        const f32x4 a0 = acc[t], a1 = acc[3 + (t < 2 ? t : 1)];
        f32x4 a = *(const LAS f32x4*)(size_t)(ra + tt * 1024) + (kk ? a1 : a0);
        u32x2 yv = lds_tr(lb + L_Y + (16 * tt + 4 * g4 + (fr >> 2)) * 128 + (sw_ * 16 + 4 * (fr & 3)) * 2);
        float xs[4] = {bflo(xv[t].x), bfhi(xv[t].x), bflo(xv[t].y), bfhi(xv[t].y)};
        float ys[4] = {bflo(yv.x), bfhi(yv.x), bflo(yv.y), bfhi(yv.y)};
        float r_[4];
#pragma unroll
        for (int q = 0; q < 4; q++) r_[q] = rbf(g * rbf(a[q])) + rbf(omg * rbf(xs[q] * ys[q]));
        *(u32x2*)(out + (size_t)srow * D + ct * 80 + 16 * tt + 4 * g4) = (u32x2){pk2(r_[0], r_[1]), pk2(r_[2], r_[3])};
      }
    }
    TS(10); TS_OUT(21);
  }
}

extern "C" void kernel_launch(void* const* d_in, const int* in_sizes, int n_in, void* d_out, int out_size, void* d_ws, size_t ws_size,
                              hipStream_t stream) {
  const u16* x = (const u16*)d_in[0]; const u16* wqkv = (const u16*)d_in[1]; const u16* wo = (const u16*)d_in[2];
  const u16* Bk = (const u16*)d_in[4]; const u16* Bv = (const u16*)d_in[5]; const u16* shk = (const u16*)d_in[6];
  const u16* hk = (const u16*)d_in[7]; const u16* Dv = (const u16*)d_in[8]; const u16* gw = (const u16*)d_in[9];
  (void)hipFuncSetAttribute((const void*)fused, hipFuncAttributeMaxDynamicSharedMemorySize, LDS_TOTAL);
  fused<<<256, 512, LDS_TOTAL, stream>>>(x, wqkv, wo, Bk, Bv, shk, hk, Dv, gw, (u16*)d_out, (char*)d_ws);
}
```

```cpp
#include <hip/hip_runtime.h>
#include <hip/hip_bf16.h>
#include <type_traits>
#define NO_PF

typedef unsigned short u16;
typedef __bf16 bf16x2 __attribute__((ext_vector_type(2)));
typedef __bf16 bf16x8 __attribute__((ext_vector_type(8)));
typedef float f32x4 __attribute__((ext_vector_type(4)));
typedef unsigned u32x4 __attribute__((ext_vector_type(4)));
typedef unsigned u32x2 __attribute__((ext_vector_type(2)));
typedef short v4i16_t __attribute__((ext_vector_type(4)));
typedef int i32x4 __attribute__((ext_vector_type(4)));
typedef __attribute__((address_space(1))) unsigned gu32;
#define LAS __attribute__((address_space(3)))
#define SBAR() __builtin_amdgcn_sched_barrier(0)
#define SGB(mask, n) __builtin_amdgcn_sched_group_barrier(mask, n, 0)
#define BAR() asm volatile("s_waitcnt lgkmcnt(0)\n\ts_barrier" ::: "memory")
#define DRAIN() asm volatile("s_waitcnt vmcnt(0)" ::: "memory")
#define SC1 16

#define TS_DECL
#define TSARG
#define TSPAR
#define TS(i)
#define TS_OUT(n)

constexpr int S = 2048, D = 640;
constexpr unsigned SZ = S * D * 2;
constexpr unsigned F_E = 0, F_U = 128, F_PQ = F_U + 64 * 128, F_P = F_PQ + 64 * 128, F_O = F_P + 128 * 128, F_Y = F_O + 128 * 128, F_W = F_Y + 128 * 128;
constexpr unsigned OFF_PQ = 0x20000, OFF_P = OFF_PQ + 128 * 8 * 3 * 1024, OFF_UT = OFF_P + (1u << 19), OFF_G = OFF_UT + SZ, OFF_WOP = OFF_G + 8192,
                   OFF_O = OFF_WOP + (1u << 20), OFF_YT = OFF_O + SZ, WS_USED = OFF_YT + SZ;
constexpr int XS = 16384, WSB = 20480, STG = XS + WSB;
constexpr int L_EXTRA = 4 * STG;
constexpr int L_TAG = L_EXTRA, L_CNT = L_EXTRA + 16, L_T = L_EXTRA + 64;
constexpr int LDS_TOTAL = L_T + 6144 + 5120;
constexpr int CPS = 4160, UOFF = 4 * CPS, CHS = UOFF + 4608;
constexpr int L_UIMG = 5 * CHS;
constexpr int L_Y = 102400, L_RED = L_Y + 10240;

__device__ __forceinline__ unsigned pk2(float a, float b) { bf16x2 r = {(__bf16)a, (__bf16)b}; return __builtin_bit_cast(unsigned, r); }
__device__ __forceinline__ float rbf(float a) { return (float)(__bf16)a; }
__device__ __forceinline__ float bflo(unsigned v) { return __uint_as_float(v << 16); }
__device__ __forceinline__ float bfhi(unsigned v) { return __uint_as_float(v & 0xffff0000u); }
__device__ __forceinline__ float phi(float t) {
  return __builtin_amdgcn_fmed3f(t, 0.f, 3.0e38f) + (__builtin_amdgcn_fmed3f(__builtin_amdgcn_exp2f(t * 1.44269504f), 0.f, 1.f) + 1e-6f);
}
__device__ __forceinline__ i32x4 make_srd(const void* p, unsigned bytes) {
  unsigned long long a = (unsigned long long)p; i32x4 r;
  r.x = __builtin_amdgcn_readfirstlane((int)(unsigned)a); r.y = __builtin_amdgcn_readfirstlane((int)(unsigned)(a >> 32));
  r.z = __builtin_amdgcn_readfirstlane((int)bytes); r.w = 0x00020000; return r;
}
__device__ __forceinline__ void glds16(unsigned ldsaddr, unsigned voff, i32x4 srd, unsigned soff) {
  asm volatile("s_mov_b32 m0, %0\n\ts_nop 0\n\tbuffer_load_dwordx4 %1, %2, %3 offen lds" :: "s"(ldsaddr), "v"(voff), "s"(srd), "s"(soff) : "memory");
}
__device__ __forceinline__ void glds16_sc1(unsigned ldsaddr, unsigned voff, i32x4 srd, unsigned soff) {
  asm volatile("s_mov_b32 m0, %0\n\ts_nop 0\n\tbuffer_load_dwordx4 %1, %2, %3 offen sc1 lds" :: "s"(ldsaddr), "v"(voff), "s"(srd), "s"(soff) : "memory");
}
template <int N> __device__ __forceinline__ void wait_bar() { asm volatile("s_waitcnt vmcnt(%0) lgkmcnt(0)\n\ts_barrier" :: "n"(N) : "memory"); }
__device__ __forceinline__ bf16x8 lds_b128(unsigned addr) { return *(const LAS bf16x8*)(size_t)addr; }
__device__ __forceinline__ u32x2 lds_b64(unsigned addr) { return *(const LAS u32x2*)(size_t)addr; }
__device__ __forceinline__ u32x2 lds_tr(unsigned addr) { return __builtin_bit_cast(u32x2, __builtin_amdgcn_ds_read_tr16_b64_v4i16((LAS v4i16_t*)(size_t)addr)); }
__device__ __forceinline__ bf16x8 mk8(u32x2 lo, u32x2 hi) { u32x4 t = {lo.x, lo.y, hi.x, hi.y}; return __builtin_bit_cast(bf16x8, t); }
__device__ __forceinline__ f32x4 mfma16(bf16x8 a, bf16x8 b, f32x4 c) { return __builtin_amdgcn_mfma_f32_16x16x32_bf16(a, b, c, 0, 0, 0); }

__device__ __forceinline__ u32x4 pair16(u32x2 a, u32x2 b) {
  const auto r0 = __builtin_amdgcn_permlane16_swap(a.x, b.x, false, false);
  const auto r1 = __builtin_amdgcn_permlane16_swap(a.y, b.y, false, false);
  return (u32x4){r0[0], r1[0], r0[1], r1[1]};
}
__device__ __forceinline__ void store_frags(const u32x2 (&v)[5], __amdgpu_buffer_rsrc_t rw, unsigned base, int k0, int fr, int g4) {
#pragma unroll
  for (int j = 0; j < 4; j += 2) {
    const int k = k0 + 16 * (j + (g4 & 1)) + 8 * (g4 >> 1);
    __builtin_amdgcn_raw_buffer_store_b128(pair16(v[j], v[j + 1]), rw, base + (k >> 5) * 1024 + (((k >> 3) & 3) * 16 + fr) * 16, 0, SC1);
  }
  const int k = k0 + 64 + 4 * g4;
  __builtin_amdgcn_raw_buffer_store_b64(v[4], rw, base + (k >> 5) * 1024 + (((k >> 3) & 3) * 16 + fr) * 16 + ((k >> 2) & 1) * 8, 0, SC1);
}

__device__ __forceinline__ void lds_sync(unsigned cnt_addr, int lane, unsigned n) {
  asm volatile("s_waitcnt lgkmcnt(0)" ::: "memory");
  if (lane == 0) __hip_atomic_fetch_add((LAS unsigned*)(size_t)cnt_addr, 1u, __ATOMIC_RELAXED, __HIP_MEMORY_SCOPE_WORKGROUP);
  while (__builtin_amdgcn_readfirstlane(__hip_atomic_load((LAS unsigned*)(size_t)cnt_addr, __ATOMIC_RELAXED, __HIP_MEMORY_SCOPE_WORKGROUP)) < n) {}
  asm volatile("" ::: "memory");
}
__device__ __forceinline__ bool lds_last(unsigned cnt_addr, int lane, unsigned n) {
  unsigned old = 0;
  if (lane == 0) old = __hip_atomic_fetch_add((LAS unsigned*)(size_t)cnt_addr, 1u, __ATOMIC_RELAXED, __HIP_MEMORY_SCOPE_WORKGROUP);
  return __builtin_amdgcn_readfirstlane(old) == n - 1;
}

__device__ __forceinline__ unsigned flag_ld(const char* ws, unsigned off) { return __hip_atomic_load((const gu32*)(ws + off), __ATOMIC_RELAXED, __HIP_MEMORY_SCOPE_AGENT); }
__device__ __forceinline__ void flag_st(char* ws, unsigned off, unsigned v) { __hip_atomic_store((gu32*)(ws + off), v, __ATOMIC_RELAXED, __HIP_MEMORY_SCOPE_AGENT); }
__device__ __forceinline__ void poll(const char* ws, unsigned off, bool act, unsigned tag) {
  for (int spins = 0; spins < (1 << 18); spins++) {
    const unsigned v = act ? flag_ld(ws, off) : tag;
    if (__all(v == tag)) break;
    __builtin_amdgcn_s_sleep(2);
  }
  asm volatile("" ::: "memory");
}

template <bool KV>
__device__ __forceinline__ void gemm_compute(f32x4 (&acc)[4][5], unsigned xa0, unsigned wa0, unsigned wa1) {
  bf16x8 xa[4], xb[4], wa[5], wb[5];
  auto load = [&](bf16x8 (&xf)[4], bf16x8 (&wf)[5], int buf, int kk) {
    const unsigned bo = buf * STG;
    const unsigned xa = (xa0 + bo) ^ (kk * 64);
    const unsigned a0 = wa0 + bo + kk * 10240, a1 = wa1 + bo + kk * 10240;
#pragma unroll
    for (int i = 0; i < 4; i++) xf[i] = lds_b128(xa + 2048 * i);
#pragma unroll
    for (int t = 0; t < 5; t++) { const unsigned a = ((t & 1) ? a1 : a0) + 32 * t; wf[t] = mk8(lds_tr(a), lds_tr(a + 1280)); }
  };
  auto mma = [&](const bf16x8 (&xf)[4], const bf16x8 (&wf)[5]) {
#pragma unroll
    for (int t = 0; t < 5; t++)
#pragma unroll
      for (int i = 0; i < 4; i++) acc[i][t] = KV ? mfma16(xf[i], wf[t], acc[i][t]) : mfma16(wf[t], xf[i], acc[i][t]);
  };
  auto interleave = [&]() {
#pragma unroll
    for (int t = 0; t < 14; t++) { SGB(0x8, 1); SGB(0x100, 1); }
    SGB(0x8, 6);
  };
  BAR();
  load(xa, wa, 0, 0);
  SBAR();
  for (int s = 0; s < 9; s++) {
    load(xb, wb, s & 3, 1); mma(xa, wa); interleave();
    SBAR();
    BAR();
    load(xa, wa, (s + 1) & 3, 0); mma(xb, wb); interleave();
    SBAR();
  }
  load(xb, wb, 1, 1); mma(xa, wa); interleave();
  SBAR();
  mma(xb, wb);
}

__device__ __forceinline__ void gemm_loader(int L, int lane, unsigned lb, const u16* x, const u16* wqkv, int rt, int cg, bool isKV) {
  const int s0 = (((rt & 3) * 5 + 1) / 2 + (isKV ? 2 * (cg & 3) : 8 + (cg & 1))) % 10;
  const i32x4 sx = make_srd(x, S * D * 2), sw = make_srd(wqkv, D * 3 * D * 2);
  unsigned vx[2], vw[5];
#pragma unroll
  for (int i = 0; i < 2; i++) vx[i] = (rt * 128 + L * 64 + i * 8 + (lane >> 3)) * 1280 + (((lane & 7) ^ ((4 * i + (lane >> 4)) & 7)) << 4);
#pragma unroll
  for (int i = 0; i < 5; i++) {
    const int ci = i * 64 + lane, k = (ci * 3277) >> 16, pos = ci - k * 20, gc = pos ^ (((k >> 3) & 1) << 1), tl = gc >> 1;
    unsigned col;
    if (isKV) { const int isv = (tl >= 3 && tl < 5) || tl >= 7; const int tt = tl < 3 ? tl : tl < 5 ? tl - 3 : tl < 7 ? tl - 2 : tl - 5;
                col = (isv ? 2560u : 1280u) + 160u * cg + tt * 32; }
    else col = 320u * cg + tl * 32;
    vw[i] = (L * 32 + k) * 3840 + col + (gc & 1) * 16;
  }
  auto stage = [&](int st_, int buf) {
    const unsigned base = lb + buf * STG;
    int st = st_ + s0; st = st >= 10 ? st - 10 : st;
#pragma unroll
    for (int p = 0; p < 10; p++) {
      glds16(base + XS + (L * 10 + p) * 1024, vw[p % 5], sw, st * (64 * 3840) + (p / 5) * (16 * 3840));
      if (p < 8) glds16(base + (L * 8 + p) * 1024, vx[p & 1], sx, st * 128 + (p >> 1) * (16 * 1280));
    }
  };
  stage(0, 0); stage(1, 1);
  wait_bar<18>();
  stage(2, 2); stage(3, 3);
  for (int s = 0; s < 9; s++) {
    if (s < 7) wait_bar<36>(); else if (s == 7) wait_bar<18>(); else wait_bar<0>();
    if (s < 6) stage(s + 4, s & 3);
  }
}

__device__ __forceinline__ void conv_load_h(u32x4 (&cur)[4], u32x4 (&prv)[4], const u16* __restrict__ hrow, int lane) {
#pragma unroll
  for (int it = 0; it < 4; it++) {
    const int i = it * 64 + lane;
    cur[it] = *(const u32x4*)(hrow + i * 8); prv[it] = *(const u32x4*)(hrow + (i > 0 ? i * 8 - 8 : 0));
  }
}
__device__ __forceinline__ void conv_prep_h(const u32x4 (&cur)[4], const u32x4 (&prv_)[4], unsigned cb, int lane) {
#pragma unroll
  for (int it = 0; it < 4; it++) {
    u32x4 prv = prv_[it];
    if (it == 0 && lane == 0) prv = (u32x4){0, 0, 0, 0};
    const unsigned W_[8] = {prv.x, prv.y, prv.z, prv.w, cur[it].x, cur[it].y, cur[it].z, cur[it].w};
    const int c = 255 - (it * 64 + lane);
#pragma unroll
    for (int e = 0; e < 4; e++) {
      u32x4 ov;
#pragma unroll
      for (int q = 0; q < 4; q++) {
        const int i0 = 15 - e - 2 * q, i1 = i0 - 1;
        const unsigned sel = ((i0 & 1) ? 0x0706u : 0x0504u) | (((i1 & 1) ? 0x0302u : 0x0100u) << 16);
        ov[q] = __builtin_amdgcn_perm(W_[i0 >> 1], W_[i1 >> 1], sel);
      }
      *(LAS u32x4*)(size_t)(cb + e * CPS + c * 16) = ov;
    }
  }
  if (lane < 8) *(LAS u32x4*)(size_t)(cb + (lane >> 1) * CPS + (256 + (lane & 1)) * 16) = (u32x4){0, 0, 0, 0};
  if (lane < 32) *(LAS u32x4*)(size_t)(cb + UOFF + lane * 16) = (u32x4){0, 0, 0, 0};
}
template <class F>
__device__ __forceinline__ void conv_load_u(__amdgpu_buffer_rsrc_t rw, int d, unsigned cb, int lane, F&& between) {
  u32x4 uv[4];
#pragma unroll
  for (int it = 0; it < 4; it++) uv[it] = __builtin_amdgcn_raw_buffer_load_b128(rw, OFF_UT + d * 4096 + (it * 64 + lane) * 16, 0, SC1);
  SBAR();
  between();
  SBAR();
#pragma unroll
  for (int it = 0; it < 4; it++) *(LAS u32x4*)(size_t)(cb + UOFF + 512 + (it * 64 + lane) * 16) = uv[it];
}
struct ConvFrags { u32x2 al[8], ah[8]; u32x4 B[8]; };
__device__ __forceinline__ void conv_read(ConvFrags& f, unsigned aa, unsigned ba) {
  asm volatile(
      "ds_read_b128 %16, %25\n\tds_read_b64 %7, %24 offset:3584\n\tds_read_b64 %15, %24 offset:3592\n\t"
      "ds_read_b128 %17, %25 offset:512\n\tds_read_b128 %18, %25 offset:1024\n\tds_read_b128 %19, %25 offset:1536\n\t"
      "ds_read_b128 %20, %25 offset:2048\n\tds_read_b128 %21, %25 offset:2560\n\tds_read_b128 %22, %25 offset:3072\n\t"
      "ds_read_b128 %23, %25 offset:3584\n\t"
      "ds_read_b64 %6, %24 offset:3072\n\tds_read_b64 %14, %24 offset:3080\n\t"
      "ds_read_b64 %5, %24 offset:2560\n\tds_read_b64 %13, %24 offset:2568\n\t"
      "ds_read_b64 %4, %24 offset:2048\n\tds_read_b64 %12, %24 offset:2056\n\t"
      "ds_read_b64 %3, %24 offset:1536\n\tds_read_b64 %11, %24 offset:1544\n\t"
      "ds_read_b64 %2, %24 offset:1024\n\tds_read_b64 %10, %24 offset:1032\n\t"
      "ds_read_b64 %1, %24 offset:512\n\tds_read_b64 %9, %24 offset:520\n\t"
      "ds_read_b64 %0, %24\n\tds_read_b64 %8, %24 offset:8"
      : "=&v"(f.al[0]), "=&v"(f.al[1]), "=&v"(f.al[2]), "=&v"(f.al[3]), "=&v"(f.al[4]), "=&v"(f.al[5]), "=&v"(f.al[6]), "=&v"(f.al[7]),
        "=&v"(f.ah[0]), "=&v"(f.ah[1]), "=&v"(f.ah[2]), "=&v"(f.ah[3]), "=&v"(f.ah[4]), "=&v"(f.ah[5]), "=&v"(f.ah[6]), "=&v"(f.ah[7]),
        "=&v"(f.B[0]), "=&v"(f.B[1]), "=&v"(f.B[2]), "=&v"(f.B[3]), "=&v"(f.B[4]), "=&v"(f.B[5]), "=&v"(f.B[6]), "=&v"(f.B[7])
      : "v"(aa), "v"(ba) : "memory");
}
__device__ __forceinline__ void conv_wait(ConvFrags& f) {
  asm volatile("s_waitcnt lgkmcnt(0)"
      : "+v"(f.al[0]), "+v"(f.al[1]), "+v"(f.al[2]), "+v"(f.al[3]), "+v"(f.al[4]), "+v"(f.al[5]), "+v"(f.al[6]), "+v"(f.al[7]),
        "+v"(f.ah[0]), "+v"(f.ah[1]), "+v"(f.ah[2]), "+v"(f.ah[3]), "+v"(f.ah[4]), "+v"(f.ah[5]), "+v"(f.ah[6]), "+v"(f.ah[7]),
        "+v"(f.B[0]), "+v"(f.B[1]), "+v"(f.B[2]), "+v"(f.B[3]), "+v"(f.B[4]), "+v"(f.B[5]), "+v"(f.B[6]), "+v"(f.B[7]) :: "memory");
}
template <int K> __device__ __forceinline__ void conv_read1(ConvFrags& f, unsigned aa, unsigned ba) {
  if constexpr (K < 8) asm volatile("ds_read_b128 %0, %1 offset:%2" : "=&v"(f.B[K]) : "v"(ba), "n"(512 * K) : "memory");
  else if constexpr (K < 16) asm volatile("ds_read_b64 %0, %1 offset:%2" : "=&v"(f.al[K - 8]) : "v"(aa), "n"(512 * (K - 8)) : "memory");
  else if constexpr (K < 24) asm volatile("ds_read_b64 %0, %1 offset:%2" : "=&v"(f.ah[K - 16]) : "v"(aa), "n"(512 * (K - 16) + 8) : "memory");
}
template <int K = 0> __device__ __forceinline__ void conv_step(f32x4 (&acc)[8], const ConvFrags& f, ConvFrags& nx, unsigned aa, unsigned ba) {
  if constexpr (K < 36) {
    constexpr int a = K < 8 ? 0 : K < 15 ? 1 : K < 21 ? 2 : K < 26 ? 3 : K < 30 ? 4 : K < 33 ? 5 : K < 35 ? 6 : 7;
    constexpr int i = K - (a == 0 ? 0 : a == 1 ? 8 : a == 2 ? 15 : a == 3 ? 21 : a == 4 ? 26 : a == 5 ? 30 : a == 6 ? 33 : 35);
    acc[a + i] = mfma16(mk8(f.al[7 - a], f.ah[7 - a]), __builtin_bit_cast(bf16x8, f.B[i]), acc[a + i]);
    conv_read1<K>(nx, aa, ba);
    SBAR();
    conv_step<K + 1>(acc, f, nx, aa, ba);
  }
}
__device__ __forceinline__ void conv_main(f32x4 (&acc)[8], unsigned cb, int lane, int c0, int c1) {
  const int fr = lane & 15, g4 = lane >> 4;
  const unsigned abase = cb + (3 - (fr & 3)) * CPS + 2 * (2044 - 16 * 112 - 16 * (g4 >> 1) + 8 * (g4 & 1) - (fr & ~3));
  const unsigned bbase = cb + UOFF + 512 + 2 * (16 * fr - 16 * (g4 >> 1) + 8 * (g4 & 1));
#pragma unroll
  for (int c = 0; c < 8; c++) acc[c] = (f32x4){0.f, 0.f, 0.f, 0.f};
  ConvFrags f0, f1;
  asm volatile("s_waitcnt lgkmcnt(0)" ::: "memory");
  conv_read(f0, abase - 64 * c0, bbase - 64 * c0);
  for (int cc = c0; cc < c1; cc += 2) {
    conv_wait(f0);
    SBAR();
    conv_step(acc, f0, f1, abase - 64 * (cc + 1), bbase - 64 * (cc + 1));
    conv_wait(f1);
    SBAR();
    const int cn = cc + 2 < c1 ? cc + 2 : cc + 1;
    conv_step(acc, f1, f0, abase - 64 * cn, bbase - 64 * cn);
  }
  conv_wait(f0);
}
__device__ __forceinline__ void conv_store(f32x4 y, unsigned cb, int c, int lane, float Dd, __amdgpu_buffer_rsrc_t rw, int d) {
  const int fr = lane & 15, g4 = lane >> 4;
  const u32x2 uu = lds_b64(cb + UOFF + 512 + 2 * (256 * c + 16 * fr + 4 * g4));
  const float r0 = fmaf(bflo(uu.x), Dd, y[0]), r1 = fmaf(bfhi(uu.x), Dd, y[1]);
  const float r2 = fmaf(bflo(uu.y), Dd, y[2]), r3 = fmaf(bfhi(uu.y), Dd, y[3]);
  __builtin_amdgcn_raw_buffer_store_b64((u32x2){pk2(r0, r1), pk2(r2, r3)}, rw, OFF_YT + d * 4096 + (256 * c + 16 * fr + 4 * g4) * 2, 0, SC1);
}

__global__ __launch_bounds__(512, 2) void fused(const u16* __restrict__ x, const u16* __restrict__ wqkv, const u16* __restrict__ wo,
                                                const u16* __restrict__ Bk, const u16* __restrict__ Bv, const u16* __restrict__ shk,
                                                const u16* __restrict__ hk, const u16* __restrict__ Dv, const u16* __restrict__ gw,
                                                u16* __restrict__ out, char* __restrict__ ws) {
  extern __shared__ __attribute__((aligned(16))) char sm[];
  const int tid = threadIdx.x, lane = tid & 63;
  const int w = __builtin_amdgcn_readfirstlane(tid >> 6);
  const int b = blockIdx.x, xcd = b & 7, r = b >> 3;
  const int fr = lane & 15, g4 = lane >> 4;
  const unsigned lb = (unsigned)(size_t)(LAS char*)sm;
  const __amdgpu_buffer_rsrc_t rw = __builtin_amdgcn_make_buffer_rsrc(ws, 0, WS_USED, 0x00020000);
  TS_DECL TS(0);
  unsigned e0 = 0;
#define PUT_TAG() do { if (w == 7 && lane == 0) { *(LAS unsigned*)(size_t)(lb + L_TAG) = e0 + 1u; *(LAS u32x4*)(size_t)(lb + L_CNT) = (u32x4){0u, 0u, 0u, 0u}; } } while (0)
#define TAG (*(const LAS unsigned*)(size_t)(lb + L_TAG))
  bool wo_issued = false;
  if (r < 24) {
    const bool isKV = r < 16;
    const int rt = (xcd & 3) * 4 + (r & 3);
    const int cg = isKV ? (xcd >> 2) * 4 + (r >> 2) : (xcd >> 2) * 2 + ((r - 16) >> 2);
    f32x4 acc[4][5];
    const int rp = (w >> 1) & 1, hf = w & 1;
    if (w < 4) {
      const unsigned xa0 = lb + (rp * 64 + fr) * 128 + ((g4 ^ ((fr >> 1) & 7)) << 4);
      const unsigned wl = lb + XS + (8 * g4 + (fr >> 2)) * 320 + ((lane & 3) >> 1) * 16 + (lane & 1) * 8 + hf * 160;
      const unsigned sgn = (g4 & 1) * 32;
      const unsigned wa0 = hf ? wl - sgn : wl + sgn, wa1 = hf ? wl + sgn : wl - sgn;
#pragma unroll
      for (int i = 0; i < 4; i++)
#pragma unroll
        for (int t = 0; t < 5; t++) acc[i][t] = (f32x4){0.f, 0.f, 0.f, 0.f};
      if (isKV) gemm_compute<true>(acc, xa0, wa0, wa1); else gemm_compute<false>(acc, xa0, wa0, wa1);
    } else if (w < 6) {
      gemm_loader(w - 4, lane, lb, x, wqkv, rt, cg, isKV);
    } else {
      const int w6 = w - 6;
      unsigned pf[5] = {0, 0, 0, 0, 0};
      SBAR();
      if (w == 7) e0 = flag_ld(ws, F_E);
      if (isKV) {
        const int l16 = lane & 15, row0 = rt * 128 + cg * 16 + w6 * 8;
        const u32x4 g0 = *(const u32x4*)(gw + lane * 8), g1 = *(const u32x4*)(gw + 512 + l16 * 8);
        u32x4 gx0[8], gx1[8];
#pragma unroll
        for (int i = 0; i < 8; i++) {
          const u16* xr = x + (size_t)(row0 + i) * D;
          gx0[i] = *(const u32x4*)(xr + lane * 8); gx1[i] = *(const u32x4*)(xr + 512 + l16 * 8);
        }
        SBAR();
        BAR(); BAR(); BAR();
        float a[8];
        const float m1 = lane < 16 ? 1.f : 0.f;
#pragma unroll
        for (int i = 0; i < 8; i++) {
          float a0 = 0.f, a1 = 0.f;
#pragma unroll
          for (int j = 0; j < 4; j++) {
            a0 = fmaf(bflo(gx0[i][j]), bflo(g0[j]), a0); a0 = fmaf(bfhi(gx0[i][j]), bfhi(g0[j]), a0);
            a1 = fmaf(bflo(gx1[i][j]), bflo(g1[j]), a1); a1 = fmaf(bfhi(gx1[i][j]), bfhi(g1[j]), a1);
          }
          a[i] = fmaf(a1, m1, a0);
          if (i == 3) { SBAR(); BAR(); SBAR(); }
        }
        SBAR(); BAR(); SBAR();
#pragma unroll
        for (int i = 0; i < 4; i++) { const float lo = a[i], hi = a[i + 4]; const bool up = lane & 4; a[i] = (up ? hi : lo) + __shfl_xor(up ? lo : hi, 4); }
#pragma unroll
        for (int i = 0; i < 2; i++) { const float lo = a[i], hi = a[i + 2]; const bool up = lane & 2; a[i] = (up ? hi : lo) + __shfl_xor(up ? lo : hi, 2); }
        { const float lo = a[0], hi = a[1]; const bool up = lane & 1; a[0] = (up ? hi : lo) + __shfl_xor(up ? lo : hi, 1); }
        a[0] += __shfl_xor(a[0], 8); a[0] += __shfl_xor(a[0], 16); a[0] += __shfl_xor(a[0], 32);
        if (lane < 8) __builtin_amdgcn_raw_buffer_store_b32(__float_as_uint(rbf(1.f / (1.f + __expf(-rbf(a[0]))))), rw, OFF_G + (row0 + lane) * 4, 0, SC1);
        PUT_TAG();
        asm volatile("" :: "v"(pf[0]), "v"(pf[1]), "v"(pf[2]), "v"(pf[3]), "v"(pf[4]));
        SBAR();
        BAR(); BAR(); BAR(); BAR(); BAR();
      } else {
        const int hw = (xcd * 8 + (r - 16)) * 2 + w6;
        const unsigned scr = lb + L_T + w6 * 6144;
#pragma unroll
        for (int bt = 0; bt < 2; bt++) {
          const int blk = bt == 0 ? hw : (hw < 32 ? 128 + hw : hw), ct = (blk * 3277) >> 16, ks = blk - 20 * ct;
          u32x4 v[5];
#pragma unroll
          for (int i = 0; i < 5; i++) {
            const int c = i * 64 + lane, row = (c * 6554) >> 16, ch = c - 10 * row;
            v[i] = *(const u32x4*)(wo + (size_t)(32 * ks + row) * D + 80 * ct + 8 * ch);
          }
          SBAR();
          BAR(); BAR();
          if (bt == 0 || hw < 32) {
#pragma unroll
            for (int i = 0; i < 5; i++) *(LAS u32x4*)(size_t)(scr + (i * 64 + lane) * 16) = v[i];
            const unsigned ta = scr + (8 * g4 + (fr >> 2)) * 160 + (lane & 3) * 8;
#pragma unroll
            for (int t = 0; t < 5; t++) {
              const u32x2 lo = lds_tr(ta + 32 * t), hi = lds_tr(ta + 32 * t + 640);
              __builtin_amdgcn_raw_buffer_store_b128((u32x4){lo.x, lo.y, hi.x, hi.y}, rw, OFF_WOP + (blk * 5 + t) * 1024 + lane * 16, 0, SC1);
            }
          }
          SBAR();
          BAR(); BAR();
        }
        PUT_TAG();
        asm volatile("" :: "v"(pf[0]), "v"(pf[1]), "v"(pf[2]), "v"(pf[3]), "v"(pf[4]));
        BAR(); BAR();
      }
    }
    TS(1);
    BAR();
    TS(11);
    const unsigned tag = TAG;
    if (isKV) {
      if (w < 4) {
#pragma unroll
        for (int t = 0; t < 5; t++) {
          const int slot = hf ? (t < 2 ? 3 + t : 5 + t) : (t < 3 ? t : 2 + t);
#pragma unroll
          for (int m = 0; m < 2; m++)
            *(LAS u32x4*)(size_t)(lb + ((rp * 2 + m) * 10 + slot) * 1024 + lane * 16) =
                (u32x4){pk2(acc[2 * m][t][0], acc[2 * m][t][1]), pk2(acc[2 * m][t][2], acc[2 * m][t][3]),
                        pk2(acc[2 * m + 1][t][0], acc[2 * m + 1][t][1]), pk2(acc[2 * m + 1][t][2], acc[2 * m + 1][t][3])};
        }
      } else {
        for (int i = tid - 256; i < 6048 / 16; i += 256) *(LAS u32x4*)(size_t)(lb + L_T + i * 16) = (u32x4){0, 0, 0, 0};
      }
      TS(12);
      BAR();
      {
        const int f0 = w < 4 ? 2 * w : 8 + 3 * (w - 4);
        u32x4 v[3];
#pragma unroll
        for (int q = 0; q < 3; q++) { const int f = f0 + (q < 2 || w >= 4 ? q : 0); v[q] = *(const LAS u32x4*)(size_t)(lb + ((f / 5) * 10 + (f % 5)) * 1024 + lane * 16); }
#pragma unroll
        for (int q = 0; q < 3; q++) {
          if (q < 2 || w >= 4) {
            const int f = f0 + q;
            *(LAS u32x4*)(size_t)(lb + ((f / 5) * 10 + (f % 5)) * 1024 + lane * 16) =
                (u32x4){pk2(phi(bflo(v[q].x)), phi(bfhi(v[q].x))), pk2(phi(bflo(v[q].y)), phi(bfhi(v[q].y))),
                        pk2(phi(bflo(v[q].z)), phi(bfhi(v[q].z))), pk2(phi(bflo(v[q].w)), phi(bfhi(v[q].w)))};
          }
        }
      }
      TS(13);
      BAR();
      TS(2);
      {
        const u32x4 o1 = {0x3F803F80u, 0x3F803F80u, 0x3F803F80u, 0x3F803F80u};
        bf16x8 A[3][4], B[3][4];
        int ti[3], tj[3];
#pragma unroll
        for (int q = 0; q < 3; q++) {
          const int T = (q < 2 || w < 2) ? w + 8 * q : w;
          const bool isz = T >= 13;
          ti[q] = isz ? T - 13 : (T + 1) / 3;
          tj[q] = isz ? -1 : T - (ti[q] == 0 ? 0 : 3 * ti[q] - 1) + (ti[q] == 0 ? 0 : ti[q] - 1);
          const unsigned aA = lb + ti[q] * 1024 + lane * 16, aB = lb + (5 + (isz ? 0 : tj[q])) * 1024 + lane * 16;
#pragma unroll
          for (int m = 0; m < 4; m++) { A[q][m] = lds_b128(aA + m * 10240); B[q][m] = lds_b128(aB + m * 10240); }
        }
        f32x4 d4[3];
#pragma unroll
        for (int q = 0; q < 3; q++) {
          d4[q] = (f32x4){0.f, 0.f, 0.f, 0.f};
          if (tj[q] < 0) {
#pragma unroll
            for (int m = 0; m < 4; m++) B[q][m] = __builtin_bit_cast(bf16x8, o1);
          }
        }
#pragma unroll
        for (int m = 0; m < 4; m++)
#pragma unroll
          for (int q = 0; q < 3; q++) d4[q] = mfma16(A[q][m], B[q][m], d4[q]);
#pragma unroll
        for (int q = 0; q < 3; q++) {
          if (q < 2 || w < 2) {
            const bool isz = tj[q] < 0;
            const int Dg = 16 * ti[q] + 4 * g4, h = (Dg * 205) >> 12, dd = Dg - 20 * h;
            const int ee = isz ? 20 : 16 * tj[q] + fr - 20 * h;
            if (isz ? fr == 0 : (ee >= 0 && ee < 20))
              __builtin_amdgcn_raw_buffer_store_b64((u32x2){pk2(d4[q][0], d4[q][1]), pk2(d4[q][2], d4[q][3])}, rw,
                                                    OFF_P + (rt * 8 + cg) * 3360 + (h * 420 + ee * 20 + dd) * 2, 0, SC1);
          }
        }
      }
      TS(14);
      DRAIN();
      TS(15);
      BAR();
      TS(3);
      if (w == 0) {
        if (lane == 0) flag_st(ws, F_P + (rt * 8 + cg) * 128, tag);
        poll(ws, lane < 16 ? F_P + (lane * 8 + cg) * 128 : F_PQ + (rt * 4 + (cg >> 1)) * 128, lane <= 16, tag);
      } else if (w == 7) {
        poll(ws, F_W + lane * 128, true, tag);
      }
      constexpr int PJ[9] = {0, 1, 1, 2, 2, 3, 3, 4, 4}, PK[9] = {0, 0, 1, 0, 1, 1, 2, 1, 2};
      unsigned an[9], ad[9];
#pragma unroll
      for (int p = 0; p < 9; p++) {
        const int E = 16 * PJ[p] + fr, h = (E * 205) >> 12, e = E - 20 * h;
        int o_ = 32 * PK[p] + 8 * g4 - 20 * h; o_ = o_ < -8 ? -8 : o_ > 20 ? 20 : o_;
        an[p] = lb + L_T + (h * 21 + e) * 72 + (8 + o_) * 2; ad[p] = lb + L_T + (h * 21 + 20) * 72 + (8 + o_) * 2;
      }
      BAR();
      TS(4);
      bf16x8 Bq[3];
      {
        const unsigned prow = OFF_PQ + ((rt * 8 + w) * 8 + cg) * 3072 + lane * 16;
#pragma unroll
        for (int ks = 0; ks < 3; ks++) Bq[ks] = __builtin_bit_cast(bf16x8, __builtin_amdgcn_raw_buffer_load_b128(rw, prow + 1024 * ks, 0, SC1));
      }
      if (tid < 210) {
        const unsigned pb = OFF_P + cg * 3360 + tid * 16;
        u32x4 v[16];
#pragma unroll
        for (int k = 0; k < 16; k++) v[k] = __builtin_amdgcn_raw_buffer_load_b128(rw, pb, (k ^ rt) * (8 * 3360), SC1);
        unsigned o4[4];
#pragma unroll
        for (int c = 0; c < 4; c++) {
          float lo[16], hi[16];
#pragma unroll
          for (int k = 0; k < 16; k++) { lo[k] = bflo(v[k][c]); hi[k] = bfhi(v[k][c]); }
#pragma unroll
          for (int st = 1; st < 16; st <<= 1)
#pragma unroll
            for (int k = 0; k < 16; k += 2 * st) { lo[k] += lo[k + st]; hi[k] += hi[k + st]; }
          o4[c] = pk2(lo[0], hi[0]);
        }
        TS(16);
#pragma unroll
        for (int q = 0; q < 2; q++) {
          const int i4 = 2 * tid + q, row = (i4 * 13108) >> 16, d = 4 * (i4 - row * 5);
          *(LAS u32x2*)(size_t)(lb + L_T + row * 72 + (8 + d) * 2) = (u32x2){o4[2 * q], o4[2 * q + 1]};
        }
      }
      BAR();
      TS(5);
      {
        if (g4 >= 2) Bq[2] = __builtin_bit_cast(bf16x8, (u32x4){0, 0, 0, 0});
        f32x4 nu[5], de[5];
#pragma unroll
        for (int j = 0; j < 5; j++) { nu[j] = (f32x4){0.f, 0.f, 0.f, 0.f}; de[j] = nu[j]; }
        bf16x8 An[9], Ad[9];
#pragma unroll
        for (int p = 0; p < 9; p++) {
          unsigned an8 = an[p] + 8, ad8 = ad[p] + 8;
          asm volatile("" : "+v"(an8), "+v"(ad8));
          An[p] = mk8(lds_b64(an[p]), lds_b64(an8)); Ad[p] = mk8(lds_b64(ad[p]), lds_b64(ad8));
        }
#pragma unroll
        for (int p = 0; p < 9; p++) { de[PJ[p]] = mfma16(Ad[p], Bq[PK[p]], de[PJ[p]]); nu[PJ[p]] = mfma16(An[p], Bq[PK[p]], nu[PJ[p]]); }
        u32x2 ov[5];
#pragma unroll
        for (int j = 0; j < 5; j++) {
          const float rd = __builtin_amdgcn_rcpf(de[j][0] + 1e-6f);
          ov[j] = (u32x2){pk2(nu[j][0] * rd, nu[j][1] * rd), pk2(nu[j][2] * rd, nu[j][3] * rd)};
        }
        store_frags(ov, rw, OFF_O + (rt * 8 + w) * 20480, 80 * cg, fr, g4);
      }
      TS(17);
      DRAIN();
      BAR();
      if (tid == 0) flag_st(ws, F_O + (rt * 8 + cg) * 128, tag);
      TS(6);
      {
        const i32x4 sws = make_srd(ws, WS_USED);
        const unsigned vwv = OFF_WOP + ((xcd >> 2) * 4 + (r >> 3)) * 102400 + lane * 16;
        for (int i = w; i < 100; i += 8) {
          int pc = 12 * (r & 7) + i; pc = __builtin_amdgcn_readfirstlane(pc >= 100 ? pc - 100 : pc);
          glds16_sc1(lb + pc * 1024, vwv, sws, pc * 1024);
        }
      }
      wo_issued = true;
    } else {
      const int vb = xcd * 8 + (r - 16);
      const int d = 80 * (vb >> 3) + 48 + 4 * (vb & 7) + (w & 3);
      const unsigned cb = lb + (w & 3) * CHS;
      if (w < 4) {
#pragma unroll
        for (int i = 0; i < 4; i++) {
          u32x2 pv[5];
#pragma unroll
          for (int t = 0; t < 5; t++) pv[t] = (u32x2){pk2(phi(acc[i][t][0]), phi(acc[i][t][1])), pk2(phi(acc[i][t][2]), phi(acc[i][t][3]))};
          store_frags(pv, rw, OFF_PQ + ((rt * 8 + rp * 4 + i) * 8 + cg * 2 + hf) * 3072, 0, fr, g4);
        }
      } else {
        u32x4 hc[4], hp[4];
        conv_load_h(hc, hp, hk + (size_t)d * S, lane);
        conv_prep_h(hc, hp, cb, lane);
        if (w == 7) poll(ws, F_U + lane * 128, true, tag);
      }
      TS(12);
      DRAIN();
      BAR();
      TS(2);
      if (tid == 0) { flag_st(ws, F_PQ + (rt * 4 + cg) * 128, tag); flag_st(ws, F_W + vb * 128, tag); }
      if (w < 4) {
        const float Dd = bflo(Dv[d]);
        conv_load_u(rw, d, cb, lane, [&]() {});
        TS(3);
        f32x4 y[8];
        conv_main(y, cb, lane, 0, 8);
        TS(4);
#pragma unroll
        for (int c = 0; c < 8; c++) conv_store(y[c], cb, c, lane, Dd, rw, d);
        DRAIN();
      }
      BAR();
      if (tid == 0) flag_st(ws, F_Y + (64 + vb) * 128, tag);
      TS(5); TS(6);
    }
  } else {
    const int pw = xcd * 8 + (r - 24);
    const int c6 = w < 4 ? w : 4 + ((w - 4) >> 1);
    const int d = 80 * (pw >> 3) + 6 * (pw & 7) + c6;
    const unsigned cb = lb + c6 * CHS;
    const bool prep = w < 4 || !(w & 1);
    u32x4 hc[4], hp[4];
    {
      const int rbase = xcd * 256 + ((r - 24) >> 1) * 64, cbase = ((r - 24) & 1) * 320;
      u32x4 kr[2][7], vr[2][4], sp[2][4];
#pragma unroll
      for (int q = 0; q < 2; q++) {
        const int it = w + 8 * q;
        if (q == 0 || w < 2) {
          const int ch = cbase + (it >> 1) * 64 + (lane & 7) * 8;
          const int s0 = rbase + (it & 1) * 32 + (lane >> 3) * 4;
#pragma unroll
          for (int j = 0; j < 7; j++) { int s = s0 - 3 + j; kr[q][j] = *(const u32x4*)(Bk + (size_t)(s < 0 ? 0 : s) * D + ch); }
#pragma unroll
          for (int j = 0; j < 4; j++) vr[q][j] = *(const u32x4*)(Bv + (size_t)(s0 + j) * D + ch);
#pragma unroll
          for (int j = 0; j < 4; j++) sp[q][j] = ((const u32x4*)(shk + ch * 4))[j];
        }
      }
      if (prep) conv_load_h(hc, hp, hk + (size_t)d * S, lane);
      if (w == 7) e0 = flag_ld(ws, F_E);
      SBAR();
#pragma unroll
      for (int q = 0; q < 2; q++) {
        const int it = w + 8 * q;
        if (q == 0 || w < 2) {
          const int s0 = rbase + (it & 1) * 32 + (lane >> 3) * 4;
          if (s0 == 0) { kr[q][0] = (u32x4){0, 0, 0, 0}; kr[q][1] = kr[q][0]; kr[q][2] = kr[q][0]; }
          const int rg = (it & 1) * 8 + (lane >> 3);
          const unsigned la = lb + L_UIMG + ((it >> 1) * 64 + (lane & 7) * 8) * 128 + (((rg >> 1) ^ (lane & 7)) << 4) + (rg & 1) * 8;
#pragma unroll
          for (int c = 0; c < 8; c++) {
            const unsigned s01 = sp[q][c >> 1][(c & 1) * 2], s23 = sp[q][c >> 1][(c & 1) * 2 + 1];
            const float w0 = bflo(s01), w1 = bfhi(s01), w2 = bflo(s23), w3 = bfhi(s23);
            float kk[7], u[4];
#pragma unroll
            for (int j = 0; j < 7; j++) { unsigned dd = kr[q][j][c >> 1]; kk[j] = (c & 1) ? bfhi(dd) : bflo(dd); }
#pragma unroll
            for (int j = 0; j < 4; j++) {
              unsigned dd = vr[q][j][c >> 1]; float v = (c & 1) ? bfhi(dd) : bflo(dd);
              float ks = kk[j] * w3; ks = fmaf(kk[j + 1], w2, ks); ks = fmaf(kk[j + 2], w1, ks); ks = fmaf(kk[j + 3], w0, ks);
              u[j] = ks * v;
            }
            *(LAS u32x2*)(size_t)(la + c * 128) = (u32x2){pk2(u[0], u[1]), pk2(u[2], u[3])};
          }
        }
      }
      PUT_TAG();
      BAR();
#pragma unroll
      for (int i = 0; i < 5; i++) {
        const int chl = (w * 5 + i) * 8 + (lane >> 3), k = lane & 7;
        const u32x4 v = *(const LAS u32x4*)(size_t)(lb + L_UIMG + chl * 128 + ((k ^ ((chl >> 3) & 7)) << 4));
        __builtin_amdgcn_raw_buffer_store_b128(v, rw, OFF_UT + (cbase + chl) * 4096 + rbase * 2 + k * 16, 0, SC1);
      }
      TS(1);
    }
    if (w < 5 && prep) conv_prep_h(hc, hp, cb, lane);
    DRAIN();
    BAR();
    TS(2);
    const unsigned tag = TAG;
    if (tid == 0) flag_st(ws, F_U + pw * 128, tag);
    const float Dd = bflo(Dv[d]);
    if (w == 6) conv_prep_h(hc, hp, cb, lane);
    if (w == 7) poll(ws, F_U + lane * 128, true, tag);
    BAR();
    if (prep) conv_load_u(rw, d, cb, lane, [&]() {});
    BAR();
    TS(3);
    f32x4 y[8];
    conv_main(y, cb, lane, w < 4 ? 0 : (w & 1) * 4, w < 4 ? 8 : (w & 1) * 4 + 4);
    TS(4);
    if (w < 4) {
#pragma unroll
      for (int c = 0; c < 8; c++) conv_store(y[c], cb, c, lane, Dd, rw, d);
    }
    BAR();
    if (w >= 4) {
#pragma unroll
      for (int c = 0; c < 4; c++) { const f32x4 lo = y[c], hi = y[4 + c]; *(LAS f32x4*)(size_t)(cb + ((w & 1) * 4 + c) * 1024 + lane * 16) = (w & 1) ? lo : hi; }
    }
    BAR();
    if (w >= 4) {
#pragma unroll
      for (int c = 0; c < 4; c++) {
        const f32x4 o_ = *(const LAS f32x4*)(size_t)(cb + (((w & 1) ^ 1) * 4 + c) * 1024 + lane * 16);
        const f32x4 lo = y[c], hi = y[4 + c];
        const f32x4 mine = (w & 1) ? hi : lo;
        conv_store((w & 1) ? o_ + mine : mine + o_, cb, (w & 1) * 4 + c, lane, Dd, rw, d);
      }
    }
    DRAIN();
    BAR();
    if (tid == 0) flag_st(ws, F_Y + pw * 128, tag);
    TS(5); TS(6);
  }

  {
    const unsigned tag = TAG;
    const int rt = (xcd & 3) * 8 + (r & 7), ct = (xcd >> 2) * 4 + (r >> 3);
    const int sw_ = w & 3, kk = w >> 2;
    const i32x4 sws = make_srd(ws, WS_USED);
    if (!wo_issued) {
      if (w == 0) poll(ws, F_W + lane * 128, true, tag);
      BAR();
      const unsigned vwv = OFF_WOP + ct * 102400 + lane * 16;
      for (int i = w; i < 100; i += 8) { int pc = 12 * (r & 7) + i; pc = __builtin_amdgcn_readfirstlane(pc >= 100 ? pc - 100 : pc); glds16_sc1(lb + pc * 1024, vwv, sws, pc * 1024); }
    }
    TS(18);
    TS(19);
    const int srow = rt * 64 + sw_ * 16 + fr;
    u32x2 xv[3];
#pragma unroll
    for (int t = 0; t < 3; t++) { const int tt = kk ? (t < 2 ? 3 + t : 4) : t; xv[t] = *(const u32x2*)(x + (size_t)srow * D + ct * 80 + 16 * tt + 4 * g4); }
    if (w == 0) {
      poll(ws, F_O + ((rt >> 1) * 8 + (lane & 7)) * 128, lane < 8, tag);
    } else if (w == 7) {
      poll(ws, F_Y + ((lane & 8 ? 64 : 0) + ct * 8 + (lane & 7)) * 128, lane < 16, tag);
      for (int pc = 0; pc < 10; pc++)
        glds16_sc1(lb + L_Y + pc * 1024, OFF_YT + (ct * 80 + pc * 8 + (lane >> 3)) * 4096 + rt * 128 + (lane & 7) * 16, sws, 0);
    } else if (w == 6 && b == 0) {
      poll(ws, F_U + lane * 128, true, tag); poll(ws, F_PQ + lane * 128, true, tag);
      poll(ws, F_P + lane * 128, true, tag); poll(ws, F_P + (64 + lane) * 128, true, tag);
      if (lane == 0) flag_st(ws, F_E, tag);
    }
    DRAIN();
    BAR();
    TS(7);
    const float g = __uint_as_float(__builtin_amdgcn_raw_buffer_load_b32(rw, OFF_G + srow * 4, 0, SC1));
    bf16x8 of[10];
    const int rot = ((ct & 3) * 5) >> 1;
    {
      const unsigned vo = OFF_O + (rt * 4 + sw_) * 20480 + kk * 1024 + lane * 16;
#pragma unroll
      for (int s = 0; s < 10; s++) { int q = s + rot; q = q >= 10 ? q - 10 : q; of[s] = __builtin_bit_cast(bf16x8, __builtin_amdgcn_raw_buffer_load_b128(rw, vo, q * 2048, SC1)); }
    }
    TS(8);
    f32x4 acc[5];
#pragma unroll
    for (int t = 0; t < 5; t++) acc[t] = (f32x4){0.f, 0.f, 0.f, 0.f};
    const unsigned wa0 = lb + kk * 5120 + lane * 16;
#pragma unroll
    for (int hh = 0; hh < 2; hh++) {
      bf16x8 wf[5][5];
#pragma unroll
      for (int s = 0; s < 5; s++) {
        int q = hh * 5 + s + rot; q = q >= 10 ? q - 10 : q;
        const unsigned wa = wa0 + q * 10240;
#pragma unroll
        for (int t = 0; t < 5; t++) wf[s][t] = lds_b128(wa + t * 1024);
      }
#pragma unroll
      for (int s = 0; s < 5; s++)
#pragma unroll
        for (int t = 0; t < 5; t++) acc[t] = mfma16(wf[s][t], of[hh * 5 + s], acc[t]);
      SGB(0x100, 10);
#pragma unroll
      for (int i = 0; i < 15; i++) { SGB(0x8, 1); SGB(0x100, 1); }
      SGB(0x8, 10);
      SBAR();
    }
    TS(9);
    const unsigned ra = lb + L_RED + (sw_ * 5) * 1024 + lane * 16;
    if (kk == 0) { *(LAS f32x4*)(size_t)(ra + 3 * 1024) = acc[3]; *(LAS f32x4*)(size_t)(ra + 4 * 1024) = acc[4]; }
    else { *(LAS f32x4*)(size_t)(ra) = acc[0]; *(LAS f32x4*)(size_t)(ra + 1024) = acc[1]; *(LAS f32x4*)(size_t)(ra + 2048) = acc[2]; }
    BAR();
    TS(20);
    const float omg = 1.f - g;
    const int nt = kk ? 2 : 3;
#pragma unroll
    for (int t = 0; t < 3; t++) {
      if (t < nt) {
        const int tt = kk ? 3 + t : t;
        const f32x4 a0 = acc[t], a1 = acc[3 + (t < 2 ? t : 1)];
        f32x4 a = *(const LAS f32x4*)(size_t)(ra + tt * 1024) + (kk ? a1 : a0);
        u32x2 yv = lds_tr(lb + L_Y + (16 * tt + 4 * g4 + (fr >> 2)) * 128 + (sw_ * 16 + 4 * (fr & 3)) * 2);
        float xs[4] = {bflo(xv[t].x), bfhi(xv[t].x), bflo(xv[t].y), bfhi(xv[t].y)};
        float ys[4] = {bflo(yv.x), bfhi(yv.x), bflo(yv.y), bfhi(yv.y)};
        float r_[4];
#pragma unroll
        for (int q = 0; q < 4; q++) r_[q] = fmaf(g, a[q], omg * (xs[q] * ys[q]));
        *(u32x2*)(out + (size_t)srow * D + ct * 80 + 16 * tt + 4 * g4) = (u32x2){pk2(r_[0], r_[1]), pk2(r_[2], r_[3])};
      }
    }
    TS(10); TS_OUT(21);
  }
}

extern "C" void kernel_launch(void* const* d_in, const int* in_sizes, int n_in, void* d_out, int out_size, void* d_ws, size_t ws_size,
                              hipStream_t stream) {
  const u16* x = (const u16*)d_in[0]; const u16* wqkv = (const u16*)d_in[1]; const u16* wo = (const u16*)d_in[2];
  const u16* Bk = (const u16*)d_in[4]; const u16* Bv = (const u16*)d_in[5]; const u16* shk = (const u16*)d_in[6];
  const u16* hk = (const u16*)d_in[7]; const u16* Dv = (const u16*)d_in[8]; const u16* gw = (const u16*)d_in[9];
  (void)hipFuncSetAttribute((const void*)fused, hipFuncAttributeMaxDynamicSharedMemorySize, LDS_TOTAL);
  fused<<<256, 512, LDS_TOTAL, stream>>>(x, wqkv, wo, Bk, Bv, shk, hk, Dv, gw, (u16*)d_out, (char*)d_ws);
}
```

```cpp
#include <hip/hip_runtime.h>
#include <hip/hip_bf16.h>
#include <type_traits>
#define NO_PF

typedef unsigned short u16;
typedef __bf16 bf16x2 __attribute__((ext_vector_type(2)));
typedef __bf16 bf16x8 __attribute__((ext_vector_type(8)));
typedef float f32x4 __attribute__((ext_vector_type(4)));
typedef unsigned u32x4 __attribute__((ext_vector_type(4)));
typedef unsigned u32x2 __attribute__((ext_vector_type(2)));
typedef short v4i16_t __attribute__((ext_vector_type(4)));
typedef int i32x4 __attribute__((ext_vector_type(4)));
typedef __attribute__((address_space(1))) unsigned gu32;
#define LAS __attribute__((address_space(3)))
#define SBAR() __builtin_amdgcn_sched_barrier(0)
#define SGB(mask, n) __builtin_amdgcn_sched_group_barrier(mask, n, 0)
#define BAR() asm volatile("s_waitcnt lgkmcnt(0)\n\ts_barrier" ::: "memory")
#define DRAIN() asm volatile("s_waitcnt vmcnt(0)" ::: "memory")
#define SC1 16

#define TS_DECL unsigned long long tevt_ = 0;
#define TSARG , tevt_
#define TSPAR , unsigned long long& tevt_
#define TS(i) do { if ((i) == 21 && blockIdx.x == 0 && threadIdx.x == 0) tevt_ = __builtin_amdgcn_s_memrealtime(); } while (0)
#define TS_OUT(n) do { if (blockIdx.x == 0 && threadIdx.x == 0) { while (__builtin_amdgcn_s_memrealtime() < tevt_ + 3000ull) __builtin_amdgcn_s_sleep(1); } } while (0)

constexpr int S = 2048, D = 640;
constexpr unsigned SZ = S * D * 2;
constexpr unsigned F_E = 0, F_U = 128, F_PQ = F_U + 64 * 128, F_P = F_PQ + 64 * 128, F_O = F_P + 128 * 128, F_Y = F_O + 128 * 128, F_W = F_Y + 128 * 128;
constexpr unsigned OFF_PQ = 0x20000, OFF_P = OFF_PQ + 128 * 8 * 3 * 1024, OFF_UT = OFF_P + (1u << 19), OFF_G = OFF_UT + SZ, OFF_WOP = OFF_G + 8192,
                   OFF_O = OFF_WOP + (1u << 20), OFF_YT = OFF_O + SZ, WS_USED = OFF_YT + SZ;
constexpr int XS = 16384, WSB = 20480, STG = XS + WSB;
constexpr int L_EXTRA = 4 * STG;
constexpr int L_TAG = L_EXTRA, L_CNT = L_EXTRA + 16, L_T = L_EXTRA + 64;
constexpr int LDS_TOTAL = L_T + 6144 + 5120;
constexpr int CPS = 4160, UOFF = 4 * CPS, CHS = UOFF + 4608;
constexpr int L_UIMG = 5 * CHS;
constexpr int L_Y = 102400, L_RED = L_Y + 10240;

__device__ __forceinline__ unsigned pk2(float a, float b) { bf16x2 r = {(__bf16)a, (__bf16)b}; return __builtin_bit_cast(unsigned, r); }
__device__ __forceinline__ float rbf(float a) { return (float)(__bf16)a; }
__device__ __forceinline__ float bflo(unsigned v) { return __uint_as_float(v << 16); }
__device__ __forceinline__ float bfhi(unsigned v) { return __uint_as_float(v & 0xffff0000u); }
__device__ __forceinline__ float phi(float t) {
  return __builtin_amdgcn_fmed3f(t, 0.f, 3.0e38f) + (__builtin_amdgcn_fmed3f(__builtin_amdgcn_exp2f(t * 1.44269504f), 0.f, 1.f) + 1e-6f);
}
__device__ __forceinline__ i32x4 make_srd(const void* p, unsigned bytes) {
  unsigned long long a = (unsigned long long)p; i32x4 r;
  r.x = __builtin_amdgcn_readfirstlane((int)(unsigned)a); r.y = __builtin_amdgcn_readfirstlane((int)(unsigned)(a >> 32));
  r.z = __builtin_amdgcn_readfirstlane((int)bytes); r.w = 0x00020000; return r;
}
__device__ __forceinline__ void glds16(unsigned ldsaddr, unsigned voff, i32x4 srd, unsigned soff) {
  asm volatile("s_mov_b32 m0, %0\n\ts_nop 0\n\tbuffer_load_dwordx4 %1, %2, %3 offen lds" :: "s"(ldsaddr), "v"(voff), "s"(srd), "s"(soff) : "memory");
}
__device__ __forceinline__ void glds16_sc1(unsigned ldsaddr, unsigned voff, i32x4 srd, unsigned soff) {
  asm volatile("s_mov_b32 m0, %0\n\ts_nop 0\n\tbuffer_load_dwordx4 %1, %2, %3 offen sc1 lds" :: "s"(ldsaddr), "v"(voff), "s"(srd), "s"(soff) : "memory");
}
template <int N> __device__ __forceinline__ void wait_bar() { asm volatile("s_waitcnt vmcnt(%0) lgkmcnt(0)\n\ts_barrier" :: "n"(N) : "memory"); }
__device__ __forceinline__ bf16x8 lds_b128(unsigned addr) { return *(const LAS bf16x8*)(size_t)addr; }
__device__ __forceinline__ u32x2 lds_b64(unsigned addr) { return *(const LAS u32x2*)(size_t)addr; }
__device__ __forceinline__ u32x2 lds_tr(unsigned addr) { return __builtin_bit_cast(u32x2, __builtin_amdgcn_ds_read_tr16_b64_v4i16((LAS v4i16_t*)(size_t)addr)); }
__device__ __forceinline__ bf16x8 mk8(u32x2 lo, u32x2 hi) { u32x4 t = {lo.x, lo.y, hi.x, hi.y}; return __builtin_bit_cast(bf16x8, t); }
__device__ __forceinline__ f32x4 mfma16(bf16x8 a, bf16x8 b, f32x4 c) { return __builtin_amdgcn_mfma_f32_16x16x32_bf16(a, b, c, 0, 0, 0); }

__device__ __forceinline__ u32x4 pair16(u32x2 a, u32x2 b) {
  const auto r0 = __builtin_amdgcn_permlane16_swap(a.x, b.x, false, false);
  const auto r1 = __builtin_amdgcn_permlane16_swap(a.y, b.y, false, false);
  return (u32x4){r0[0], r1[0], r0[1], r1[1]};
}
__device__ __forceinline__ void store_frags(const u32x2 (&v)[5], __amdgpu_buffer_rsrc_t rw, unsigned base, int k0, int fr, int g4) {
#pragma unroll
  for (int j = 0; j < 4; j += 2) {
    const int k = k0 + 16 * (j + (g4 & 1)) + 8 * (g4 >> 1);
    __builtin_amdgcn_raw_buffer_store_b128(pair16(v[j], v[j + 1]), rw, base + (k >> 5) * 1024 + (((k >> 3) & 3) * 16 + fr) * 16, 0, SC1);
  }
  const int k = k0 + 64 + 4 * g4;
  __builtin_amdgcn_raw_buffer_store_b64(v[4], rw, base + (k >> 5) * 1024 + (((k >> 3) & 3) * 16 + fr) * 16 + ((k >> 2) & 1) * 8, 0, SC1);
}

__device__ __forceinline__ void lds_sync(unsigned cnt_addr, int lane, unsigned n) {
  asm volatile("s_waitcnt lgkmcnt(0)" ::: "memory");
  if (lane == 0) __hip_atomic_fetch_add((LAS unsigned*)(size_t)cnt_addr, 1u, __ATOMIC_RELAXED, __HIP_MEMORY_SCOPE_WORKGROUP);
  while (__builtin_amdgcn_readfirstlane(__hip_atomic_load((LAS unsigned*)(size_t)cnt_addr, __ATOMIC_RELAXED, __HIP_MEMORY_SCOPE_WORKGROUP)) < n) {}
  asm volatile("" ::: "memory");
}
__device__ __forceinline__ bool lds_last(unsigned cnt_addr, int lane, unsigned n) {
  unsigned old = 0;
  if (lane == 0) old = __hip_atomic_fetch_add((LAS unsigned*)(size_t)cnt_addr, 1u, __ATOMIC_RELAXED, __HIP_MEMORY_SCOPE_WORKGROUP);
  return __builtin_amdgcn_readfirstlane(old) == n - 1;
}

__device__ __forceinline__ unsigned flag_ld(const char* ws, unsigned off) { return __hip_atomic_load((const gu32*)(ws + off), __ATOMIC_RELAXED, __HIP_MEMORY_SCOPE_AGENT); }
__device__ __forceinline__ void flag_st(char* ws, unsigned off, unsigned v) { __hip_atomic_store((gu32*)(ws + off), v, __ATOMIC_RELAXED, __HIP_MEMORY_SCOPE_AGENT); }
__device__ __forceinline__ void poll(const char* ws, unsigned off, bool act, unsigned tag) {
  for (int spins = 0; spins < (1 << 18); spins++) {
    const unsigned v = act ? flag_ld(ws, off) : tag;
    if (__all(v == tag)) break;
    __builtin_amdgcn_s_sleep(2);
  }
  asm volatile("" ::: "memory");
}

template <bool KV>
__device__ __forceinline__ void gemm_compute(f32x4 (&acc)[4][5], unsigned xa0, unsigned wa0, unsigned wa1 TSPAR) {
  bf16x8 xa[4], xb[4], wa[5], wb[5];
  auto load = [&](bf16x8 (&xf)[4], bf16x8 (&wf)[5], int buf, int kk) {
    const unsigned bo = buf * STG;
    const unsigned xa = (xa0 + bo) ^ (kk * 64);
    const unsigned a0 = wa0 + bo + kk * 10240, a1 = wa1 + bo + kk * 10240;
#pragma unroll
    for (int i = 0; i < 4; i++) xf[i] = lds_b128(xa + 2048 * i);
#pragma unroll
    for (int t = 0; t < 5; t++) { const unsigned a = ((t & 1) ? a1 : a0) + 32 * t; wf[t] = mk8(lds_tr(a), lds_tr(a + 1280)); }
  };
  auto mma = [&](const bf16x8 (&xf)[4], const bf16x8 (&wf)[5]) {
#pragma unroll
    for (int t = 0; t < 5; t++)
#pragma unroll
      for (int i = 0; i < 4; i++) acc[i][t] = KV ? mfma16(xf[i], wf[t], acc[i][t]) : mfma16(wf[t], xf[i], acc[i][t]);
  };
  auto interleave = [&]() {
#pragma unroll
    for (int t = 0; t < 14; t++) { SGB(0x8, 1); SGB(0x100, 1); }
    SGB(0x8, 6);
  };
  BAR();
  TS(21);
  load(xa, wa, 0, 0);
  SBAR();
  for (int s = 0; s < 9; s++) {
    load(xb, wb, s & 3, 1); mma(xa, wa); interleave();
    SBAR();
    BAR();
    if (s == 0) TS(22);
    if (s == 4) TS(23);
    load(xa, wa, (s + 1) & 3, 0); mma(xb, wb); interleave();
    SBAR();
  }
  load(xb, wb, 1, 1); mma(xa, wa); interleave();
  SBAR();
  mma(xb, wb);
}

__device__ __forceinline__ void gemm_loader(int L, int lane, unsigned lb, const u16* x, const u16* wqkv, int rt, int cg, bool isKV) {
  const int s0 = (((rt & 3) * 5 + 1) / 2 + (isKV ? 2 * (cg & 3) : 8 + (cg & 1))) % 10;
  const i32x4 sx = make_srd(x, S * D * 2), sw = make_srd(wqkv, D * 3 * D * 2);
  unsigned vx[2], vw[5];
#pragma unroll
  for (int i = 0; i < 2; i++) vx[i] = (rt * 128 + L * 64 + i * 8 + (lane >> 3)) * 1280 + (((lane & 7) ^ ((4 * i + (lane >> 4)) & 7)) << 4);
#pragma unroll
  for (int i = 0; i < 5; i++) {
    const int ci = i * 64 + lane, k = (ci * 3277) >> 16, pos = ci - k * 20, gc = pos ^ (((k >> 3) & 1) << 1), tl = gc >> 1;
    unsigned col;
    if (isKV) { const int isv = (tl >= 3 && tl < 5) || tl >= 7; const int tt = tl < 3 ? tl : tl < 5 ? tl - 3 : tl < 7 ? tl - 2 : tl - 5;
                col = (isv ? 2560u : 1280u) + 160u * cg + tt * 32; }
    else col = 320u * cg + tl * 32;
    vw[i] = (L * 32 + k) * 3840 + col + (gc & 1) * 16;
  }
  auto stage = [&](int st_, int buf) {
    const unsigned base = lb + buf * STG;
    int st = st_ + s0; st = st >= 10 ? st - 10 : st;
#pragma unroll
    for (int p = 0; p < 10; p++) {
      glds16(base + XS + (L * 10 + p) * 1024, vw[p % 5], sw, st * (64 * 3840) + (p / 5) * (16 * 3840));
      if (p < 8) glds16(base + (L * 8 + p) * 1024, vx[p & 1], sx, st * 128 + (p >> 1) * (16 * 1280));
    }
  };
  stage(0, 0); stage(1, 1);
  wait_bar<18>();
  stage(2, 2); stage(3, 3);
  for (int s = 0; s < 9; s++) {
    if (s < 7) wait_bar<36>(); else if (s == 7) wait_bar<18>(); else wait_bar<0>();
    if (s < 6) stage(s + 4, s & 3);
  }
}

__device__ __forceinline__ void conv_load_h(u32x4 (&cur)[4], u32x4 (&prv)[4], const u16* __restrict__ hrow, int lane) {
#pragma unroll
  for (int it = 0; it < 4; it++) {
    const int i = it * 64 + lane;
    cur[it] = *(const u32x4*)(hrow + i * 8); prv[it] = *(const u32x4*)(hrow + (i > 0 ? i * 8 - 8 : 0));
  }
}
__device__ __forceinline__ void conv_prep_h(const u32x4 (&cur)[4], const u32x4 (&prv_)[4], unsigned cb, int lane) {
#pragma unroll
  for (int it = 0; it < 4; it++) {
    u32x4 prv = prv_[it];
    if (it == 0 && lane == 0) prv = (u32x4){0, 0, 0, 0};
    const unsigned W_[8] = {prv.x, prv.y, prv.z, prv.w, cur[it].x, cur[it].y, cur[it].z, cur[it].w};
    const int c = 255 - (it * 64 + lane);
#pragma unroll
    for (int e = 0; e < 4; e++) {
      u32x4 ov;
#pragma unroll
      for (int q = 0; q < 4; q++) {
        const int i0 = 15 - e - 2 * q, i1 = i0 - 1;
        const unsigned sel = ((i0 & 1) ? 0x0706u : 0x0504u) | (((i1 & 1) ? 0x0302u : 0x0100u) << 16);
        ov[q] = __builtin_amdgcn_perm(W_[i0 >> 1], W_[i1 >> 1], sel);
      }
      *(LAS u32x4*)(size_t)(cb + e * CPS + c * 16) = ov;
    }
  }
  if (lane < 8) *(LAS u32x4*)(size_t)(cb + (lane >> 1) * CPS + (256 + (lane & 1)) * 16) = (u32x4){0, 0, 0, 0};
  if (lane < 32) *(LAS u32x4*)(size_t)(cb + UOFF + lane * 16) = (u32x4){0, 0, 0, 0};
}
template <class F>
__device__ __forceinline__ void conv_load_u(__amdgpu_buffer_rsrc_t rw, int d, unsigned cb, int lane, F&& between) {
  u32x4 uv[4];
#pragma unroll
  for (int it = 0; it < 4; it++) uv[it] = __builtin_amdgcn_raw_buffer_load_b128(rw, OFF_UT + d * 4096 + (it * 64 + lane) * 16, 0, SC1);
  SBAR();
  between();
  SBAR();
#pragma unroll
  for (int it = 0; it < 4; it++) *(LAS u32x4*)(size_t)(cb + UOFF + 512 + (it * 64 + lane) * 16) = uv[it];
}
struct ConvFrags { u32x2 al[8], ah[8]; u32x4 B[8]; };
__device__ __forceinline__ void conv_read(ConvFrags& f, unsigned aa, unsigned ba) {
  asm volatile(
      "ds_read_b128 %16, %25\n\tds_read_b64 %7, %24 offset:3584\n\tds_read_b64 %15, %24 offset:3592\n\t"
      "ds_read_b128 %17, %25 offset:512\n\tds_read_b128 %18, %25 offset:1024\n\tds_read_b128 %19, %25 offset:1536\n\t"
      "ds_read_b128 %20, %25 offset:2048\n\tds_read_b128 %21, %25 offset:2560\n\tds_read_b128 %22, %25 offset:3072\n\t"
      "ds_read_b128 %23, %25 offset:3584\n\t"
      "ds_read_b64 %6, %24 offset:3072\n\tds_read_b64 %14, %24 offset:3080\n\t"
      "ds_read_b64 %5, %24 offset:2560\n\tds_read_b64 %13, %24 offset:2568\n\t"
      "ds_read_b64 %4, %24 offset:2048\n\tds_read_b64 %12, %24 offset:2056\n\t"
      "ds_read_b64 %3, %24 offset:1536\n\tds_read_b64 %11, %24 offset:1544\n\t"
      "ds_read_b64 %2, %24 offset:1024\n\tds_read_b64 %10, %24 offset:1032\n\t"
      "ds_read_b64 %1, %24 offset:512\n\tds_read_b64 %9, %24 offset:520\n\t"
      "ds_read_b64 %0, %24\n\tds_read_b64 %8, %24 offset:8"
      : "=&v"(f.al[0]), "=&v"(f.al[1]), "=&v"(f.al[2]), "=&v"(f.al[3]), "=&v"(f.al[4]), "=&v"(f.al[5]), "=&v"(f.al[6]), "=&v"(f.al[7]),
        "=&v"(f.ah[0]), "=&v"(f.ah[1]), "=&v"(f.ah[2]), "=&v"(f.ah[3]), "=&v"(f.ah[4]), "=&v"(f.ah[5]), "=&v"(f.ah[6]), "=&v"(f.ah[7]),
        "=&v"(f.B[0]), "=&v"(f.B[1]), "=&v"(f.B[2]), "=&v"(f.B[3]), "=&v"(f.B[4]), "=&v"(f.B[5]), "=&v"(f.B[6]), "=&v"(f.B[7])
      : "v"(aa), "v"(ba) : "memory");
}
__device__ __forceinline__ void conv_wait(ConvFrags& f) {
  asm volatile("s_waitcnt lgkmcnt(0)"
      : "+v"(f.al[0]), "+v"(f.al[1]), "+v"(f.al[2]), "+v"(f.al[3]), "+v"(f.al[4]), "+v"(f.al[5]), "+v"(f.al[6]), "+v"(f.al[7]),
        "+v"(f.ah[0]), "+v"(f.ah[1]), "+v"(f.ah[2]), "+v"(f.ah[3]), "+v"(f.ah[4]), "+v"(f.ah[5]), "+v"(f.ah[6]), "+v"(f.ah[7]),
        "+v"(f.B[0]), "+v"(f.B[1]), "+v"(f.B[2]), "+v"(f.B[3]), "+v"(f.B[4]), "+v"(f.B[5]), "+v"(f.B[6]), "+v"(f.B[7]) :: "memory");
}
template <int K> __device__ __forceinline__ void conv_read1(ConvFrags& f, unsigned aa, unsigned ba) {
  if constexpr (K < 8) asm volatile("ds_read_b128 %0, %1 offset:%2" : "=&v"(f.B[K]) : "v"(ba), "n"(512 * K) : "memory");
  else if constexpr (K < 16) asm volatile("ds_read_b64 %0, %1 offset:%2" : "=&v"(f.al[K - 8]) : "v"(aa), "n"(512 * (K - 8)) : "memory");
  else if constexpr (K < 24) asm volatile("ds_read_b64 %0, %1 offset:%2" : "=&v"(f.ah[K - 16]) : "v"(aa), "n"(512 * (K - 16) + 8) : "memory");
}
template <int K = 0> __device__ __forceinline__ void conv_step(f32x4 (&acc)[8], const ConvFrags& f, ConvFrags& nx, unsigned aa, unsigned ba) {
  if constexpr (K < 36) {
    constexpr int a = K < 8 ? 0 : K < 15 ? 1 : K < 21 ? 2 : K < 26 ? 3 : K < 30 ? 4 : K < 33 ? 5 : K < 35 ? 6 : 7;
    constexpr int i = K - (a == 0 ? 0 : a == 1 ? 8 : a == 2 ? 15 : a == 3 ? 21 : a == 4 ? 26 : a == 5 ? 30 : a == 6 ? 33 : 35);
    acc[a + i] = mfma16(mk8(f.al[7 - a], f.ah[7 - a]), __builtin_bit_cast(bf16x8, f.B[i]), acc[a + i]);
    conv_read1<K>(nx, aa, ba);
    SBAR();
    conv_step<K + 1>(acc, f, nx, aa, ba);
  }
}
__device__ __forceinline__ void conv_main(f32x4 (&acc)[8], unsigned cb, int lane, int c0, int c1) {
  const int fr = lane & 15, g4 = lane >> 4;
  const unsigned abase = cb + (3 - (fr & 3)) * CPS + 2 * (2044 - 16 * 112 - 16 * (g4 >> 1) + 8 * (g4 & 1) - (fr & ~3));
  const unsigned bbase = cb + UOFF + 512 + 2 * (16 * fr - 16 * (g4 >> 1) + 8 * (g4 & 1));
#pragma unroll
  for (int c = 0; c < 8; c++) acc[c] = (f32x4){0.f, 0.f, 0.f, 0.f};
  ConvFrags f0, f1;
  asm volatile("s_waitcnt lgkmcnt(0)" ::: "memory");
  conv_read(f0, abase - 64 * c0, bbase - 64 * c0);
  for (int cc = c0; cc < c1; cc += 2) {
    conv_wait(f0);
    SBAR();
    conv_step(acc, f0, f1, abase - 64 * (cc + 1), bbase - 64 * (cc + 1));
    conv_wait(f1);
    SBAR();
    const int cn = cc + 2 < c1 ? cc + 2 : cc + 1;
    conv_step(acc, f1, f0, abase - 64 * cn, bbase - 64 * cn);
  }
  conv_wait(f0);
}
__device__ __forceinline__ void conv_store(f32x4 y, unsigned cb, int c, int lane, float Dd, __amdgpu_buffer_rsrc_t rw, int d) {
  const int fr = lane & 15, g4 = lane >> 4;
  const u32x2 uu = lds_b64(cb + UOFF + 512 + 2 * (256 * c + 16 * fr + 4 * g4));
  const float r0 = fmaf(bflo(uu.x), Dd, y[0]), r1 = fmaf(bfhi(uu.x), Dd, y[1]);
  const float r2 = fmaf(bflo(uu.y), Dd, y[2]), r3 = fmaf(bfhi(uu.y), Dd, y[3]);
  __builtin_amdgcn_raw_buffer_store_b64((u32x2){pk2(r0, r1), pk2(r2, r3)}, rw, OFF_YT + d * 4096 + (256 * c + 16 * fr + 4 * g4) * 2, 0, SC1);
}

__global__ __launch_bounds__(512, 2) void fused(const u16* __restrict__ x, const u16* __restrict__ wqkv, const u16* __restrict__ wo,
                                                const u16* __restrict__ Bk, const u16* __restrict__ Bv, const u16* __restrict__ shk,
                                                const u16* __restrict__ hk, const u16* __restrict__ Dv, const u16* __restrict__ gw,
                                                u16* __restrict__ out, char* __restrict__ ws) {
  extern __shared__ __attribute__((aligned(16))) char sm[];
  const int tid = threadIdx.x, lane = tid & 63;
  const int w = __builtin_amdgcn_readfirstlane(tid >> 6);
  const int b = blockIdx.x, xcd = b & 7, r = b >> 3;
  const int fr = lane & 15, g4 = lane >> 4;
  const unsigned lb = (unsigned)(size_t)(LAS char*)sm;
  const __amdgpu_buffer_rsrc_t rw = __builtin_amdgcn_make_buffer_rsrc(ws, 0, WS_USED, 0x00020000);
  TS_DECL TS(0);
  unsigned e0 = 0;
#define PUT_TAG() do { if (w == 7 && lane == 0) { *(LAS unsigned*)(size_t)(lb + L_TAG) = e0 + 1u; *(LAS u32x4*)(size_t)(lb + L_CNT) = (u32x4){0u, 0u, 0u, 0u}; } } while (0)
#define TAG (*(const LAS unsigned*)(size_t)(lb + L_TAG))
  bool wo_issued = false;
  if (r < 24) {
    const bool isKV = r < 16;
    const int rt = (xcd & 3) * 4 + (r & 3);
    const int cg = isKV ? (xcd >> 2) * 4 + (r >> 2) : (xcd >> 2) * 2 + ((r - 16) >> 2);
    f32x4 acc[4][5];
    const int rp = (w >> 1) & 1, hf = w & 1;
    if (w < 4) {
      const unsigned xa0 = lb + (rp * 64 + fr) * 128 + ((g4 ^ ((fr >> 1) & 7)) << 4);
      const unsigned wl = lb + XS + (8 * g4 + (fr >> 2)) * 320 + ((lane & 3) >> 1) * 16 + (lane & 1) * 8 + hf * 160;
      const unsigned sgn = (g4 & 1) * 32;
      const unsigned wa0 = hf ? wl - sgn : wl + sgn, wa1 = hf ? wl + sgn : wl - sgn;
#pragma unroll
      for (int i = 0; i < 4; i++)
#pragma unroll
        for (int t = 0; t < 5; t++) acc[i][t] = (f32x4){0.f, 0.f, 0.f, 0.f};
      if (isKV) gemm_compute<true>(acc, xa0, wa0, wa1 TSARG); else gemm_compute<false>(acc, xa0, wa0, wa1 TSARG);
    } else if (w < 6) {
      gemm_loader(w - 4, lane, lb, x, wqkv, rt, cg, isKV);
    } else {
      const int w6 = w - 6;
      unsigned pf[5] = {0, 0, 0, 0, 0};
      SBAR();
      if (w == 7) e0 = flag_ld(ws, F_E);
      if (isKV) {
        const int l16 = lane & 15, row0 = rt * 128 + cg * 16 + w6 * 8;
        const u32x4 g0 = *(const u32x4*)(gw + lane * 8), g1 = *(const u32x4*)(gw + 512 + l16 * 8);
        u32x4 gx0[8], gx1[8];
#pragma unroll
        for (int i = 0; i < 8; i++) {
          const u16* xr = x + (size_t)(row0 + i) * D;
          gx0[i] = *(const u32x4*)(xr + lane * 8); gx1[i] = *(const u32x4*)(xr + 512 + l16 * 8);
        }
        SBAR();
        BAR(); BAR(); BAR();
        float a[8];
        const float m1 = lane < 16 ? 1.f : 0.f;
#pragma unroll
        for (int i = 0; i < 8; i++) {
          float a0 = 0.f, a1 = 0.f;
#pragma unroll
          for (int j = 0; j < 4; j++) {
            a0 = fmaf(bflo(gx0[i][j]), bflo(g0[j]), a0); a0 = fmaf(bfhi(gx0[i][j]), bfhi(g0[j]), a0);
            a1 = fmaf(bflo(gx1[i][j]), bflo(g1[j]), a1); a1 = fmaf(bfhi(gx1[i][j]), bfhi(g1[j]), a1);
          }
          a[i] = fmaf(a1, m1, a0);
          if (i == 3) { SBAR(); BAR(); SBAR(); }
        }
        SBAR(); BAR(); SBAR();
#pragma unroll
        for (int i = 0; i < 4; i++) { const float lo = a[i], hi = a[i + 4]; const bool up = lane & 4; a[i] = (up ? hi : lo) + __shfl_xor(up ? lo : hi, 4); }
#pragma unroll
        for (int i = 0; i < 2; i++) { const float lo = a[i], hi = a[i + 2]; const bool up = lane & 2; a[i] = (up ? hi : lo) + __shfl_xor(up ? lo : hi, 2); }
        { const float lo = a[0], hi = a[1]; const bool up = lane & 1; a[0] = (up ? hi : lo) + __shfl_xor(up ? lo : hi, 1); }
        a[0] += __shfl_xor(a[0], 8); a[0] += __shfl_xor(a[0], 16); a[0] += __shfl_xor(a[0], 32);
        if (lane < 8) __builtin_amdgcn_raw_buffer_store_b32(__float_as_uint(rbf(1.f / (1.f + __expf(-rbf(a[0]))))), rw, OFF_G + (row0 + lane) * 4, 0, SC1);
        PUT_TAG();
        asm volatile("" :: "v"(pf[0]), "v"(pf[1]), "v"(pf[2]), "v"(pf[3]), "v"(pf[4]));
        SBAR();
        BAR(); BAR(); BAR(); BAR(); BAR();
      } else {
        const int hw = (xcd * 8 + (r - 16)) * 2 + w6;
        const unsigned scr = lb + L_T + w6 * 6144;
#pragma unroll
        for (int bt = 0; bt < 2; bt++) {
          const int blk = bt == 0 ? hw : (hw < 32 ? 128 + hw : hw), ct = (blk * 3277) >> 16, ks = blk - 20 * ct;
          u32x4 v[5];
#pragma unroll
          for (int i = 0; i < 5; i++) {
            const int c = i * 64 + lane, row = (c * 6554) >> 16, ch = c - 10 * row;
            v[i] = *(const u32x4*)(wo + (size_t)(32 * ks + row) * D + 80 * ct + 8 * ch);
          }
          SBAR();
          BAR(); BAR();
          if (bt == 0 || hw < 32) {
#pragma unroll
            for (int i = 0; i < 5; i++) *(LAS u32x4*)(size_t)(scr + (i * 64 + lane) * 16) = v[i];
            const unsigned ta = scr + (8 * g4 + (fr >> 2)) * 160 + (lane & 3) * 8;
#pragma unroll
            for (int t = 0; t < 5; t++) {
              const u32x2 lo = lds_tr(ta + 32 * t), hi = lds_tr(ta + 32 * t + 640);
              __builtin_amdgcn_raw_buffer_store_b128((u32x4){lo.x, lo.y, hi.x, hi.y}, rw, OFF_WOP + (blk * 5 + t) * 1024 + lane * 16, 0, SC1);
            }
          }
          SBAR();
          BAR(); BAR();
        }
        PUT_TAG();
        asm volatile("" :: "v"(pf[0]), "v"(pf[1]), "v"(pf[2]), "v"(pf[3]), "v"(pf[4]));
        BAR(); BAR();
      }
    }
    TS(1);
    BAR();
    TS(11);
    const unsigned tag = TAG;
    if (isKV) {
      if (w < 4) {
#pragma unroll
        for (int t = 0; t < 5; t++) {
          const int slot = hf ? (t < 2 ? 3 + t : 5 + t) : (t < 3 ? t : 2 + t);
#pragma unroll
          for (int m = 0; m < 2; m++)
            *(LAS u32x4*)(size_t)(lb + ((rp * 2 + m) * 10 + slot) * 1024 + lane * 16) =
                (u32x4){pk2(acc[2 * m][t][0], acc[2 * m][t][1]), pk2(acc[2 * m][t][2], acc[2 * m][t][3]),
                        pk2(acc[2 * m + 1][t][0], acc[2 * m + 1][t][1]), pk2(acc[2 * m + 1][t][2], acc[2 * m + 1][t][3])};
        }
      } else {
        for (int i = tid - 256; i < 6048 / 16; i += 256) *(LAS u32x4*)(size_t)(lb + L_T + i * 16) = (u32x4){0, 0, 0, 0};
      }
      TS(12);
      BAR();
      {
        const int f0 = w < 4 ? 2 * w : 8 + 3 * (w - 4);
        u32x4 v[3];
#pragma unroll
        for (int q = 0; q < 3; q++) { const int f = f0 + (q < 2 || w >= 4 ? q : 0); v[q] = *(const LAS u32x4*)(size_t)(lb + ((f / 5) * 10 + (f % 5)) * 1024 + lane * 16); }
#pragma unroll
        for (int q = 0; q < 3; q++) {
          if (q < 2 || w >= 4) {
            const int f = f0 + q;
            *(LAS u32x4*)(size_t)(lb + ((f / 5) * 10 + (f % 5)) * 1024 + lane * 16) =
                (u32x4){pk2(phi(bflo(v[q].x)), phi(bfhi(v[q].x))), pk2(phi(bflo(v[q].y)), phi(bfhi(v[q].y))),
                        pk2(phi(bflo(v[q].z)), phi(bfhi(v[q].z))), pk2(phi(bflo(v[q].w)), phi(bfhi(v[q].w)))};
          }
        }
      }
      TS(13);
      BAR();
      TS(2);
      {
        const u32x4 o1 = {0x3F803F80u, 0x3F803F80u, 0x3F803F80u, 0x3F803F80u};
        bf16x8 A[3][4], B[3][4];
        int ti[3], tj[3];
#pragma unroll
        for (int q = 0; q < 3; q++) {
          const int T = (q < 2 || w < 2) ? w + 8 * q : w;
          const bool isz = T >= 13;
          ti[q] = isz ? T - 13 : (T + 1) / 3;
          tj[q] = isz ? -1 : T - (ti[q] == 0 ? 0 : 3 * ti[q] - 1) + (ti[q] == 0 ? 0 : ti[q] - 1);
          const unsigned aA = lb + ti[q] * 1024 + lane * 16, aB = lb + (5 + (isz ? 0 : tj[q])) * 1024 + lane * 16;
#pragma unroll
          for (int m = 0; m < 4; m++) { A[q][m] = lds_b128(aA + m * 10240); B[q][m] = lds_b128(aB + m * 10240); }
        }
        f32x4 d4[3];
#pragma unroll
        for (int q = 0; q < 3; q++) {
          d4[q] = (f32x4){0.f, 0.f, 0.f, 0.f};
          if (tj[q] < 0) {
#pragma unroll
            for (int m = 0; m < 4; m++) B[q][m] = __builtin_bit_cast(bf16x8, o1);
          }
        }
#pragma unroll
        for (int m = 0; m < 4; m++)
#pragma unroll
          for (int q = 0; q < 3; q++) d4[q] = mfma16(A[q][m], B[q][m], d4[q]);
#pragma unroll
        for (int q = 0; q < 3; q++) {
          if (q < 2 || w < 2) {
            const bool isz = tj[q] < 0;
            const int Dg = 16 * ti[q] + 4 * g4, h = (Dg * 205) >> 12, dd = Dg - 20 * h;
            const int ee = isz ? 20 : 16 * tj[q] + fr - 20 * h;
            if (isz ? fr == 0 : (ee >= 0 && ee < 20))
              __builtin_amdgcn_raw_buffer_store_b64((u32x2){pk2(d4[q][0], d4[q][1]), pk2(d4[q][2], d4[q][3])}, rw,
                                                    OFF_P + (rt * 8 + cg) * 3360 + (h * 420 + ee * 20 + dd) * 2, 0, SC1);
          }
        }
      }
      TS(14);
      DRAIN();
      TS(15);
      BAR();
      TS(3);
      if (w == 0) {
        if (lane == 0) flag_st(ws, F_P + (rt * 8 + cg) * 128, tag);
        poll(ws, lane < 16 ? F_P + (lane * 8 + cg) * 128 : F_PQ + (rt * 4 + (cg >> 1)) * 128, lane <= 16, tag);
      } else if (w == 7) {
        poll(ws, F_W + lane * 128, true, tag);
      }
      constexpr int PJ[9] = {0, 1, 1, 2, 2, 3, 3, 4, 4}, PK[9] = {0, 0, 1, 0, 1, 1, 2, 1, 2};
      unsigned an[9], ad[9];
#pragma unroll
      for (int p = 0; p < 9; p++) {
        const int E = 16 * PJ[p] + fr, h = (E * 205) >> 12, e = E - 20 * h;
        int o_ = 32 * PK[p] + 8 * g4 - 20 * h; o_ = o_ < -8 ? -8 : o_ > 20 ? 20 : o_;
        an[p] = lb + L_T + (h * 21 + e) * 72 + (8 + o_) * 2; ad[p] = lb + L_T + (h * 21 + 20) * 72 + (8 + o_) * 2;
      }
      BAR();
      TS(4);
      bf16x8 Bq[3];
      {
        const unsigned prow = OFF_PQ + ((rt * 8 + w) * 8 + cg) * 3072 + lane * 16;
#pragma unroll
        for (int ks = 0; ks < 3; ks++) Bq[ks] = __builtin_bit_cast(bf16x8, __builtin_amdgcn_raw_buffer_load_b128(rw, prow + 1024 * ks, 0, SC1));
      }
      if (tid < 210) {
        const unsigned pb = OFF_P + cg * 3360 + tid * 16;
        u32x4 v[16];
#pragma unroll
        for (int k = 0; k < 16; k++) v[k] = __builtin_amdgcn_raw_buffer_load_b128(rw, pb, (k ^ rt) * (8 * 3360), SC1);
        unsigned o4[4];
#pragma unroll
        for (int c = 0; c < 4; c++) {
          float lo[16], hi[16];
#pragma unroll
          for (int k = 0; k < 16; k++) { lo[k] = bflo(v[k][c]); hi[k] = bfhi(v[k][c]); }
#pragma unroll
          for (int st = 1; st < 16; st <<= 1)
#pragma unroll
            for (int k = 0; k < 16; k += 2 * st) { lo[k] += lo[k + st]; hi[k] += hi[k + st]; }
          o4[c] = pk2(lo[0], hi[0]);
        }
        TS(16);
#pragma unroll
        for (int q = 0; q < 2; q++) {
          const int i4 = 2 * tid + q, row = (i4 * 13108) >> 16, d = 4 * (i4 - row * 5);
          *(LAS u32x2*)(size_t)(lb + L_T + row * 72 + (8 + d) * 2) = (u32x2){o4[2 * q], o4[2 * q + 1]};
        }
      }
      BAR();
      TS(5);
      {
        if (g4 >= 2) Bq[2] = __builtin_bit_cast(bf16x8, (u32x4){0, 0, 0, 0});
        f32x4 nu[5], de[5];
#pragma unroll
        for (int j = 0; j < 5; j++) { nu[j] = (f32x4){0.f, 0.f, 0.f, 0.f}; de[j] = nu[j]; }
        bf16x8 An[9], Ad[9];
#pragma unroll
        for (int p = 0; p < 9; p++) {
          unsigned an8 = an[p] + 8, ad8 = ad[p] + 8;
          asm volatile("" : "+v"(an8), "+v"(ad8));
          An[p] = mk8(lds_b64(an[p]), lds_b64(an8)); Ad[p] = mk8(lds_b64(ad[p]), lds_b64(ad8));
        }
#pragma unroll
        for (int p = 0; p < 9; p++) { de[PJ[p]] = mfma16(Ad[p], Bq[PK[p]], de[PJ[p]]); nu[PJ[p]] = mfma16(An[p], Bq[PK[p]], nu[PJ[p]]); }
        u32x2 ov[5];
#pragma unroll
        for (int j = 0; j < 5; j++) {
          const float rd = __builtin_amdgcn_rcpf(de[j][0] + 1e-6f);
          ov[j] = (u32x2){pk2(nu[j][0] * rd, nu[j][1] * rd), pk2(nu[j][2] * rd, nu[j][3] * rd)};
        }
        store_frags(ov, rw, OFF_O + (rt * 8 + w) * 20480, 80 * cg, fr, g4);
      }
      TS(17);
      DRAIN();
      BAR();
      if (tid == 0) flag_st(ws, F_O + (rt * 8 + cg) * 128, tag);
      TS(6);
      {
        const i32x4 sws = make_srd(ws, WS_USED);
        const unsigned vwv = OFF_WOP + ((xcd >> 2) * 4 + (r >> 3)) * 102400 + lane * 16;
        for (int i = w; i < 100; i += 8) {
          int pc = 12 * (r & 7) + i; pc = __builtin_amdgcn_readfirstlane(pc >= 100 ? pc - 100 : pc);
          glds16_sc1(lb + pc * 1024, vwv, sws, pc * 1024);
        }
      }
      wo_issued = true;
    } else {
      const int vb = xcd * 8 + (r - 16);
      const int d = 80 * (vb >> 3) + 48 + 4 * (vb & 7) + (w & 3);
      const unsigned cb = lb + (w & 3) * CHS;
      if (w < 4) {
#pragma unroll
        for (int i = 0; i < 4; i++) {
          u32x2 pv[5];
#pragma unroll
          for (int t = 0; t < 5; t++) pv[t] = (u32x2){pk2(phi(acc[i][t][0]), phi(acc[i][t][1])), pk2(phi(acc[i][t][2]), phi(acc[i][t][3]))};
          store_frags(pv, rw, OFF_PQ + ((rt * 8 + rp * 4 + i) * 8 + cg * 2 + hf) * 3072, 0, fr, g4);
        }
      } else {
        u32x4 hc[4], hp[4];
        conv_load_h(hc, hp, hk + (size_t)d * S, lane);
        conv_prep_h(hc, hp, cb, lane);
        if (w == 7) poll(ws, F_U + lane * 128, true, tag);
      }
      TS(12);
      DRAIN();
      BAR();
      TS(2);
      if (tid == 0) { flag_st(ws, F_PQ + (rt * 4 + cg) * 128, tag); flag_st(ws, F_W + vb * 128, tag); }
      if (w < 4) {
        const float Dd = bflo(Dv[d]);
        conv_load_u(rw, d, cb, lane, [&]() {});
        TS(3);
        f32x4 y[8];
        conv_main(y, cb, lane, 0, 8);
        TS(4);
#pragma unroll
        for (int c = 0; c < 8; c++) conv_store(y[c], cb, c, lane, Dd, rw, d);
        DRAIN();
      }
      BAR();
      if (tid == 0) flag_st(ws, F_Y + (64 + vb) * 128, tag);
      TS(5); TS(6);
    }
  } else {
    const int pw = xcd * 8 + (r - 24);
    const int c6 = w < 4 ? w : 4 + ((w - 4) >> 1);
    const int d = 80 * (pw >> 3) + 6 * (pw & 7) + c6;
    const unsigned cb = lb + c6 * CHS;
    const bool prep = w < 4 || !(w & 1);
    u32x4 hc[4], hp[4];
    {
      const int rbase = xcd * 256 + ((r - 24) >> 1) * 64, cbase = ((r - 24) & 1) * 320;
      u32x4 kr[2][7], vr[2][4], sp[2][4];
#pragma unroll
      for (int q = 0; q < 2; q++) {
        const int it = w + 8 * q;
        if (q == 0 || w < 2) {
          const int ch = cbase + (it >> 1) * 64 + (lane & 7) * 8;
          const int s0 = rbase + (it & 1) * 32 + (lane >> 3) * 4;
#pragma unroll
          for (int j = 0; j < 7; j++) { int s = s0 - 3 + j; kr[q][j] = *(const u32x4*)(Bk + (size_t)(s < 0 ? 0 : s) * D + ch); }
#pragma unroll
          for (int j = 0; j < 4; j++) vr[q][j] = *(const u32x4*)(Bv + (size_t)(s0 + j) * D + ch);
#pragma unroll
          for (int j = 0; j < 4; j++) sp[q][j] = ((const u32x4*)(shk + ch * 4))[j];
        }
      }
      if (prep) conv_load_h(hc, hp, hk + (size_t)d * S, lane);
      if (w == 7) e0 = flag_ld(ws, F_E);
      SBAR();
#pragma unroll
      for (int q = 0; q < 2; q++) {
        const int it = w + 8 * q;
        if (q == 0 || w < 2) {
          const int s0 = rbase + (it & 1) * 32 + (lane >> 3) * 4;
          if (s0 == 0) { kr[q][0] = (u32x4){0, 0, 0, 0}; kr[q][1] = kr[q][0]; kr[q][2] = kr[q][0]; }
          const int rg = (it & 1) * 8 + (lane >> 3);
          const unsigned la = lb + L_UIMG + ((it >> 1) * 64 + (lane & 7) * 8) * 128 + (((rg >> 1) ^ (lane & 7)) << 4) + (rg & 1) * 8;
#pragma unroll
          for (int c = 0; c < 8; c++) {
            const unsigned s01 = sp[q][c >> 1][(c & 1) * 2], s23 = sp[q][c >> 1][(c & 1) * 2 + 1];
            const float w0 = bflo(s01), w1 = bfhi(s01), w2 = bflo(s23), w3 = bfhi(s23);
            float kk[7], u[4];
#pragma unroll
            for (int j = 0; j < 7; j++) { unsigned dd = kr[q][j][c >> 1]; kk[j] = (c & 1) ? bfhi(dd) : bflo(dd); }
#pragma unroll
            for (int j = 0; j < 4; j++) {
              unsigned dd = vr[q][j][c >> 1]; float v = (c & 1) ? bfhi(dd) : bflo(dd);
              float ks = kk[j] * w3; ks = fmaf(kk[j + 1], w2, ks); ks = fmaf(kk[j + 2], w1, ks); ks = fmaf(kk[j + 3], w0, ks);
              u[j] = ks * v;
            }
            *(LAS u32x2*)(size_t)(la + c * 128) = (u32x2){pk2(u[0], u[1]), pk2(u[2], u[3])};
          }
        }
      }
      PUT_TAG();
      BAR();
#pragma unroll
      for (int i = 0; i < 5; i++) {
        const int chl = (w * 5 + i) * 8 + (lane >> 3), k = lane & 7;
        const u32x4 v = *(const LAS u32x4*)(size_t)(lb + L_UIMG + chl * 128 + ((k ^ ((chl >> 3) & 7)) << 4));
        __builtin_amdgcn_raw_buffer_store_b128(v, rw, OFF_UT + (cbase + chl) * 4096 + rbase * 2 + k * 16, 0, SC1);
      }
      TS(1);
    }
    if (w < 5 && prep) conv_prep_h(hc, hp, cb, lane);
    DRAIN();
    BAR();
    TS(2);
    const unsigned tag = TAG;
    if (tid == 0) flag_st(ws, F_U + pw * 128, tag);
    const float Dd = bflo(Dv[d]);
    if (w == 6) conv_prep_h(hc, hp, cb, lane);
    if (w == 7) poll(ws, F_U + lane * 128, true, tag);
    BAR();
    if (prep) conv_load_u(rw, d, cb, lane, [&]() {});
    BAR();
    TS(3);
    f32x4 y[8];
    conv_main(y, cb, lane, w < 4 ? 0 : (w & 1) * 4, w < 4 ? 8 : (w & 1) * 4 + 4);
    TS(4);
    if (w < 4) {
#pragma unroll
      for (int c = 0; c < 8; c++) conv_store(y[c], cb, c, lane, Dd, rw, d);
    }
    BAR();
    if (w >= 4) {
#pragma unroll
      for (int c = 0; c < 4; c++) { const f32x4 lo = y[c], hi = y[4 + c]; *(LAS f32x4*)(size_t)(cb + ((w & 1) * 4 + c) * 1024 + lane * 16) = (w & 1) ? lo : hi; }
    }
    BAR();
    if (w >= 4) {
#pragma unroll
      for (int c = 0; c < 4; c++) {
        const f32x4 o_ = *(const LAS f32x4*)(size_t)(cb + (((w & 1) ^ 1) * 4 + c) * 1024 + lane * 16);
        const f32x4 lo = y[c], hi = y[4 + c];
        const f32x4 mine = (w & 1) ? hi : lo;
        conv_store((w & 1) ? o_ + mine : mine + o_, cb, (w & 1) * 4 + c, lane, Dd, rw, d);
      }
    }
    DRAIN();
    BAR();
    if (tid == 0) flag_st(ws, F_Y + pw * 128, tag);
    TS(5); TS(6);
  }

  {
    const unsigned tag = TAG;
    const int rt = (xcd & 3) * 8 + (r & 7), ct = (xcd >> 2) * 4 + (r >> 3);
    const int sw_ = w & 3, kk = w >> 2;
    const i32x4 sws = make_srd(ws, WS_USED);
    if (!wo_issued) {
      if (w == 0) poll(ws, F_W + lane * 128, true, tag);
      BAR();
      const unsigned vwv = OFF_WOP + ct * 102400 + lane * 16;
      for (int i = w; i < 100; i += 8) { int pc = 12 * (r & 7) + i; pc = __builtin_amdgcn_readfirstlane(pc >= 100 ? pc - 100 : pc); glds16_sc1(lb + pc * 1024, vwv, sws, pc * 1024); }
    }
    TS(18);
    TS(19);
    const int srow = rt * 64 + sw_ * 16 + fr;
    u32x2 xv[3];
#pragma unroll
    for (int t = 0; t < 3; t++) { const int tt = kk ? (t < 2 ? 3 + t : 4) : t; xv[t] = *(const u32x2*)(x + (size_t)srow * D + ct * 80 + 16 * tt + 4 * g4); }
    if (w == 0) {
      poll(ws, F_O + ((rt >> 1) * 8 + (lane & 7)) * 128, lane < 8, tag);
    } else if (w == 7) {
      poll(ws, F_Y + ((lane & 8 ? 64 : 0) + ct * 8 + (lane & 7)) * 128, lane < 16, tag);
      for (int pc = 0; pc < 10; pc++)
        glds16_sc1(lb + L_Y + pc * 1024, OFF_YT + (ct * 80 + pc * 8 + (lane >> 3)) * 4096 + rt * 128 + (lane & 7) * 16, sws, 0);
    } else if (w == 6 && b == 0) {
      poll(ws, F_U + lane * 128, true, tag); poll(ws, F_PQ + lane * 128, true, tag);
      poll(ws, F_P + lane * 128, true, tag); poll(ws, F_P + (64 + lane) * 128, true, tag);
      if (lane == 0) flag_st(ws, F_E, tag);
    }
    DRAIN();
    BAR();
    TS(7);
    const float g = __uint_as_float(__builtin_amdgcn_raw_buffer_load_b32(rw, OFF_G + srow * 4, 0, SC1));
    bf16x8 of[10];
    const int rot = ((ct & 3) * 5) >> 1;
    {
      const unsigned vo = OFF_O + (rt * 4 + sw_) * 20480 + kk * 1024 + lane * 16;
#pragma unroll
      for (int s = 0; s < 10; s++) { int q = s + rot; q = q >= 10 ? q - 10 : q; of[s] = __builtin_bit_cast(bf16x8, __builtin_amdgcn_raw_buffer_load_b128(rw, vo, q * 2048, SC1)); }
    }
    TS(8);
    f32x4 acc[5];
#pragma unroll
    for (int t = 0; t < 5; t++) acc[t] = (f32x4){0.f, 0.f, 0.f, 0.f};
    const unsigned wa0 = lb + kk * 5120 + lane * 16;
#pragma unroll
    for (int hh = 0; hh < 2; hh++) {
      bf16x8 wf[5][5];
#pragma unroll
      for (int s = 0; s < 5; s++) {
        int q = hh * 5 + s + rot; q = q >= 10 ? q - 10 : q;
        const unsigned wa = wa0 + q * 10240;
#pragma unroll
        for (int t = 0; t < 5; t++) wf[s][t] = lds_b128(wa + t * 1024);
      }
#pragma unroll
      for (int s = 0; s < 5; s++)
#pragma unroll
        for (int t = 0; t < 5; t++) acc[t] = mfma16(wf[s][t], of[hh * 5 + s], acc[t]);
      SGB(0x100, 10);
#pragma unroll
      for (int i = 0; i < 15; i++) { SGB(0x8, 1); SGB(0x100, 1); }
      SGB(0x8, 10);
      SBAR();
    }
    TS(9);
    const unsigned ra = lb + L_RED + (sw_ * 5) * 1024 + lane * 16;
    if (kk == 0) { *(LAS f32x4*)(size_t)(ra + 3 * 1024) = acc[3]; *(LAS f32x4*)(size_t)(ra + 4 * 1024) = acc[4]; }
    else { *(LAS f32x4*)(size_t)(ra) = acc[0]; *(LAS f32x4*)(size_t)(ra + 1024) = acc[1]; *(LAS f32x4*)(size_t)(ra + 2048) = acc[2]; }
    BAR();
    TS(20);
    const float omg = 1.f - g;
    const int nt = kk ? 2 : 3;
#pragma unroll
    for (int t = 0; t < 3; t++) {
      if (t < nt) {
        const int tt = kk ? 3 + t : t;
        const f32x4 a0 = acc[t], a1 = acc[3 + (t < 2 ? t : 1)];
        f32x4 a = *(const LAS f32x4*)(size_t)(ra + tt * 1024) + (kk ? a1 : a0);
        u32x2 yv = lds_tr(lb + L_Y + (16 * tt + 4 * g4 + (fr >> 2)) * 128 + (sw_ * 16 + 4 * (fr & 3)) * 2);
        float xs[4] = {bflo(xv[t].x), bfhi(xv[t].x), bflo(xv[t].y), bfhi(xv[t].y)};
        float ys[4] = {bflo(yv.x), bfhi(yv.x), bflo(yv.y), bfhi(yv.y)};
        float r_[4];
#pragma unroll
        for (int q = 0; q < 4; q++) r_[q] = fmaf(g, a[q], omg * (xs[q] * ys[q]));
        *(u32x2*)(out + (size_t)srow * D + ct * 80 + 16 * tt + 4 * g4) = (u32x2){pk2(r_[0], r_[1]), pk2(r_[2], r_[3])};
      }
    }
    TS(10); TS_OUT(21);
  }
}

extern "C" void kernel_launch(void* const* d_in, const int* in_sizes, int n_in, void* d_out, int out_size, void* d_ws, size_t ws_size,
                              hipStream_t stream) {
  const u16* x = (const u16*)d_in[0]; const u16* wqkv = (const u16*)d_in[1]; const u16* wo = (const u16*)d_in[2];
  const u16* Bk = (const u16*)d_in[4]; const u16* Bv = (const u16*)d_in[5]; const u16* shk = (const u16*)d_in[6];
  const u16* hk = (const u16*)d_in[7]; const u16* Dv = (const u16*)d_in[8]; const u16* gw = (const u16*)d_in[9];
  (void)hipFuncSetAttribute((const void*)fused, hipFuncAttributeMaxDynamicSharedMemorySize, LDS_TOTAL);
  fused<<<256, 512, LDS_TOTAL, stream>>>(x, wqkv, wo, Bk, Bv, shk, hk, Dv, gw, (u16*)d_out, (char*)d_ws);
}
```

```cpp
#include <hip/hip_runtime.h>
#include <hip/hip_bf16.h>
#include <type_traits>
#define NO_PF

typedef unsigned short u16;
typedef __bf16 bf16x2 __attribute__((ext_vector_type(2)));
typedef __bf16 bf16x8 __attribute__((ext_vector_type(8)));
typedef float f32x4 __attribute__((ext_vector_type(4)));
typedef unsigned u32x4 __attribute__((ext_vector_type(4)));
typedef unsigned u32x2 __attribute__((ext_vector_type(2)));
typedef short v4i16_t __attribute__((ext_vector_type(4)));
typedef int i32x4 __attribute__((ext_vector_type(4)));
typedef __attribute__((address_space(1))) unsigned gu32;
#define LAS __attribute__((address_space(3)))
#define SBAR() __builtin_amdgcn_sched_barrier(0)
#define SGB(mask, n) __builtin_amdgcn_sched_group_barrier(mask, n, 0)
#define BAR() asm volatile("s_waitcnt lgkmcnt(0)\n\ts_barrier" ::: "memory")
#define DRAIN() asm volatile("s_waitcnt vmcnt(0)" ::: "memory")
#define SC1 16

#define TS_DECL
#define TSARG
#define TSPAR
#define TS(i)
#define TS_OUT(n)

constexpr int S = 2048, D = 640;
constexpr unsigned SZ = S * D * 2;
constexpr unsigned F_E = 0, F_U = 128, F_PQ = F_U + 64 * 128, F_P = F_PQ + 64 * 128, F_O = F_P + 128 * 128, F_Y = F_O + 128 * 128, F_W = F_Y + 128 * 128;
constexpr unsigned OFF_PQ = 0x20000, OFF_P = OFF_PQ + 128 * 8 * 3 * 1024, OFF_UT = OFF_P + (1u << 19), OFF_G = OFF_UT + SZ, OFF_WOP = OFF_G + 8192,
                   OFF_O = OFF_WOP + (1u << 20), OFF_YT = OFF_O + SZ, WS_USED = OFF_YT + SZ;
constexpr int XS = 16384, WSB = 20480, STG = XS + WSB;
constexpr int L_EXTRA = 4 * STG;
constexpr int L_TAG = L_EXTRA, L_CNT = L_EXTRA + 16, L_T = L_EXTRA + 64;
constexpr int LDS_TOTAL = L_T + 6144 + 5120;
constexpr int CPS = 4160, UOFF = 4 * CPS, CHS = UOFF + 4608;
constexpr int L_UIMG = 5 * CHS;
constexpr int L_Y = 102400, L_RED = L_Y + 10240;

__device__ __forceinline__ unsigned pk2(float a, float b) { bf16x2 r = {(__bf16)a, (__bf16)b}; return __builtin_bit_cast(unsigned, r); }
__device__ __forceinline__ float rbf(float a) { return (float)(__bf16)a; }
__device__ __forceinline__ float bflo(unsigned v) { return __uint_as_float(v << 16); }
__device__ __forceinline__ float bfhi(unsigned v) { return __uint_as_float(v & 0xffff0000u); }
__device__ __forceinline__ float phi(float t) {
  return __builtin_amdgcn_fmed3f(t, 0.f, 3.0e38f) + (__builtin_amdgcn_fmed3f(__builtin_amdgcn_exp2f(t * 1.44269504f), 0.f, 1.f) + 1e-6f);
}
__device__ __forceinline__ i32x4 make_srd(const void* p, unsigned bytes) {
  unsigned long long a = (unsigned long long)p; i32x4 r;
  r.x = __builtin_amdgcn_readfirstlane((int)(unsigned)a); r.y = __builtin_amdgcn_readfirstlane((int)(unsigned)(a >> 32));
  r.z = __builtin_amdgcn_readfirstlane((int)bytes); r.w = 0x00020000; return r;
}
__device__ __forceinline__ void glds16(unsigned ldsaddr, unsigned voff, i32x4 srd, unsigned soff) {
  asm volatile("s_mov_b32 m0, %0\n\ts_nop 0\n\tbuffer_load_dwordx4 %1, %2, %3 offen lds" :: "s"(ldsaddr), "v"(voff), "s"(srd), "s"(soff) : "memory");
}
__device__ __forceinline__ void glds16_sc1(unsigned ldsaddr, unsigned voff, i32x4 srd, unsigned soff) {
  asm volatile("s_mov_b32 m0, %0\n\ts_nop 0\n\tbuffer_load_dwordx4 %1, %2, %3 offen sc1 lds" :: "s"(ldsaddr), "v"(voff), "s"(srd), "s"(soff) : "memory");
}
__device__ __forceinline__ void bload16_sc1(bf16x8& dst, unsigned voff, i32x4 srd, unsigned soff) {
  asm volatile("buffer_load_dwordx4 %0, %1, %2, %3 offen sc1" : "=v"(dst) : "v"(voff), "s"(srd), "s"(soff) : "memory");
}
template <int N> __device__ __forceinline__ void wait_bar() { asm volatile("s_waitcnt vmcnt(%0) lgkmcnt(0)\n\ts_barrier" :: "n"(N) : "memory"); }
__device__ __forceinline__ bf16x8 lds_b128(unsigned addr) { return *(const LAS bf16x8*)(size_t)addr; }
__device__ __forceinline__ u32x2 lds_b64(unsigned addr) { return *(const LAS u32x2*)(size_t)addr; }
__device__ __forceinline__ u32x2 lds_tr(unsigned addr) { return __builtin_bit_cast(u32x2, __builtin_amdgcn_ds_read_tr16_b64_v4i16((LAS v4i16_t*)(size_t)addr)); }
__device__ __forceinline__ bf16x8 mk8(u32x2 lo, u32x2 hi) { u32x4 t = {lo.x, lo.y, hi.x, hi.y}; return __builtin_bit_cast(bf16x8, t); }
__device__ __forceinline__ f32x4 mfma16(bf16x8 a, bf16x8 b, f32x4 c) { return __builtin_amdgcn_mfma_f32_16x16x32_bf16(a, b, c, 0, 0, 0); }

__device__ __forceinline__ u32x4 pair16(u32x2 a, u32x2 b) {
  const auto r0 = __builtin_amdgcn_permlane16_swap(a.x, b.x, false, false);
  const auto r1 = __builtin_amdgcn_permlane16_swap(a.y, b.y, false, false);
  return (u32x4){r0[0], r1[0], r0[1], r1[1]};
}
__device__ __forceinline__ void store_frags(const u32x2 (&v)[5], __amdgpu_buffer_rsrc_t rw, unsigned base, int k0, int fr, int g4) {
#pragma unroll
  for (int j = 0; j < 4; j += 2) {
    const int k = k0 + 16 * (j + (g4 & 1)) + 8 * (g4 >> 1);
    __builtin_amdgcn_raw_buffer_store_b128(pair16(v[j], v[j + 1]), rw, base + (k >> 5) * 1024 + (((k >> 3) & 3) * 16 + fr) * 16, 0, SC1);
  }
  const int k = k0 + 64 + 4 * g4;
  __builtin_amdgcn_raw_buffer_store_b64(v[4], rw, base + (k >> 5) * 1024 + (((k >> 3) & 3) * 16 + fr) * 16 + ((k >> 2) & 1) * 8, 0, SC1);
}

__device__ __forceinline__ void lds_sync(unsigned cnt_addr, int lane, unsigned n) {
  asm volatile("s_waitcnt lgkmcnt(0)" ::: "memory");
  if (lane == 0) __hip_atomic_fetch_add((LAS unsigned*)(size_t)cnt_addr, 1u, __ATOMIC_RELAXED, __HIP_MEMORY_SCOPE_WORKGROUP);
  while (__builtin_amdgcn_readfirstlane(__hip_atomic_load((LAS unsigned*)(size_t)cnt_addr, __ATOMIC_RELAXED, __HIP_MEMORY_SCOPE_WORKGROUP)) < n) {}
  asm volatile("" ::: "memory");
}
__device__ __forceinline__ bool lds_last(unsigned cnt_addr, int lane, unsigned n) {
  unsigned old = 0;
  if (lane == 0) old = __hip_atomic_fetch_add((LAS unsigned*)(size_t)cnt_addr, 1u, __ATOMIC_RELAXED, __HIP_MEMORY_SCOPE_WORKGROUP);
  return __builtin_amdgcn_readfirstlane(old) == n - 1;
}

__device__ __forceinline__ unsigned flag_ld(const char* ws, unsigned off) { return __hip_atomic_load((const gu32*)(ws + off), __ATOMIC_RELAXED, __HIP_MEMORY_SCOPE_AGENT); }
__device__ __forceinline__ void flag_st(char* ws, unsigned off, unsigned v) { __hip_atomic_store((gu32*)(ws + off), v, __ATOMIC_RELAXED, __HIP_MEMORY_SCOPE_AGENT); }
__device__ __forceinline__ void poll(const char* ws, unsigned off, bool act, unsigned tag) {
  const unsigned o0 = __builtin_amdgcn_readfirstlane(off);
  off = act ? off : o0;
  unsigned a, b, n = 1u << 17;
  asm volatile(
      "global_load_dword %0, %3, %4 sc1\n\t"
      "s_sleep 6\n"
      "1:\n\t"
      "global_load_dword %1, %3, %4 sc1\n\t"
      "s_waitcnt vmcnt(1)\n\t"
      "v_cmp_ne_u32 vcc, %0, %5\n\t"
      "s_cbranch_vccz 2f\n\t"
      "global_load_dword %0, %3, %4 sc1\n\t"
      "s_waitcnt vmcnt(1)\n\t"
      "v_cmp_ne_u32 vcc, %1, %5\n\t"
      "s_cbranch_vccz 2f\n\t"
      "s_sub_u32 %2, %2, 1\n\t"
      "s_cbranch_scc0 1b\n"
      "2:\n\t"
      "s_waitcnt vmcnt(0)"
      : "=&v"(a), "=&v"(b), "+s"(n) : "v"(off), "s"(ws), "v"(tag) : "vcc", "scc", "memory");
}

template <bool KV>
__device__ __forceinline__ void gemm_compute(f32x4 (&acc)[4][5], unsigned xa0, unsigned wa0, unsigned wa1 TSPAR) {
  bf16x8 xa[4], xb[4], wa[5], wb[5];
  auto load = [&](bf16x8 (&xf)[4], bf16x8 (&wf)[5], int buf, int kk) {
    const unsigned bo = buf * STG;
    const unsigned xa = (xa0 + bo) ^ (kk * 64);
    const unsigned a0 = wa0 + bo + kk * 10240, a1 = wa1 + bo + kk * 10240;
#pragma unroll
    for (int i = 0; i < 4; i++) xf[i] = lds_b128(xa + 2048 * i);
#pragma unroll
    for (int t = 0; t < 5; t++) { const unsigned a = ((t & 1) ? a1 : a0) + 32 * t; wf[t] = mk8(lds_tr(a), lds_tr(a + 1280)); }
  };
  auto mma = [&](const bf16x8 (&xf)[4], const bf16x8 (&wf)[5]) {
#pragma unroll
    for (int t = 0; t < 5; t++)
#pragma unroll
      for (int i = 0; i < 4; i++) acc[i][t] = KV ? mfma16(xf[i], wf[t], acc[i][t]) : mfma16(wf[t], xf[i], acc[i][t]);
  };
  auto interleave = [&]() {
#pragma unroll
    for (int t = 0; t < 14; t++) { SGB(0x8, 1); SGB(0x100, 1); }
    SGB(0x8, 6);
  };
  BAR();
  TS(21);
  load(xa, wa, 0, 0);
  SBAR();
  for (int s = 0; s < 9; s++) {
    load(xb, wb, s & 3, 1); mma(xa, wa); interleave();
    SBAR();
    BAR();
    if (s == 0) TS(22);
    if (s == 4) TS(23);
    load(xa, wa, (s + 1) & 3, 0); mma(xb, wb); interleave();
    SBAR();
  }
  load(xb, wb, 1, 1); mma(xa, wa); interleave();
  SBAR();
  mma(xb, wb);
}

__device__ __forceinline__ void gemm_loader(int L, int lane, unsigned lb, const u16* x, const u16* wqkv, int rt, int cg, bool isKV) {
  const int s0 = (((rt & 3) * 5 + 1) / 2 + (isKV ? 2 * (cg & 3) : 8 + (cg & 1))) % 10;
  const i32x4 sx = make_srd(x, S * D * 2), sw = make_srd(wqkv, D * 3 * D * 2);
  unsigned vx[2], vw[5];
#pragma unroll
  for (int i = 0; i < 2; i++) vx[i] = (rt * 128 + L * 64 + i * 8 + (lane >> 3)) * 1280 + (((lane & 7) ^ ((4 * i + (lane >> 4)) & 7)) << 4);
#pragma unroll
  for (int i = 0; i < 5; i++) {
    const int ci = i * 64 + lane, k = (ci * 3277) >> 16, pos = ci - k * 20, gc = pos ^ (((k >> 3) & 1) << 1), tl = gc >> 1;
    unsigned col;
    if (isKV) { const int isv = (tl >= 3 && tl < 5) || tl >= 7; const int tt = tl < 3 ? tl : tl < 5 ? tl - 3 : tl < 7 ? tl - 2 : tl - 5;
                col = (isv ? 2560u : 1280u) + 160u * cg + tt * 32; }
    else col = 320u * cg + tl * 32;
    vw[i] = (L * 32 + k) * 3840 + col + (gc & 1) * 16;
  }
  auto stage = [&](int st_, int buf) {
    const unsigned base = lb + buf * STG;
    int st = st_ + s0; st = st >= 10 ? st - 10 : st;
#pragma unroll
    for (int p = 0; p < 10; p++) {
      glds16(base + XS + (L * 10 + p) * 1024, vw[p % 5], sw, st * (64 * 3840) + (p / 5) * (16 * 3840));
      if (p < 8) glds16(base + (L * 8 + p) * 1024, vx[p & 1], sx, st * 128 + (p >> 1) * (16 * 1280));
    }
  };
  stage(0, 0); stage(1, 1);
  wait_bar<18>();
  stage(2, 2);
  for (int s = 0; s < 9; s++) {
    if (s < 8) wait_bar<18>(); else wait_bar<0>();
    if (s < 7) stage(s + 3, (s + 3) & 3);
  }
}

template <int T> struct StatTile {
  static constexpr bool isz = T >= 13;
  static constexpr int i = isz ? T - 13 : (T + 1) / 3;
  static constexpr int j = isz ? 0 : T - (i == 0 ? 0 : 3 * i - 1) + (i == 0 ? 0 : i - 1);
};
template <int T> __device__ __forceinline__ f32x4 stat_mma(unsigned la) {
  const u32x4 o1 = {0x3F803F80u, 0x3F803F80u, 0x3F803F80u, 0x3F803F80u};
  bf16x8 A[4], B[4];
#pragma unroll
  for (int m = 0; m < 4; m++) {
    A[m] = lds_b128(la + StatTile<T>::i * 1024 + m * 10240);
    B[m] = StatTile<T>::isz ? __builtin_bit_cast(bf16x8, o1) : lds_b128(la + (5 + StatTile<T>::j) * 1024 + m * 10240);
  }
  f32x4 d4 = {0.f, 0.f, 0.f, 0.f};
#pragma unroll
  for (int m = 0; m < 4; m++) d4 = mfma16(A[m], B[m], d4);
  return d4;
}
template <int T> __device__ __forceinline__ void stat_store(f32x4 d4, int lane, __amdgpu_buffer_rsrc_t rw, unsigned pbase) {
  const int fr = lane & 15, g4 = lane >> 4;
  const int Dg = 16 * StatTile<T>::i + 4 * g4, h = (Dg * 205) >> 12, dd = Dg - 20 * h;
  const int ee = StatTile<T>::isz ? 20 : 16 * StatTile<T>::j + fr - 20 * h;
  if (StatTile<T>::isz ? fr == 0 : (ee >= 0 && ee < 20))
    __builtin_amdgcn_raw_buffer_store_b64((u32x2){pk2(d4[0], d4[1]), pk2(d4[2], d4[3])}, rw, pbase + (h * 420 + ee * 20 + dd) * 2, 0, SC1);
}
template <int W> __device__ __forceinline__ void stat_tiles(unsigned lb, int lane, __amdgpu_buffer_rsrc_t rw, unsigned pbase) {
  const unsigned la = lb + lane * 16;
  const f32x4 a = stat_mma<W>(la), b = stat_mma<W + 8>(la);
  if constexpr (W < 2) { const f32x4 c = stat_mma<W + 16>(la); stat_store<W + 16>(c, lane, rw, pbase); }
  stat_store<W>(a, lane, rw, pbase);
  stat_store<W + 8>(b, lane, rw, pbase);
}

__device__ __forceinline__ void conv_load_h(u32x4 (&cur)[4], u32x4 (&prv)[4], const u16* __restrict__ hrow, int lane) {
#pragma unroll
  for (int it = 0; it < 4; it++) {
    const int i = it * 64 + lane;
    cur[it] = *(const u32x4*)(hrow + i * 8); prv[it] = *(const u32x4*)(hrow + (i > 0 ? i * 8 - 8 : 0));
  }
}
__device__ __forceinline__ void conv_prep_h(const u32x4 (&cur)[4], const u32x4 (&prv_)[4], unsigned cb, int lane) {
#pragma unroll
  for (int it = 0; it < 4; it++) {
    u32x4 prv = prv_[it];
    if (it == 0 && lane == 0) prv = (u32x4){0, 0, 0, 0};
    const unsigned W_[8] = {prv.x, prv.y, prv.z, prv.w, cur[it].x, cur[it].y, cur[it].z, cur[it].w};
    const int c = 255 - (it * 64 + lane);
#pragma unroll
    for (int e = 0; e < 4; e++) {
      u32x4 ov;
#pragma unroll
      for (int q = 0; q < 4; q++) {
        const int i0 = 15 - e - 2 * q, i1 = i0 - 1;
        const unsigned sel = ((i0 & 1) ? 0x0706u : 0x0504u) | (((i1 & 1) ? 0x0302u : 0x0100u) << 16);
        ov[q] = __builtin_amdgcn_perm(W_[i0 >> 1], W_[i1 >> 1], sel);
      }
      *(LAS u32x4*)(size_t)(cb + e * CPS + c * 16) = ov;
    }
  }
  if (lane < 8) *(LAS u32x4*)(size_t)(cb + (lane >> 1) * CPS + (256 + (lane & 1)) * 16) = (u32x4){0, 0, 0, 0};
  if (lane < 32) *(LAS u32x4*)(size_t)(cb + UOFF + lane * 16) = (u32x4){0, 0, 0, 0};
}
template <class F>
__device__ __forceinline__ void conv_load_u(__amdgpu_buffer_rsrc_t rw, int d, unsigned cb, int lane, F&& between) {
  u32x4 uv[4];
#pragma unroll
  for (int it = 0; it < 4; it++) uv[it] = __builtin_amdgcn_raw_buffer_load_b128(rw, OFF_UT + d * 4096 + (it * 64 + lane) * 16, 0, SC1);
  SBAR();
  between();
  SBAR();
#pragma unroll
  for (int it = 0; it < 4; it++) *(LAS u32x4*)(size_t)(cb + UOFF + 512 + (it * 64 + lane) * 16) = uv[it];
}
struct ConvFrags { u32x2 al[8], ah[8]; u32x4 B[8]; };
__device__ __forceinline__ void conv_read(ConvFrags& f, unsigned aa, unsigned ba) {
  asm volatile(
      "ds_read_b128 %16, %25\n\tds_read_b64 %7, %24 offset:3584\n\tds_read_b64 %15, %24 offset:3592\n\t"
      "ds_read_b128 %17, %25 offset:512\n\tds_read_b128 %18, %25 offset:1024\n\tds_read_b128 %19, %25 offset:1536\n\t"
      "ds_read_b128 %20, %25 offset:2048\n\tds_read_b128 %21, %25 offset:2560\n\tds_read_b128 %22, %25 offset:3072\n\t"
      "ds_read_b128 %23, %25 offset:3584\n\t"
      "ds_read_b64 %6, %24 offset:3072\n\tds_read_b64 %14, %24 offset:3080\n\t"
      "ds_read_b64 %5, %24 offset:2560\n\tds_read_b64 %13, %24 offset:2568\n\t"
      "ds_read_b64 %4, %24 offset:2048\n\tds_read_b64 %12, %24 offset:2056\n\t"
      "ds_read_b64 %3, %24 offset:1536\n\tds_read_b64 %11, %24 offset:1544\n\t"
      "ds_read_b64 %2, %24 offset:1024\n\tds_read_b64 %10, %24 offset:1032\n\t"
      "ds_read_b64 %1, %24 offset:512\n\tds_read_b64 %9, %24 offset:520\n\t"
      "ds_read_b64 %0, %24\n\tds_read_b64 %8, %24 offset:8"
      : "=&v"(f.al[0]), "=&v"(f.al[1]), "=&v"(f.al[2]), "=&v"(f.al[3]), "=&v"(f.al[4]), "=&v"(f.al[5]), "=&v"(f.al[6]), "=&v"(f.al[7]),
        "=&v"(f.ah[0]), "=&v"(f.ah[1]), "=&v"(f.ah[2]), "=&v"(f.ah[3]), "=&v"(f.ah[4]), "=&v"(f.ah[5]), "=&v"(f.ah[6]), "=&v"(f.ah[7]),
        "=&v"(f.B[0]), "=&v"(f.B[1]), "=&v"(f.B[2]), "=&v"(f.B[3]), "=&v"(f.B[4]), "=&v"(f.B[5]), "=&v"(f.B[6]), "=&v"(f.B[7])
      : "v"(aa), "v"(ba) : "memory");
}
__device__ __forceinline__ void conv_wait(ConvFrags& f) {
  asm volatile("s_waitcnt lgkmcnt(0)"
      : "+v"(f.al[0]), "+v"(f.al[1]), "+v"(f.al[2]), "+v"(f.al[3]), "+v"(f.al[4]), "+v"(f.al[5]), "+v"(f.al[6]), "+v"(f.al[7]),
        "+v"(f.ah[0]), "+v"(f.ah[1]), "+v"(f.ah[2]), "+v"(f.ah[3]), "+v"(f.ah[4]), "+v"(f.ah[5]), "+v"(f.ah[6]), "+v"(f.ah[7]),
        "+v"(f.B[0]), "+v"(f.B[1]), "+v"(f.B[2]), "+v"(f.B[3]), "+v"(f.B[4]), "+v"(f.B[5]), "+v"(f.B[6]), "+v"(f.B[7]) :: "memory");
}
template <int K> __device__ __forceinline__ void conv_read1(ConvFrags& f, unsigned aa, unsigned ba) {
  if constexpr (K < 8) asm volatile("ds_read_b128 %0, %1 offset:%2" : "=&v"(f.B[K]) : "v"(ba), "n"(512 * K) : "memory");
  else if constexpr (K < 16) asm volatile("ds_read_b64 %0, %1 offset:%2" : "=&v"(f.al[K - 8]) : "v"(aa), "n"(512 * (K - 8)) : "memory");
  else if constexpr (K < 24) asm volatile("ds_read_b64 %0, %1 offset:%2" : "=&v"(f.ah[K - 16]) : "v"(aa), "n"(512 * (K - 16) + 8) : "memory");
}
template <int K = 0> __device__ __forceinline__ void conv_step(f32x4 (&acc)[8], const ConvFrags& f, ConvFrags& nx, unsigned aa, unsigned ba) {
  if constexpr (K < 36) {
    constexpr int a = K < 8 ? 0 : K < 15 ? 1 : K < 21 ? 2 : K < 26 ? 3 : K < 30 ? 4 : K < 33 ? 5 : K < 35 ? 6 : 7;
    constexpr int i = K - (a == 0 ? 0 : a == 1 ? 8 : a == 2 ? 15 : a == 3 ? 21 : a == 4 ? 26 : a == 5 ? 30 : a == 6 ? 33 : 35);
    acc[a + i] = mfma16(mk8(f.al[7 - a], f.ah[7 - a]), __builtin_bit_cast(bf16x8, f.B[i]), acc[a + i]);
    conv_read1<K>(nx, aa, ba);
    SBAR();
    conv_step<K + 1>(acc, f, nx, aa, ba);
  }
}
__device__ __forceinline__ void conv_main(f32x4 (&acc)[8], unsigned cb, int lane, int c0, int c1) {
  const int fr = lane & 15, g4 = lane >> 4;
  const unsigned abase = cb + (3 - (fr & 3)) * CPS + 2 * (2044 - 16 * 112 - 16 * (g4 >> 1) + 8 * (g4 & 1) - (fr & ~3));
  const unsigned bbase = cb + UOFF + 512 + 2 * (16 * fr - 16 * (g4 >> 1) + 8 * (g4 & 1));
#pragma unroll
  for (int c = 0; c < 8; c++) acc[c] = (f32x4){0.f, 0.f, 0.f, 0.f};
  ConvFrags f0, f1;
  asm volatile("s_waitcnt lgkmcnt(0)" ::: "memory");
  conv_read(f0, abase - 64 * c0, bbase - 64 * c0);
  for (int cc = c0; cc < c1; cc += 2) {
    conv_wait(f0);
    SBAR();
    conv_step(acc, f0, f1, abase - 64 * (cc + 1), bbase - 64 * (cc + 1));
    conv_wait(f1);
    SBAR();
    const int cn = cc + 2 < c1 ? cc + 2 : cc + 1;
    conv_step(acc, f1, f0, abase - 64 * cn, bbase - 64 * cn);
  }
  conv_wait(f0);
}
__device__ __forceinline__ void conv_store(f32x4 y, unsigned cb, int c, int lane, float Dd, __amdgpu_buffer_rsrc_t rw, int d) {
  const int fr = lane & 15, g4 = lane >> 4;
  const u32x2 uu = lds_b64(cb + UOFF + 512 + 2 * (256 * c + 16 * fr + 4 * g4));
  const float r0 = fmaf(bflo(uu.x), Dd, y[0]), r1 = fmaf(bfhi(uu.x), Dd, y[1]);
  const float r2 = fmaf(bflo(uu.y), Dd, y[2]), r3 = fmaf(bfhi(uu.y), Dd, y[3]);
  __builtin_amdgcn_raw_buffer_store_b64((u32x2){pk2(r0, r1), pk2(r2, r3)}, rw, OFF_YT + d * 4096 + (256 * c + 16 * fr + 4 * g4) * 2, 0, SC1);
}

__global__ __launch_bounds__(512, 2) void fused(const u16* __restrict__ x, const u16* __restrict__ wqkv, const u16* __restrict__ wo,
                                                const u16* __restrict__ Bk, const u16* __restrict__ Bv, const u16* __restrict__ shk,
                                                const u16* __restrict__ hk, const u16* __restrict__ Dv, const u16* __restrict__ gw,
                                                u16* __restrict__ out, char* __restrict__ ws) {
  extern __shared__ __attribute__((aligned(16))) char sm[];
  const int tid = threadIdx.x, lane = tid & 63;
  const int w = __builtin_amdgcn_readfirstlane(tid >> 6);
  const int b = blockIdx.x, xcd = b & 7, r = b >> 3;
  const int fr = lane & 15, g4 = lane >> 4;
  const unsigned lb = (unsigned)(size_t)(LAS char*)sm;
  const __amdgpu_buffer_rsrc_t rw = __builtin_amdgcn_make_buffer_rsrc(ws, 0, WS_USED, 0x00020000);
  TS_DECL TS(0);
  unsigned e0 = 0;
#define PUT_TAG() do { if (w == 7 && lane == 0) { *(LAS unsigned*)(size_t)(lb + L_TAG) = e0 + 1u; *(LAS u32x4*)(size_t)(lb + L_CNT) = (u32x4){0u, 0u, 0u, 0u}; } } while (0)
#define TAG (*(const LAS unsigned*)(size_t)(lb + L_TAG))
  if (r < 24) {
    const bool isKV = r < 16;
    const int rt = (xcd & 3) * 4 + (r & 3);
    const int cg = isKV ? (xcd >> 2) * 4 + (r >> 2) : (xcd >> 2) * 2 + ((r - 16) >> 2);
    f32x4 acc[4][5];
    const int rp = (w >> 1) & 1, hf = w & 1;
    if (w < 4) {
      const unsigned xa0 = lb + (rp * 64 + fr) * 128 + ((g4 ^ ((fr >> 1) & 7)) << 4);
      const unsigned wl = lb + XS + (8 * g4 + (fr >> 2)) * 320 + ((lane & 3) >> 1) * 16 + (lane & 1) * 8 + hf * 160;
      const unsigned sgn = (g4 & 1) * 32;
      const unsigned wa0 = hf ? wl - sgn : wl + sgn, wa1 = hf ? wl + sgn : wl - sgn;
#pragma unroll
      for (int i = 0; i < 4; i++)
#pragma unroll
        for (int t = 0; t < 5; t++) acc[i][t] = (f32x4){0.f, 0.f, 0.f, 0.f};
      if (isKV) gemm_compute<true>(acc, xa0, wa0, wa1 TSARG); else gemm_compute<false>(acc, xa0, wa0, wa1 TSARG);
    } else if (w < 6) {
      gemm_loader(w - 4, lane, lb, x, wqkv, rt, cg, isKV);
    } else {
      const int w6 = w - 6;
      unsigned pf[5] = {0, 0, 0, 0, 0};
      SBAR();
      if (w == 7) e0 = flag_ld(ws, F_E);
      if (isKV) {
        const int l16 = lane & 15, row0 = rt * 128 + cg * 16 + w6 * 8;
        const u32x4 g0 = *(const u32x4*)(gw + lane * 8), g1 = *(const u32x4*)(gw + 512 + l16 * 8);
        u32x4 gx0[8], gx1[8];
#pragma unroll
        for (int i = 0; i < 8; i++) {
          const u16* xr = x + (size_t)(row0 + i) * D;
          gx0[i] = *(const u32x4*)(xr + lane * 8); gx1[i] = *(const u32x4*)(xr + 512 + l16 * 8);
        }
        SBAR();
        BAR(); BAR(); BAR();
        float a[8];
        const float m1 = lane < 16 ? 1.f : 0.f;
#pragma unroll
        for (int i = 0; i < 8; i++) {
          float a0 = 0.f, a1 = 0.f;
#pragma unroll
          for (int j = 0; j < 4; j++) {
            a0 = fmaf(bflo(gx0[i][j]), bflo(g0[j]), a0); a0 = fmaf(bfhi(gx0[i][j]), bfhi(g0[j]), a0);
            a1 = fmaf(bflo(gx1[i][j]), bflo(g1[j]), a1); a1 = fmaf(bfhi(gx1[i][j]), bfhi(g1[j]), a1);
          }
          a[i] = fmaf(a1, m1, a0);
          if (i == 3) { SBAR(); BAR(); SBAR(); }
        }
        SBAR(); BAR(); SBAR();
#pragma unroll
        for (int i = 0; i < 4; i++) { const float lo = a[i], hi = a[i + 4]; const bool up = lane & 4; a[i] = (up ? hi : lo) + __shfl_xor(up ? lo : hi, 4); }
#pragma unroll
        for (int i = 0; i < 2; i++) { const float lo = a[i], hi = a[i + 2]; const bool up = lane & 2; a[i] = (up ? hi : lo) + __shfl_xor(up ? lo : hi, 2); }
        { const float lo = a[0], hi = a[1]; const bool up = lane & 1; a[0] = (up ? hi : lo) + __shfl_xor(up ? lo : hi, 1); }
        a[0] += __shfl_xor(a[0], 8); a[0] += __shfl_xor(a[0], 16); a[0] += __shfl_xor(a[0], 32);
        if (lane < 8) __builtin_amdgcn_raw_buffer_store_b32(__float_as_uint(rbf(1.f / (1.f + __expf(-rbf(a[0]))))), rw, OFF_G + (row0 + lane) * 4, 0, SC1);
        PUT_TAG();
        asm volatile("" :: "v"(pf[0]), "v"(pf[1]), "v"(pf[2]), "v"(pf[3]), "v"(pf[4]));
        SBAR();
        BAR(); BAR(); BAR(); BAR(); BAR();
      } else {
        const int hw = (xcd * 8 + (r - 16)) * 2 + w6;
        const unsigned scr = lb + L_T + w6 * 6144;
#pragma unroll
        for (int bt = 0; bt < 2; bt++) {
          const int blk = bt == 0 ? hw : (hw < 32 ? 128 + hw : hw), ct = (blk * 3277) >> 16, ks = blk - 20 * ct;
          u32x4 v[5];
#pragma unroll
          for (int i = 0; i < 5; i++) {
            const int c = i * 64 + lane, row = (c * 6554) >> 16, ch = c - 10 * row;
            v[i] = *(const u32x4*)(wo + (size_t)(32 * ks + row) * D + 80 * ct + 8 * ch);
          }
          SBAR();
          BAR(); BAR();
          if (bt == 0 || hw < 32) {
#pragma unroll
            for (int i = 0; i < 5; i++) *(LAS u32x4*)(size_t)(scr + (i * 64 + lane) * 16) = v[i];
            const unsigned ta = scr + (8 * g4 + (fr >> 2)) * 160 + (lane & 3) * 8;
#pragma unroll
            for (int t = 0; t < 5; t++) {
              const u32x2 lo = lds_tr(ta + 32 * t), hi = lds_tr(ta + 32 * t + 640);
              __builtin_amdgcn_raw_buffer_store_b128((u32x4){lo.x, lo.y, hi.x, hi.y}, rw, OFF_WOP + (blk * 5 + t) * 1024 + lane * 16, 0, SC1);
            }
          }
          SBAR();
          BAR(); BAR();
        }
        PUT_TAG();
        asm volatile("" :: "v"(pf[0]), "v"(pf[1]), "v"(pf[2]), "v"(pf[3]), "v"(pf[4]));
        BAR(); BAR();
      }
    }
    TS(1);
    BAR();
    TS(11);
    const unsigned tag = TAG;
    if (isKV) {
      if (w < 4) {
#pragma unroll
        for (int t = 0; t < 5; t++) {
          const bool isk = hf ? t < 2 : t < 3;
          const int slot = hf ? (t < 2 ? 3 + t : 5 + t) : (t < 3 ? t : 2 + t);
#pragma unroll
          for (int m = 0; m < 2; m++) {
            u32x4 f;
            if (isk) f = (u32x4){pk2(phi(acc[2 * m][t][0]), phi(acc[2 * m][t][1])), pk2(phi(acc[2 * m][t][2]), phi(acc[2 * m][t][3])),
                                 pk2(phi(acc[2 * m + 1][t][0]), phi(acc[2 * m + 1][t][1])), pk2(phi(acc[2 * m + 1][t][2]), phi(acc[2 * m + 1][t][3]))};
            else f = (u32x4){pk2(acc[2 * m][t][0], acc[2 * m][t][1]), pk2(acc[2 * m][t][2], acc[2 * m][t][3]),
                             pk2(acc[2 * m + 1][t][0], acc[2 * m + 1][t][1]), pk2(acc[2 * m + 1][t][2], acc[2 * m + 1][t][3])};
            *(LAS u32x4*)(size_t)(lb + ((rp * 2 + m) * 10 + slot) * 1024 + lane * 16) = f;
          }
        }
      } else {
        for (int i = tid - 256; i < 6048 / 16; i += 256) *(LAS u32x4*)(size_t)(lb + L_T + i * 16) = (u32x4){0, 0, 0, 0};
      }
      TS(12);
      BAR();
      TS(2);
      switch (w) {
        case 0: stat_tiles<0>(lb, lane, rw, OFF_P + (rt * 8 + cg) * 3360); break;
        case 1: stat_tiles<1>(lb, lane, rw, OFF_P + (rt * 8 + cg) * 3360); break;
        case 2: stat_tiles<2>(lb, lane, rw, OFF_P + (rt * 8 + cg) * 3360); break;
        case 3: stat_tiles<3>(lb, lane, rw, OFF_P + (rt * 8 + cg) * 3360); break;
        case 4: stat_tiles<4>(lb, lane, rw, OFF_P + (rt * 8 + cg) * 3360); break;
        case 5: stat_tiles<5>(lb, lane, rw, OFF_P + (rt * 8 + cg) * 3360); break;
        case 6: stat_tiles<6>(lb, lane, rw, OFF_P + (rt * 8 + cg) * 3360); break;
        default: stat_tiles<7>(lb, lane, rw, OFF_P + (rt * 8 + cg) * 3360); break;
      }
      TS(14);
      DRAIN();
      TS(15);
      BAR();
      TS(3);
      if (w == 0) {
        if (lane == 0) flag_st(ws, F_P + (rt * 8 + cg) * 128, tag);
        poll(ws, lane < 16 ? F_P + (lane * 8 + cg) * 128 : F_PQ + (rt * 4 + (cg >> 1)) * 128, lane <= 16, tag);
      } else if (w == 7) {
        poll(ws, F_W + lane * 128, true, tag);
      }
      constexpr int PJ[9] = {0, 1, 1, 2, 2, 3, 3, 4, 4}, PK[9] = {0, 0, 1, 0, 1, 1, 2, 1, 2};
      unsigned an[9], ad[9];
#pragma unroll
      for (int p = 0; p < 9; p++) {
        const int E = 16 * PJ[p] + fr, h = (E * 205) >> 12, e = E - 20 * h;
        int o_ = 32 * PK[p] + 8 * g4 - 20 * h; o_ = o_ < -8 ? -8 : o_ > 20 ? 20 : o_;
        an[p] = lb + L_T + (h * 21 + e) * 72 + (8 + o_) * 2; ad[p] = lb + L_T + (h * 21 + 20) * 72 + (8 + o_) * 2;
      }
      BAR();
      TS(4);
      bf16x8 Bq[3];
      {
        const unsigned prow = OFF_PQ + ((rt * 8 + w) * 8 + cg) * 3072 + lane * 16;
#pragma unroll
        for (int ks = 0; ks < 3; ks++) Bq[ks] = __builtin_bit_cast(bf16x8, __builtin_amdgcn_raw_buffer_load_b128(rw, prow + 1024 * ks, 0, SC1));
      }
      if (tid < 210) {
        const unsigned pb = OFF_P + cg * 3360 + tid * 16;
        u32x4 v[16];
#pragma unroll
        for (int k = 0; k < 16; k++) v[k] = __builtin_amdgcn_raw_buffer_load_b128(rw, pb, (k ^ rt) * (8 * 3360), SC1);
        unsigned o4[4];
#pragma unroll
        for (int c = 0; c < 4; c++) {
          float lo[16], hi[16];
#pragma unroll
          for (int k = 0; k < 16; k++) { lo[k] = bflo(v[k][c]); hi[k] = bfhi(v[k][c]); }
#pragma unroll
          for (int st = 1; st < 16; st <<= 1)
#pragma unroll
            for (int k = 0; k < 16; k += 2 * st) { lo[k] += lo[k + st]; hi[k] += hi[k + st]; }
          o4[c] = pk2(lo[0], hi[0]);
        }
        TS(16);
#pragma unroll
        for (int q = 0; q < 2; q++) {
          const int i4 = 2 * tid + q, row = (i4 * 13108) >> 16, d = 4 * (i4 - row * 5);
          *(LAS u32x2*)(size_t)(lb + L_T + row * 72 + (8 + d) * 2) = (u32x2){o4[2 * q], o4[2 * q + 1]};
        }
      }
      BAR();
      TS(5);
      {
        if (g4 >= 2) Bq[2] = __builtin_bit_cast(bf16x8, (u32x4){0, 0, 0, 0});
        f32x4 nu[5], de[5];
#pragma unroll
        for (int j = 0; j < 5; j++) { nu[j] = (f32x4){0.f, 0.f, 0.f, 0.f}; de[j] = nu[j]; }
        bf16x8 An[9], Ad[9];
#pragma unroll
        for (int p = 0; p < 9; p++) {
          unsigned an8 = an[p] + 8, ad8 = ad[p] + 8;
          asm volatile("" : "+v"(an8), "+v"(ad8));
          An[p] = mk8(lds_b64(an[p]), lds_b64(an8)); Ad[p] = mk8(lds_b64(ad[p]), lds_b64(ad8));
        }
#pragma unroll
        for (int p = 0; p < 9; p++) { de[PJ[p]] = mfma16(Ad[p], Bq[PK[p]], de[PJ[p]]); nu[PJ[p]] = mfma16(An[p], Bq[PK[p]], nu[PJ[p]]); }
        u32x2 ov[5];
#pragma unroll
        for (int j = 0; j < 5; j++) {
          const float rd = __builtin_amdgcn_rcpf(de[j][0] + 1e-6f);
          ov[j] = (u32x2){pk2(nu[j][0] * rd, nu[j][1] * rd), pk2(nu[j][2] * rd, nu[j][3] * rd)};
        }
        store_frags(ov, rw, OFF_O + (rt * 8 + w) * 20480, 80 * cg, fr, g4);
      }
      TS(17);
      DRAIN();
      BAR();
      if (tid == 0) flag_st(ws, F_O + (rt * 8 + cg) * 128, tag);
      TS(6);
    } else {
      const int vb = xcd * 8 + (r - 16);
      const int d = 80 * (vb >> 3) + 48 + 4 * (vb & 7) + (w & 3);
      const unsigned cb = lb + (w & 3) * CHS;
      if (w < 4) {
#pragma unroll
        for (int i = 0; i < 4; i++) {
          u32x2 pv[5];
#pragma unroll
          for (int t = 0; t < 5; t++) pv[t] = (u32x2){pk2(phi(acc[i][t][0]), phi(acc[i][t][1])), pk2(phi(acc[i][t][2]), phi(acc[i][t][3]))};
          store_frags(pv, rw, OFF_PQ + ((rt * 8 + rp * 4 + i) * 8 + cg * 2 + hf) * 3072, 0, fr, g4);
        }
      } else {
        u32x4 hc[4], hp[4];
        conv_load_h(hc, hp, hk + (size_t)d * S, lane);
        conv_prep_h(hc, hp, cb, lane);
        if (w == 7) poll(ws, F_U + lane * 128, true, tag);
      }
      TS(12);
      DRAIN();
      BAR();
      TS(2);
      if (tid == 0) { flag_st(ws, F_PQ + (rt * 4 + cg) * 128, tag); flag_st(ws, F_W + vb * 128, tag); }
      if (w < 4) {
        const float Dd = bflo(Dv[d]);
        conv_load_u(rw, d, cb, lane, [&]() {});
        TS(3);
        f32x4 y[8];
        conv_main(y, cb, lane, 0, 8);
        TS(4);
#pragma unroll
        for (int c = 0; c < 8; c++) conv_store(y[c], cb, c, lane, Dd, rw, d);
        DRAIN();
      } else if (w == 7) {
        poll(ws, F_W + lane * 128, true, tag);
      }
      BAR();
      if (tid == 0) flag_st(ws, F_Y + (64 + vb) * 128, tag);
      TS(5); TS(6);
    }
  } else {
    const int pw = xcd * 8 + (r - 24);
    const int c6 = w < 4 ? w : 4 + ((w - 4) >> 1);
    const int d = 80 * (pw >> 3) + 6 * (pw & 7) + c6;
    const unsigned cb = lb + c6 * CHS;
    const bool prep = w < 4 || !(w & 1);
    u32x4 hc[4], hp[4];
    {
      const int rbase = xcd * 256 + ((r - 24) >> 1) * 64, cbase = ((r - 24) & 1) * 320;
      u32x4 kr[2][7], vr[2][4], sp[2][4];
#pragma unroll
      for (int q = 0; q < 2; q++) {
        const int it = w + 8 * q;
        if (q == 0 || w < 2) {
          const int ch = cbase + (it >> 1) * 64 + (lane & 7) * 8;
          const int s0 = rbase + (it & 1) * 32 + (lane >> 3) * 4;
#pragma unroll
          for (int j = 0; j < 7; j++) { int s = s0 - 3 + j; kr[q][j] = *(const u32x4*)(Bk + (size_t)(s < 0 ? 0 : s) * D + ch); }
#pragma unroll
          for (int j = 0; j < 4; j++) vr[q][j] = *(const u32x4*)(Bv + (size_t)(s0 + j) * D + ch);
#pragma unroll
          for (int j = 0; j < 4; j++) sp[q][j] = ((const u32x4*)(shk + ch * 4))[j];
        }
      }
      if (prep) conv_load_h(hc, hp, hk + (size_t)d * S, lane);
      if (w == 7) e0 = flag_ld(ws, F_E);
      SBAR();
#pragma unroll
      for (int q = 0; q < 2; q++) {
        const int it = w + 8 * q;
        if (q == 0 || w < 2) {
          const int s0 = rbase + (it & 1) * 32 + (lane >> 3) * 4;
          if (s0 == 0) { kr[q][0] = (u32x4){0, 0, 0, 0}; kr[q][1] = kr[q][0]; kr[q][2] = kr[q][0]; }
          const int rg = (it & 1) * 8 + (lane >> 3);
          const unsigned la = lb + L_UIMG + ((it >> 1) * 64 + (lane & 7) * 8) * 128 + (((rg >> 1) ^ (lane & 7)) << 4) + (rg & 1) * 8;
#pragma unroll
          for (int c = 0; c < 8; c++) {
            const unsigned s01 = sp[q][c >> 1][(c & 1) * 2], s23 = sp[q][c >> 1][(c & 1) * 2 + 1];
            const float w0 = bflo(s01), w1 = bfhi(s01), w2 = bflo(s23), w3 = bfhi(s23);
            float kk[7], u[4];
#pragma unroll
            for (int j = 0; j < 7; j++) { unsigned dd = kr[q][j][c >> 1]; kk[j] = (c & 1) ? bfhi(dd) : bflo(dd); }
#pragma unroll
            for (int j = 0; j < 4; j++) {
              unsigned dd = vr[q][j][c >> 1]; float v = (c & 1) ? bfhi(dd) : bflo(dd);
              float ks = kk[j] * w3; ks = fmaf(kk[j + 1], w2, ks); ks = fmaf(kk[j + 2], w1, ks); ks = fmaf(kk[j + 3], w0, ks);
              u[j] = ks * v;
            }
            *(LAS u32x2*)(size_t)(la + c * 128) = (u32x2){pk2(u[0], u[1]), pk2(u[2], u[3])};
          }
        }
      }
      PUT_TAG();
      BAR();
#pragma unroll
      for (int i = 0; i < 5; i++) {
        const int chl = (w * 5 + i) * 8 + (lane >> 3), k = lane & 7;
        const u32x4 v = *(const LAS u32x4*)(size_t)(lb + L_UIMG + chl * 128 + ((k ^ ((chl >> 3) & 7)) << 4));
        __builtin_amdgcn_raw_buffer_store_b128(v, rw, OFF_UT + (cbase + chl) * 4096 + rbase * 2 + k * 16, 0, SC1);
      }
      TS(1);
    }
    if (w < 5 && prep) conv_prep_h(hc, hp, cb, lane);
    DRAIN();
    BAR();
    TS(2);
    const unsigned tag = TAG;
    if (tid == 0) flag_st(ws, F_U + pw * 128, tag);
    const float Dd = bflo(Dv[d]);
    if (w == 6) conv_prep_h(hc, hp, cb, lane);
    if (w == 7) poll(ws, F_U + lane * 128, true, tag);
    BAR();
    if (prep) conv_load_u(rw, d, cb, lane, [&]() {});
    BAR();
    TS(3);
    f32x4 y[8];
    conv_main(y, cb, lane, w < 4 ? 0 : (w & 1) * 4, w < 4 ? 8 : (w & 1) * 4 + 4);
    TS(4);
    if (w < 4) {
#pragma unroll
      for (int c = 0; c < 8; c++) conv_store(y[c], cb, c, lane, Dd, rw, d);
    }
    BAR();
    if (w >= 4) {
#pragma unroll
      for (int c = 0; c < 4; c++) { const f32x4 lo = y[c], hi = y[4 + c]; *(LAS f32x4*)(size_t)(cb + ((w & 1) * 4 + c) * 1024 + lane * 16) = (w & 1) ? lo : hi; }
    }
    BAR();
    if (w >= 4) {
#pragma unroll
      for (int c = 0; c < 4; c++) {
        const f32x4 o_ = *(const LAS f32x4*)(size_t)(cb + (((w & 1) ^ 1) * 4 + c) * 1024 + lane * 16);
        const f32x4 lo = y[c], hi = y[4 + c];
        const f32x4 mine = (w & 1) ? hi : lo;
        conv_store((w & 1) ? o_ + mine : mine + o_, cb, (w & 1) * 4 + c, lane, Dd, rw, d);
      }
    }
    if (w == 0) poll(ws, F_W + lane * 128, true, tag);
    DRAIN();
    BAR();
    if (tid == 0) flag_st(ws, F_Y + pw * 128, tag);
    TS(5); TS(6);
  }

  {
    const unsigned tag = TAG;
    const int rt = (xcd & 3) * 8 + (r & 7), ct = (xcd >> 2) * 4 + (r >> 3);
    const int sw_ = w & 3, kk = w >> 2;
    const i32x4 sws = make_srd(ws, WS_USED);
    TS(18);
    const int srow = rt * 64 + sw_ * 16 + fr;
    u32x2 xv[3];
#pragma unroll
    for (int t = 0; t < 3; t++) { const int tt = kk ? (t < 2 ? 3 + t : 4) : t; xv[t] = *(const u32x2*)(x + (size_t)srow * D + ct * 80 + 16 * tt + 4 * g4); }
    SBAR();
    if (w == 0) {
      poll(ws, F_O + ((rt >> 1) * 8 + (lane & 7)) * 128, lane < 8, tag);
    } else if (w == 7) {
      poll(ws, F_Y + ((lane & 8 ? 64 : 0) + ct * 8 + (lane & 7)) * 128, lane < 16, tag);
    } else {
      const unsigned vwv = OFF_WOP + ct * 102400 + lane * 16;
      for (int i = w - 1; i < 100; i += 6) { int pc = 12 * (r & 7) + i; pc = __builtin_amdgcn_readfirstlane(pc >= 100 ? pc - 100 : pc); glds16_sc1(lb + pc * 1024, vwv, sws, pc * 1024); }
      if (w == 6 && b == 0) {
        poll(ws, F_U + lane * 128, true, tag); poll(ws, F_PQ + lane * 128, true, tag);
        poll(ws, F_P + lane * 128, true, tag); poll(ws, F_P + (64 + lane) * 128, true, tag);
        if (lane == 0) flag_st(ws, F_E, tag);
      }
      DRAIN();
    }
    BAR();
    TS(7);
    asm volatile("" :: "v"(xv[0]), "v"(xv[1]), "v"(xv[2]));
    bf16x8 of[10];
    const int rot = ((ct & 3) * 5) >> 1;
    {
      const unsigned vo = OFF_O + (rt * 4 + sw_) * 20480 + kk * 1024 + lane * 16;
#pragma unroll
      for (int s = 0; s < 10; s++) { int q = s + rot; q = __builtin_amdgcn_readfirstlane(q >= 10 ? q - 10 : q); bload16_sc1(of[s], vo, sws, q * 2048); }
    }
    if (w == 7) {
      for (int pc = 0; pc < 10; pc++)
        glds16_sc1(lb + L_Y + pc * 1024, OFF_YT + (ct * 80 + pc * 8 + (lane >> 3)) * 4096 + rt * 128 + (lane & 7) * 16, sws, 0);
    }
    float g;
    asm volatile("buffer_load_dword %0, %1, %2, 0 offen sc1" : "=v"(g) : "v"(OFF_G + srow * 4), "s"(sws) : "memory");
    TS(8);
    f32x4 acc[5];
#pragma unroll
    for (int t = 0; t < 5; t++) acc[t] = (f32x4){0.f, 0.f, 0.f, 0.f};
    const unsigned wa0 = lb + kk * 5120 + lane * 16;
#pragma unroll
    for (int hh = 0; hh < 2; hh++) {
      bf16x8 wf[5][5];
#pragma unroll
      for (int s = 0; s < 5; s++) {
        int q = hh * 5 + s + rot; q = q >= 10 ? q - 10 : q;
        const unsigned wa = wa0 + q * 10240;
#pragma unroll
        for (int t = 0; t < 5; t++) wf[s][t] = lds_b128(wa + t * 1024);
      }
      if (hh == 0) {
        if (w == 7) asm volatile("s_waitcnt vmcnt(16)" ::: "memory"); else asm volatile("s_waitcnt vmcnt(6)" ::: "memory");
        asm volatile("" : "+v"(of[0]), "+v"(of[1]), "+v"(of[2]), "+v"(of[3]), "+v"(of[4]) :: "memory");
      } else {
        if (w == 7) asm volatile("s_waitcnt vmcnt(11)" ::: "memory"); else asm volatile("s_waitcnt vmcnt(1)" ::: "memory");
        asm volatile("" : "+v"(of[5]), "+v"(of[6]), "+v"(of[7]), "+v"(of[8]), "+v"(of[9]) :: "memory");
      }
      SBAR();
#pragma unroll
      for (int s = 0; s < 5; s++)
#pragma unroll
        for (int t = 0; t < 5; t++) acc[t] = mfma16(wf[s][t], of[hh * 5 + s], acc[t]);
      SBAR();
    }
    TS(9);
    const unsigned ra = lb + L_RED + (sw_ * 5) * 1024 + lane * 16;
    if (kk == 0) { *(LAS f32x4*)(size_t)(ra + 3 * 1024) = acc[3]; *(LAS f32x4*)(size_t)(ra + 4 * 1024) = acc[4]; }
    else { *(LAS f32x4*)(size_t)(ra) = acc[0]; *(LAS f32x4*)(size_t)(ra + 1024) = acc[1]; *(LAS f32x4*)(size_t)(ra + 2048) = acc[2]; }
    asm volatile("s_waitcnt vmcnt(0)" : "+v"(g) :: "memory");
    BAR();
    TS(20);
    const float omg = 1.f - g;
    const int nt = kk ? 2 : 3;
#pragma unroll
    for (int t = 0; t < 3; t++) {
      if (t < nt) {
        const int tt = kk ? 3 + t : t;
        const f32x4 a0 = acc[t], a1 = acc[3 + (t < 2 ? t : 1)];
        f32x4 a = *(const LAS f32x4*)(size_t)(ra + tt * 1024) + (kk ? a1 : a0);
        u32x2 yv = lds_tr(lb + L_Y + (16 * tt + 4 * g4 + (fr >> 2)) * 128 + (sw_ * 16 + 4 * (fr & 3)) * 2);
        float xs[4] = {bflo(xv[t].x), bfhi(xv[t].x), bflo(xv[t].y), bfhi(xv[t].y)};
        float ys[4] = {bflo(yv.x), bfhi(yv.x), bflo(yv.y), bfhi(yv.y)};
        float r_[4];
#pragma unroll
        for (int q = 0; q < 4; q++) r_[q] = fmaf(g, a[q], omg * (xs[q] * ys[q]));
        *(u32x2*)(out + (size_t)srow * D + ct * 80 + 16 * tt + 4 * g4) = (u32x2){pk2(r_[0], r_[1]), pk2(r_[2], r_[3])};
      }
    }
    TS(10); TS_OUT(21);
  }
}

extern "C" void kernel_launch(void* const* d_in, const int* in_sizes, int n_in, void* d_out, int out_size, void* d_ws, size_t ws_size,
                              hipStream_t stream) {
  const u16* x = (const u16*)d_in[0]; const u16* wqkv = (const u16*)d_in[1]; const u16* wo = (const u16*)d_in[2];
  const u16* Bk = (const u16*)d_in[4]; const u16* Bv = (const u16*)d_in[5]; const u16* shk = (const u16*)d_in[6];
  const u16* hk = (const u16*)d_in[7]; const u16* Dv = (const u16*)d_in[8]; const u16* gw = (const u16*)d_in[9];
  (void)hipFuncSetAttribute((const void*)fused, hipFuncAttributeMaxDynamicSharedMemorySize, LDS_TOTAL);
  fused<<<256, 512, LDS_TOTAL, stream>>>(x, wqkv, wo, Bk, Bv, shk, hk, Dv, gw, (u16*)d_out, (char*)d_ws);
}
```

```cpp
#include <hip/hip_runtime.h>
#include <hip/hip_bf16.h>
#include <type_traits>
#define NO_PF

typedef unsigned short u16;
typedef __bf16 bf16x2 __attribute__((ext_vector_type(2)));
typedef __bf16 bf16x8 __attribute__((ext_vector_type(8)));
typedef float f32x4 __attribute__((ext_vector_type(4)));
typedef unsigned u32x4 __attribute__((ext_vector_type(4)));
typedef unsigned u32x2 __attribute__((ext_vector_type(2)));
typedef short v4i16_t __attribute__((ext_vector_type(4)));
typedef int i32x4 __attribute__((ext_vector_type(4)));
typedef __attribute__((address_space(1))) unsigned gu32;
#define LAS __attribute__((address_space(3)))
#define SBAR() __builtin_amdgcn_sched_barrier(0)
#define SGB(mask, n) __builtin_amdgcn_sched_group_barrier(mask, n, 0)
#define BAR() asm volatile("s_waitcnt lgkmcnt(0)\n\ts_barrier" ::: "memory")
#define DRAIN() asm volatile("s_waitcnt vmcnt(0)" ::: "memory")
#define SC1 16

#define TS_DECL unsigned long long tevt_ = 0;
#define TSARG , tevt_
#define TSPAR , unsigned long long& tevt_
#define TS(i) do { if ((i) == 0 && blockIdx.x == 0 && threadIdx.x == 0) tevt_ = __builtin_amdgcn_s_memrealtime(); } while (0)
#define TS_OUT(n) do { if (blockIdx.x == 0 && threadIdx.x == 0) { while (__builtin_amdgcn_s_memrealtime() < tevt_ + 3000ull) __builtin_amdgcn_s_sleep(1); } } while (0)

constexpr int S = 2048, D = 640;
constexpr unsigned SZ = S * D * 2;
constexpr unsigned F_E = 0, F_U = 128, F_PQ = F_U + 64 * 128, F_P = F_PQ + 64 * 128, F_O = F_P + 128 * 128, F_Y = F_O + 128 * 128, F_W = F_Y + 128 * 128;
constexpr unsigned OFF_PQ = 0x20000, OFF_P = OFF_PQ + 128 * 8 * 3 * 1024, OFF_UT = OFF_P + (1u << 19), OFF_G = OFF_UT + SZ, OFF_WOP = OFF_G + 8192,
                   OFF_O = OFF_WOP + (1u << 20), OFF_YT = OFF_O + SZ, WS_USED = OFF_YT + SZ;
constexpr int XS = 16384, WSB = 20480, STG = XS + WSB;
constexpr int L_EXTRA = 4 * STG;
constexpr int L_TAG = L_EXTRA, L_CNT = L_EXTRA + 16, L_T = L_EXTRA + 64;
constexpr int LDS_TOTAL = L_T + 6144 + 5120;
constexpr int CPS = 4160, UOFF = 4 * CPS, CHS = UOFF + 4608;
constexpr int L_UIMG = 5 * CHS;
constexpr int L_Y = 102400, L_RED = L_Y + 10240;

__device__ __forceinline__ unsigned pk2(float a, float b) { bf16x2 r = {(__bf16)a, (__bf16)b}; return __builtin_bit_cast(unsigned, r); }
__device__ __forceinline__ float rbf(float a) { return (float)(__bf16)a; }
__device__ __forceinline__ float bflo(unsigned v) { return __uint_as_float(v << 16); }
__device__ __forceinline__ float bfhi(unsigned v) { return __uint_as_float(v & 0xffff0000u); }
__device__ __forceinline__ float phi(float t) {
  return __builtin_amdgcn_fmed3f(t, 0.f, 3.0e38f) + (__builtin_amdgcn_fmed3f(__builtin_amdgcn_exp2f(t * 1.44269504f), 0.f, 1.f) + 1e-6f);
}
__device__ __forceinline__ i32x4 make_srd(const void* p, unsigned bytes) {
  unsigned long long a = (unsigned long long)p; i32x4 r;
  r.x = __builtin_amdgcn_readfirstlane((int)(unsigned)a); r.y = __builtin_amdgcn_readfirstlane((int)(unsigned)(a >> 32));
  r.z = __builtin_amdgcn_readfirstlane((int)bytes); r.w = 0x00020000; return r;
}
__device__ __forceinline__ void glds16(unsigned ldsaddr, unsigned voff, i32x4 srd, unsigned soff) {
  asm volatile("s_mov_b32 m0, %0\n\ts_nop 0\n\tbuffer_load_dwordx4 %1, %2, %3 offen lds" :: "s"(ldsaddr), "v"(voff), "s"(srd), "s"(soff) : "memory");
}
__device__ __forceinline__ void glds16_sc1(unsigned ldsaddr, unsigned voff, i32x4 srd, unsigned soff) {
  asm volatile("s_mov_b32 m0, %0\n\ts_nop 0\n\tbuffer_load_dwordx4 %1, %2, %3 offen sc1 lds" :: "s"(ldsaddr), "v"(voff), "s"(srd), "s"(soff) : "memory");
}
__device__ __forceinline__ void bload16_sc1(bf16x8& dst, unsigned voff, i32x4 srd, unsigned soff) {
  asm volatile("buffer_load_dwordx4 %0, %1, %2, %3 offen sc1" : "=v"(dst) : "v"(voff), "s"(srd), "s"(soff) : "memory");
}
template <int N> __device__ __forceinline__ void wait_bar() { asm volatile("s_waitcnt vmcnt(%0) lgkmcnt(0)\n\ts_barrier" :: "n"(N) : "memory"); }
__device__ __forceinline__ bf16x8 lds_b128(unsigned addr) { return *(const LAS bf16x8*)(size_t)addr; }
__device__ __forceinline__ u32x2 lds_b64(unsigned addr) { return *(const LAS u32x2*)(size_t)addr; }
__device__ __forceinline__ u32x2 lds_tr(unsigned addr) { return __builtin_bit_cast(u32x2, __builtin_amdgcn_ds_read_tr16_b64_v4i16((LAS v4i16_t*)(size_t)addr)); }
__device__ __forceinline__ bf16x8 mk8(u32x2 lo, u32x2 hi) { u32x4 t = {lo.x, lo.y, hi.x, hi.y}; return __builtin_bit_cast(bf16x8, t); }
__device__ __forceinline__ f32x4 mfma16(bf16x8 a, bf16x8 b, f32x4 c) { return __builtin_amdgcn_mfma_f32_16x16x32_bf16(a, b, c, 0, 0, 0); }

__device__ __forceinline__ u32x4 pair16(u32x2 a, u32x2 b) {
  const auto r0 = __builtin_amdgcn_permlane16_swap(a.x, b.x, false, false);
  const auto r1 = __builtin_amdgcn_permlane16_swap(a.y, b.y, false, false);
  return (u32x4){r0[0], r1[0], r0[1], r1[1]};
}
__device__ __forceinline__ void store_frags(const u32x2 (&v)[5], __amdgpu_buffer_rsrc_t rw, unsigned base, int k0, int fr, int g4) {
#pragma unroll
  for (int j = 0; j < 4; j += 2) {
    const int k = k0 + 16 * (j + (g4 & 1)) + 8 * (g4 >> 1);
    __builtin_amdgcn_raw_buffer_store_b128(pair16(v[j], v[j + 1]), rw, base + (k >> 5) * 1024 + (((k >> 3) & 3) * 16 + fr) * 16, 0, SC1);
  }
  const int k = k0 + 64 + 4 * g4;
  __builtin_amdgcn_raw_buffer_store_b64(v[4], rw, base + (k >> 5) * 1024 + (((k >> 3) & 3) * 16 + fr) * 16 + ((k >> 2) & 1) * 8, 0, SC1);
}

__device__ __forceinline__ void lds_sync(unsigned cnt_addr, int lane, unsigned n) {
  asm volatile("s_waitcnt lgkmcnt(0)" ::: "memory");
  if (lane == 0) __hip_atomic_fetch_add((LAS unsigned*)(size_t)cnt_addr, 1u, __ATOMIC_RELAXED, __HIP_MEMORY_SCOPE_WORKGROUP);
  while (__builtin_amdgcn_readfirstlane(__hip_atomic_load((LAS unsigned*)(size_t)cnt_addr, __ATOMIC_RELAXED, __HIP_MEMORY_SCOPE_WORKGROUP)) < n) {}
  asm volatile("" ::: "memory");
}
__device__ __forceinline__ bool lds_last(unsigned cnt_addr, int lane, unsigned n) {
  unsigned old = 0;
  if (lane == 0) old = __hip_atomic_fetch_add((LAS unsigned*)(size_t)cnt_addr, 1u, __ATOMIC_RELAXED, __HIP_MEMORY_SCOPE_WORKGROUP);
  return __builtin_amdgcn_readfirstlane(old) == n - 1;
}

__device__ __forceinline__ unsigned flag_ld(const char* ws, unsigned off) { return __hip_atomic_load((const gu32*)(ws + off), __ATOMIC_RELAXED, __HIP_MEMORY_SCOPE_AGENT); }
__device__ __forceinline__ void flag_st(char* ws, unsigned off, unsigned v) { __hip_atomic_store((gu32*)(ws + off), v, __ATOMIC_RELAXED, __HIP_MEMORY_SCOPE_AGENT); }
__device__ __forceinline__ void poll(const char* ws, unsigned off, bool act, unsigned tag) {
  const unsigned o0 = __builtin_amdgcn_readfirstlane(off);
  off = act ? off : o0;
  unsigned a, b, n = 1u << 17;
  asm volatile(
      "global_load_dword %0, %3, %4 sc1\n\t"
      "s_sleep 6\n"
      "1:\n\t"
      "global_load_dword %1, %3, %4 sc1\n\t"
      "s_waitcnt vmcnt(1)\n\t"
      "v_cmp_ne_u32 vcc, %0, %5\n\t"
      "s_cbranch_vccz 2f\n\t"
      "global_load_dword %0, %3, %4 sc1\n\t"
      "s_waitcnt vmcnt(1)\n\t"
      "v_cmp_ne_u32 vcc, %1, %5\n\t"
      "s_cbranch_vccz 2f\n\t"
      "s_sub_u32 %2, %2, 1\n\t"
      "s_cbranch_scc0 1b\n"
      "2:\n\t"
      "s_waitcnt vmcnt(0)"
      : "=&v"(a), "=&v"(b), "+s"(n) : "v"(off), "s"(ws), "v"(tag) : "vcc", "scc", "memory");
}

template <bool KV>
__device__ __forceinline__ void gemm_compute(f32x4 (&acc)[4][5], unsigned xa0, unsigned wa0, unsigned wa1 TSPAR) {
  bf16x8 xa[4], xb[4], wa[5], wb[5];
  auto load = [&](bf16x8 (&xf)[4], bf16x8 (&wf)[5], int buf, int kk) {
    const unsigned bo = buf * STG;
    const unsigned xa = (xa0 + bo) ^ (kk * 64);
    const unsigned a0 = wa0 + bo + kk * 10240, a1 = wa1 + bo + kk * 10240;
#pragma unroll
    for (int i = 0; i < 4; i++) xf[i] = lds_b128(xa + 2048 * i);
#pragma unroll
    for (int t = 0; t < 5; t++) { const unsigned a = ((t & 1) ? a1 : a0) + 32 * t; wf[t] = mk8(lds_tr(a), lds_tr(a + 1280)); }
  };
  auto mma = [&](const bf16x8 (&xf)[4], const bf16x8 (&wf)[5]) {
#pragma unroll
    for (int t = 0; t < 5; t++)
#pragma unroll
      for (int i = 0; i < 4; i++) acc[i][t] = KV ? mfma16(xf[i], wf[t], acc[i][t]) : mfma16(wf[t], xf[i], acc[i][t]);
  };
  auto interleave = [&]() {
#pragma unroll
    for (int t = 0; t < 14; t++) { SGB(0x8, 1); SGB(0x100, 1); }
    SGB(0x8, 6);
  };
  BAR();
  TS(21);
  load(xa, wa, 0, 0);
  SBAR();
  for (int s = 0; s < 9; s++) {
    load(xb, wb, s & 3, 1); mma(xa, wa); interleave();
    SBAR();
    BAR();
    if (s == 0) TS(22);
    if (s == 4) TS(23);
    load(xa, wa, (s + 1) & 3, 0); mma(xb, wb); interleave();
    SBAR();
  }
  load(xb, wb, 1, 1); mma(xa, wa); interleave();
  SBAR();
  mma(xb, wb);
}

__device__ __forceinline__ void gemm_loader(int L, int lane, unsigned lb, const u16* x, const u16* wqkv, int rt, int cg, bool isKV) {
  const int s0 = (((rt & 3) * 5 + 1) / 2 + (isKV ? 2 * (cg & 3) : 8 + (cg & 1))) % 10;
  const i32x4 sx = make_srd(x, S * D * 2), sw = make_srd(wqkv, D * 3 * D * 2);
  unsigned vx[2], vw[5];
#pragma unroll
  for (int i = 0; i < 2; i++) vx[i] = (rt * 128 + L * 64 + i * 8 + (lane >> 3)) * 1280 + (((lane & 7) ^ ((4 * i + (lane >> 4)) & 7)) << 4);
#pragma unroll
  for (int i = 0; i < 5; i++) {
    const int ci = i * 64 + lane, k = (ci * 3277) >> 16, pos = ci - k * 20, gc = pos ^ (((k >> 3) & 1) << 1), tl = gc >> 1;
    unsigned col;
    if (isKV) { const int isv = (tl >= 3 && tl < 5) || tl >= 7; const int tt = tl < 3 ? tl : tl < 5 ? tl - 3 : tl < 7 ? tl - 2 : tl - 5;
                col = (isv ? 2560u : 1280u) + 160u * cg + tt * 32; }
    else col = 320u * cg + tl * 32;
    vw[i] = (L * 32 + k) * 3840 + col + (gc & 1) * 16;
  }
  auto stage = [&](int st_, int buf) {
    const unsigned base = lb + buf * STG;
    int st = st_ + s0; st = st >= 10 ? st - 10 : st;
#pragma unroll
    for (int p = 0; p < 10; p++) {
      glds16(base + XS + (L * 10 + p) * 1024, vw[p % 5], sw, st * (64 * 3840) + (p / 5) * (16 * 3840));
      if (p < 8) glds16(base + (L * 8 + p) * 1024, vx[p & 1], sx, st * 128 + (p >> 1) * (16 * 1280));
    }
  };
  stage(0, 0); stage(1, 1);
  wait_bar<18>();
  stage(2, 2);
  for (int s = 0; s < 9; s++) {
    if (s < 8) wait_bar<18>(); else wait_bar<0>();
    if (s < 7) stage(s + 3, (s + 3) & 3);
  }
}

template <int T> struct StatTile {
  static constexpr bool isz = T >= 13;
  static constexpr int i = isz ? T - 13 : (T + 1) / 3;
  static constexpr int j = isz ? 0 : T - (i == 0 ? 0 : 3 * i - 1) + (i == 0 ? 0 : i - 1);
};
template <int T> __device__ __forceinline__ f32x4 stat_mma(unsigned la) {
  const u32x4 o1 = {0x3F803F80u, 0x3F803F80u, 0x3F803F80u, 0x3F803F80u};
  bf16x8 A[4], B[4];
#pragma unroll
  for (int m = 0; m < 4; m++) {
    A[m] = lds_b128(la + StatTile<T>::i * 1024 + m * 10240);
    B[m] = StatTile<T>::isz ? __builtin_bit_cast(bf16x8, o1) : lds_b128(la + (5 + StatTile<T>::j) * 1024 + m * 10240);
  }
  f32x4 d4 = {0.f, 0.f, 0.f, 0.f};
#pragma unroll
  for (int m = 0; m < 4; m++) d4 = mfma16(A[m], B[m], d4);
  return d4;
}
template <int T> __device__ __forceinline__ void stat_store(f32x4 d4, int lane, __amdgpu_buffer_rsrc_t rw, unsigned pbase) {
  const int fr = lane & 15, g4 = lane >> 4;
  const int Dg = 16 * StatTile<T>::i + 4 * g4, h = (Dg * 205) >> 12, dd = Dg - 20 * h;
  const int ee = StatTile<T>::isz ? 20 : 16 * StatTile<T>::j + fr - 20 * h;
  if (StatTile<T>::isz ? fr == 0 : (ee >= 0 && ee < 20))
    __builtin_amdgcn_raw_buffer_store_b64((u32x2){pk2(d4[0], d4[1]), pk2(d4[2], d4[3])}, rw, pbase + (h * 420 + ee * 20 + dd) * 2, 0, SC1);
}
template <int W> __device__ __forceinline__ void stat_tiles(unsigned lb, int lane, __amdgpu_buffer_rsrc_t rw, unsigned pbase) {
  const unsigned la = lb + lane * 16;
  const f32x4 a = stat_mma<W>(la), b = stat_mma<W + 8>(la);
  if constexpr (W < 2) { const f32x4 c = stat_mma<W + 16>(la); stat_store<W + 16>(c, lane, rw, pbase); }
  stat_store<W>(a, lane, rw, pbase);
  stat_store<W + 8>(b, lane, rw, pbase);
}

__device__ __forceinline__ void conv_load_h(u32x4 (&cur)[4], u32x4 (&prv)[4], const u16* __restrict__ hrow, int lane) {
#pragma unroll
  for (int it = 0; it < 4; it++) {
    const int i = it * 64 + lane;
    cur[it] = *(const u32x4*)(hrow + i * 8); prv[it] = *(const u32x4*)(hrow + (i > 0 ? i * 8 - 8 : 0));
  }
}
__device__ __forceinline__ void conv_prep_h(const u32x4 (&cur)[4], const u32x4 (&prv_)[4], unsigned cb, int lane) {
#pragma unroll
  for (int it = 0; it < 4; it++) {
    u32x4 prv = prv_[it];
    if (it == 0 && lane == 0) prv = (u32x4){0, 0, 0, 0};
    const unsigned W_[8] = {prv.x, prv.y, prv.z, prv.w, cur[it].x, cur[it].y, cur[it].z, cur[it].w};
    const int c = 255 - (it * 64 + lane);
#pragma unroll
    for (int e = 0; e < 4; e++) {
      u32x4 ov;
#pragma unroll
      for (int q = 0; q < 4; q++) {
        const int i0 = 15 - e - 2 * q, i1 = i0 - 1;
        const unsigned sel = ((i0 & 1) ? 0x0706u : 0x0504u) | (((i1 & 1) ? 0x0302u : 0x0100u) << 16);
        ov[q] = __builtin_amdgcn_perm(W_[i0 >> 1], W_[i1 >> 1], sel);
      }
      *(LAS u32x4*)(size_t)(cb + e * CPS + c * 16) = ov;
    }
  }
  if (lane < 8) *(LAS u32x4*)(size_t)(cb + (lane >> 1) * CPS + (256 + (lane & 1)) * 16) = (u32x4){0, 0, 0, 0};
  if (lane < 32) *(LAS u32x4*)(size_t)(cb + UOFF + lane * 16) = (u32x4){0, 0, 0, 0};
}
template <class F>
__device__ __forceinline__ void conv_load_u(__amdgpu_buffer_rsrc_t rw, int d, unsigned cb, int lane, F&& between) {
  u32x4 uv[4];
#pragma unroll
  for (int it = 0; it < 4; it++) uv[it] = __builtin_amdgcn_raw_buffer_load_b128(rw, OFF_UT + d * 4096 + (it * 64 + lane) * 16, 0, SC1);
  SBAR();
  between();
  SBAR();
#pragma unroll
  for (int it = 0; it < 4; it++) *(LAS u32x4*)(size_t)(cb + UOFF + 512 + (it * 64 + lane) * 16) = uv[it];
}
struct ConvFrags { u32x2 al[8], ah[8]; u32x4 B[8]; };
__device__ __forceinline__ void conv_read(ConvFrags& f, unsigned aa, unsigned ba) {
  asm volatile(
      "ds_read_b128 %16, %25\n\tds_read_b64 %7, %24 offset:3584\n\tds_read_b64 %15, %24 offset:3592\n\t"
      "ds_read_b128 %17, %25 offset:512\n\tds_read_b128 %18, %25 offset:1024\n\tds_read_b128 %19, %25 offset:1536\n\t"
      "ds_read_b128 %20, %25 offset:2048\n\tds_read_b128 %21, %25 offset:2560\n\tds_read_b128 %22, %25 offset:3072\n\t"
      "ds_read_b128 %23, %25 offset:3584\n\t"
      "ds_read_b64 %6, %24 offset:3072\n\tds_read_b64 %14, %24 offset:3080\n\t"
      "ds_read_b64 %5, %24 offset:2560\n\tds_read_b64 %13, %24 offset:2568\n\t"
      "ds_read_b64 %4, %24 offset:2048\n\tds_read_b64 %12, %24 offset:2056\n\t"
      "ds_read_b64 %3, %24 offset:1536\n\tds_read_b64 %11, %24 offset:1544\n\t"
      "ds_read_b64 %2, %24 offset:1024\n\tds_read_b64 %10, %24 offset:1032\n\t"
      "ds_read_b64 %1, %24 offset:512\n\tds_read_b64 %9, %24 offset:520\n\t"
      "ds_read_b64 %0, %24\n\tds_read_b64 %8, %24 offset:8"
      : "=&v"(f.al[0]), "=&v"(f.al[1]), "=&v"(f.al[2]), "=&v"(f.al[3]), "=&v"(f.al[4]), "=&v"(f.al[5]), "=&v"(f.al[6]), "=&v"(f.al[7]),
        "=&v"(f.ah[0]), "=&v"(f.ah[1]), "=&v"(f.ah[2]), "=&v"(f.ah[3]), "=&v"(f.ah[4]), "=&v"(f.ah[5]), "=&v"(f.ah[6]), "=&v"(f.ah[7]),
        "=&v"(f.B[0]), "=&v"(f.B[1]), "=&v"(f.B[2]), "=&v"(f.B[3]), "=&v"(f.B[4]), "=&v"(f.B[5]), "=&v"(f.B[6]), "=&v"(f.B[7])
      : "v"(aa), "v"(ba) : "memory");
}
__device__ __forceinline__ void conv_wait(ConvFrags& f) {
  asm volatile("s_waitcnt lgkmcnt(0)"
      : "+v"(f.al[0]), "+v"(f.al[1]), "+v"(f.al[2]), "+v"(f.al[3]), "+v"(f.al[4]), "+v"(f.al[5]), "+v"(f.al[6]), "+v"(f.al[7]),
        "+v"(f.ah[0]), "+v"(f.ah[1]), "+v"(f.ah[2]), "+v"(f.ah[3]), "+v"(f.ah[4]), "+v"(f.ah[5]), "+v"(f.ah[6]), "+v"(f.ah[7]),
        "+v"(f.B[0]), "+v"(f.B[1]), "+v"(f.B[2]), "+v"(f.B[3]), "+v"(f.B[4]), "+v"(f.B[5]), "+v"(f.B[6]), "+v"(f.B[7]) :: "memory");
}
template <int K> __device__ __forceinline__ void conv_read1(ConvFrags& f, unsigned aa, unsigned ba) {
  if constexpr (K < 8) asm volatile("ds_read_b128 %0, %1 offset:%2" : "=&v"(f.B[K]) : "v"(ba), "n"(512 * K) : "memory");
  else if constexpr (K < 16) asm volatile("ds_read_b64 %0, %1 offset:%2" : "=&v"(f.al[K - 8]) : "v"(aa), "n"(512 * (K - 8)) : "memory");
  else if constexpr (K < 24) asm volatile("ds_read_b64 %0, %1 offset:%2" : "=&v"(f.ah[K - 16]) : "v"(aa), "n"(512 * (K - 16) + 8) : "memory");
}
template <int K = 0> __device__ __forceinline__ void conv_step(f32x4 (&acc)[8], const ConvFrags& f, ConvFrags& nx, unsigned aa, unsigned ba) {
  if constexpr (K < 36) {
    constexpr int a = K < 8 ? 0 : K < 15 ? 1 : K < 21 ? 2 : K < 26 ? 3 : K < 30 ? 4 : K < 33 ? 5 : K < 35 ? 6 : 7;
    constexpr int i = K - (a == 0 ? 0 : a == 1 ? 8 : a == 2 ? 15 : a == 3 ? 21 : a == 4 ? 26 : a == 5 ? 30 : a == 6 ? 33 : 35);
    acc[a + i] = mfma16(mk8(f.al[7 - a], f.ah[7 - a]), __builtin_bit_cast(bf16x8, f.B[i]), acc[a + i]);
    conv_read1<K>(nx, aa, ba);
    SBAR();
    conv_step<K + 1>(acc, f, nx, aa, ba);
  }
}
__device__ __forceinline__ void conv_main(f32x4 (&acc)[8], unsigned cb, int lane, int c0, int c1) {
  const int fr = lane & 15, g4 = lane >> 4;
  const unsigned abase = cb + (3 - (fr & 3)) * CPS + 2 * (2044 - 16 * 112 - 16 * (g4 >> 1) + 8 * (g4 & 1) - (fr & ~3));
  const unsigned bbase = cb + UOFF + 512 + 2 * (16 * fr - 16 * (g4 >> 1) + 8 * (g4 & 1));
#pragma unroll
  for (int c = 0; c < 8; c++) acc[c] = (f32x4){0.f, 0.f, 0.f, 0.f};
  ConvFrags f0, f1;
  asm volatile("s_waitcnt lgkmcnt(0)" ::: "memory");
  conv_read(f0, abase - 64 * c0, bbase - 64 * c0);
  for (int cc = c0; cc < c1; cc += 2) {
    conv_wait(f0);
    SBAR();
    conv_step(acc, f0, f1, abase - 64 * (cc + 1), bbase - 64 * (cc + 1));
    conv_wait(f1);
    SBAR();
    const int cn = cc + 2 < c1 ? cc + 2 : cc + 1;
    conv_step(acc, f1, f0, abase - 64 * cn, bbase - 64 * cn);
  }
  conv_wait(f0);
}
__device__ __forceinline__ void conv_store(f32x4 y, unsigned cb, int c, int lane, float Dd, __amdgpu_buffer_rsrc_t rw, int d) {
  const int fr = lane & 15, g4 = lane >> 4;
  const u32x2 uu = lds_b64(cb + UOFF + 512 + 2 * (256 * c + 16 * fr + 4 * g4));
  const float r0 = fmaf(bflo(uu.x), Dd, y[0]), r1 = fmaf(bfhi(uu.x), Dd, y[1]);
  const float r2 = fmaf(bflo(uu.y), Dd, y[2]), r3 = fmaf(bfhi(uu.y), Dd, y[3]);
  __builtin_amdgcn_raw_buffer_store_b64((u32x2){pk2(r0, r1), pk2(r2, r3)}, rw, OFF_YT + d * 4096 + (256 * c + 16 * fr + 4 * g4) * 2, 0, SC1);
}

__global__ __launch_bounds__(512, 2) void fused(const u16* __restrict__ x, const u16* __restrict__ wqkv, const u16* __restrict__ wo,
                                                const u16* __restrict__ Bk, const u16* __restrict__ Bv, const u16* __restrict__ shk,
                                                const u16* __restrict__ hk, const u16* __restrict__ Dv, const u16* __restrict__ gw,
                                                u16* __restrict__ out, char* __restrict__ ws) {
  extern __shared__ __attribute__((aligned(16))) char sm[];
  const int tid = threadIdx.x, lane = tid & 63;
  const int w = __builtin_amdgcn_readfirstlane(tid >> 6);
  const int b = blockIdx.x, xcd = b & 7, r = b >> 3;
  const int fr = lane & 15, g4 = lane >> 4;
  const unsigned lb = (unsigned)(size_t)(LAS char*)sm;
  const __amdgpu_buffer_rsrc_t rw = __builtin_amdgcn_make_buffer_rsrc(ws, 0, WS_USED, 0x00020000);
  TS_DECL TS(0);
  unsigned e0 = 0;
#define PUT_TAG() do { if (w == 7 && lane == 0) { *(LAS unsigned*)(size_t)(lb + L_TAG) = e0 + 1u; *(LAS u32x4*)(size_t)(lb + L_CNT) = (u32x4){0u, 0u, 0u, 0u}; } } while (0)
#define TAG (*(const LAS unsigned*)(size_t)(lb + L_TAG))
  if (r < 24) {
    const bool isKV = r < 16;
    const int rt = (xcd & 3) * 4 + (r & 3);
    const int cg = isKV ? (xcd >> 2) * 4 + (r >> 2) : (xcd >> 2) * 2 + ((r - 16) >> 2);
    f32x4 acc[4][5];
    const int rp = (w >> 1) & 1, hf = w & 1;
    if (w < 4) {
      const unsigned xa0 = lb + (rp * 64 + fr) * 128 + ((g4 ^ ((fr >> 1) & 7)) << 4);
      const unsigned wl = lb + XS + (8 * g4 + (fr >> 2)) * 320 + ((lane & 3) >> 1) * 16 + (lane & 1) * 8 + hf * 160;
      const unsigned sgn = (g4 & 1) * 32;
      const unsigned wa0 = hf ? wl - sgn : wl + sgn, wa1 = hf ? wl + sgn : wl - sgn;
#pragma unroll
      for (int i = 0; i < 4; i++)
#pragma unroll
        for (int t = 0; t < 5; t++) acc[i][t] = (f32x4){0.f, 0.f, 0.f, 0.f};
      if (isKV) gemm_compute<true>(acc, xa0, wa0, wa1 TSARG); else gemm_compute<false>(acc, xa0, wa0, wa1 TSARG);
    } else if (w < 6) {
      gemm_loader(w - 4, lane, lb, x, wqkv, rt, cg, isKV);
    } else {
      const int w6 = w - 6;
      unsigned pf[5] = {0, 0, 0, 0, 0};
      SBAR();
      if (w == 7) e0 = flag_ld(ws, F_E);
      if (isKV) {
        const int l16 = lane & 15, row0 = rt * 128 + cg * 16 + w6 * 8;
        const u32x4 g0 = *(const u32x4*)(gw + lane * 8), g1 = *(const u32x4*)(gw + 512 + l16 * 8);
        u32x4 gx0[8], gx1[8];
#pragma unroll
        for (int i = 0; i < 8; i++) {
          const u16* xr = x + (size_t)(row0 + i) * D;
          gx0[i] = *(const u32x4*)(xr + lane * 8); gx1[i] = *(const u32x4*)(xr + 512 + l16 * 8);
        }
        SBAR();
        BAR(); BAR(); BAR();
        float a[8];
        const float m1 = lane < 16 ? 1.f : 0.f;
#pragma unroll
        for (int i = 0; i < 8; i++) {
          float a0 = 0.f, a1 = 0.f;
#pragma unroll
          for (int j = 0; j < 4; j++) {
            a0 = fmaf(bflo(gx0[i][j]), bflo(g0[j]), a0); a0 = fmaf(bfhi(gx0[i][j]), bfhi(g0[j]), a0);
            a1 = fmaf(bflo(gx1[i][j]), bflo(g1[j]), a1); a1 = fmaf(bfhi(gx1[i][j]), bfhi(g1[j]), a1);
          }
          a[i] = fmaf(a1, m1, a0);
          if (i == 3) { SBAR(); BAR(); SBAR(); }
        }
        SBAR(); BAR(); SBAR();
#pragma unroll
        for (int i = 0; i < 4; i++) { const float lo = a[i], hi = a[i + 4]; const bool up = lane & 4; a[i] = (up ? hi : lo) + __shfl_xor(up ? lo : hi, 4); }
#pragma unroll
        for (int i = 0; i < 2; i++) { const float lo = a[i], hi = a[i + 2]; const bool up = lane & 2; a[i] = (up ? hi : lo) + __shfl_xor(up ? lo : hi, 2); }
        { const float lo = a[0], hi = a[1]; const bool up = lane & 1; a[0] = (up ? hi : lo) + __shfl_xor(up ? lo : hi, 1); }
        a[0] += __shfl_xor(a[0], 8); a[0] += __shfl_xor(a[0], 16); a[0] += __shfl_xor(a[0], 32);
        if (lane < 8) __builtin_amdgcn_raw_buffer_store_b32(__float_as_uint(rbf(1.f / (1.f + __expf(-rbf(a[0]))))), rw, OFF_G + (row0 + lane) * 4, 0, SC1);
        PUT_TAG();
        asm volatile("" :: "v"(pf[0]), "v"(pf[1]), "v"(pf[2]), "v"(pf[3]), "v"(pf[4]));
        SBAR();
        BAR(); BAR(); BAR(); BAR(); BAR();
      } else {
        const int hw = (xcd * 8 + (r - 16)) * 2 + w6;
        const unsigned scr = lb + L_T + w6 * 6144;
#pragma unroll
        for (int bt = 0; bt < 2; bt++) {
          const int blk = bt == 0 ? hw : (hw < 32 ? 128 + hw : hw), ct = (blk * 3277) >> 16, ks = blk - 20 * ct;
          u32x4 v[5];
#pragma unroll
          for (int i = 0; i < 5; i++) {
            const int c = i * 64 + lane, row = (c * 6554) >> 16, ch = c - 10 * row;
            v[i] = *(const u32x4*)(wo + (size_t)(32 * ks + row) * D + 80 * ct + 8 * ch);
          }
          SBAR();
          BAR(); BAR();
          if (bt == 0 || hw < 32) {
#pragma unroll
            for (int i = 0; i < 5; i++) *(LAS u32x4*)(size_t)(scr + (i * 64 + lane) * 16) = v[i];
            const unsigned ta = scr + (8 * g4 + (fr >> 2)) * 160 + (lane & 3) * 8;
#pragma unroll
            for (int t = 0; t < 5; t++) {
              const u32x2 lo = lds_tr(ta + 32 * t), hi = lds_tr(ta + 32 * t + 640);
              __builtin_amdgcn_raw_buffer_store_b128((u32x4){lo.x, lo.y, hi.x, hi.y}, rw, OFF_WOP + (blk * 5 + t) * 1024 + lane * 16, 0, SC1);
            }
          }
          SBAR();
          BAR(); BAR();
        }
        PUT_TAG();
        asm volatile("" :: "v"(pf[0]), "v"(pf[1]), "v"(pf[2]), "v"(pf[3]), "v"(pf[4]));
        BAR(); BAR();
      }
    }
    TS(1);
    BAR();
    TS(11);
    const unsigned tag = TAG;
    if (isKV) {
      if (w < 4) {
#pragma unroll
        for (int t = 0; t < 5; t++) {
          const bool isk = hf ? t < 2 : t < 3;
          const int slot = hf ? (t < 2 ? 3 + t : 5 + t) : (t < 3 ? t : 2 + t);
#pragma unroll
          for (int m = 0; m < 2; m++) {
            u32x4 f;
            if (isk) f = (u32x4){pk2(phi(acc[2 * m][t][0]), phi(acc[2 * m][t][1])), pk2(phi(acc[2 * m][t][2]), phi(acc[2 * m][t][3])),
                                 pk2(phi(acc[2 * m + 1][t][0]), phi(acc[2 * m + 1][t][1])), pk2(phi(acc[2 * m + 1][t][2]), phi(acc[2 * m + 1][t][3]))};
            else f = (u32x4){pk2(acc[2 * m][t][0], acc[2 * m][t][1]), pk2(acc[2 * m][t][2], acc[2 * m][t][3]),
                             pk2(acc[2 * m + 1][t][0], acc[2 * m + 1][t][1]), pk2(acc[2 * m + 1][t][2], acc[2 * m + 1][t][3])};
            *(LAS u32x4*)(size_t)(lb + ((rp * 2 + m) * 10 + slot) * 1024 + lane * 16) = f;
          }
        }
      } else {
        for (int i = tid - 256; i < 6048 / 16; i += 256) *(LAS u32x4*)(size_t)(lb + L_T + i * 16) = (u32x4){0, 0, 0, 0};
      }
      TS(12);
      BAR();
      TS(2);
      switch (w) {
        case 0: stat_tiles<0>(lb, lane, rw, OFF_P + (rt * 8 + cg) * 3360); break;
        case 1: stat_tiles<1>(lb, lane, rw, OFF_P + (rt * 8 + cg) * 3360); break;
        case 2: stat_tiles<2>(lb, lane, rw, OFF_P + (rt * 8 + cg) * 3360); break;
        case 3: stat_tiles<3>(lb, lane, rw, OFF_P + (rt * 8 + cg) * 3360); break;
        case 4: stat_tiles<4>(lb, lane, rw, OFF_P + (rt * 8 + cg) * 3360); break;
        case 5: stat_tiles<5>(lb, lane, rw, OFF_P + (rt * 8 + cg) * 3360); break;
        case 6: stat_tiles<6>(lb, lane, rw, OFF_P + (rt * 8 + cg) * 3360); break;
        default: stat_tiles<7>(lb, lane, rw, OFF_P + (rt * 8 + cg) * 3360); break;
      }
      TS(14);
      DRAIN();
      TS(15);
      BAR();
      TS(3);
      if (w == 0) {
        if (lane == 0) flag_st(ws, F_P + (rt * 8 + cg) * 128, tag);
        poll(ws, lane < 16 ? F_P + (lane * 8 + cg) * 128 : F_PQ + (rt * 4 + (cg >> 1)) * 128, lane <= 16, tag);
      } else if (w == 7) {
        poll(ws, F_W + lane * 128, true, tag);
      }
      constexpr int PJ[9] = {0, 1, 1, 2, 2, 3, 3, 4, 4}, PK[9] = {0, 0, 1, 0, 1, 1, 2, 1, 2};
      unsigned an[9], ad[9];
#pragma unroll
      for (int p = 0; p < 9; p++) {
        const int E = 16 * PJ[p] + fr, h = (E * 205) >> 12, e = E - 20 * h;
        int o_ = 32 * PK[p] + 8 * g4 - 20 * h; o_ = o_ < -8 ? -8 : o_ > 20 ? 20 : o_;
        an[p] = lb + L_T + (h * 21 + e) * 72 + (8 + o_) * 2; ad[p] = lb + L_T + (h * 21 + 20) * 72 + (8 + o_) * 2;
      }
      BAR();
      TS(4);
      bf16x8 Bq[3];
      {
        const unsigned prow = OFF_PQ + ((rt * 8 + w) * 8 + cg) * 3072 + lane * 16;
#pragma unroll
        for (int ks = 0; ks < 3; ks++) Bq[ks] = __builtin_bit_cast(bf16x8, __builtin_amdgcn_raw_buffer_load_b128(rw, prow + 1024 * ks, 0, SC1));
      }
      if (tid < 210) {
        const unsigned pb = OFF_P + cg * 3360 + tid * 16;
        u32x4 v[16];
#pragma unroll
        for (int k = 0; k < 16; k++) v[k] = __builtin_amdgcn_raw_buffer_load_b128(rw, pb, (k ^ rt) * (8 * 3360), SC1);
        unsigned o4[4];
#pragma unroll
        for (int c = 0; c < 4; c++) {
          float lo[16], hi[16];
#pragma unroll
          for (int k = 0; k < 16; k++) { lo[k] = bflo(v[k][c]); hi[k] = bfhi(v[k][c]); }
#pragma unroll
          for (int st = 1; st < 16; st <<= 1)
#pragma unroll
            for (int k = 0; k < 16; k += 2 * st) { lo[k] += lo[k + st]; hi[k] += hi[k + st]; }
          o4[c] = pk2(lo[0], hi[0]);
        }
        TS(16);
#pragma unroll
        for (int q = 0; q < 2; q++) {
          const int i4 = 2 * tid + q, row = (i4 * 13108) >> 16, d = 4 * (i4 - row * 5);
          *(LAS u32x2*)(size_t)(lb + L_T + row * 72 + (8 + d) * 2) = (u32x2){o4[2 * q], o4[2 * q + 1]};
        }
      }
      BAR();
      TS(5);
      {
        if (g4 >= 2) Bq[2] = __builtin_bit_cast(bf16x8, (u32x4){0, 0, 0, 0});
        f32x4 nu[5], de[5];
#pragma unroll
        for (int j = 0; j < 5; j++) { nu[j] = (f32x4){0.f, 0.f, 0.f, 0.f}; de[j] = nu[j]; }
        bf16x8 An[9], Ad[9];
#pragma unroll
        for (int p = 0; p < 9; p++) {
          unsigned an8 = an[p] + 8, ad8 = ad[p] + 8;
          asm volatile("" : "+v"(an8), "+v"(ad8));
          An[p] = mk8(lds_b64(an[p]), lds_b64(an8)); Ad[p] = mk8(lds_b64(ad[p]), lds_b64(ad8));
        }
#pragma unroll
        for (int p = 0; p < 9; p++) { de[PJ[p]] = mfma16(Ad[p], Bq[PK[p]], de[PJ[p]]); nu[PJ[p]] = mfma16(An[p], Bq[PK[p]], nu[PJ[p]]); }
        u32x2 ov[5];
#pragma unroll
        for (int j = 0; j < 5; j++) {
          const float rd = __builtin_amdgcn_rcpf(de[j][0] + 1e-6f);
          ov[j] = (u32x2){pk2(nu[j][0] * rd, nu[j][1] * rd), pk2(nu[j][2] * rd, nu[j][3] * rd)};
        }
        store_frags(ov, rw, OFF_O + (rt * 8 + w) * 20480, 80 * cg, fr, g4);
      }
      TS(17);
      DRAIN();
      BAR();
      if (tid == 0) flag_st(ws, F_O + (rt * 8 + cg) * 128, tag);
      TS(6);
    } else {
      const int vb = xcd * 8 + (r - 16);
      const int d = 80 * (vb >> 3) + 48 + 4 * (vb & 7) + (w & 3);
      const unsigned cb = lb + (w & 3) * CHS;
      if (w < 4) {
#pragma unroll
        for (int i = 0; i < 4; i++) {
          u32x2 pv[5];
#pragma unroll
          for (int t = 0; t < 5; t++) pv[t] = (u32x2){pk2(phi(acc[i][t][0]), phi(acc[i][t][1])), pk2(phi(acc[i][t][2]), phi(acc[i][t][3]))};
          store_frags(pv, rw, OFF_PQ + ((rt * 8 + rp * 4 + i) * 8 + cg * 2 + hf) * 3072, 0, fr, g4);
        }
      } else {
        u32x4 hc[4], hp[4];
        conv_load_h(hc, hp, hk + (size_t)d * S, lane);
        conv_prep_h(hc, hp, cb, lane);
        if (w == 7) poll(ws, F_U + lane * 128, true, tag);
      }
      TS(12);
      DRAIN();
      BAR();
      TS(2);
      if (tid == 0) { flag_st(ws, F_PQ + (rt * 4 + cg) * 128, tag); flag_st(ws, F_W + vb * 128, tag); }
      if (w < 4) {
        const float Dd = bflo(Dv[d]);
        conv_load_u(rw, d, cb, lane, [&]() {});
        TS(3);
        f32x4 y[8];
        conv_main(y, cb, lane, 0, 8);
        TS(4);
#pragma unroll
        for (int c = 0; c < 8; c++) conv_store(y[c], cb, c, lane, Dd, rw, d);
        DRAIN();
      } else if (w == 7) {
        poll(ws, F_W + lane * 128, true, tag);
      }
      BAR();
      if (tid == 0) flag_st(ws, F_Y + (64 + vb) * 128, tag);
      TS(5); TS(6);
    }
  } else {
    const int pw = xcd * 8 + (r - 24);
    const int c6 = w < 4 ? w : 4 + ((w - 4) >> 1);
    const int d = 80 * (pw >> 3) + 6 * (pw & 7) + c6;
    const unsigned cb = lb + c6 * CHS;
    const bool prep = w < 4 || !(w & 1);
    u32x4 hc[4], hp[4];
    {
      const int rbase = xcd * 256 + ((r - 24) >> 1) * 64, cbase = ((r - 24) & 1) * 320;
      u32x4 kr[2][7], vr[2][4], sp[2][4];
#pragma unroll
      for (int q = 0; q < 2; q++) {
        const int it = w + 8 * q;
        if (q == 0 || w < 2) {
          const int ch = cbase + (it >> 1) * 64 + (lane & 7) * 8;
          const int s0 = rbase + (it & 1) * 32 + (lane >> 3) * 4;
#pragma unroll
          for (int j = 0; j < 7; j++) { int s = s0 - 3 + j; kr[q][j] = *(const u32x4*)(Bk + (size_t)(s < 0 ? 0 : s) * D + ch); }
#pragma unroll
          for (int j = 0; j < 4; j++) vr[q][j] = *(const u32x4*)(Bv + (size_t)(s0 + j) * D + ch);
#pragma unroll
          for (int j = 0; j < 4; j++) sp[q][j] = ((const u32x4*)(shk + ch * 4))[j];
        }
      }
      if (prep) conv_load_h(hc, hp, hk + (size_t)d * S, lane);
      if (w == 7) e0 = flag_ld(ws, F_E);
      SBAR();
#pragma unroll
      for (int q = 0; q < 2; q++) {
        const int it = w + 8 * q;
        if (q == 0 || w < 2) {
          const int s0 = rbase + (it & 1) * 32 + (lane >> 3) * 4;
          if (s0 == 0) { kr[q][0] = (u32x4){0, 0, 0, 0}; kr[q][1] = kr[q][0]; kr[q][2] = kr[q][0]; }
          const int rg = (it & 1) * 8 + (lane >> 3);
          const unsigned la = lb + L_UIMG + ((it >> 1) * 64 + (lane & 7) * 8) * 128 + (((rg >> 1) ^ (lane & 7)) << 4) + (rg & 1) * 8;
#pragma unroll
          for (int c = 0; c < 8; c++) {
            const unsigned s01 = sp[q][c >> 1][(c & 1) * 2], s23 = sp[q][c >> 1][(c & 1) * 2 + 1];
            const float w0 = bflo(s01), w1 = bfhi(s01), w2 = bflo(s23), w3 = bfhi(s23);
            float kk[7], u[4];
#pragma unroll
            for (int j = 0; j < 7; j++) { unsigned dd = kr[q][j][c >> 1]; kk[j] = (c & 1) ? bfhi(dd) : bflo(dd); }
#pragma unroll
            for (int j = 0; j < 4; j++) {
              unsigned dd = vr[q][j][c >> 1]; float v = (c & 1) ? bfhi(dd) : bflo(dd);
              float ks = kk[j] * w3; ks = fmaf(kk[j + 1], w2, ks); ks = fmaf(kk[j + 2], w1, ks); ks = fmaf(kk[j + 3], w0, ks);
              u[j] = ks * v;
            }
            *(LAS u32x2*)(size_t)(la + c * 128) = (u32x2){pk2(u[0], u[1]), pk2(u[2], u[3])};
          }
        }
      }
      PUT_TAG();
      BAR();
#pragma unroll
      for (int i = 0; i < 5; i++) {
        const int chl = (w * 5 + i) * 8 + (lane >> 3), k = lane & 7;
        const u32x4 v = *(const LAS u32x4*)(size_t)(lb + L_UIMG + chl * 128 + ((k ^ ((chl >> 3) & 7)) << 4));
        __builtin_amdgcn_raw_buffer_store_b128(v, rw, OFF_UT + (cbase + chl) * 4096 + rbase * 2 + k * 16, 0, SC1);
      }
      TS(1);
    }
    if (w < 5 && prep) conv_prep_h(hc, hp, cb, lane);
    DRAIN();
    BAR();
    TS(2);
    const unsigned tag = TAG;
    if (tid == 0) flag_st(ws, F_U + pw * 128, tag);
    const float Dd = bflo(Dv[d]);
    if (w == 6) conv_prep_h(hc, hp, cb, lane);
    if (w == 7) poll(ws, F_U + lane * 128, true, tag);
    BAR();
    if (prep) conv_load_u(rw, d, cb, lane, [&]() {});
    BAR();
    TS(3);
    f32x4 y[8];
    conv_main(y, cb, lane, w < 4 ? 0 : (w & 1) * 4, w < 4 ? 8 : (w & 1) * 4 + 4);
    TS(4);
    if (w < 4) {
#pragma unroll
      for (int c = 0; c < 8; c++) conv_store(y[c], cb, c, lane, Dd, rw, d);
    }
    BAR();
    if (w >= 4) {
#pragma unroll
      for (int c = 0; c < 4; c++) { const f32x4 lo = y[c], hi = y[4 + c]; *(LAS f32x4*)(size_t)(cb + ((w & 1) * 4 + c) * 1024 + lane * 16) = (w & 1) ? lo : hi; }
    }
    BAR();
    if (w >= 4) {
#pragma unroll
      for (int c = 0; c < 4; c++) {
        const f32x4 o_ = *(const LAS f32x4*)(size_t)(cb + (((w & 1) ^ 1) * 4 + c) * 1024 + lane * 16);
        const f32x4 lo = y[c], hi = y[4 + c];
        const f32x4 mine = (w & 1) ? hi : lo;
        conv_store((w & 1) ? o_ + mine : mine + o_, cb, (w & 1) * 4 + c, lane, Dd, rw, d);
      }
    }
    if (w == 0) poll(ws, F_W + lane * 128, true, tag);
    DRAIN();
    BAR();
    if (tid == 0) flag_st(ws, F_Y + pw * 128, tag);
    TS(5); TS(6);
  }

  {
    const unsigned tag = TAG;
    const int rt = (xcd & 3) * 8 + (r & 7), ct = (xcd >> 2) * 4 + (r >> 3);
    const int sw_ = w & 3, kk = w >> 2;
    const i32x4 sws = make_srd(ws, WS_USED);
    TS(18);
    const int srow = rt * 64 + sw_ * 16 + fr;
    u32x2 xv[3];
#pragma unroll
    for (int t = 0; t < 3; t++) { const int tt = kk ? (t < 2 ? 3 + t : 4) : t; xv[t] = *(const u32x2*)(x + (size_t)srow * D + ct * 80 + 16 * tt + 4 * g4); }
    SBAR();
    if (w == 0) {
      poll(ws, F_O + ((rt >> 1) * 8 + (lane & 7)) * 128, lane < 8, tag);
    } else if (w == 7) {
      poll(ws, F_Y + ((lane & 8 ? 64 : 0) + ct * 8 + (lane & 7)) * 128, lane < 16, tag);
    } else {
      const unsigned vwv = OFF_WOP + ct * 102400 + lane * 16;
      for (int i = w - 1; i < 100; i += 6) { int pc = 12 * (r & 7) + i; pc = __builtin_amdgcn_readfirstlane(pc >= 100 ? pc - 100 : pc); glds16_sc1(lb + pc * 1024, vwv, sws, pc * 1024); }
      if (w == 6 && b == 0) {
        poll(ws, F_U + lane * 128, true, tag); poll(ws, F_PQ + lane * 128, true, tag);
        poll(ws, F_P + lane * 128, true, tag); poll(ws, F_P + (64 + lane) * 128, true, tag);
        if (lane == 0) flag_st(ws, F_E, tag);
      }
      DRAIN();
    }
    BAR();
    TS(7);
    asm volatile("" :: "v"(xv[0]), "v"(xv[1]), "v"(xv[2]));
    bf16x8 of[10];
    const int rot = ((ct & 3) * 5) >> 1;
    {
      const unsigned vo = OFF_O + (rt * 4 + sw_) * 20480 + kk * 1024 + lane * 16;
#pragma unroll
      for (int s = 0; s < 10; s++) { int q = s + rot; q = __builtin_amdgcn_readfirstlane(q >= 10 ? q - 10 : q); bload16_sc1(of[s], vo, sws, q * 2048); }
    }
    if (w == 7) {
      for (int pc = 0; pc < 10; pc++)
        glds16_sc1(lb + L_Y + pc * 1024, OFF_YT + (ct * 80 + pc * 8 + (lane >> 3)) * 4096 + rt * 128 + (lane & 7) * 16, sws, 0);
    }
    float g;
    asm volatile("buffer_load_dword %0, %1, %2, 0 offen sc1" : "=v"(g) : "v"(OFF_G + srow * 4), "s"(sws) : "memory");
    TS(8);
    f32x4 acc[5];
#pragma unroll
    for (int t = 0; t < 5; t++) acc[t] = (f32x4){0.f, 0.f, 0.f, 0.f};
    const unsigned wa0 = lb + kk * 5120 + lane * 16;
#pragma unroll
    for (int hh = 0; hh < 2; hh++) {
      bf16x8 wf[5][5];
#pragma unroll
      for (int s = 0; s < 5; s++) {
        int q = hh * 5 + s + rot; q = q >= 10 ? q - 10 : q;
        const unsigned wa = wa0 + q * 10240;
#pragma unroll
        for (int t = 0; t < 5; t++) wf[s][t] = lds_b128(wa + t * 1024);
      }
      if (hh == 0) {
        if (w == 7) asm volatile("s_waitcnt vmcnt(16)" ::: "memory"); else asm volatile("s_waitcnt vmcnt(6)" ::: "memory");
        asm volatile("" : "+v"(of[0]), "+v"(of[1]), "+v"(of[2]), "+v"(of[3]), "+v"(of[4]) :: "memory");
      } else {
        if (w == 7) asm volatile("s_waitcnt vmcnt(11)" ::: "memory"); else asm volatile("s_waitcnt vmcnt(1)" ::: "memory");
        asm volatile("" : "+v"(of[5]), "+v"(of[6]), "+v"(of[7]), "+v"(of[8]), "+v"(of[9]) :: "memory");
      }
      SBAR();
#pragma unroll
      for (int s = 0; s < 5; s++)
#pragma unroll
        for (int t = 0; t < 5; t++) acc[t] = mfma16(wf[s][t], of[hh * 5 + s], acc[t]);
      SBAR();
    }
    TS(9);
    const unsigned ra = lb + L_RED + (sw_ * 5) * 1024 + lane * 16;
    if (kk == 0) { *(LAS f32x4*)(size_t)(ra + 3 * 1024) = acc[3]; *(LAS f32x4*)(size_t)(ra + 4 * 1024) = acc[4]; }
    else { *(LAS f32x4*)(size_t)(ra) = acc[0]; *(LAS f32x4*)(size_t)(ra + 1024) = acc[1]; *(LAS f32x4*)(size_t)(ra + 2048) = acc[2]; }
    asm volatile("s_waitcnt vmcnt(0)" : "+v"(g) :: "memory");
    BAR();
    TS(20);
    const float omg = 1.f - g;
    const int nt = kk ? 2 : 3;
#pragma unroll
    for (int t = 0; t < 3; t++) {
      if (t < nt) {
        const int tt = kk ? 3 + t : t;
        const f32x4 a0 = acc[t], a1 = acc[3 + (t < 2 ? t : 1)];
        f32x4 a = *(const LAS f32x4*)(size_t)(ra + tt * 1024) + (kk ? a1 : a0);
        u32x2 yv = lds_tr(lb + L_Y + (16 * tt + 4 * g4 + (fr >> 2)) * 128 + (sw_ * 16 + 4 * (fr & 3)) * 2);
        float xs[4] = {bflo(xv[t].x), bfhi(xv[t].x), bflo(xv[t].y), bfhi(xv[t].y)};
        float ys[4] = {bflo(yv.x), bfhi(yv.x), bflo(yv.y), bfhi(yv.y)};
        float r_[4];
#pragma unroll
        for (int q = 0; q < 4; q++) r_[q] = fmaf(g, a[q], omg * (xs[q] * ys[q]));
        *(u32x2*)(out + (size_t)srow * D + ct * 80 + 16 * tt + 4 * g4) = (u32x2){pk2(r_[0], r_[1]), pk2(r_[2], r_[3])};
      }
    }
    TS(10); TS_OUT(21);
  }
}

extern "C" void kernel_launch(void* const* d_in, const int* in_sizes, int n_in, void* d_out, int out_size, void* d_ws, size_t ws_size,
                              hipStream_t stream) {
  const u16* x = (const u16*)d_in[0]; const u16* wqkv = (const u16*)d_in[1]; const u16* wo = (const u16*)d_in[2];
  const u16* Bk = (const u16*)d_in[4]; const u16* Bv = (const u16*)d_in[5]; const u16* shk = (const u16*)d_in[6];
  const u16* hk = (const u16*)d_in[7]; const u16* Dv = (const u16*)d_in[8]; const u16* gw = (const u16*)d_in[9];
  (void)hipFuncSetAttribute((const void*)fused, hipFuncAttributeMaxDynamicSharedMemorySize, LDS_TOTAL);
  fused<<<256, 512, LDS_TOTAL, stream>>>(x, wqkv, wo, Bk, Bv, shk, hk, Dv, gw, (u16*)d_out, (char*)d_ws);
}
```

```cpp
#include <hip/hip_runtime.h>
#include <hip/hip_bf16.h>
#include <type_traits>
#define NO_PF

typedef unsigned short u16;
typedef __bf16 bf16x2 __attribute__((ext_vector_type(2)));
typedef __bf16 bf16x8 __attribute__((ext_vector_type(8)));
typedef float f32x4 __attribute__((ext_vector_type(4)));
typedef unsigned u32x4 __attribute__((ext_vector_type(4)));
typedef unsigned u32x2 __attribute__((ext_vector_type(2)));
typedef short v4i16_t __attribute__((ext_vector_type(4)));
typedef int i32x4 __attribute__((ext_vector_type(4)));
typedef __attribute__((address_space(1))) unsigned gu32;
#define LAS __attribute__((address_space(3)))
#define SBAR() __builtin_amdgcn_sched_barrier(0)
#define SGB(mask, n) __builtin_amdgcn_sched_group_barrier(mask, n, 0)
#define BAR() asm volatile("s_waitcnt lgkmcnt(0)\n\ts_barrier" ::: "memory")
#define DRAIN() asm volatile("s_waitcnt vmcnt(0)" ::: "memory")
#define SC1 16

#define TS_DECL
#define TSARG
#define TSPAR
#define TS(i)
#define TS_OUT(n)

constexpr int S = 2048, D = 640;
constexpr unsigned SZ = S * D * 2;
constexpr unsigned F_E = 0, F_U = 128, F_PQ = F_U + 64 * 128, F_P = F_PQ + 64 * 128, F_O = F_P + 128 * 128, F_Y = F_O + 128 * 128, F_W = F_Y + 128 * 128;
constexpr unsigned OFF_PQ = 0x20000, OFF_P = OFF_PQ + 128 * 8 * 3 * 1024, OFF_UT = OFF_P + (1u << 19), OFF_G = OFF_UT + SZ, OFF_WOP = OFF_G + 8192,
                   OFF_O = OFF_WOP + (1u << 20), OFF_YT = OFF_O + SZ, WS_USED = OFF_YT + SZ;
constexpr int XS = 16384, WSB = 20480, STG = XS + WSB;
constexpr int L_EXTRA = 4 * STG;
constexpr int L_TAG = L_EXTRA, L_CNT = L_EXTRA + 16, L_T = L_EXTRA + 64;
constexpr int LDS_TOTAL = L_T + 6144 + 5120;
constexpr int CPS = 4160, UOFF = 4 * CPS, CHS = UOFF + 4608;
constexpr int L_UIMG = 5 * CHS;
constexpr int L_Y = 102400, L_RED = L_Y + 10240;

__device__ __forceinline__ unsigned pk2(float a, float b) { bf16x2 r = {(__bf16)a, (__bf16)b}; return __builtin_bit_cast(unsigned, r); }
__device__ __forceinline__ float rbf(float a) { return (float)(__bf16)a; }
__device__ __forceinline__ float bflo(unsigned v) { return __uint_as_float(v << 16); }
__device__ __forceinline__ float bfhi(unsigned v) { return __uint_as_float(v & 0xffff0000u); }
__device__ __forceinline__ float phi(float t) {
  return __builtin_amdgcn_fmed3f(t, 0.f, 3.0e38f) + (__builtin_amdgcn_fmed3f(__builtin_amdgcn_exp2f(t * 1.44269504f), 0.f, 1.f) + 1e-6f);
}
__device__ __forceinline__ i32x4 make_srd(const void* p, unsigned bytes) {
  unsigned long long a = (unsigned long long)p; i32x4 r;
  r.x = __builtin_amdgcn_readfirstlane((int)(unsigned)a); r.y = __builtin_amdgcn_readfirstlane((int)(unsigned)(a >> 32));
  r.z = __builtin_amdgcn_readfirstlane((int)bytes); r.w = 0x00020000; return r;
}
__device__ __forceinline__ void glds16(unsigned ldsaddr, unsigned voff, i32x4 srd, unsigned soff) {
  asm volatile("s_mov_b32 m0, %0\n\ts_nop 0\n\tbuffer_load_dwordx4 %1, %2, %3 offen lds" :: "s"(ldsaddr), "v"(voff), "s"(srd), "s"(soff) : "memory");
}
__device__ __forceinline__ void glds16_sc1(unsigned ldsaddr, unsigned voff, i32x4 srd, unsigned soff) {
  asm volatile("s_mov_b32 m0, %0\n\ts_nop 0\n\tbuffer_load_dwordx4 %1, %2, %3 offen sc1 lds" :: "s"(ldsaddr), "v"(voff), "s"(srd), "s"(soff) : "memory");
}
__device__ __forceinline__ void bload16_sc1(bf16x8& dst, unsigned voff, i32x4 srd, unsigned soff) {
  asm volatile("buffer_load_dwordx4 %0, %1, %2, %3 offen sc1" : "=v"(dst) : "v"(voff), "s"(srd), "s"(soff) : "memory");
}
template <int N> __device__ __forceinline__ void wait_bar() { asm volatile("s_waitcnt vmcnt(%0) lgkmcnt(0)\n\ts_barrier" :: "n"(N) : "memory"); }
__device__ __forceinline__ bf16x8 lds_b128(unsigned addr) { return *(const LAS bf16x8*)(size_t)addr; }
__device__ __forceinline__ u32x2 lds_b64(unsigned addr) { return *(const LAS u32x2*)(size_t)addr; }
__device__ __forceinline__ u32x2 lds_tr(unsigned addr) { return __builtin_bit_cast(u32x2, __builtin_amdgcn_ds_read_tr16_b64_v4i16((LAS v4i16_t*)(size_t)addr)); }
__device__ __forceinline__ bf16x8 mk8(u32x2 lo, u32x2 hi) { u32x4 t = {lo.x, lo.y, hi.x, hi.y}; return __builtin_bit_cast(bf16x8, t); }
__device__ __forceinline__ f32x4 mfma16(bf16x8 a, bf16x8 b, f32x4 c) { return __builtin_amdgcn_mfma_f32_16x16x32_bf16(a, b, c, 0, 0, 0); }

__device__ __forceinline__ u32x4 pair16(u32x2 a, u32x2 b) {
  const auto r0 = __builtin_amdgcn_permlane16_swap(a.x, b.x, false, false);
  const auto r1 = __builtin_amdgcn_permlane16_swap(a.y, b.y, false, false);
  return (u32x4){r0[0], r1[0], r0[1], r1[1]};
}
__device__ __forceinline__ void store_frags(const u32x2 (&v)[5], __amdgpu_buffer_rsrc_t rw, unsigned base, int k0, int fr, int g4) {
#pragma unroll
  for (int j = 0; j < 4; j += 2) {
    const int k = k0 + 16 * (j + (g4 & 1)) + 8 * (g4 >> 1);
    __builtin_amdgcn_raw_buffer_store_b128(pair16(v[j], v[j + 1]), rw, base + (k >> 5) * 1024 + (((k >> 3) & 3) * 16 + fr) * 16, 0, SC1);
  }
  const int k = k0 + 64 + 4 * g4;
  __builtin_amdgcn_raw_buffer_store_b64(v[4], rw, base + (k >> 5) * 1024 + (((k >> 3) & 3) * 16 + fr) * 16 + ((k >> 2) & 1) * 8, 0, SC1);
}

__device__ __forceinline__ void lds_sync(unsigned cnt_addr, int lane, unsigned n) {
  asm volatile("s_waitcnt lgkmcnt(0)" ::: "memory");
  if (lane == 0) __hip_atomic_fetch_add((LAS unsigned*)(size_t)cnt_addr, 1u, __ATOMIC_RELAXED, __HIP_MEMORY_SCOPE_WORKGROUP);
  while (__builtin_amdgcn_readfirstlane(__hip_atomic_load((LAS unsigned*)(size_t)cnt_addr, __ATOMIC_RELAXED, __HIP_MEMORY_SCOPE_WORKGROUP)) < n) {}
  asm volatile("" ::: "memory");
}
__device__ __forceinline__ bool lds_last(unsigned cnt_addr, int lane, unsigned n) {
  unsigned old = 0;
  if (lane == 0) old = __hip_atomic_fetch_add((LAS unsigned*)(size_t)cnt_addr, 1u, __ATOMIC_RELAXED, __HIP_MEMORY_SCOPE_WORKGROUP);
  return __builtin_amdgcn_readfirstlane(old) == n - 1;
}

__device__ __forceinline__ unsigned flag_ld(const char* ws, unsigned off) { return __hip_atomic_load((const gu32*)(ws + off), __ATOMIC_RELAXED, __HIP_MEMORY_SCOPE_AGENT); }
__device__ __forceinline__ void flag_st(char* ws, unsigned off, unsigned v) { __hip_atomic_store((gu32*)(ws + off), v, __ATOMIC_RELAXED, __HIP_MEMORY_SCOPE_AGENT); }
__device__ __forceinline__ void poll(const char* ws, unsigned off, bool act, unsigned tag) {
  const unsigned o0 = __builtin_amdgcn_readfirstlane(off);
  off = act ? off : o0;
  unsigned a, b, n = 1u << 17;
  asm volatile(
      "global_load_dword %0, %3, %4 sc1\n\t"
      "s_sleep 6\n"
      "1:\n\t"
      "global_load_dword %1, %3, %4 sc1\n\t"
      "s_waitcnt vmcnt(1)\n\t"
      "v_cmp_ne_u32 vcc, %0, %5\n\t"
      "s_cbranch_vccz 2f\n\t"
      "global_load_dword %0, %3, %4 sc1\n\t"
      "s_waitcnt vmcnt(1)\n\t"
      "v_cmp_ne_u32 vcc, %1, %5\n\t"
      "s_cbranch_vccz 2f\n\t"
      "s_sub_u32 %2, %2, 1\n\t"
      "s_cbranch_scc0 1b\n"
      "2:\n\t"
      "s_waitcnt vmcnt(0)"
      : "=&v"(a), "=&v"(b), "+s"(n) : "v"(off), "s"(ws), "v"(tag) : "vcc", "scc", "memory");
}

template <bool KV>
__device__ __forceinline__ void gemm_compute(f32x4 (&acc)[4][5], unsigned xa0, unsigned wa0, unsigned wa1 TSPAR) {
  bf16x8 xa[4], xb[4], wa[5], wb[5];
  auto load = [&](bf16x8 (&xf)[4], bf16x8 (&wf)[5], int buf, int kk) {
    const unsigned bo = buf * STG;
    const unsigned xa = (xa0 + bo) ^ (kk * 64);
    const unsigned a0 = wa0 + bo + kk * 10240, a1 = wa1 + bo + kk * 10240;
#pragma unroll
    for (int i = 0; i < 4; i++) xf[i] = lds_b128(xa + 2048 * i);
#pragma unroll
    for (int t = 0; t < 5; t++) { const unsigned a = ((t & 1) ? a1 : a0) + 32 * t; wf[t] = mk8(lds_tr(a), lds_tr(a + 1280)); }
  };
  auto mma = [&](const bf16x8 (&xf)[4], const bf16x8 (&wf)[5]) {
#pragma unroll
    for (int t = 0; t < 5; t++)
#pragma unroll
      for (int i = 0; i < 4; i++) acc[i][t] = KV ? mfma16(xf[i], wf[t], acc[i][t]) : mfma16(wf[t], xf[i], acc[i][t]);
  };
  auto interleave = [&]() {
#pragma unroll
    for (int t = 0; t < 14; t++) { SGB(0x8, 1); SGB(0x100, 1); }
    SGB(0x8, 6);
  };
  BAR();
  TS(21);
  load(xa, wa, 0, 0);
  SBAR();
  for (int s = 0; s < 9; s++) {
    load(xb, wb, s & 3, 1); mma(xa, wa); interleave();
    SBAR();
    BAR();
    if (s == 0) TS(22);
    if (s == 4) TS(23);
    load(xa, wa, (s + 1) & 3, 0); mma(xb, wb); interleave();
    SBAR();
  }
  load(xb, wb, 1, 1); mma(xa, wa); interleave();
  SBAR();
  mma(xb, wb);
}

__device__ __forceinline__ void gemm_loader(int L, int lane, unsigned lb, const u16* x, const u16* wqkv, int rt, int cg, bool isKV) {
  const int s0 = (((rt & 3) * 5 + 1) / 2 + (isKV ? 2 * (cg & 3) : 8 + (cg & 1))) % 10;
  const i32x4 sx = make_srd(x, S * D * 2), sw = make_srd(wqkv, D * 3 * D * 2);
  unsigned vx[2], vw[5];
#pragma unroll
  for (int i = 0; i < 2; i++) vx[i] = (rt * 128 + L * 64 + i * 8 + (lane >> 3)) * 1280 + (((lane & 7) ^ ((4 * i + (lane >> 4)) & 7)) << 4);
#pragma unroll
  for (int i = 0; i < 5; i++) {
    const int ci = i * 64 + lane, k = (ci * 3277) >> 16, pos = ci - k * 20, gc = pos ^ (((k >> 3) & 1) << 1), tl = gc >> 1;
    unsigned col;
    if (isKV) { const int isv = (tl >= 3 && tl < 5) || tl >= 7; const int tt = tl < 3 ? tl : tl < 5 ? tl - 3 : tl < 7 ? tl - 2 : tl - 5;
                col = (isv ? 2560u : 1280u) + 160u * cg + tt * 32; }
    else col = 320u * cg + tl * 32;
    vw[i] = (L * 32 + k) * 3840 + col + (gc & 1) * 16;
  }
  auto stage = [&](int st_, int buf) {
    const unsigned base = lb + buf * STG;
    int st = st_ + s0; st = st >= 10 ? st - 10 : st;
#pragma unroll
    for (int p = 0; p < 10; p++) {
      glds16(base + XS + (L * 10 + p) * 1024, vw[p % 5], sw, st * (64 * 3840) + (p / 5) * (16 * 3840));
      if (p < 8) glds16(base + (L * 8 + p) * 1024, vx[p & 1], sx, st * 128 + (p >> 1) * (16 * 1280));
    }
  };
  stage(0, 0); stage(1, 1);
  wait_bar<18>();
  stage(2, 2);
  for (int s = 0; s < 9; s++) {
    if (s < 8) wait_bar<18>(); else wait_bar<0>();
    if (s < 7) stage(s + 3, (s + 3) & 3);
  }
}

template <int T> struct StatTile {
  static constexpr bool isz = T >= 13;
  static constexpr int i = isz ? T - 13 : (T + 1) / 3;
  static constexpr int j = isz ? 0 : T - (i == 0 ? 0 : 3 * i - 1) + (i == 0 ? 0 : i - 1);
};
template <int T> __device__ __forceinline__ f32x4 stat_mma(unsigned la) {
  const u32x4 o1 = {0x3F803F80u, 0x3F803F80u, 0x3F803F80u, 0x3F803F80u};
  bf16x8 A[4], B[4];
#pragma unroll
  for (int m = 0; m < 4; m++) {
    A[m] = lds_b128(la + StatTile<T>::i * 1024 + m * 10240);
    B[m] = StatTile<T>::isz ? __builtin_bit_cast(bf16x8, o1) : lds_b128(la + (5 + StatTile<T>::j) * 1024 + m * 10240);
  }
  f32x4 d4 = {0.f, 0.f, 0.f, 0.f};
#pragma unroll
  for (int m = 0; m < 4; m++) d4 = mfma16(A[m], B[m], d4);
  return d4;
}
template <int T> __device__ __forceinline__ void stat_store(f32x4 d4, int lane, __amdgpu_buffer_rsrc_t rw, unsigned pbase) {
  const int fr = lane & 15, g4 = lane >> 4;
  const int Dg = 16 * StatTile<T>::i + 4 * g4, h = (Dg * 205) >> 12, dd = Dg - 20 * h;
  const int ee = StatTile<T>::isz ? 20 : 16 * StatTile<T>::j + fr - 20 * h;
  if (StatTile<T>::isz ? fr == 0 : (ee >= 0 && ee < 20))
    __builtin_amdgcn_raw_buffer_store_b64((u32x2){pk2(d4[0], d4[1]), pk2(d4[2], d4[3])}, rw, pbase + (h * 420 + ee * 20 + dd) * 2, 0, SC1);
}
template <int W> __device__ __forceinline__ void stat_tiles(unsigned lb, int lane, __amdgpu_buffer_rsrc_t rw, unsigned pbase) {
  const unsigned la = lb + lane * 16;
  const f32x4 a = stat_mma<W>(la), b = stat_mma<W + 8>(la);
  if constexpr (W < 2) { const f32x4 c = stat_mma<W + 16>(la); stat_store<W + 16>(c, lane, rw, pbase); }
  stat_store<W>(a, lane, rw, pbase);
  stat_store<W + 8>(b, lane, rw, pbase);
}

__device__ __forceinline__ void conv_load_h(u32x4 (&cur)[4], u32x4 (&prv)[4], const u16* __restrict__ hrow, int lane) {
#pragma unroll
  for (int it = 0; it < 4; it++) {
    const int i = it * 64 + lane;
    cur[it] = *(const u32x4*)(hrow + i * 8); prv[it] = *(const u32x4*)(hrow + (i > 0 ? i * 8 - 8 : 0));
  }
}
__device__ __forceinline__ void conv_prep_h(const u32x4 (&cur)[4], const u32x4 (&prv_)[4], unsigned cb, int lane) {
#pragma unroll
  for (int it = 0; it < 4; it++) {
    u32x4 prv = prv_[it];
    if (it == 0 && lane == 0) prv = (u32x4){0, 0, 0, 0};
    const unsigned W_[8] = {prv.x, prv.y, prv.z, prv.w, cur[it].x, cur[it].y, cur[it].z, cur[it].w};
    const int c = 255 - (it * 64 + lane);
#pragma unroll
    for (int e = 0; e < 4; e++) {
      u32x4 ov;
#pragma unroll
      for (int q = 0; q < 4; q++) {
        const int i0 = 15 - e - 2 * q, i1 = i0 - 1;
        const unsigned sel = ((i0 & 1) ? 0x0706u : 0x0504u) | (((i1 & 1) ? 0x0302u : 0x0100u) << 16);
        ov[q] = __builtin_amdgcn_perm(W_[i0 >> 1], W_[i1 >> 1], sel);
      }
      *(LAS u32x4*)(size_t)(cb + e * CPS + c * 16) = ov;
    }
  }
  if (lane < 8) *(LAS u32x4*)(size_t)(cb + (lane >> 1) * CPS + (256 + (lane & 1)) * 16) = (u32x4){0, 0, 0, 0};
  if (lane < 32) *(LAS u32x4*)(size_t)(cb + UOFF + lane * 16) = (u32x4){0, 0, 0, 0};
}
template <class F>
__device__ __forceinline__ void conv_load_u(__amdgpu_buffer_rsrc_t rw, int d, unsigned cb, int lane, F&& between) {
  u32x4 uv[4];
#pragma unroll
  for (int it = 0; it < 4; it++) uv[it] = __builtin_amdgcn_raw_buffer_load_b128(rw, OFF_UT + d * 4096 + (it * 64 + lane) * 16, 0, SC1);
  SBAR();
  between();
  SBAR();
#pragma unroll
  for (int it = 0; it < 4; it++) *(LAS u32x4*)(size_t)(cb + UOFF + 512 + (it * 64 + lane) * 16) = uv[it];
}
struct ConvFrags { u32x2 al[8], ah[8]; u32x4 B[8]; };
__device__ __forceinline__ void conv_read(ConvFrags& f, unsigned aa, unsigned ba) {
  asm volatile(
      "ds_read_b128 %16, %25\n\tds_read_b64 %7, %24 offset:3584\n\tds_read_b64 %15, %24 offset:3592\n\t"
      "ds_read_b128 %17, %25 offset:512\n\tds_read_b128 %18, %25 offset:1024\n\tds_read_b128 %19, %25 offset:1536\n\t"
      "ds_read_b128 %20, %25 offset:2048\n\tds_read_b128 %21, %25 offset:2560\n\tds_read_b128 %22, %25 offset:3072\n\t"
      "ds_read_b128 %23, %25 offset:3584\n\t"
      "ds_read_b64 %6, %24 offset:3072\n\tds_read_b64 %14, %24 offset:3080\n\t"
      "ds_read_b64 %5, %24 offset:2560\n\tds_read_b64 %13, %24 offset:2568\n\t"
      "ds_read_b64 %4, %24 offset:2048\n\tds_read_b64 %12, %24 offset:2056\n\t"
      "ds_read_b64 %3, %24 offset:1536\n\tds_read_b64 %11, %24 offset:1544\n\t"
      "ds_read_b64 %2, %24 offset:1024\n\tds_read_b64 %10, %24 offset:1032\n\t"
      "ds_read_b64 %1, %24 offset:512\n\tds_read_b64 %9, %24 offset:520\n\t"
      "ds_read_b64 %0, %24\n\tds_read_b64 %8, %24 offset:8"
      : "=&v"(f.al[0]), "=&v"(f.al[1]), "=&v"(f.al[2]), "=&v"(f.al[3]), "=&v"(f.al[4]), "=&v"(f.al[5]), "=&v"(f.al[6]), "=&v"(f.al[7]),
        "=&v"(f.ah[0]), "=&v"(f.ah[1]), "=&v"(f.ah[2]), "=&v"(f.ah[3]), "=&v"(f.ah[4]), "=&v"(f.ah[5]), "=&v"(f.ah[6]), "=&v"(f.ah[7]),
        "=&v"(f.B[0]), "=&v"(f.B[1]), "=&v"(f.B[2]), "=&v"(f.B[3]), "=&v"(f.B[4]), "=&v"(f.B[5]), "=&v"(f.B[6]), "=&v"(f.B[7])
      : "v"(aa), "v"(ba) : "memory");
}
__device__ __forceinline__ void conv_wait(ConvFrags& f) {
  asm volatile("s_waitcnt lgkmcnt(0)"
      : "+v"(f.al[0]), "+v"(f.al[1]), "+v"(f.al[2]), "+v"(f.al[3]), "+v"(f.al[4]), "+v"(f.al[5]), "+v"(f.al[6]), "+v"(f.al[7]),
        "+v"(f.ah[0]), "+v"(f.ah[1]), "+v"(f.ah[2]), "+v"(f.ah[3]), "+v"(f.ah[4]), "+v"(f.ah[5]), "+v"(f.ah[6]), "+v"(f.ah[7]),
        "+v"(f.B[0]), "+v"(f.B[1]), "+v"(f.B[2]), "+v"(f.B[3]), "+v"(f.B[4]), "+v"(f.B[5]), "+v"(f.B[6]), "+v"(f.B[7]) :: "memory");
}
template <int K> __device__ __forceinline__ void conv_read1(ConvFrags& f, unsigned aa, unsigned ba) {
  if constexpr (K < 8) asm volatile("ds_read_b128 %0, %1 offset:%2" : "=&v"(f.B[K]) : "v"(ba), "n"(512 * K) : "memory");
  else if constexpr (K < 16) asm volatile("ds_read_b64 %0, %1 offset:%2" : "=&v"(f.al[K - 8]) : "v"(aa), "n"(512 * (K - 8)) : "memory");
  else if constexpr (K < 24) asm volatile("ds_read_b64 %0, %1 offset:%2" : "=&v"(f.ah[K - 16]) : "v"(aa), "n"(512 * (K - 16) + 8) : "memory");
}
template <int K = 0> __device__ __forceinline__ void conv_step(f32x4 (&acc)[8], const ConvFrags& f, ConvFrags& nx, unsigned aa, unsigned ba) {
  if constexpr (K < 36) {
    constexpr int a = K < 8 ? 0 : K < 15 ? 1 : K < 21 ? 2 : K < 26 ? 3 : K < 30 ? 4 : K < 33 ? 5 : K < 35 ? 6 : 7;
    constexpr int i = K - (a == 0 ? 0 : a == 1 ? 8 : a == 2 ? 15 : a == 3 ? 21 : a == 4 ? 26 : a == 5 ? 30 : a == 6 ? 33 : 35);
    acc[a + i] = mfma16(mk8(f.al[7 - a], f.ah[7 - a]), __builtin_bit_cast(bf16x8, f.B[i]), acc[a + i]);
    conv_read1<K>(nx, aa, ba);
    SBAR();
    conv_step<K + 1>(acc, f, nx, aa, ba);
  }
}
__device__ __forceinline__ void conv_main(f32x4 (&acc)[8], unsigned cb, int lane, int c0, int c1) {
  const int fr = lane & 15, g4 = lane >> 4;
  const unsigned abase = cb + (3 - (fr & 3)) * CPS + 2 * (2044 - 16 * 112 - 16 * (g4 >> 1) + 8 * (g4 & 1) - (fr & ~3));
  const unsigned bbase = cb + UOFF + 512 + 2 * (16 * fr - 16 * (g4 >> 1) + 8 * (g4 & 1));
#pragma unroll
  for (int c = 0; c < 8; c++) acc[c] = (f32x4){0.f, 0.f, 0.f, 0.f};
  ConvFrags f0, f1;
  asm volatile("s_waitcnt lgkmcnt(0)" ::: "memory");
  conv_read(f0, abase - 64 * c0, bbase - 64 * c0);
  for (int cc = c0; cc < c1; cc += 2) {
    conv_wait(f0);
    SBAR();
    conv_step(acc, f0, f1, abase - 64 * (cc + 1), bbase - 64 * (cc + 1));
    conv_wait(f1);
    SBAR();
    const int cn = cc + 2 < c1 ? cc + 2 : cc + 1;
    conv_step(acc, f1, f0, abase - 64 * cn, bbase - 64 * cn);
  }
  conv_wait(f0);
}
__device__ __forceinline__ void conv_store(f32x4 y, unsigned cb, int c, int lane, float Dd, __amdgpu_buffer_rsrc_t rw, int d) {
  const int fr = lane & 15, g4 = lane >> 4;
  const u32x2 uu = lds_b64(cb + UOFF + 512 + 2 * (256 * c + 16 * fr + 4 * g4));
  const float r0 = fmaf(bflo(uu.x), Dd, y[0]), r1 = fmaf(bfhi(uu.x), Dd, y[1]);
  const float r2 = fmaf(bflo(uu.y), Dd, y[2]), r3 = fmaf(bfhi(uu.y), Dd, y[3]);
  __builtin_amdgcn_raw_buffer_store_b64((u32x2){pk2(r0, r1), pk2(r2, r3)}, rw, OFF_YT + d * 4096 + (256 * c + 16 * fr + 4 * g4) * 2, 0, SC1);
}

__global__ __launch_bounds__(512, 2) void fused(const u16* __restrict__ x, const u16* __restrict__ wqkv, const u16* __restrict__ wo,
                                                const u16* __restrict__ Bk, const u16* __restrict__ Bv, const u16* __restrict__ shk,
                                                const u16* __restrict__ hk, const u16* __restrict__ Dv, const u16* __restrict__ gw,
                                                u16* __restrict__ out, char* __restrict__ ws) {
  extern __shared__ __attribute__((aligned(16))) char sm[];
  const int tid = threadIdx.x, lane = tid & 63;
  const int w = __builtin_amdgcn_readfirstlane(tid >> 6);
  const int b = blockIdx.x, xcd = b & 7, r = b >> 3;
  const int fr = lane & 15, g4 = lane >> 4;
  const unsigned lb = (unsigned)(size_t)(LAS char*)sm;
  const __amdgpu_buffer_rsrc_t rw = __builtin_amdgcn_make_buffer_rsrc(ws, 0, WS_USED, 0x00020000);
  TS_DECL TS(0);
  unsigned e0 = 0;
#define PUT_TAG() do { if (w == 7 && lane == 0) { *(LAS unsigned*)(size_t)(lb + L_TAG) = e0 + 1u; *(LAS u32x4*)(size_t)(lb + L_CNT) = (u32x4){0u, 0u, 0u, 0u}; } } while (0)
#define TAG (*(const LAS unsigned*)(size_t)(lb + L_TAG))
  if (r < 24) {
    const bool isKV = r < 16;
    const int rt = (xcd & 3) * 4 + (r & 3);
    const int cg = isKV ? (xcd >> 2) * 4 + (r >> 2) : (xcd >> 2) * 2 + ((r - 16) >> 2);
    f32x4 acc[4][5];
    const int rp = (w >> 1) & 1, hf = w & 1;
    if (w < 4) {
      const unsigned xa0 = lb + (rp * 64 + fr) * 128 + ((g4 ^ ((fr >> 1) & 7)) << 4);
      const unsigned wl = lb + XS + (8 * g4 + (fr >> 2)) * 320 + ((lane & 3) >> 1) * 16 + (lane & 1) * 8 + hf * 160;
      const unsigned sgn = (g4 & 1) * 32;
      const unsigned wa0 = hf ? wl - sgn : wl + sgn, wa1 = hf ? wl + sgn : wl - sgn;
#pragma unroll
      for (int i = 0; i < 4; i++)
#pragma unroll
        for (int t = 0; t < 5; t++) acc[i][t] = (f32x4){0.f, 0.f, 0.f, 0.f};
      if (isKV) gemm_compute<true>(acc, xa0, wa0, wa1 TSARG); else gemm_compute<false>(acc, xa0, wa0, wa1 TSARG);
    } else if (w < 6) {
      gemm_loader(w - 4, lane, lb, x, wqkv, rt, cg, isKV);
    } else {
      const int w6 = w - 6;
      unsigned pf[5] = {0, 0, 0, 0, 0};
      SBAR();
      if (w == 7) e0 = flag_ld(ws, F_E);
      if (isKV) {
        const int l16 = lane & 15, row0 = rt * 128 + cg * 16 + w6 * 8;
        const u32x4 g0 = *(const u32x4*)(gw + lane * 8), g1 = *(const u32x4*)(gw + 512 + l16 * 8);
        u32x4 gx0[8], gx1[8];
#pragma unroll
        for (int i = 0; i < 8; i++) {
          const u16* xr = x + (size_t)(row0 + i) * D;
          gx0[i] = *(const u32x4*)(xr + lane * 8); gx1[i] = *(const u32x4*)(xr + 512 + l16 * 8);
        }
        SBAR();
        BAR(); BAR(); BAR();
        float a[8];
        const float m1 = lane < 16 ? 1.f : 0.f;
#pragma unroll
        for (int i = 0; i < 8; i++) {
          float a0 = 0.f, a1 = 0.f;
#pragma unroll
          for (int j = 0; j < 4; j++) {
            a0 = fmaf(bflo(gx0[i][j]), bflo(g0[j]), a0); a0 = fmaf(bfhi(gx0[i][j]), bfhi(g0[j]), a0);
            a1 = fmaf(bflo(gx1[i][j]), bflo(g1[j]), a1); a1 = fmaf(bfhi(gx1[i][j]), bfhi(g1[j]), a1);
          }
          a[i] = fmaf(a1, m1, a0);
          if (i == 3) { SBAR(); BAR(); SBAR(); }
        }
        SBAR(); BAR(); SBAR();
#pragma unroll
        for (int i = 0; i < 4; i++) { const float lo = a[i], hi = a[i + 4]; const bool up = lane & 4; a[i] = (up ? hi : lo) + __shfl_xor(up ? lo : hi, 4); }
#pragma unroll
        for (int i = 0; i < 2; i++) { const float lo = a[i], hi = a[i + 2]; const bool up = lane & 2; a[i] = (up ? hi : lo) + __shfl_xor(up ? lo : hi, 2); }
        { const float lo = a[0], hi = a[1]; const bool up = lane & 1; a[0] = (up ? hi : lo) + __shfl_xor(up ? lo : hi, 1); }
        a[0] += __shfl_xor(a[0], 8); a[0] += __shfl_xor(a[0], 16); a[0] += __shfl_xor(a[0], 32);
        if (lane < 8) __builtin_amdgcn_raw_buffer_store_b32(__float_as_uint(rbf(1.f / (1.f + __expf(-rbf(a[0]))))), rw, OFF_G + (row0 + lane) * 4, 0, SC1);
        PUT_TAG();
        asm volatile("" :: "v"(pf[0]), "v"(pf[1]), "v"(pf[2]), "v"(pf[3]), "v"(pf[4]));
        SBAR();
        BAR(); BAR(); BAR(); BAR(); BAR();
      } else {
        const int hw = (xcd * 8 + (r - 16)) * 2 + w6;
        const unsigned scr = lb + L_T + w6 * 6144;
#pragma unroll
        for (int bt = 0; bt < 2; bt++) {
          const int blk = bt == 0 ? hw : (hw < 32 ? 128 + hw : hw), ct = (blk * 3277) >> 16, ks = blk - 20 * ct;
          u32x4 v[5];
#pragma unroll
          for (int i = 0; i < 5; i++) {
            const int c = i * 64 + lane, row = (c * 6554) >> 16, ch = c - 10 * row;
            v[i] = *(const u32x4*)(wo + (size_t)(32 * ks + row) * D + 80 * ct + 8 * ch);
          }
          SBAR();
          BAR();
          if (bt == 0) PUT_TAG();
          BAR();
          if (bt == 0 || hw < 32) {
#pragma unroll
            for (int i = 0; i < 5; i++) *(LAS u32x4*)(size_t)(scr + (i * 64 + lane) * 16) = v[i];
            const unsigned ta = scr + (8 * g4 + (fr >> 2)) * 160 + (lane & 3) * 8;
#pragma unroll
            for (int t = 0; t < 5; t++) {
              const u32x2 lo = lds_tr(ta + 32 * t), hi = lds_tr(ta + 32 * t + 640);
              __builtin_amdgcn_raw_buffer_store_b128((u32x4){lo.x, lo.y, hi.x, hi.y}, rw, OFF_WOP + (blk * 5 + t) * 1024 + lane * 16, 0, SC1);
            }
          }
          SBAR();
          BAR(); BAR();
        }
        asm volatile("" :: "v"(pf[0]), "v"(pf[1]), "v"(pf[2]), "v"(pf[3]), "v"(pf[4]));
        DRAIN();
        if (lds_last(lb + L_CNT + 8, lane, 2) && lane == 0) flag_st(ws, F_W + (xcd * 8 + (r - 16)) * 128, TAG);
        BAR(); BAR();
      }
    }
    TS(1);
    BAR();
    TS(11);
    const unsigned tag = TAG;
    if (isKV) {
      if (w < 4) {
#pragma unroll
        for (int t = 0; t < 5; t++) {
          const bool isk = hf ? t < 2 : t < 3;
          const int slot = hf ? (t < 2 ? 3 + t : 5 + t) : (t < 3 ? t : 2 + t);
#pragma unroll
          for (int m = 0; m < 2; m++) {
            u32x4 f;
            if (isk) f = (u32x4){pk2(phi(acc[2 * m][t][0]), phi(acc[2 * m][t][1])), pk2(phi(acc[2 * m][t][2]), phi(acc[2 * m][t][3])),
                                 pk2(phi(acc[2 * m + 1][t][0]), phi(acc[2 * m + 1][t][1])), pk2(phi(acc[2 * m + 1][t][2]), phi(acc[2 * m + 1][t][3]))};
            else f = (u32x4){pk2(acc[2 * m][t][0], acc[2 * m][t][1]), pk2(acc[2 * m][t][2], acc[2 * m][t][3]),
                             pk2(acc[2 * m + 1][t][0], acc[2 * m + 1][t][1]), pk2(acc[2 * m + 1][t][2], acc[2 * m + 1][t][3])};
            *(LAS u32x4*)(size_t)(lb + ((rp * 2 + m) * 10 + slot) * 1024 + lane * 16) = f;
          }
        }
      } else {
        for (int i = tid - 256; i < 6048 / 16; i += 256) *(LAS u32x4*)(size_t)(lb + L_T + i * 16) = (u32x4){0, 0, 0, 0};
        if (w == 7) poll(ws, F_W + lane * 128, true, tag);
      }
      TS(12);
      BAR();
      TS(2);
      switch (w) {
        case 0: stat_tiles<0>(lb, lane, rw, OFF_P + (rt * 8 + cg) * 3360); break;
        case 1: stat_tiles<1>(lb, lane, rw, OFF_P + (rt * 8 + cg) * 3360); break;
        case 2: stat_tiles<2>(lb, lane, rw, OFF_P + (rt * 8 + cg) * 3360); break;
        case 3: stat_tiles<3>(lb, lane, rw, OFF_P + (rt * 8 + cg) * 3360); break;
        case 4: stat_tiles<4>(lb, lane, rw, OFF_P + (rt * 8 + cg) * 3360); break;
        case 5: stat_tiles<5>(lb, lane, rw, OFF_P + (rt * 8 + cg) * 3360); break;
        case 6: stat_tiles<6>(lb, lane, rw, OFF_P + (rt * 8 + cg) * 3360); break;
        default: stat_tiles<7>(lb, lane, rw, OFF_P + (rt * 8 + cg) * 3360); break;
      }
      TS(14);
      DRAIN();
      TS(15);
      BAR();
      TS(3);
      if (w == 0) {
        if (lane == 0) flag_st(ws, F_P + (rt * 8 + cg) * 128, tag);
        poll(ws, lane < 16 ? F_P + (lane * 8 + cg) * 128 : F_PQ + (rt * 4 + (cg >> 1)) * 128, lane <= 16, tag);
      } else if (w < 7) {
        const i32x4 sws = make_srd(ws, WS_USED);
        const unsigned vwv = OFF_WOP + ((xcd >> 2) * 4 + (r >> 3)) * 102400 + lane * 16;
        for (int i = w - 1; i < 100; i += 6) {
          int pc = 12 * (r & 7) + i; pc = __builtin_amdgcn_readfirstlane(pc >= 100 ? pc - 100 : pc);
          glds16_sc1(lb + pc * 1024, vwv, sws, pc * 1024);
        }
      }
      constexpr int PJ[9] = {0, 1, 1, 2, 2, 3, 3, 4, 4}, PK[9] = {0, 0, 1, 0, 1, 1, 2, 1, 2};
      unsigned an[9], ad[9];
#pragma unroll
      for (int p = 0; p < 9; p++) {
        const int E = 16 * PJ[p] + fr, h = (E * 205) >> 12, e = E - 20 * h;
        int o_ = 32 * PK[p] + 8 * g4 - 20 * h; o_ = o_ < -8 ? -8 : o_ > 20 ? 20 : o_;
        an[p] = lb + L_T + (h * 21 + e) * 72 + (8 + o_) * 2; ad[p] = lb + L_T + (h * 21 + 20) * 72 + (8 + o_) * 2;
      }
      BAR();
      TS(4);
      bf16x8 Bq[3];
      {
        const unsigned prow = OFF_PQ + ((rt * 8 + w) * 8 + cg) * 3072 + lane * 16;
#pragma unroll
        for (int ks = 0; ks < 3; ks++) Bq[ks] = __builtin_bit_cast(bf16x8, __builtin_amdgcn_raw_buffer_load_b128(rw, prow + 1024 * ks, 0, SC1));
      }
      if (tid < 210) {
        const unsigned pb = OFF_P + cg * 3360 + tid * 16;
        u32x4 v[16];
#pragma unroll
        for (int k = 0; k < 16; k++) v[k] = __builtin_amdgcn_raw_buffer_load_b128(rw, pb, (k ^ rt) * (8 * 3360), SC1);
        unsigned o4[4];
#pragma unroll
        for (int c = 0; c < 4; c++) {
          float lo[16], hi[16];
#pragma unroll
          for (int k = 0; k < 16; k++) { lo[k] = bflo(v[k][c]); hi[k] = bfhi(v[k][c]); }
#pragma unroll
          for (int st = 1; st < 16; st <<= 1)
#pragma unroll
            for (int k = 0; k < 16; k += 2 * st) { lo[k] += lo[k + st]; hi[k] += hi[k + st]; }
          o4[c] = pk2(lo[0], hi[0]);
        }
        TS(16);
#pragma unroll
        for (int q = 0; q < 2; q++) {
          const int i4 = 2 * tid + q, row = (i4 * 13108) >> 16, d = 4 * (i4 - row * 5);
          *(LAS u32x2*)(size_t)(lb + L_T + row * 72 + (8 + d) * 2) = (u32x2){o4[2 * q], o4[2 * q + 1]};
        }
      }
      BAR();
      TS(5);
      {
        if (g4 >= 2) Bq[2] = __builtin_bit_cast(bf16x8, (u32x4){0, 0, 0, 0});
        f32x4 nu[5], de[5];
#pragma unroll
        for (int j = 0; j < 5; j++) { nu[j] = (f32x4){0.f, 0.f, 0.f, 0.f}; de[j] = nu[j]; }
        bf16x8 An[9], Ad[9];
#pragma unroll
        for (int p = 0; p < 9; p++) {
          unsigned an8 = an[p] + 8, ad8 = ad[p] + 8;
          asm volatile("" : "+v"(an8), "+v"(ad8));
          An[p] = mk8(lds_b64(an[p]), lds_b64(an8)); Ad[p] = mk8(lds_b64(ad[p]), lds_b64(ad8));
        }
#pragma unroll
        for (int p = 0; p < 9; p++) { de[PJ[p]] = mfma16(Ad[p], Bq[PK[p]], de[PJ[p]]); nu[PJ[p]] = mfma16(An[p], Bq[PK[p]], nu[PJ[p]]); }
        u32x2 ov[5];
#pragma unroll
        for (int j = 0; j < 5; j++) {
          const float rd = __builtin_amdgcn_rcpf(de[j][0] + 1e-6f);
          ov[j] = (u32x2){pk2(nu[j][0] * rd, nu[j][1] * rd), pk2(nu[j][2] * rd, nu[j][3] * rd)};
        }
        store_frags(ov, rw, OFF_O + (rt * 8 + w) * 20480, 80 * cg, fr, g4);
      }
      TS(17);
      DRAIN();
      BAR();
      if (tid == 0) flag_st(ws, F_O + (rt * 8 + cg) * 128, tag);
      TS(6);
    } else {
      const int vb = xcd * 8 + (r - 16);
      const int d = 80 * (vb >> 3) + 48 + 4 * (vb & 7) + (w & 3);
      const unsigned cb = lb + (w & 3) * CHS;
      if (w < 4) {
#pragma unroll
        for (int i = 0; i < 4; i++) {
          u32x2 pv[5];
#pragma unroll
          for (int t = 0; t < 5; t++) pv[t] = (u32x2){pk2(phi(acc[i][t][0]), phi(acc[i][t][1])), pk2(phi(acc[i][t][2]), phi(acc[i][t][3]))};
          store_frags(pv, rw, OFF_PQ + ((rt * 8 + rp * 4 + i) * 8 + cg * 2 + hf) * 3072, 0, fr, g4);
        }
      } else {
        u32x4 hc[4], hp[4];
        conv_load_h(hc, hp, hk + (size_t)d * S, lane);
        poll(ws, F_U + lane * 128, true, tag);
        conv_load_u(rw, d, cb, lane, [&]() { conv_prep_h(hc, hp, cb, lane); });
      }
      TS(12);
      DRAIN();
      BAR();
      TS(2);
      if (tid == 0) flag_st(ws, F_PQ + (rt * 4 + cg) * 128, tag);
      if (w < 4) {
        const float Dd = bflo(Dv[d]);
        TS(3);
        f32x4 y[8];
        conv_main(y, cb, lane, 0, 8);
        TS(4);
#pragma unroll
        for (int c = 0; c < 8; c++) conv_store(y[c], cb, c, lane, Dd, rw, d);
        DRAIN();
      } else if (w == 7) {
        poll(ws, F_W + lane * 128, true, tag);
      }
      BAR();
      if (tid == 0) flag_st(ws, F_Y + (64 + vb) * 128, tag);
      TS(5); TS(6);
    }
  } else {
    const int pw = xcd * 8 + (r - 24);
    const int c6 = w < 4 ? w : 4 + ((w - 4) >> 1);
    const int d = 80 * (pw >> 3) + 6 * (pw & 7) + c6;
    const unsigned cb = lb + c6 * CHS;
    const bool prep = w < 4 || !(w & 1);
    u32x4 hc[4], hp[4];
    {
      const int rbase = xcd * 256 + ((r - 24) >> 1) * 64, cbase = ((r - 24) & 1) * 320;
      u32x4 kr[2][7], vr[2][4], sp[2][4];
#pragma unroll
      for (int q = 0; q < 2; q++) {
        const int it = w + 8 * q;
        if (q == 0 || w < 2) {
          const int ch = cbase + (it >> 1) * 64 + (lane & 7) * 8;
          const int s0 = rbase + (it & 1) * 32 + (lane >> 3) * 4;
#pragma unroll
          for (int j = 0; j < 7; j++) { int s = s0 - 3 + j; kr[q][j] = *(const u32x4*)(Bk + (size_t)(s < 0 ? 0 : s) * D + ch); }
#pragma unroll
          for (int j = 0; j < 4; j++) vr[q][j] = *(const u32x4*)(Bv + (size_t)(s0 + j) * D + ch);
#pragma unroll
          for (int j = 0; j < 4; j++) sp[q][j] = ((const u32x4*)(shk + ch * 4))[j];
        }
      }
      if (prep) conv_load_h(hc, hp, hk + (size_t)d * S, lane);
      if (w == 7) e0 = flag_ld(ws, F_E);
      SBAR();
#pragma unroll
      for (int q = 0; q < 2; q++) {
        const int it = w + 8 * q;
        if (q == 0 || w < 2) {
          const int s0 = rbase + (it & 1) * 32 + (lane >> 3) * 4;
          if (s0 == 0) { kr[q][0] = (u32x4){0, 0, 0, 0}; kr[q][1] = kr[q][0]; kr[q][2] = kr[q][0]; }
          const int rg = (it & 1) * 8 + (lane >> 3);
          const unsigned la = lb + L_UIMG + ((it >> 1) * 64 + (lane & 7) * 8) * 128 + (((rg >> 1) ^ (lane & 7)) << 4) + (rg & 1) * 8;
#pragma unroll
          for (int c = 0; c < 8; c++) {
            const unsigned s01 = sp[q][c >> 1][(c & 1) * 2], s23 = sp[q][c >> 1][(c & 1) * 2 + 1];
            const float w0 = bflo(s01), w1 = bfhi(s01), w2 = bflo(s23), w3 = bfhi(s23);
            float kk[7], u[4];
#pragma unroll
            for (int j = 0; j < 7; j++) { unsigned dd = kr[q][j][c >> 1]; kk[j] = (c & 1) ? bfhi(dd) : bflo(dd); }
#pragma unroll
            for (int j = 0; j < 4; j++) {
              unsigned dd = vr[q][j][c >> 1]; float v = (c & 1) ? bfhi(dd) : bflo(dd);
              float ks = kk[j] * w3; ks = fmaf(kk[j + 1], w2, ks); ks = fmaf(kk[j + 2], w1, ks); ks = fmaf(kk[j + 3], w0, ks);
              u[j] = ks * v;
            }
            *(LAS u32x2*)(size_t)(la + c * 128) = (u32x2){pk2(u[0], u[1]), pk2(u[2], u[3])};
          }
        }
      }
      PUT_TAG();
      BAR();
#pragma unroll
      for (int i = 0; i < 5; i++) {
        const int chl = (w * 5 + i) * 8 + (lane >> 3), k = lane & 7;
        const u32x4 v = *(const LAS u32x4*)(size_t)(lb + L_UIMG + chl * 128 + ((k ^ ((chl >> 3) & 7)) << 4));
        __builtin_amdgcn_raw_buffer_store_b128(v, rw, OFF_UT + (cbase + chl) * 4096 + rbase * 2 + k * 16, 0, SC1);
      }
      TS(1);
    }
    if (w < 5 && prep) conv_prep_h(hc, hp, cb, lane);
    DRAIN();
    BAR();
    TS(2);
    const unsigned tag = TAG;
    if (tid == 0) flag_st(ws, F_U + pw * 128, tag);
    const float Dd = bflo(Dv[d]);
    if (prep) {
      if (w == 6) conv_prep_h(hc, hp, cb, lane);
      poll(ws, F_U + lane * 128, true, tag);
      conv_load_u(rw, d, cb, lane, [&]() {});
    }
    BAR();
    TS(3);
    f32x4 y[8];
    conv_main(y, cb, lane, w < 4 ? 0 : (w & 1) * 4, w < 4 ? 8 : (w & 1) * 4 + 4);
    TS(4);
    if (w < 4) {
#pragma unroll
      for (int c = 0; c < 8; c++) conv_store(y[c], cb, c, lane, Dd, rw, d);
    }
    BAR();
    if (w >= 4) {
#pragma unroll
      for (int c = 0; c < 4; c++) { const f32x4 lo = y[c], hi = y[4 + c]; *(LAS f32x4*)(size_t)(cb + ((w & 1) * 4 + c) * 1024 + lane * 16) = (w & 1) ? lo : hi; }
    }
    BAR();
    if (w >= 4) {
#pragma unroll
      for (int c = 0; c < 4; c++) {
        const f32x4 o_ = *(const LAS f32x4*)(size_t)(cb + (((w & 1) ^ 1) * 4 + c) * 1024 + lane * 16);
        const f32x4 lo = y[c], hi = y[4 + c];
        const f32x4 mine = (w & 1) ? hi : lo;
        conv_store((w & 1) ? o_ + mine : mine + o_, cb, (w & 1) * 4 + c, lane, Dd, rw, d);
      }
    }
    if (w == 0) poll(ws, F_W + lane * 128, true, tag);
    DRAIN();
    BAR();
    if (tid == 0) flag_st(ws, F_Y + pw * 128, tag);
    TS(5); TS(6);
  }

  {
    const unsigned tag = TAG;
    const int rt = (xcd & 3) * 8 + (r & 7), ct = (xcd >> 2) * 4 + (r >> 3);
    const int sw_ = w & 3, kk = w >> 2;
    const i32x4 sws = make_srd(ws, WS_USED);
    TS(18);
    const int srow = rt * 64 + sw_ * 16 + fr;
    u32x2 xv[3];
#pragma unroll
    for (int t = 0; t < 3; t++) { const int tt = kk ? (t < 2 ? 3 + t : 4) : t; xv[t] = *(const u32x2*)(x + (size_t)srow * D + ct * 80 + 16 * tt + 4 * g4); }
    SBAR();
    if (w == 0) {
      poll(ws, F_O + ((rt >> 1) * 8 + (lane & 7)) * 128, lane < 8, tag);
    } else if (w == 7) {
      poll(ws, F_Y + ((lane & 8 ? 64 : 0) + ct * 8 + (lane & 7)) * 128, lane < 16, tag);
    } else {
      if (r >= 16) {
        const unsigned vwv = OFF_WOP + ct * 102400 + lane * 16;
        for (int i = w - 1; i < 100; i += 6) { int pc = 12 * (r & 7) + i; pc = __builtin_amdgcn_readfirstlane(pc >= 100 ? pc - 100 : pc); glds16_sc1(lb + pc * 1024, vwv, sws, pc * 1024); }
      }
      if (w == 6 && b == 0) {
        poll(ws, F_U + lane * 128, true, tag); poll(ws, F_PQ + lane * 128, true, tag);
        poll(ws, F_P + lane * 128, true, tag); poll(ws, F_P + (64 + lane) * 128, true, tag);
        if (lane == 0) flag_st(ws, F_E, tag);
      }
      DRAIN();
    }
    BAR();
    TS(7);
    asm volatile("" :: "v"(xv[0]), "v"(xv[1]), "v"(xv[2]));
    bf16x8 of[10];
    const int rot = ((ct & 3) * 5) >> 1;
    {
      const unsigned vo = OFF_O + (rt * 4 + sw_) * 20480 + kk * 1024 + lane * 16;
#pragma unroll
      for (int s = 0; s < 10; s++) { int q = s + rot; q = __builtin_amdgcn_readfirstlane(q >= 10 ? q - 10 : q); bload16_sc1(of[s], vo, sws, q * 2048); }
    }
    if (w == 7) {
      for (int pc = 0; pc < 10; pc++)
        glds16_sc1(lb + L_Y + pc * 1024, OFF_YT + (ct * 80 + pc * 8 + (lane >> 3)) * 4096 + rt * 128 + (lane & 7) * 16, sws, 0);
    }
    float g;
    asm volatile("buffer_load_dword %0, %1, %2, 0 offen sc1" : "=v"(g) : "v"(OFF_G + srow * 4), "s"(sws) : "memory");
    TS(8);
    f32x4 acc[5];
#pragma unroll
    for (int t = 0; t < 5; t++) acc[t] = (f32x4){0.f, 0.f, 0.f, 0.f};
    const unsigned wa0 = lb + kk * 5120 + lane * 16;
#pragma unroll
    for (int hh = 0; hh < 2; hh++) {
      bf16x8 wf[5][5];
#pragma unroll
      for (int s = 0; s < 5; s++) {
        int q = hh * 5 + s + rot; q = q >= 10 ? q - 10 : q;
        const unsigned wa = wa0 + q * 10240;
#pragma unroll
        for (int t = 0; t < 5; t++) wf[s][t] = lds_b128(wa + t * 1024);
      }
      if (hh == 0) {
        if (w == 7) asm volatile("s_waitcnt vmcnt(16)" ::: "memory"); else asm volatile("s_waitcnt vmcnt(6)" ::: "memory");
        asm volatile("" : "+v"(of[0]), "+v"(of[1]), "+v"(of[2]), "+v"(of[3]), "+v"(of[4]) :: "memory");
      } else {
        if (w == 7) asm volatile("s_waitcnt vmcnt(11)" ::: "memory"); else asm volatile("s_waitcnt vmcnt(1)" ::: "memory");
        asm volatile("" : "+v"(of[5]), "+v"(of[6]), "+v"(of[7]), "+v"(of[8]), "+v"(of[9]) :: "memory");
      }
      SBAR();
#pragma unroll
      for (int s = 0; s < 5; s++)
#pragma unroll
        for (int t = 0; t < 5; t++) acc[t] = mfma16(wf[s][t], of[hh * 5 + s], acc[t]);
      SBAR();
    }
    TS(9);
    const unsigned ra = lb + L_RED + (sw_ * 5) * 1024 + lane * 16;
    if (kk == 0) { *(LAS f32x4*)(size_t)(ra + 3 * 1024) = acc[3]; *(LAS f32x4*)(size_t)(ra + 4 * 1024) = acc[4]; }
    else { *(LAS f32x4*)(size_t)(ra) = acc[0]; *(LAS f32x4*)(size_t)(ra + 1024) = acc[1]; *(LAS f32x4*)(size_t)(ra + 2048) = acc[2]; }
    asm volatile("s_waitcnt vmcnt(0)" : "+v"(g) :: "memory");
    BAR();
    TS(20);
    const float omg = 1.f - g;
    const int nt = kk ? 2 : 3;
#pragma unroll
    for (int t = 0; t < 3; t++) {
      if (t < nt) {
        const int tt = kk ? 3 + t : t;
        const f32x4 a0 = acc[t], a1 = acc[3 + (t < 2 ? t : 1)];
        f32x4 a = *(const LAS f32x4*)(size_t)(ra + tt * 1024) + (kk ? a1 : a0);
        u32x2 yv = lds_tr(lb + L_Y + (16 * tt + 4 * g4 + (fr >> 2)) * 128 + (sw_ * 16 + 4 * (fr & 3)) * 2);
        float xs[4] = {bflo(xv[t].x), bfhi(xv[t].x), bflo(xv[t].y), bfhi(xv[t].y)};
        float ys[4] = {bflo(yv.x), bfhi(yv.x), bflo(yv.y), bfhi(yv.y)};
        float r_[4];
#pragma unroll
        for (int q = 0; q < 4; q++) r_[q] = fmaf(g, a[q], omg * (xs[q] * ys[q]));
        *(u32x2*)(out + (size_t)srow * D + ct * 80 + 16 * tt + 4 * g4) = (u32x2){pk2(r_[0], r_[1]), pk2(r_[2], r_[3])};
      }
    }
    TS(10); TS_OUT(21);
  }
}

extern "C" void kernel_launch(void* const* d_in, const int* in_sizes, int n_in, void* d_out, int out_size, void* d_ws, size_t ws_size,
                              hipStream_t stream) {
  const u16* x = (const u16*)d_in[0]; const u16* wqkv = (const u16*)d_in[1]; const u16* wo = (const u16*)d_in[2];
  const u16* Bk = (const u16*)d_in[4]; const u16* Bv = (const u16*)d_in[5]; const u16* shk = (const u16*)d_in[6];
  const u16* hk = (const u16*)d_in[7]; const u16* Dv = (const u16*)d_in[8]; const u16* gw = (const u16*)d_in[9];
  (void)hipFuncSetAttribute((const void*)fused, hipFuncAttributeMaxDynamicSharedMemorySize, LDS_TOTAL);
  fused<<<256, 512, LDS_TOTAL, stream>>>(x, wqkv, wo, Bk, Bv, shk, hk, Dv, gw, (u16*)d_out, (char*)d_ws);
}
```

```cpp
#include <hip/hip_runtime.h>
#include <hip/hip_bf16.h>
#include <type_traits>
#define CODE_PF 41984
#define NO_PF

typedef unsigned short u16;
typedef __bf16 bf16x2 __attribute__((ext_vector_type(2)));
typedef __bf16 bf16x8 __attribute__((ext_vector_type(8)));
typedef float f32x4 __attribute__((ext_vector_type(4)));
typedef unsigned u32x4 __attribute__((ext_vector_type(4)));
typedef unsigned u32x2 __attribute__((ext_vector_type(2)));
typedef short v4i16_t __attribute__((ext_vector_type(4)));
typedef int i32x4 __attribute__((ext_vector_type(4)));
typedef __attribute__((address_space(1))) unsigned gu32;
#define LAS __attribute__((address_space(3)))
#define SBAR() __builtin_amdgcn_sched_barrier(0)
#define SGB(mask, n) __builtin_amdgcn_sched_group_barrier(mask, n, 0)
#define BAR() asm volatile("s_waitcnt lgkmcnt(0)\n\ts_barrier" ::: "memory")
#define DRAIN() asm volatile("s_waitcnt vmcnt(0)" ::: "memory")
#define SC1 16

#define TS_DECL
#define TSARG
#define TSPAR
#define TS(i)
#define TS_OUT(n)

constexpr int S = 2048, D = 640;
constexpr unsigned SZ = S * D * 2;
constexpr unsigned F_E = 0, F_U = 128, F_PQ = F_U + 64 * 128, F_P = F_PQ + 64 * 128, F_O = F_P + 128 * 128, F_Y = F_O + 128 * 128, F_W = F_Y + 128 * 128;
constexpr unsigned OFF_PQ = 0x20000, OFF_P = OFF_PQ + 128 * 8 * 3 * 1024, OFF_UT = OFF_P + (1u << 19), OFF_G = OFF_UT + SZ, OFF_WOP = OFF_G + 8192,
                   OFF_O = OFF_WOP + (1u << 20), OFF_YT = OFF_O + SZ, WS_USED = OFF_YT + SZ;
constexpr int XS = 16384, WSB = 20480, STG = XS + WSB;
constexpr int L_EXTRA = 4 * STG;
constexpr int L_TAG = L_EXTRA, L_CNT = L_EXTRA + 16, L_T = L_EXTRA + 64;
constexpr int L_SCR = L_T + 6144 + 5120;
constexpr int LDS_TOTAL = L_SCR + 256;
constexpr int CPS = 4160, UOFF = 4 * CPS, CHS = UOFF + 4608;
constexpr int L_UIMG = 5 * CHS;
constexpr int L_Y = 102400, L_RED = L_Y + 10240;

__device__ __forceinline__ unsigned pk2(float a, float b) { bf16x2 r = {(__bf16)a, (__bf16)b}; return __builtin_bit_cast(unsigned, r); }
__device__ __forceinline__ float rbf(float a) { return (float)(__bf16)a; }
__device__ __forceinline__ float bflo(unsigned v) { return __uint_as_float(v << 16); }
__device__ __forceinline__ float bfhi(unsigned v) { return __uint_as_float(v & 0xffff0000u); }
__device__ __forceinline__ float phi(float t) {
  return __builtin_amdgcn_fmed3f(t, 0.f, 3.0e38f) + (__builtin_amdgcn_fmed3f(__builtin_amdgcn_exp2f(t * 1.44269504f), 0.f, 1.f) + 1e-6f);
}
__device__ __forceinline__ i32x4 make_srd(const void* p, unsigned bytes) {
  unsigned long long a = (unsigned long long)p; i32x4 r;
  r.x = __builtin_amdgcn_readfirstlane((int)(unsigned)a); r.y = __builtin_amdgcn_readfirstlane((int)(unsigned)(a >> 32));
  r.z = __builtin_amdgcn_readfirstlane((int)bytes); r.w = 0x00020000; return r;
}
__device__ __forceinline__ void glds16(unsigned ldsaddr, unsigned voff, i32x4 srd, unsigned soff) {
  asm volatile("s_mov_b32 m0, %0\n\ts_nop 0\n\tbuffer_load_dwordx4 %1, %2, %3 offen lds" :: "s"(ldsaddr), "v"(voff), "s"(srd), "s"(soff) : "memory");
}
__device__ __forceinline__ void glds16_sc1(unsigned ldsaddr, unsigned voff, i32x4 srd, unsigned soff) {
  asm volatile("s_mov_b32 m0, %0\n\ts_nop 0\n\tbuffer_load_dwordx4 %1, %2, %3 offen sc1 lds" :: "s"(ldsaddr), "v"(voff), "s"(srd), "s"(soff) : "memory");
}
__device__ __forceinline__ void bload16_sc1(bf16x8& dst, unsigned voff, i32x4 srd, unsigned soff) {
  asm volatile("buffer_load_dwordx4 %0, %1, %2, %3 offen sc1" : "=v"(dst) : "v"(voff), "s"(srd), "s"(soff) : "memory");
}
template <int N> __device__ __forceinline__ void wait_bar() { asm volatile("s_waitcnt vmcnt(%0) lgkmcnt(0)\n\ts_barrier" :: "n"(N) : "memory"); }
__device__ __forceinline__ bf16x8 lds_b128(unsigned addr) { return *(const LAS bf16x8*)(size_t)addr; }
__device__ __forceinline__ u32x2 lds_b64(unsigned addr) { return *(const LAS u32x2*)(size_t)addr; }
__device__ __forceinline__ u32x2 lds_tr(unsigned addr) { return __builtin_bit_cast(u32x2, __builtin_amdgcn_ds_read_tr16_b64_v4i16((LAS v4i16_t*)(size_t)addr)); }
__device__ __forceinline__ bf16x8 mk8(u32x2 lo, u32x2 hi) { u32x4 t = {lo.x, lo.y, hi.x, hi.y}; return __builtin_bit_cast(bf16x8, t); }
__device__ __forceinline__ f32x4 mfma16(bf16x8 a, bf16x8 b, f32x4 c) { return __builtin_amdgcn_mfma_f32_16x16x32_bf16(a, b, c, 0, 0, 0); }

__device__ __forceinline__ u32x4 pair16(u32x2 a, u32x2 b) {
  const auto r0 = __builtin_amdgcn_permlane16_swap(a.x, b.x, false, false);
  const auto r1 = __builtin_amdgcn_permlane16_swap(a.y, b.y, false, false);
  return (u32x4){r0[0], r1[0], r0[1], r1[1]};
}
__device__ __forceinline__ void store_frags(const u32x2 (&v)[5], __amdgpu_buffer_rsrc_t rw, unsigned base, int k0, int fr, int g4) {
#pragma unroll
  for (int j = 0; j < 4; j += 2) {
    const int k = k0 + 16 * (j + (g4 & 1)) + 8 * (g4 >> 1);
    __builtin_amdgcn_raw_buffer_store_b128(pair16(v[j], v[j + 1]), rw, base + (k >> 5) * 1024 + (((k >> 3) & 3) * 16 + fr) * 16, 0, SC1);
  }
  const int k = k0 + 64 + 4 * g4;
  __builtin_amdgcn_raw_buffer_store_b64(v[4], rw, base + (k >> 5) * 1024 + (((k >> 3) & 3) * 16 + fr) * 16 + ((k >> 2) & 1) * 8, 0, SC1);
}

__device__ __forceinline__ void lds_sync(unsigned cnt_addr, int lane, unsigned n) {
  asm volatile("s_waitcnt lgkmcnt(0)" ::: "memory");
  if (lane == 0) __hip_atomic_fetch_add((LAS unsigned*)(size_t)cnt_addr, 1u, __ATOMIC_RELAXED, __HIP_MEMORY_SCOPE_WORKGROUP);
  while (__builtin_amdgcn_readfirstlane(__hip_atomic_load((LAS unsigned*)(size_t)cnt_addr, __ATOMIC_RELAXED, __HIP_MEMORY_SCOPE_WORKGROUP)) < n) {}
  asm volatile("" ::: "memory");
}
__device__ __forceinline__ bool lds_last(unsigned cnt_addr, int lane, unsigned n) {
  unsigned old = 0;
  if (lane == 0) old = __hip_atomic_fetch_add((LAS unsigned*)(size_t)cnt_addr, 1u, __ATOMIC_RELAXED, __HIP_MEMORY_SCOPE_WORKGROUP);
  return __builtin_amdgcn_readfirstlane(old) == n - 1;
}

__device__ __forceinline__ unsigned flag_ld(const char* ws, unsigned off) { return __hip_atomic_load((const gu32*)(ws + off), __ATOMIC_RELAXED, __HIP_MEMORY_SCOPE_AGENT); }
__device__ __forceinline__ void flag_st(char* ws, unsigned off, unsigned v) { __hip_atomic_store((gu32*)(ws + off), v, __ATOMIC_RELAXED, __HIP_MEMORY_SCOPE_AGENT); }
__device__ __forceinline__ void poll(const char* ws, unsigned off, bool act, unsigned tag) {
  const unsigned o0 = __builtin_amdgcn_readfirstlane(off);
  off = act ? off : o0;
  unsigned a, b, n = 1u << 17;
  asm volatile(
      "global_load_dword %0, %3, %4 sc1\n\t"
      "s_sleep 6\n"
      "1:\n\t"
      "global_load_dword %1, %3, %4 sc1\n\t"
      "s_waitcnt vmcnt(1)\n\t"
      "v_cmp_ne_u32 vcc, %0, %5\n\t"
      "s_cbranch_vccz 2f\n\t"
      "global_load_dword %0, %3, %4 sc1\n\t"
      "s_waitcnt vmcnt(1)\n\t"
      "v_cmp_ne_u32 vcc, %1, %5\n\t"
      "s_cbranch_vccz 2f\n\t"
      "s_sub_u32 %2, %2, 1\n\t"
      "s_cbranch_scc0 1b\n"
      "2:\n\t"
      "s_waitcnt vmcnt(0)"
      : "=&v"(a), "=&v"(b), "+s"(n) : "v"(off), "s"(ws), "v"(tag) : "vcc", "scc", "memory");
}

template <bool KV>
__device__ __forceinline__ void gemm_compute(f32x4 (&acc)[4][5], unsigned xa0, unsigned wa0, unsigned wa1 TSPAR) {
  bf16x8 xa[4], xb[4], wa[5], wb[5];
  auto load = [&](bf16x8 (&xf)[4], bf16x8 (&wf)[5], int buf, int kk) {
    const unsigned bo = buf * STG;
    const unsigned xa = (xa0 + bo) ^ (kk * 64);
    const unsigned a0 = wa0 + bo + kk * 10240, a1 = wa1 + bo + kk * 10240;
#pragma unroll
    for (int i = 0; i < 4; i++) xf[i] = lds_b128(xa + 2048 * i);
#pragma unroll
    for (int t = 0; t < 5; t++) { const unsigned a = ((t & 1) ? a1 : a0) + 32 * t; wf[t] = mk8(lds_tr(a), lds_tr(a + 1280)); }
  };
  auto mma = [&](const bf16x8 (&xf)[4], const bf16x8 (&wf)[5]) {
#pragma unroll
    for (int t = 0; t < 5; t++)
#pragma unroll
      for (int i = 0; i < 4; i++) acc[i][t] = KV ? mfma16(xf[i], wf[t], acc[i][t]) : mfma16(wf[t], xf[i], acc[i][t]);
  };
  auto interleave = [&]() {
#pragma unroll
    for (int t = 0; t < 14; t++) { SGB(0x8, 1); SGB(0x100, 1); }
    SGB(0x8, 6);
  };
  BAR();
  TS(21);
  load(xa, wa, 0, 0);
  SBAR();
  for (int s = 0; s < 9; s++) {
    load(xb, wb, s & 3, 1); mma(xa, wa); interleave();
    SBAR();
    BAR();
    if (s == 0) TS(22);
    if (s == 4) TS(23);
    load(xa, wa, (s + 1) & 3, 0); mma(xb, wb); interleave();
    SBAR();
  }
  load(xb, wb, 1, 1); mma(xa, wa); interleave();
  SBAR();
  mma(xb, wb);
}

__device__ __forceinline__ void gemm_loader(int L, int lane, unsigned lb, const u16* x, const u16* wqkv, int rt, int cg, bool isKV) {
  const int s0 = (((rt & 3) * 5 + 1) / 2 + (isKV ? 2 * (cg & 3) : 8 + (cg & 1))) % 10;
  const i32x4 sx = make_srd(x, S * D * 2), sw = make_srd(wqkv, D * 3 * D * 2);
  unsigned vx[2], vw[5];
#pragma unroll
  for (int i = 0; i < 2; i++) vx[i] = (rt * 128 + L * 64 + i * 8 + (lane >> 3)) * 1280 + (((lane & 7) ^ ((4 * i + (lane >> 4)) & 7)) << 4);
#pragma unroll
  for (int i = 0; i < 5; i++) {
    const int ci = i * 64 + lane, k = (ci * 3277) >> 16, pos = ci - k * 20, gc = pos ^ (((k >> 3) & 1) << 1), tl = gc >> 1;
    unsigned col;
    if (isKV) { const int isv = (tl >= 3 && tl < 5) || tl >= 7; const int tt = tl < 3 ? tl : tl < 5 ? tl - 3 : tl < 7 ? tl - 2 : tl - 5;
                col = (isv ? 2560u : 1280u) + 160u * cg + tt * 32; }
    else col = 320u * cg + tl * 32;
    vw[i] = (L * 32 + k) * 3840 + col + (gc & 1) * 16;
  }
  auto stage = [&](int st_, int buf) {
    const unsigned base = lb + buf * STG;
    int st = st_ + s0; st = st >= 10 ? st - 10 : st;
#pragma unroll
    for (int p = 0; p < 10; p++) {
      glds16(base + XS + (L * 10 + p) * 1024, vw[p % 5], sw, st * (64 * 3840) + (p / 5) * (16 * 3840));
      if (p < 8) glds16(base + (L * 8 + p) * 1024, vx[p & 1], sx, st * 128 + (p >> 1) * (16 * 1280));
    }
  };
  stage(0, 0); stage(1, 1);
  wait_bar<18>();
  stage(2, 2);
  for (int s = 0; s < 9; s++) {
    if (s < 8) wait_bar<18>(); else wait_bar<0>();
    if (s < 7) stage(s + 3, (s + 3) & 3);
  }
}

template <int T> struct StatTile {
  static constexpr bool isz = T >= 13;
  static constexpr int i = isz ? T - 13 : (T + 1) / 3;
  static constexpr int j = isz ? 0 : T - (i == 0 ? 0 : 3 * i - 1) + (i == 0 ? 0 : i - 1);
};
template <int T> __device__ __forceinline__ f32x4 stat_mma(unsigned la) {
  const u32x4 o1 = {0x3F803F80u, 0x3F803F80u, 0x3F803F80u, 0x3F803F80u};
  bf16x8 A[4], B[4];
#pragma unroll
  for (int m = 0; m < 4; m++) {
    A[m] = lds_b128(la + StatTile<T>::i * 1024 + m * 10240);
    B[m] = StatTile<T>::isz ? __builtin_bit_cast(bf16x8, o1) : lds_b128(la + (5 + StatTile<T>::j) * 1024 + m * 10240);
  }
  f32x4 d4 = {0.f, 0.f, 0.f, 0.f};
#pragma unroll
  for (int m = 0; m < 4; m++) d4 = mfma16(A[m], B[m], d4);
  return d4;
}
template <int T> __device__ __forceinline__ void stat_store(f32x4 d4, int lane, __amdgpu_buffer_rsrc_t rw, unsigned pbase) {
  const int fr = lane & 15, g4 = lane >> 4;
  const int Dg = 16 * StatTile<T>::i + 4 * g4, h = (Dg * 205) >> 12, dd = Dg - 20 * h;
  const int ee = StatTile<T>::isz ? 20 : 16 * StatTile<T>::j + fr - 20 * h;
  if (StatTile<T>::isz ? fr == 0 : (ee >= 0 && ee < 20))
    __builtin_amdgcn_raw_buffer_store_b64((u32x2){pk2(d4[0], d4[1]), pk2(d4[2], d4[3])}, rw, pbase + (h * 420 + ee * 20 + dd) * 2, 0, SC1);
}
template <int W> __device__ __forceinline__ void stat_tiles(unsigned lb, int lane, __amdgpu_buffer_rsrc_t rw, unsigned pbase) {
  const unsigned la = lb + lane * 16;
  const f32x4 a = stat_mma<W>(la), b = stat_mma<W + 8>(la);
  if constexpr (W < 2) { const f32x4 c = stat_mma<W + 16>(la); stat_store<W + 16>(c, lane, rw, pbase); }
  stat_store<W>(a, lane, rw, pbase);
  stat_store<W + 8>(b, lane, rw, pbase);
}

__device__ __forceinline__ void conv_load_h(u32x4 (&cur)[4], u32x4 (&prv)[4], const u16* __restrict__ hrow, int lane) {
#pragma unroll
  for (int it = 0; it < 4; it++) {
    const int i = it * 64 + lane;
    cur[it] = *(const u32x4*)(hrow + i * 8); prv[it] = *(const u32x4*)(hrow + (i > 0 ? i * 8 - 8 : 0));
  }
}
__device__ __forceinline__ void conv_prep_h(const u32x4 (&cur)[4], const u32x4 (&prv_)[4], unsigned cb, int lane) {
#pragma unroll
  for (int it = 0; it < 4; it++) {
    u32x4 prv = prv_[it];
    if (it == 0 && lane == 0) prv = (u32x4){0, 0, 0, 0};
    const unsigned W_[8] = {prv.x, prv.y, prv.z, prv.w, cur[it].x, cur[it].y, cur[it].z, cur[it].w};
    const int c = 255 - (it * 64 + lane);
#pragma unroll
    for (int e = 0; e < 4; e++) {
      u32x4 ov;
#pragma unroll
      for (int q = 0; q < 4; q++) {
        const int i0 = 15 - e - 2 * q, i1 = i0 - 1;
        const unsigned sel = ((i0 & 1) ? 0x0706u : 0x0504u) | (((i1 & 1) ? 0x0302u : 0x0100u) << 16);
        ov[q] = __builtin_amdgcn_perm(W_[i0 >> 1], W_[i1 >> 1], sel);
      }
      *(LAS u32x4*)(size_t)(cb + e * CPS + c * 16) = ov;
    }
  }
  if (lane < 8) *(LAS u32x4*)(size_t)(cb + (lane >> 1) * CPS + (256 + (lane & 1)) * 16) = (u32x4){0, 0, 0, 0};
  if (lane < 32) *(LAS u32x4*)(size_t)(cb + UOFF + lane * 16) = (u32x4){0, 0, 0, 0};
}
template <class F>
__device__ __forceinline__ void conv_load_u(__amdgpu_buffer_rsrc_t rw, int d, unsigned cb, int lane, F&& between) {
  u32x4 uv[4];
#pragma unroll
  for (int it = 0; it < 4; it++) uv[it] = __builtin_amdgcn_raw_buffer_load_b128(rw, OFF_UT + d * 4096 + (it * 64 + lane) * 16, 0, SC1);
  SBAR();
  between();
  SBAR();
#pragma unroll
  for (int it = 0; it < 4; it++) *(LAS u32x4*)(size_t)(cb + UOFF + 512 + (it * 64 + lane) * 16) = uv[it];
}
struct ConvFrags { u32x2 al[8], ah[8]; u32x4 B[8]; };
__device__ __forceinline__ void conv_read(ConvFrags& f, unsigned aa, unsigned ba) {
  asm volatile(
      "ds_read_b128 %16, %25\n\tds_read_b64 %7, %24 offset:3584\n\tds_read_b64 %15, %24 offset:3592\n\t"
      "ds_read_b128 %17, %25 offset:512\n\tds_read_b128 %18, %25 offset:1024\n\tds_read_b128 %19, %25 offset:1536\n\t"
      "ds_read_b128 %20, %25 offset:2048\n\tds_read_b128 %21, %25 offset:2560\n\tds_read_b128 %22, %25 offset:3072\n\t"
      "ds_read_b128 %23, %25 offset:3584\n\t"
      "ds_read_b64 %6, %24 offset:3072\n\tds_read_b64 %14, %24 offset:3080\n\t"
      "ds_read_b64 %5, %24 offset:2560\n\tds_read_b64 %13, %24 offset:2568\n\t"
      "ds_read_b64 %4, %24 offset:2048\n\tds_read_b64 %12, %24 offset:2056\n\t"
      "ds_read_b64 %3, %24 offset:1536\n\tds_read_b64 %11, %24 offset:1544\n\t"
      "ds_read_b64 %2, %24 offset:1024\n\tds_read_b64 %10, %24 offset:1032\n\t"
      "ds_read_b64 %1, %24 offset:512\n\tds_read_b64 %9, %24 offset:520\n\t"
      "ds_read_b64 %0, %24\n\tds_read_b64 %8, %24 offset:8"
      : "=&v"(f.al[0]), "=&v"(f.al[1]), "=&v"(f.al[2]), "=&v"(f.al[3]), "=&v"(f.al[4]), "=&v"(f.al[5]), "=&v"(f.al[6]), "=&v"(f.al[7]),
        "=&v"(f.ah[0]), "=&v"(f.ah[1]), "=&v"(f.ah[2]), "=&v"(f.ah[3]), "=&v"(f.ah[4]), "=&v"(f.ah[5]), "=&v"(f.ah[6]), "=&v"(f.ah[7]),
        "=&v"(f.B[0]), "=&v"(f.B[1]), "=&v"(f.B[2]), "=&v"(f.B[3]), "=&v"(f.B[4]), "=&v"(f.B[5]), "=&v"(f.B[6]), "=&v"(f.B[7])
      : "v"(aa), "v"(ba) : "memory");
}
__device__ __forceinline__ void conv_wait(ConvFrags& f) {
  asm volatile("s_waitcnt lgkmcnt(0)"
      : "+v"(f.al[0]), "+v"(f.al[1]), "+v"(f.al[2]), "+v"(f.al[3]), "+v"(f.al[4]), "+v"(f.al[5]), "+v"(f.al[6]), "+v"(f.al[7]),
        "+v"(f.ah[0]), "+v"(f.ah[1]), "+v"(f.ah[2]), "+v"(f.ah[3]), "+v"(f.ah[4]), "+v"(f.ah[5]), "+v"(f.ah[6]), "+v"(f.ah[7]),
        "+v"(f.B[0]), "+v"(f.B[1]), "+v"(f.B[2]), "+v"(f.B[3]), "+v"(f.B[4]), "+v"(f.B[5]), "+v"(f.B[6]), "+v"(f.B[7]) :: "memory");
}
template <int K> __device__ __forceinline__ void conv_read1(ConvFrags& f, unsigned aa, unsigned ba) {
  if constexpr (K < 8) asm volatile("ds_read_b128 %0, %1 offset:%2" : "=&v"(f.B[K]) : "v"(ba), "n"(512 * K) : "memory");
  else if constexpr (K < 16) asm volatile("ds_read_b64 %0, %1 offset:%2" : "=&v"(f.al[K - 8]) : "v"(aa), "n"(512 * (K - 8)) : "memory");
  else if constexpr (K < 24) asm volatile("ds_read_b64 %0, %1 offset:%2" : "=&v"(f.ah[K - 16]) : "v"(aa), "n"(512 * (K - 16) + 8) : "memory");
}
template <int K = 0> __device__ __forceinline__ void conv_step(f32x4 (&acc)[8], const ConvFrags& f, ConvFrags& nx, unsigned aa, unsigned ba) {
  if constexpr (K < 36) {
    constexpr int a = K < 8 ? 0 : K < 15 ? 1 : K < 21 ? 2 : K < 26 ? 3 : K < 30 ? 4 : K < 33 ? 5 : K < 35 ? 6 : 7;
    constexpr int i = K - (a == 0 ? 0 : a == 1 ? 8 : a == 2 ? 15 : a == 3 ? 21 : a == 4 ? 26 : a == 5 ? 30 : a == 6 ? 33 : 35);
    acc[a + i] = mfma16(mk8(f.al[7 - a], f.ah[7 - a]), __builtin_bit_cast(bf16x8, f.B[i]), acc[a + i]);
    conv_read1<K>(nx, aa, ba);
    SBAR();
    conv_step<K + 1>(acc, f, nx, aa, ba);
  }
}
__device__ __forceinline__ void conv_main(f32x4 (&acc)[8], unsigned cb, int lane, int c0, int c1) {
  const int fr = lane & 15, g4 = lane >> 4;
  const unsigned abase = cb + (3 - (fr & 3)) * CPS + 2 * (2044 - 16 * 112 - 16 * (g4 >> 1) + 8 * (g4 & 1) - (fr & ~3));
  const unsigned bbase = cb + UOFF + 512 + 2 * (16 * fr - 16 * (g4 >> 1) + 8 * (g4 & 1));
#pragma unroll
  for (int c = 0; c < 8; c++) acc[c] = (f32x4){0.f, 0.f, 0.f, 0.f};
  ConvFrags f0, f1;
  asm volatile("s_waitcnt lgkmcnt(0)" ::: "memory");
  conv_read(f0, abase - 64 * c0, bbase - 64 * c0);
  for (int cc = c0; cc < c1; cc += 2) {
    conv_wait(f0);
    SBAR();
    conv_step(acc, f0, f1, abase - 64 * (cc + 1), bbase - 64 * (cc + 1));
    conv_wait(f1);
    SBAR();
    const int cn = cc + 2 < c1 ? cc + 2 : cc + 1;
    conv_step(acc, f1, f0, abase - 64 * cn, bbase - 64 * cn);
  }
  conv_wait(f0);
}
__device__ __forceinline__ void conv_store(f32x4 y, unsigned cb, int c, int lane, float Dd, __amdgpu_buffer_rsrc_t rw, int d) {
  const int fr = lane & 15, g4 = lane >> 4;
  const u32x2 uu = lds_b64(cb + UOFF + 512 + 2 * (256 * c + 16 * fr + 4 * g4));
  const float r0 = fmaf(bflo(uu.x), Dd, y[0]), r1 = fmaf(bfhi(uu.x), Dd, y[1]);
  const float r2 = fmaf(bflo(uu.y), Dd, y[2]), r3 = fmaf(bfhi(uu.y), Dd, y[3]);
  __builtin_amdgcn_raw_buffer_store_b64((u32x2){pk2(r0, r1), pk2(r2, r3)}, rw, OFF_YT + d * 4096 + (256 * c + 16 * fr + 4 * g4) * 2, 0, SC1);
}

__global__ __launch_bounds__(512, 2) void fused(const u16* __restrict__ x, const u16* __restrict__ wqkv, const u16* __restrict__ wo,
                                                const u16* __restrict__ Bk, const u16* __restrict__ Bv, const u16* __restrict__ shk,
                                                const u16* __restrict__ hk, const u16* __restrict__ Dv, const u16* __restrict__ gw,
                                                u16* __restrict__ out, char* __restrict__ ws) {
  extern __shared__ __attribute__((aligned(16))) char sm[];
  const int tid = threadIdx.x, lane = tid & 63;
  const int w = __builtin_amdgcn_readfirstlane(tid >> 6);
  const int b = blockIdx.x, xcd = b & 7, r = b >> 3;
  const int fr = lane & 15, g4 = lane >> 4;
  const unsigned lb = (unsigned)(size_t)(LAS char*)sm;
  const __amdgpu_buffer_rsrc_t rw = __builtin_amdgcn_make_buffer_rsrc(ws, 0, WS_USED, 0x00020000);
  TS_DECL TS(0);
  if (w == 7) {
    const unsigned long long pc = __builtin_amdgcn_s_getpc() & ~127ull;
    i32x4 sc; sc.x = (int)(unsigned)pc; sc.y = (int)(unsigned)(pc >> 32); sc.z = CODE_PF; sc.w = 0x00020000;
    const unsigned vo = (r * 11 + (lane < 11 ? lane : 0)) * 128;
    asm volatile("s_mov_b32 m0, %0\n\ts_nop 0\n\tbuffer_load_dword %1, %2, 0 offen lds" :: "s"(lb + L_SCR), "v"(vo), "s"(sc) : "memory");
  }
  unsigned e0 = 0;
#define PUT_TAG() do { if (w == 7 && lane == 0) { *(LAS unsigned*)(size_t)(lb + L_TAG) = e0 + 1u; *(LAS u32x4*)(size_t)(lb + L_CNT) = (u32x4){0u, 0u, 0u, 0u}; } } while (0)
#define TAG (*(const LAS unsigned*)(size_t)(lb + L_TAG))
  if (r < 24) {
    const bool isKV = r < 16;
    const int rt = (xcd & 3) * 4 + (r & 3);
    const int cg = isKV ? (xcd >> 2) * 4 + (r >> 2) : (xcd >> 2) * 2 + ((r - 16) >> 2);
    f32x4 acc[4][5];
    const int rp = (w >> 1) & 1, hf = w & 1;
    if (w < 4) {
      const unsigned xa0 = lb + (rp * 64 + fr) * 128 + ((g4 ^ ((fr >> 1) & 7)) << 4);
      const unsigned wl = lb + XS + (8 * g4 + (fr >> 2)) * 320 + ((lane & 3) >> 1) * 16 + (lane & 1) * 8 + hf * 160;
      const unsigned sgn = (g4 & 1) * 32;
      const unsigned wa0 = hf ? wl - sgn : wl + sgn, wa1 = hf ? wl + sgn : wl - sgn;
#pragma unroll
      for (int i = 0; i < 4; i++)
#pragma unroll
        for (int t = 0; t < 5; t++) acc[i][t] = (f32x4){0.f, 0.f, 0.f, 0.f};
      if (isKV) gemm_compute<true>(acc, xa0, wa0, wa1 TSARG); else gemm_compute<false>(acc, xa0, wa0, wa1 TSARG);
    } else if (w < 6) {
      gemm_loader(w - 4, lane, lb, x, wqkv, rt, cg, isKV);
    } else {
      const int w6 = w - 6;
      unsigned pf[5] = {0, 0, 0, 0, 0};
      SBAR();
      if (w == 7) e0 = flag_ld(ws, F_E);
      if (isKV) {
        const int l16 = lane & 15, row0 = rt * 128 + cg * 16 + w6 * 8;
        const u32x4 g0 = *(const u32x4*)(gw + lane * 8), g1 = *(const u32x4*)(gw + 512 + l16 * 8);
        u32x4 gx0[8], gx1[8];
#pragma unroll
        for (int i = 0; i < 8; i++) {
          const u16* xr = x + (size_t)(row0 + i) * D;
          gx0[i] = *(const u32x4*)(xr + lane * 8); gx1[i] = *(const u32x4*)(xr + 512 + l16 * 8);
        }
        SBAR();
        BAR(); BAR(); BAR();
        float a[8];
        const float m1 = lane < 16 ? 1.f : 0.f;
#pragma unroll
        for (int i = 0; i < 8; i++) {
          float a0 = 0.f, a1 = 0.f;
#pragma unroll
          for (int j = 0; j < 4; j++) {
            a0 = fmaf(bflo(gx0[i][j]), bflo(g0[j]), a0); a0 = fmaf(bfhi(gx0[i][j]), bfhi(g0[j]), a0);
            a1 = fmaf(bflo(gx1[i][j]), bflo(g1[j]), a1); a1 = fmaf(bfhi(gx1[i][j]), bfhi(g1[j]), a1);
          }
          a[i] = fmaf(a1, m1, a0);
          if (i == 3) { SBAR(); BAR(); SBAR(); }
        }
        SBAR(); BAR(); SBAR();
#pragma unroll
        for (int i = 0; i < 4; i++) { const float lo = a[i], hi = a[i + 4]; const bool up = lane & 4; a[i] = (up ? hi : lo) + __shfl_xor(up ? lo : hi, 4); }
#pragma unroll
        for (int i = 0; i < 2; i++) { const float lo = a[i], hi = a[i + 2]; const bool up = lane & 2; a[i] = (up ? hi : lo) + __shfl_xor(up ? lo : hi, 2); }
        { const float lo = a[0], hi = a[1]; const bool up = lane & 1; a[0] = (up ? hi : lo) + __shfl_xor(up ? lo : hi, 1); }
        a[0] += __shfl_xor(a[0], 8); a[0] += __shfl_xor(a[0], 16); a[0] += __shfl_xor(a[0], 32);
        if (lane < 8) __builtin_amdgcn_raw_buffer_store_b32(__float_as_uint(rbf(1.f / (1.f + __expf(-rbf(a[0]))))), rw, OFF_G + (row0 + lane) * 4, 0, SC1);
        PUT_TAG();
        asm volatile("" :: "v"(pf[0]), "v"(pf[1]), "v"(pf[2]), "v"(pf[3]), "v"(pf[4]));
        SBAR();
        BAR(); BAR(); BAR(); BAR(); BAR();
      } else {
        const int hw = (xcd * 8 + (r - 16)) * 2 + w6;
        const unsigned scr = lb + L_T + w6 * 6144;
#pragma unroll
        for (int bt = 0; bt < 2; bt++) {
          const int blk = bt == 0 ? hw : (hw < 32 ? 128 + hw : hw), ct = (blk * 3277) >> 16, ks = blk - 20 * ct;
          u32x4 v[5];
#pragma unroll
          for (int i = 0; i < 5; i++) {
            const int c = i * 64 + lane, row = (c * 6554) >> 16, ch = c - 10 * row;
            v[i] = *(const u32x4*)(wo + (size_t)(32 * ks + row) * D + 80 * ct + 8 * ch);
          }
          SBAR();
          BAR();
          if (bt == 0) PUT_TAG();
          BAR();
          if (bt == 0 || hw < 32) {
#pragma unroll
            for (int i = 0; i < 5; i++) *(LAS u32x4*)(size_t)(scr + (i * 64 + lane) * 16) = v[i];
            const unsigned ta = scr + (8 * g4 + (fr >> 2)) * 160 + (lane & 3) * 8;
#pragma unroll
            for (int t = 0; t < 5; t++) {
              const u32x2 lo = lds_tr(ta + 32 * t), hi = lds_tr(ta + 32 * t + 640);
              __builtin_amdgcn_raw_buffer_store_b128((u32x4){lo.x, lo.y, hi.x, hi.y}, rw, OFF_WOP + (blk * 5 + t) * 1024 + lane * 16, 0, SC1);
            }
          }
          SBAR();
          BAR(); BAR();
        }
        asm volatile("" :: "v"(pf[0]), "v"(pf[1]), "v"(pf[2]), "v"(pf[3]), "v"(pf[4]));
        DRAIN();
        if (lds_last(lb + L_CNT + 8, lane, 2) && lane == 0) flag_st(ws, F_W + (xcd * 8 + (r - 16)) * 128, TAG);
        BAR(); BAR();
      }
    }
    TS(1);
    BAR();
    TS(11);
    const unsigned tag = TAG;
    if (isKV) {
      if (w < 4) {
#pragma unroll
        for (int t = 0; t < 5; t++) {
          const bool isk = hf ? t < 2 : t < 3;
          const int slot = hf ? (t < 2 ? 3 + t : 5 + t) : (t < 3 ? t : 2 + t);
#pragma unroll
          for (int m = 0; m < 2; m++) {
            u32x4 f;
            if (isk) f = (u32x4){pk2(phi(acc[2 * m][t][0]), phi(acc[2 * m][t][1])), pk2(phi(acc[2 * m][t][2]), phi(acc[2 * m][t][3])),
                                 pk2(phi(acc[2 * m + 1][t][0]), phi(acc[2 * m + 1][t][1])), pk2(phi(acc[2 * m + 1][t][2]), phi(acc[2 * m + 1][t][3]))};
            else f = (u32x4){pk2(acc[2 * m][t][0], acc[2 * m][t][1]), pk2(acc[2 * m][t][2], acc[2 * m][t][3]),
                             pk2(acc[2 * m + 1][t][0], acc[2 * m + 1][t][1]), pk2(acc[2 * m + 1][t][2], acc[2 * m + 1][t][3])};
            *(LAS u32x4*)(size_t)(lb + ((rp * 2 + m) * 10 + slot) * 1024 + lane * 16) = f;
          }
        }
      } else {
        for (int i = tid - 256; i < 6048 / 16; i += 256) *(LAS u32x4*)(size_t)(lb + L_T + i * 16) = (u32x4){0, 0, 0, 0};
        if (w == 7) poll(ws, F_W + lane * 128, true, tag);
      }
      TS(12);
      BAR();
      TS(2);
      switch (w) {
        case 0: stat_tiles<0>(lb, lane, rw, OFF_P + (rt * 8 + cg) * 3360); break;
        case 1: stat_tiles<1>(lb, lane, rw, OFF_P + (rt * 8 + cg) * 3360); break;
        case 2: stat_tiles<2>(lb, lane, rw, OFF_P + (rt * 8 + cg) * 3360); break;
        case 3: stat_tiles<3>(lb, lane, rw, OFF_P + (rt * 8 + cg) * 3360); break;
        case 4: stat_tiles<4>(lb, lane, rw, OFF_P + (rt * 8 + cg) * 3360); break;
        case 5: stat_tiles<5>(lb, lane, rw, OFF_P + (rt * 8 + cg) * 3360); break;
        case 6: stat_tiles<6>(lb, lane, rw, OFF_P + (rt * 8 + cg) * 3360); break;
        default: stat_tiles<7>(lb, lane, rw, OFF_P + (rt * 8 + cg) * 3360); break;
      }
      TS(14);
      DRAIN();
      TS(15);
      BAR();
      TS(3);
      if (w == 0) {
        if (lane == 0) flag_st(ws, F_P + (rt * 8 + cg) * 128, tag);
        poll(ws, lane < 16 ? F_P + (lane * 8 + cg) * 128 : F_PQ + (rt * 4 + (cg >> 1)) * 128, lane <= 16, tag);
      } else if (w < 7) {
        const i32x4 sws = make_srd(ws, WS_USED);
        const unsigned vwv = OFF_WOP + ((xcd >> 2) * 4 + (r >> 3)) * 102400 + lane * 16;
        for (int i = w - 1; i < 100; i += 6) {
          int pc = 12 * (r & 7) + i; pc = __builtin_amdgcn_readfirstlane(pc >= 100 ? pc - 100 : pc);
          glds16_sc1(lb + pc * 1024, vwv, sws, pc * 1024);
        }
      }
      constexpr int PJ[9] = {0, 1, 1, 2, 2, 3, 3, 4, 4}, PK[9] = {0, 0, 1, 0, 1, 1, 2, 1, 2};
      unsigned an[9], ad[9];
#pragma unroll
      for (int p = 0; p < 9; p++) {
        const int E = 16 * PJ[p] + fr, h = (E * 205) >> 12, e = E - 20 * h;
        int o_ = 32 * PK[p] + 8 * g4 - 20 * h; o_ = o_ < -8 ? -8 : o_ > 20 ? 20 : o_;
        an[p] = lb + L_T + (h * 21 + e) * 72 + (8 + o_) * 2; ad[p] = lb + L_T + (h * 21 + 20) * 72 + (8 + o_) * 2;
      }
      BAR();
      TS(4);
      bf16x8 Bq[3];
      {
        const unsigned prow = OFF_PQ + ((rt * 8 + w) * 8 + cg) * 3072 + lane * 16;
#pragma unroll
        for (int ks = 0; ks < 3; ks++) Bq[ks] = __builtin_bit_cast(bf16x8, __builtin_amdgcn_raw_buffer_load_b128(rw, prow + 1024 * ks, 0, SC1));
      }
      if (tid < 210) {
        const unsigned pb = OFF_P + cg * 3360 + tid * 16;
        u32x4 v[16];
#pragma unroll
        for (int k = 0; k < 16; k++) v[k] = __builtin_amdgcn_raw_buffer_load_b128(rw, pb, (k ^ rt) * (8 * 3360), SC1);
        unsigned o4[4];
#pragma unroll
        for (int c = 0; c < 4; c++) {
          float lo[16], hi[16];
#pragma unroll
          for (int k = 0; k < 16; k++) { lo[k] = bflo(v[k][c]); hi[k] = bfhi(v[k][c]); }
#pragma unroll
          for (int st = 1; st < 16; st <<= 1)
#pragma unroll
            for (int k = 0; k < 16; k += 2 * st) { lo[k] += lo[k + st]; hi[k] += hi[k + st]; }
          o4[c] = pk2(lo[0], hi[0]);
        }
        TS(16);
#pragma unroll
        for (int q = 0; q < 2; q++) {
          const int i4 = 2 * tid + q, row = (i4 * 13108) >> 16, d = 4 * (i4 - row * 5);
          *(LAS u32x2*)(size_t)(lb + L_T + row * 72 + (8 + d) * 2) = (u32x2){o4[2 * q], o4[2 * q + 1]};
        }
      }
      BAR();
      TS(5);
      {
        if (g4 >= 2) Bq[2] = __builtin_bit_cast(bf16x8, (u32x4){0, 0, 0, 0});
        f32x4 nu[5], de[5];
#pragma unroll
        for (int j = 0; j < 5; j++) { nu[j] = (f32x4){0.f, 0.f, 0.f, 0.f}; de[j] = nu[j]; }
        bf16x8 An[9], Ad[9];
#pragma unroll
        for (int p = 0; p < 9; p++) {
          unsigned an8 = an[p] + 8, ad8 = ad[p] + 8;
          asm volatile("" : "+v"(an8), "+v"(ad8));
          An[p] = mk8(lds_b64(an[p]), lds_b64(an8)); Ad[p] = mk8(lds_b64(ad[p]), lds_b64(ad8));
        }
#pragma unroll
        for (int p = 0; p < 9; p++) { de[PJ[p]] = mfma16(Ad[p], Bq[PK[p]], de[PJ[p]]); nu[PJ[p]] = mfma16(An[p], Bq[PK[p]], nu[PJ[p]]); }
        u32x2 ov[5];
#pragma unroll
        for (int j = 0; j < 5; j++) {
          const float rd = __builtin_amdgcn_rcpf(de[j][0] + 1e-6f);
          ov[j] = (u32x2){pk2(nu[j][0] * rd, nu[j][1] * rd), pk2(nu[j][2] * rd, nu[j][3] * rd)};
        }
        store_frags(ov, rw, OFF_O + (rt * 8 + w) * 20480, 80 * cg, fr, g4);
      }
      TS(17);
      DRAIN();
      BAR();
      if (tid == 0) flag_st(ws, F_O + (rt * 8 + cg) * 128, tag);
      TS(6);
    } else {
      const int vb = xcd * 8 + (r - 16);
      const int d = 80 * (vb >> 3) + 48 + 4 * (vb & 7) + (w & 3);
      const unsigned cb = lb + (w & 3) * CHS;
      if (w < 4) {
#pragma unroll
        for (int i = 0; i < 4; i++) {
          u32x2 pv[5];
#pragma unroll
          for (int t = 0; t < 5; t++) pv[t] = (u32x2){pk2(phi(acc[i][t][0]), phi(acc[i][t][1])), pk2(phi(acc[i][t][2]), phi(acc[i][t][3]))};
          store_frags(pv, rw, OFF_PQ + ((rt * 8 + rp * 4 + i) * 8 + cg * 2 + hf) * 3072, 0, fr, g4);
        }
      } else {
        u32x4 hc[4], hp[4];
        conv_load_h(hc, hp, hk + (size_t)d * S, lane);
        poll(ws, F_U + lane * 128, true, tag);
        conv_load_u(rw, d, cb, lane, [&]() { conv_prep_h(hc, hp, cb, lane); });
      }
      TS(12);
      DRAIN();
      BAR();
      TS(2);
      if (tid == 0) flag_st(ws, F_PQ + (rt * 4 + cg) * 128, tag);
      if (w < 4) {
        const float Dd = bflo(Dv[d]);
        TS(3);
        f32x4 y[8];
        conv_main(y, cb, lane, 0, 8);
        TS(4);
#pragma unroll
        for (int c = 0; c < 8; c++) conv_store(y[c], cb, c, lane, Dd, rw, d);
        DRAIN();
      } else if (w == 7) {
        poll(ws, F_W + lane * 128, true, tag);
      }
      BAR();
      if (tid == 0) flag_st(ws, F_Y + (64 + vb) * 128, tag);
      TS(5); TS(6);
    }
  } else {
    const int pw = xcd * 8 + (r - 24);
    const int c6 = w < 4 ? w : 4 + ((w - 4) >> 1);
    const int d = 80 * (pw >> 3) + 6 * (pw & 7) + c6;
    const unsigned cb = lb + c6 * CHS;
    const bool prep = w < 4 || !(w & 1);
    u32x4 hc[4], hp[4];
    {
      const int rbase = xcd * 256 + ((r - 24) >> 1) * 64, cbase = ((r - 24) & 1) * 320;
      u32x4 kr[2][7], vr[2][4], sp[2][4];
#pragma unroll
      for (int q = 0; q < 2; q++) {
        const int it = w + 8 * q;
        if (q == 0 || w < 2) {
          const int ch = cbase + (it >> 1) * 64 + (lane & 7) * 8;
          const int s0 = rbase + (it & 1) * 32 + (lane >> 3) * 4;
#pragma unroll
          for (int j = 0; j < 7; j++) { int s = s0 - 3 + j; kr[q][j] = *(const u32x4*)(Bk + (size_t)(s < 0 ? 0 : s) * D + ch); }
#pragma unroll
          for (int j = 0; j < 4; j++) vr[q][j] = *(const u32x4*)(Bv + (size_t)(s0 + j) * D + ch);
#pragma unroll
          for (int j = 0; j < 4; j++) sp[q][j] = ((const u32x4*)(shk + ch * 4))[j];
        }
      }
      if (prep) conv_load_h(hc, hp, hk + (size_t)d * S, lane);
      if (w == 7) e0 = flag_ld(ws, F_E);
      SBAR();
#pragma unroll
      for (int q = 0; q < 2; q++) {
        const int it = w + 8 * q;
        if (q == 0 || w < 2) {
          const int s0 = rbase + (it & 1) * 32 + (lane >> 3) * 4;
          if (s0 == 0) { kr[q][0] = (u32x4){0, 0, 0, 0}; kr[q][1] = kr[q][0]; kr[q][2] = kr[q][0]; }
          const int rg = (it & 1) * 8 + (lane >> 3);
          const unsigned la = lb + L_UIMG + ((it >> 1) * 64 + (lane & 7) * 8) * 128 + (((rg >> 1) ^ (lane & 7)) << 4) + (rg & 1) * 8;
#pragma unroll
          for (int c = 0; c < 8; c++) {
            const unsigned s01 = sp[q][c >> 1][(c & 1) * 2], s23 = sp[q][c >> 1][(c & 1) * 2 + 1];
            const float w0 = bflo(s01), w1 = bfhi(s01), w2 = bflo(s23), w3 = bfhi(s23);
            float kk[7], u[4];
#pragma unroll
            for (int j = 0; j < 7; j++) { unsigned dd = kr[q][j][c >> 1]; kk[j] = (c & 1) ? bfhi(dd) : bflo(dd); }
#pragma unroll
            for (int j = 0; j < 4; j++) {
              unsigned dd = vr[q][j][c >> 1]; float v = (c & 1) ? bfhi(dd) : bflo(dd);
              float ks = kk[j] * w3; ks = fmaf(kk[j + 1], w2, ks); ks = fmaf(kk[j + 2], w1, ks); ks = fmaf(kk[j + 3], w0, ks);
              u[j] = ks * v;
            }
            *(LAS u32x2*)(size_t)(la + c * 128) = (u32x2){pk2(u[0], u[1]), pk2(u[2], u[3])};
          }
        }
      }
      PUT_TAG();
      BAR();
#pragma unroll
      for (int i = 0; i < 5; i++) {
        const int chl = (w * 5 + i) * 8 + (lane >> 3), k = lane & 7;
        const u32x4 v = *(const LAS u32x4*)(size_t)(lb + L_UIMG + chl * 128 + ((k ^ ((chl >> 3) & 7)) << 4));
        __builtin_amdgcn_raw_buffer_store_b128(v, rw, OFF_UT + (cbase + chl) * 4096 + rbase * 2 + k * 16, 0, SC1);
      }
      TS(1);
    }
    if (w < 5 && prep) conv_prep_h(hc, hp, cb, lane);
    DRAIN();
    BAR();
    TS(2);
    const unsigned tag = TAG;
    if (tid == 0) flag_st(ws, F_U + pw * 128, tag);
    const float Dd = bflo(Dv[d]);
    if (prep) {
      if (w == 6) conv_prep_h(hc, hp, cb, lane);
      poll(ws, F_U + lane * 128, true, tag);
      conv_load_u(rw, d, cb, lane, [&]() {});
    }
    BAR();
    TS(3);
    f32x4 y[8];
    conv_main(y, cb, lane, w < 4 ? 0 : (w & 1) * 4, w < 4 ? 8 : (w & 1) * 4 + 4);
    TS(4);
    if (w < 4) {
#pragma unroll
      for (int c = 0; c < 8; c++) conv_store(y[c], cb, c, lane, Dd, rw, d);
    }
    BAR();
    if (w >= 4) {
#pragma unroll
      for (int c = 0; c < 4; c++) { const f32x4 lo = y[c], hi = y[4 + c]; *(LAS f32x4*)(size_t)(cb + ((w & 1) * 4 + c) * 1024 + lane * 16) = (w & 1) ? lo : hi; }
    }
    BAR();
    if (w >= 4) {
#pragma unroll
      for (int c = 0; c < 4; c++) {
        const f32x4 o_ = *(const LAS f32x4*)(size_t)(cb + (((w & 1) ^ 1) * 4 + c) * 1024 + lane * 16);
        const f32x4 lo = y[c], hi = y[4 + c];
        const f32x4 mine = (w & 1) ? hi : lo;
        conv_store((w & 1) ? o_ + mine : mine + o_, cb, (w & 1) * 4 + c, lane, Dd, rw, d);
      }
    }
    if (w == 0) poll(ws, F_W + lane * 128, true, tag);
    DRAIN();
    BAR();
    if (tid == 0) flag_st(ws, F_Y + pw * 128, tag);
    TS(5); TS(6);
  }

  {
    const unsigned tag = TAG;
    const int rt = (xcd & 3) * 8 + (r & 7), ct = (xcd >> 2) * 4 + (r >> 3);
    const int sw_ = w & 3, kk = w >> 2;
    const i32x4 sws = make_srd(ws, WS_USED);
    TS(18);
    const int srow = rt * 64 + sw_ * 16 + fr;
    u32x2 xv[3];
#pragma unroll
    for (int t = 0; t < 3; t++) { const int tt = kk ? (t < 2 ? 3 + t : 4) : t; xv[t] = *(const u32x2*)(x + (size_t)srow * D + ct * 80 + 16 * tt + 4 * g4); }
    SBAR();
    if (w == 0) {
      poll(ws, F_O + ((rt >> 1) * 8 + (lane & 7)) * 128, lane < 8, tag);
    } else if (w == 7) {
      poll(ws, F_Y + ((lane & 8 ? 64 : 0) + ct * 8 + (lane & 7)) * 128, lane < 16, tag);
    } else {
      if (r >= 16) {
        const unsigned vwv = OFF_WOP + ct * 102400 + lane * 16;
        for (int i = w - 1; i < 100; i += 6) { int pc = 12 * (r & 7) + i; pc = __builtin_amdgcn_readfirstlane(pc >= 100 ? pc - 100 : pc); glds16_sc1(lb + pc * 1024, vwv, sws, pc * 1024); }
      }
      if (w == 6 && b == 0) {
        poll(ws, F_U + lane * 128, true, tag); poll(ws, F_PQ + lane * 128, true, tag);
        poll(ws, F_P + lane * 128, true, tag); poll(ws, F_P + (64 + lane) * 128, true, tag);
        if (lane == 0) flag_st(ws, F_E, tag);
      }
      DRAIN();
    }
    BAR();
    TS(7);
    asm volatile("" :: "v"(xv[0]), "v"(xv[1]), "v"(xv[2]));
    bf16x8 of[10];
    const int rot = ((ct & 3) * 5) >> 1;
    {
      const unsigned vo = OFF_O + (rt * 4 + sw_) * 20480 + kk * 1024 + lane * 16;
#pragma unroll
      for (int s = 0; s < 10; s++) { int q = s + rot; q = __builtin_amdgcn_readfirstlane(q >= 10 ? q - 10 : q); bload16_sc1(of[s], vo, sws, q * 2048); }
    }
    if (w == 7) {
      for (int pc = 0; pc < 10; pc++)
        glds16_sc1(lb + L_Y + pc * 1024, OFF_YT + (ct * 80 + pc * 8 + (lane >> 3)) * 4096 + rt * 128 + (lane & 7) * 16, sws, 0);
    }
    float g;
    asm volatile("buffer_load_dword %0, %1, %2, 0 offen sc1" : "=v"(g) : "v"(OFF_G + srow * 4), "s"(sws) : "memory");
    TS(8);
    f32x4 acc[5];
#pragma unroll
    for (int t = 0; t < 5; t++) acc[t] = (f32x4){0.f, 0.f, 0.f, 0.f};
    const unsigned wa0 = lb + kk * 5120 + lane * 16;
#pragma unroll
    for (int hh = 0; hh < 2; hh++) {
      bf16x8 wf[5][5];
#pragma unroll
      for (int s = 0; s < 5; s++) {
        int q = hh * 5 + s + rot; q = q >= 10 ? q - 10 : q;
        const unsigned wa = wa0 + q * 10240;
#pragma unroll
        for (int t = 0; t < 5; t++) wf[s][t] = lds_b128(wa + t * 1024);
      }
      if (hh == 0) {
        if (w == 7) asm volatile("s_waitcnt vmcnt(16)" ::: "memory"); else asm volatile("s_waitcnt vmcnt(6)" ::: "memory");
        asm volatile("" : "+v"(of[0]), "+v"(of[1]), "+v"(of[2]), "+v"(of[3]), "+v"(of[4]) :: "memory");
      } else {
        if (w == 7) asm volatile("s_waitcnt vmcnt(11)" ::: "memory"); else asm volatile("s_waitcnt vmcnt(1)" ::: "memory");
        asm volatile("" : "+v"(of[5]), "+v"(of[6]), "+v"(of[7]), "+v"(of[8]), "+v"(of[9]) :: "memory");
      }
      SBAR();
#pragma unroll
      for (int s = 0; s < 5; s++)
#pragma unroll
        for (int t = 0; t < 5; t++) acc[t] = mfma16(wf[s][t], of[hh * 5 + s], acc[t]);
      SBAR();
    }
    TS(9);
    const unsigned ra = lb + L_RED + (sw_ * 5) * 1024 + lane * 16;
    if (kk == 0) { *(LAS f32x4*)(size_t)(ra + 3 * 1024) = acc[3]; *(LAS f32x4*)(size_t)(ra + 4 * 1024) = acc[4]; }
    else { *(LAS f32x4*)(size_t)(ra) = acc[0]; *(LAS f32x4*)(size_t)(ra + 1024) = acc[1]; *(LAS f32x4*)(size_t)(ra + 2048) = acc[2]; }
    asm volatile("s_waitcnt vmcnt(0)" : "+v"(g) :: "memory");
    BAR();
    TS(20);
    const float omg = 1.f - g;
    const int nt = kk ? 2 : 3;
#pragma unroll
    for (int t = 0; t < 3; t++) {
      if (t < nt) {
        const int tt = kk ? 3 + t : t;
        const f32x4 a0 = acc[t], a1 = acc[3 + (t < 2 ? t : 1)];
        f32x4 a = *(const LAS f32x4*)(size_t)(ra + tt * 1024) + (kk ? a1 : a0);
        u32x2 yv = lds_tr(lb + L_Y + (16 * tt + 4 * g4 + (fr >> 2)) * 128 + (sw_ * 16 + 4 * (fr & 3)) * 2);
        float xs[4] = {bflo(xv[t].x), bfhi(xv[t].x), bflo(xv[t].y), bfhi(xv[t].y)};
        float ys[4] = {bflo(yv.x), bfhi(yv.x), bflo(yv.y), bfhi(yv.y)};
        float r_[4];
#pragma unroll
        for (int q = 0; q < 4; q++) r_[q] = fmaf(g, a[q], omg * (xs[q] * ys[q]));
        *(u32x2*)(out + (size_t)srow * D + ct * 80 + 16 * tt + 4 * g4) = (u32x2){pk2(r_[0], r_[1]), pk2(r_[2], r_[3])};
      }
    }
    TS(10); TS_OUT(21);
  }
}

extern "C" void kernel_launch(void* const* d_in, const int* in_sizes, int n_in, void* d_out, int out_size, void* d_ws, size_t ws_size,
                              hipStream_t stream) {
  const u16* x = (const u16*)d_in[0]; const u16* wqkv = (const u16*)d_in[1]; const u16* wo = (const u16*)d_in[2];
  const u16* Bk = (const u16*)d_in[4]; const u16* Bv = (const u16*)d_in[5]; const u16* shk = (const u16*)d_in[6];
  const u16* hk = (const u16*)d_in[7]; const u16* Dv = (const u16*)d_in[8]; const u16* gw = (const u16*)d_in[9];
  (void)hipFuncSetAttribute((const void*)fused, hipFuncAttributeMaxDynamicSharedMemorySize, LDS_TOTAL);
  fused<<<256, 512, LDS_TOTAL, stream>>>(x, wqkv, wo, Bk, Bv, shk, hk, Dv, gw, (u16*)d_out, (char*)d_ws);
}
```

```cpp
#include <hip/hip_runtime.h>
#include <hip/hip_bf16.h>
#include <type_traits>
#define NO_PF

typedef unsigned short u16;
typedef __bf16 bf16x2 __attribute__((ext_vector_type(2)));
typedef __bf16 bf16x8 __attribute__((ext_vector_type(8)));
typedef float f32x4 __attribute__((ext_vector_type(4)));
typedef unsigned u32x4 __attribute__((ext_vector_type(4)));
typedef unsigned u32x2 __attribute__((ext_vector_type(2)));
typedef short v4i16_t __attribute__((ext_vector_type(4)));
typedef int i32x4 __attribute__((ext_vector_type(4)));
typedef __attribute__((address_space(1))) unsigned gu32;
#define LAS __attribute__((address_space(3)))
#define SBAR() __builtin_amdgcn_sched_barrier(0)
#define SGB(mask, n) __builtin_amdgcn_sched_group_barrier(mask, n, 0)
#define BAR() asm volatile("s_waitcnt lgkmcnt(0)\n\ts_barrier" ::: "memory")
#define DRAIN() asm volatile("s_waitcnt vmcnt(0)" ::: "memory")
#define SC1 16

#define TS_DECL unsigned long long tevt_ = 0;
#define TSARG , tevt_
#define TSPAR , unsigned long long& tevt_
#define TS(i) do { if ((i) == 0 && blockIdx.x == 0 && threadIdx.x == 0) tevt_ = __builtin_amdgcn_s_memrealtime(); } while (0)
#define TS_OUT(n) do { if (blockIdx.x == 0 && threadIdx.x == 0) { while (__builtin_amdgcn_s_memrealtime() < tevt_ + 3000ull) __builtin_amdgcn_s_sleep(1); } } while (0)

constexpr int S = 2048, D = 640;
constexpr unsigned SZ = S * D * 2;
constexpr unsigned F_E = 0, F_U = 128, F_PQ = F_U + 64 * 128, F_P = F_PQ + 64 * 128, F_O = F_P + 128 * 128, F_Y = F_O + 128 * 128, F_W = F_Y + 128 * 128;
constexpr unsigned OFF_PQ = 0x20000, OFF_P = OFF_PQ + 128 * 8 * 3 * 1024, OFF_UT = OFF_P + (1u << 19), OFF_G = OFF_UT + SZ, OFF_WOP = OFF_G + 8192,
                   OFF_O = OFF_WOP + (1u << 20), OFF_YT = OFF_O + SZ, WS_USED = OFF_YT + SZ;
constexpr int XS = 16384, WSB = 20480, STG = XS + WSB;
constexpr int L_EXTRA = 4 * STG;
constexpr int L_TAG = L_EXTRA, L_CNT = L_EXTRA + 16, L_T = L_EXTRA + 64;
constexpr int L_SCR = L_T + 6144 + 5120;
constexpr int LDS_TOTAL = L_SCR + 256;
constexpr int CPS = 4160, UOFF = 4 * CPS, CHS = UOFF + 4608;
constexpr int L_UIMG = 5 * CHS;
constexpr int L_Y = 102400, L_RED = L_Y + 10240;

__device__ __forceinline__ unsigned pk2(float a, float b) { bf16x2 r = {(__bf16)a, (__bf16)b}; return __builtin_bit_cast(unsigned, r); }
__device__ __forceinline__ float rbf(float a) { return (float)(__bf16)a; }
__device__ __forceinline__ float bflo(unsigned v) { return __uint_as_float(v << 16); }
__device__ __forceinline__ float bfhi(unsigned v) { return __uint_as_float(v & 0xffff0000u); }
__device__ __forceinline__ float phi(float t) {
  return __builtin_amdgcn_fmed3f(t, 0.f, 3.0e38f) + (__builtin_amdgcn_fmed3f(__builtin_amdgcn_exp2f(t * 1.44269504f), 0.f, 1.f) + 1e-6f);
}
__device__ __forceinline__ i32x4 make_srd(const void* p, unsigned bytes) {
  unsigned long long a = (unsigned long long)p; i32x4 r;
  r.x = __builtin_amdgcn_readfirstlane((int)(unsigned)a); r.y = __builtin_amdgcn_readfirstlane((int)(unsigned)(a >> 32));
  r.z = __builtin_amdgcn_readfirstlane((int)bytes); r.w = 0x00020000; return r;
}
__device__ __forceinline__ void glds16(unsigned ldsaddr, unsigned voff, i32x4 srd, unsigned soff) {
  asm volatile("s_mov_b32 m0, %0\n\ts_nop 0\n\tbuffer_load_dwordx4 %1, %2, %3 offen lds" :: "s"(ldsaddr), "v"(voff), "s"(srd), "s"(soff) : "memory");
}
__device__ __forceinline__ void glds16_sc1(unsigned ldsaddr, unsigned voff, i32x4 srd, unsigned soff) {
  asm volatile("s_mov_b32 m0, %0\n\ts_nop 0\n\tbuffer_load_dwordx4 %1, %2, %3 offen sc1 lds" :: "s"(ldsaddr), "v"(voff), "s"(srd), "s"(soff) : "memory");
}
__device__ __forceinline__ void bload16_sc1(bf16x8& dst, unsigned voff, i32x4 srd, unsigned soff) {
  asm volatile("buffer_load_dwordx4 %0, %1, %2, %3 offen sc1" : "=v"(dst) : "v"(voff), "s"(srd), "s"(soff) : "memory");
}
template <int N> __device__ __forceinline__ void wait_bar() { asm volatile("s_waitcnt vmcnt(%0) lgkmcnt(0)\n\ts_barrier" :: "n"(N) : "memory"); }
__device__ __forceinline__ bf16x8 lds_b128(unsigned addr) { return *(const LAS bf16x8*)(size_t)addr; }
__device__ __forceinline__ u32x2 lds_b64(unsigned addr) { return *(const LAS u32x2*)(size_t)addr; }
__device__ __forceinline__ u32x2 lds_tr(unsigned addr) { return __builtin_bit_cast(u32x2, __builtin_amdgcn_ds_read_tr16_b64_v4i16((LAS v4i16_t*)(size_t)addr)); }
__device__ __forceinline__ bf16x8 mk8(u32x2 lo, u32x2 hi) { u32x4 t = {lo.x, lo.y, hi.x, hi.y}; return __builtin_bit_cast(bf16x8, t); }
__device__ __forceinline__ f32x4 mfma16(bf16x8 a, bf16x8 b, f32x4 c) { return __builtin_amdgcn_mfma_f32_16x16x32_bf16(a, b, c, 0, 0, 0); }

__device__ __forceinline__ u32x4 pair16(u32x2 a, u32x2 b) {
  const auto r0 = __builtin_amdgcn_permlane16_swap(a.x, b.x, false, false);
  const auto r1 = __builtin_amdgcn_permlane16_swap(a.y, b.y, false, false);
  return (u32x4){r0[0], r1[0], r0[1], r1[1]};
}
__device__ __forceinline__ void store_frags(const u32x2 (&v)[5], __amdgpu_buffer_rsrc_t rw, unsigned base, int k0, int fr, int g4) {
#pragma unroll
  for (int j = 0; j < 4; j += 2) {
    const int k = k0 + 16 * (j + (g4 & 1)) + 8 * (g4 >> 1);
    __builtin_amdgcn_raw_buffer_store_b128(pair16(v[j], v[j + 1]), rw, base + (k >> 5) * 1024 + (((k >> 3) & 3) * 16 + fr) * 16, 0, SC1);
  }
  const int k = k0 + 64 + 4 * g4;
  __builtin_amdgcn_raw_buffer_store_b64(v[4], rw, base + (k >> 5) * 1024 + (((k >> 3) & 3) * 16 + fr) * 16 + ((k >> 2) & 1) * 8, 0, SC1);
}

__device__ __forceinline__ void lds_sync(unsigned cnt_addr, int lane, unsigned n) {
  asm volatile("s_waitcnt lgkmcnt(0)" ::: "memory");
  if (lane == 0) __hip_atomic_fetch_add((LAS unsigned*)(size_t)cnt_addr, 1u, __ATOMIC_RELAXED, __HIP_MEMORY_SCOPE_WORKGROUP);
  while (__builtin_amdgcn_readfirstlane(__hip_atomic_load((LAS unsigned*)(size_t)cnt_addr, __ATOMIC_RELAXED, __HIP_MEMORY_SCOPE_WORKGROUP)) < n) {}
  asm volatile("" ::: "memory");
}
__device__ __forceinline__ bool lds_last(unsigned cnt_addr, int lane, unsigned n) {
  unsigned old = 0;
  if (lane == 0) old = __hip_atomic_fetch_add((LAS unsigned*)(size_t)cnt_addr, 1u, __ATOMIC_RELAXED, __HIP_MEMORY_SCOPE_WORKGROUP);
  return __builtin_amdgcn_readfirstlane(old) == n - 1;
}

__device__ __forceinline__ unsigned flag_ld(const char* ws, unsigned off) { return __hip_atomic_load((const gu32*)(ws + off), __ATOMIC_RELAXED, __HIP_MEMORY_SCOPE_AGENT); }
__device__ __forceinline__ void flag_st(char* ws, unsigned off, unsigned v) { __hip_atomic_store((gu32*)(ws + off), v, __ATOMIC_RELAXED, __HIP_MEMORY_SCOPE_AGENT); }
__device__ __forceinline__ void poll(const char* ws, unsigned off, bool act, unsigned tag) {
  const unsigned o0 = __builtin_amdgcn_readfirstlane(off);
  off = act ? off : o0;
  unsigned a, b, n = 1u << 17;
  asm volatile(
      "global_load_dword %0, %3, %4 sc1\n\t"
      "s_sleep 6\n"
      "1:\n\t"
      "global_load_dword %1, %3, %4 sc1\n\t"
      "s_waitcnt vmcnt(1)\n\t"
      "v_cmp_ne_u32 vcc, %0, %5\n\t"
      "s_cbranch_vccz 2f\n\t"
      "global_load_dword %0, %3, %4 sc1\n\t"
      "s_waitcnt vmcnt(1)\n\t"
      "v_cmp_ne_u32 vcc, %1, %5\n\t"
      "s_cbranch_vccz 2f\n\t"
      "s_sub_u32 %2, %2, 1\n\t"
      "s_cbranch_scc0 1b\n"
      "2:\n\t"
      "s_waitcnt vmcnt(0)"
      : "=&v"(a), "=&v"(b), "+s"(n) : "v"(off), "s"(ws), "v"(tag) : "vcc", "scc", "memory");
}

template <bool KV>
__device__ __forceinline__ void gemm_compute(f32x4 (&acc)[4][5], unsigned xa0, unsigned wa0, unsigned wa1 TSPAR) {
  bf16x8 xa[4], xb[4], wa[5], wb[5];
  auto load = [&](bf16x8 (&xf)[4], bf16x8 (&wf)[5], int buf, int kk) {
    const unsigned bo = buf * STG;
    const unsigned xa = (xa0 + bo) ^ (kk * 64);
    const unsigned a0 = wa0 + bo + kk * 10240, a1 = wa1 + bo + kk * 10240;
#pragma unroll
    for (int i = 0; i < 4; i++) xf[i] = lds_b128(xa + 2048 * i);
#pragma unroll
    for (int t = 0; t < 5; t++) { const unsigned a = ((t & 1) ? a1 : a0) + 32 * t; wf[t] = mk8(lds_tr(a), lds_tr(a + 1280)); }
  };
  auto mma = [&](const bf16x8 (&xf)[4], const bf16x8 (&wf)[5]) {
#pragma unroll
    for (int t = 0; t < 5; t++)
#pragma unroll
      for (int i = 0; i < 4; i++) acc[i][t] = KV ? mfma16(xf[i], wf[t], acc[i][t]) : mfma16(wf[t], xf[i], acc[i][t]);
  };
  auto interleave = [&]() {
#pragma unroll
    for (int t = 0; t < 14; t++) { SGB(0x8, 1); SGB(0x100, 1); }
    SGB(0x8, 6);
  };
  BAR();
  TS(21);
  load(xa, wa, 0, 0);
  SBAR();
  for (int s = 0; s < 9; s++) {
    load(xb, wb, s & 3, 1); mma(xa, wa); interleave();
    SBAR();
    BAR();
    if (s == 0) TS(22);
    if (s == 4) TS(23);
    load(xa, wa, (s + 1) & 3, 0); mma(xb, wb); interleave();
    SBAR();
  }
  load(xb, wb, 1, 1); mma(xa, wa); interleave();
  SBAR();
  mma(xb, wb);
}

__device__ __forceinline__ int gemm_s0(int rt, int cg, bool isKV) { return (((rt & 3) * 5 + 1) / 2 + (isKV ? 2 * (cg & 3) : 8 + (cg & 1))) % 10; }
__device__ __forceinline__ void gemm_vx(unsigned (&vx)[2], int L, int lane, int rt) {
#pragma unroll
  for (int i = 0; i < 2; i++) vx[i] = (rt * 128 + L * 64 + i * 8 + (lane >> 3)) * 1280 + (((lane & 7) ^ ((4 * i + (lane >> 4)) & 7)) << 4);
}
__device__ __forceinline__ void gemm_first_x(int L, int lane, unsigned lb, const u16* x, int rt, int cg, bool isKV) {
  const i32x4 sx = make_srd(x, S * D * 2);
  unsigned vx[2];
  gemm_vx(vx, L, lane, rt);
  const int s0 = gemm_s0(rt, cg, isKV);
#pragma unroll
  for (int p = 0; p < 8; p++) glds16(lb + (L * 8 + p) * 1024, vx[p & 1], sx, s0 * 128 + (p >> 1) * (16 * 1280));
}
__device__ __forceinline__ void gemm_loader(int L, int lane, unsigned lb, const u16* x, const u16* wqkv, int rt, int cg, bool isKV) {
  const int s0 = gemm_s0(rt, cg, isKV);
  const i32x4 sx = make_srd(x, S * D * 2), sw = make_srd(wqkv, D * 3 * D * 2);
  unsigned vx[2], vw[5];
  gemm_vx(vx, L, lane, rt);
#pragma unroll
  for (int i = 0; i < 5; i++) {
    const int ci = i * 64 + lane, k = (ci * 3277) >> 16, pos = ci - k * 20, gc = pos ^ (((k >> 3) & 1) << 1), tl = gc >> 1;
    unsigned col;
    if (isKV) { const int isv = (tl >= 3 && tl < 5) || tl >= 7; const int tt = tl < 3 ? tl : tl < 5 ? tl - 3 : tl < 7 ? tl - 2 : tl - 5;
                col = (isv ? 2560u : 1280u) + 160u * cg + tt * 32; }
    else col = 320u * cg + tl * 32;
    vw[i] = (L * 32 + k) * 3840 + col + (gc & 1) * 16;
  }
  auto stage = [&](int st_, int buf) {
    const unsigned base = lb + buf * STG;
    int st = st_ + s0; st = st >= 10 ? st - 10 : st;
#pragma unroll
    for (int p = 0; p < 10; p++) {
      glds16(base + XS + (L * 10 + p) * 1024, vw[p % 5], sw, st * (64 * 3840) + (p / 5) * (16 * 3840));
      if (p < 8) glds16(base + (L * 8 + p) * 1024, vx[p & 1], sx, st * 128 + (p >> 1) * (16 * 1280));
    }
  };
#pragma unroll
  for (int p = 0; p < 10; p++) glds16(lb + XS + (L * 10 + p) * 1024, vw[p % 5], sw, s0 * (64 * 3840) + (p / 5) * (16 * 3840));
  stage(1, 1);
  wait_bar<18>();
  stage(2, 2);
  for (int s = 0; s < 9; s++) {
    if (s < 8) wait_bar<18>(); else wait_bar<0>();
    if (s < 7) stage(s + 3, (s + 3) & 3);
  }
}

template <int T> struct StatTile {
  static constexpr bool isz = T >= 13;
  static constexpr int i = isz ? T - 13 : (T + 1) / 3;
  static constexpr int j = isz ? 0 : T - (i == 0 ? 0 : 3 * i - 1) + (i == 0 ? 0 : i - 1);
};
template <int T> __device__ __forceinline__ f32x4 stat_mma(unsigned la) {
  const u32x4 o1 = {0x3F803F80u, 0x3F803F80u, 0x3F803F80u, 0x3F803F80u};
  bf16x8 A[4], B[4];
#pragma unroll
  for (int m = 0; m < 4; m++) {
    A[m] = lds_b128(la + StatTile<T>::i * 1024 + m * 10240);
    B[m] = StatTile<T>::isz ? __builtin_bit_cast(bf16x8, o1) : lds_b128(la + (5 + StatTile<T>::j) * 1024 + m * 10240);
  }
  f32x4 d4 = {0.f, 0.f, 0.f, 0.f};
#pragma unroll
  for (int m = 0; m < 4; m++) d4 = mfma16(A[m], B[m], d4);
  return d4;
}
template <int T> __device__ __forceinline__ void stat_store(f32x4 d4, int lane, __amdgpu_buffer_rsrc_t rw, unsigned pbase) {
  const int fr = lane & 15, g4 = lane >> 4;
  const int Dg = 16 * StatTile<T>::i + 4 * g4, h = (Dg * 205) >> 12, dd = Dg - 20 * h;
  const int ee = StatTile<T>::isz ? 20 : 16 * StatTile<T>::j + fr - 20 * h;
  if (StatTile<T>::isz ? fr == 0 : (ee >= 0 && ee < 20))
    __builtin_amdgcn_raw_buffer_store_b64((u32x2){pk2(d4[0], d4[1]), pk2(d4[2], d4[3])}, rw, pbase + (h * 420 + ee * 20 + dd) * 2, 0, SC1);
}
template <int W> __device__ __forceinline__ void stat_tiles(unsigned lb, int lane, __amdgpu_buffer_rsrc_t rw, unsigned pbase) {
  const unsigned la = lb + lane * 16;
  const f32x4 a = stat_mma<W>(la), b = stat_mma<W + 8>(la);
  if constexpr (W < 2) { const f32x4 c = stat_mma<W + 16>(la); stat_store<W + 16>(c, lane, rw, pbase); }
  stat_store<W>(a, lane, rw, pbase);
  stat_store<W + 8>(b, lane, rw, pbase);
}

__device__ __forceinline__ void conv_load_h(u32x4 (&cur)[4], u32x4 (&prv)[4], const u16* __restrict__ hrow, int lane) {
#pragma unroll
  for (int it = 0; it < 4; it++) {
    const int i = it * 64 + lane;
    cur[it] = *(const u32x4*)(hrow + i * 8); prv[it] = *(const u32x4*)(hrow + (i > 0 ? i * 8 - 8 : 0));
  }
}
__device__ __forceinline__ void conv_prep_h(const u32x4 (&cur)[4], const u32x4 (&prv_)[4], unsigned cb, int lane) {
#pragma unroll
  for (int it = 0; it < 4; it++) {
    u32x4 prv = prv_[it];
    if (it == 0 && lane == 0) prv = (u32x4){0, 0, 0, 0};
    const unsigned W_[8] = {prv.x, prv.y, prv.z, prv.w, cur[it].x, cur[it].y, cur[it].z, cur[it].w};
    const int c = 255 - (it * 64 + lane);
#pragma unroll
    for (int e = 0; e < 4; e++) {
      u32x4 ov;
#pragma unroll
      for (int q = 0; q < 4; q++) {
        const int i0 = 15 - e - 2 * q, i1 = i0 - 1;
        const unsigned sel = ((i0 & 1) ? 0x0706u : 0x0504u) | (((i1 & 1) ? 0x0302u : 0x0100u) << 16);
        ov[q] = __builtin_amdgcn_perm(W_[i0 >> 1], W_[i1 >> 1], sel);
      }
      *(LAS u32x4*)(size_t)(cb + e * CPS + c * 16) = ov;
    }
  }
  if (lane < 8) *(LAS u32x4*)(size_t)(cb + (lane >> 1) * CPS + (256 + (lane & 1)) * 16) = (u32x4){0, 0, 0, 0};
  if (lane < 32) *(LAS u32x4*)(size_t)(cb + UOFF + lane * 16) = (u32x4){0, 0, 0, 0};
}
template <class F>
__device__ __forceinline__ void conv_load_u(__amdgpu_buffer_rsrc_t rw, int d, unsigned cb, int lane, F&& between) {
  u32x4 uv[4];
#pragma unroll
  for (int it = 0; it < 4; it++) uv[it] = __builtin_amdgcn_raw_buffer_load_b128(rw, OFF_UT + d * 4096 + (it * 64 + lane) * 16, 0, SC1);
  SBAR();
  between();
  SBAR();
#pragma unroll
  for (int it = 0; it < 4; it++) *(LAS u32x4*)(size_t)(cb + UOFF + 512 + (it * 64 + lane) * 16) = uv[it];
}
struct ConvFrags { u32x2 al[8], ah[8]; u32x4 B[8]; };
__device__ __forceinline__ void conv_read(ConvFrags& f, unsigned aa, unsigned ba) {
  asm volatile(
      "ds_read_b128 %16, %25\n\tds_read_b64 %7, %24 offset:3584\n\tds_read_b64 %15, %24 offset:3592\n\t"
      "ds_read_b128 %17, %25 offset:512\n\tds_read_b128 %18, %25 offset:1024\n\tds_read_b128 %19, %25 offset:1536\n\t"
      "ds_read_b128 %20, %25 offset:2048\n\tds_read_b128 %21, %25 offset:2560\n\tds_read_b128 %22, %25 offset:3072\n\t"
      "ds_read_b128 %23, %25 offset:3584\n\t"
      "ds_read_b64 %6, %24 offset:3072\n\tds_read_b64 %14, %24 offset:3080\n\t"
      "ds_read_b64 %5, %24 offset:2560\n\tds_read_b64 %13, %24 offset:2568\n\t"
      "ds_read_b64 %4, %24 offset:2048\n\tds_read_b64 %12, %24 offset:2056\n\t"
      "ds_read_b64 %3, %24 offset:1536\n\tds_read_b64 %11, %24 offset:1544\n\t"
      "ds_read_b64 %2, %24 offset:1024\n\tds_read_b64 %10, %24 offset:1032\n\t"
      "ds_read_b64 %1, %24 offset:512\n\tds_read_b64 %9, %24 offset:520\n\t"
      "ds_read_b64 %0, %24\n\tds_read_b64 %8, %24 offset:8"
      : "=&v"(f.al[0]), "=&v"(f.al[1]), "=&v"(f.al[2]), "=&v"(f.al[3]), "=&v"(f.al[4]), "=&v"(f.al[5]), "=&v"(f.al[6]), "=&v"(f.al[7]),
        "=&v"(f.ah[0]), "=&v"(f.ah[1]), "=&v"(f.ah[2]), "=&v"(f.ah[3]), "=&v"(f.ah[4]), "=&v"(f.ah[5]), "=&v"(f.ah[6]), "=&v"(f.ah[7]),
        "=&v"(f.B[0]), "=&v"(f.B[1]), "=&v"(f.B[2]), "=&v"(f.B[3]), "=&v"(f.B[4]), "=&v"(f.B[5]), "=&v"(f.B[6]), "=&v"(f.B[7])
      : "v"(aa), "v"(ba) : "memory");
}
__device__ __forceinline__ void conv_wait(ConvFrags& f) {
  asm volatile("s_waitcnt lgkmcnt(0)"
      : "+v"(f.al[0]), "+v"(f.al[1]), "+v"(f.al[2]), "+v"(f.al[3]), "+v"(f.al[4]), "+v"(f.al[5]), "+v"(f.al[6]), "+v"(f.al[7]),
        "+v"(f.ah[0]), "+v"(f.ah[1]), "+v"(f.ah[2]), "+v"(f.ah[3]), "+v"(f.ah[4]), "+v"(f.ah[5]), "+v"(f.ah[6]), "+v"(f.ah[7]),
        "+v"(f.B[0]), "+v"(f.B[1]), "+v"(f.B[2]), "+v"(f.B[3]), "+v"(f.B[4]), "+v"(f.B[5]), "+v"(f.B[6]), "+v"(f.B[7]) :: "memory");
}
template <int K> __device__ __forceinline__ void conv_read1(ConvFrags& f, unsigned aa, unsigned ba) {
  if constexpr (K < 8) asm volatile("ds_read_b128 %0, %1 offset:%2" : "=&v"(f.B[K]) : "v"(ba), "n"(512 * K) : "memory");
  else if constexpr (K < 16) asm volatile("ds_read_b64 %0, %1 offset:%2" : "=&v"(f.al[K - 8]) : "v"(aa), "n"(512 * (K - 8)) : "memory");
  else if constexpr (K < 24) asm volatile("ds_read_b64 %0, %1 offset:%2" : "=&v"(f.ah[K - 16]) : "v"(aa), "n"(512 * (K - 16) + 8) : "memory");
}
template <int K = 0> __device__ __forceinline__ void conv_step(f32x4 (&acc)[8], const ConvFrags& f, ConvFrags& nx, unsigned aa, unsigned ba) {
  if constexpr (K < 36) {
    constexpr int a = K < 8 ? 0 : K < 15 ? 1 : K < 21 ? 2 : K < 26 ? 3 : K < 30 ? 4 : K < 33 ? 5 : K < 35 ? 6 : 7;
    constexpr int i = K - (a == 0 ? 0 : a == 1 ? 8 : a == 2 ? 15 : a == 3 ? 21 : a == 4 ? 26 : a == 5 ? 30 : a == 6 ? 33 : 35);
    acc[a + i] = mfma16(mk8(f.al[7 - a], f.ah[7 - a]), __builtin_bit_cast(bf16x8, f.B[i]), acc[a + i]);
    conv_read1<K>(nx, aa, ba);
    SBAR();
    conv_step<K + 1>(acc, f, nx, aa, ba);
  }
}
__device__ __forceinline__ void conv_main(f32x4 (&acc)[8], unsigned cb, int lane, int c0, int c1) {
  const int fr = lane & 15, g4 = lane >> 4;
  const unsigned abase = cb + (3 - (fr & 3)) * CPS + 2 * (2044 - 16 * 112 - 16 * (g4 >> 1) + 8 * (g4 & 1) - (fr & ~3));
  const unsigned bbase = cb + UOFF + 512 + 2 * (16 * fr - 16 * (g4 >> 1) + 8 * (g4 & 1));
#pragma unroll
  for (int c = 0; c < 8; c++) acc[c] = (f32x4){0.f, 0.f, 0.f, 0.f};
  ConvFrags f0, f1;
  asm volatile("s_waitcnt lgkmcnt(0)" ::: "memory");
  conv_read(f0, abase - 64 * c0, bbase - 64 * c0);
  for (int cc = c0; cc < c1; cc += 2) {
    conv_wait(f0);
    SBAR();
    conv_step(acc, f0, f1, abase - 64 * (cc + 1), bbase - 64 * (cc + 1));
    conv_wait(f1);
    SBAR();
    const int cn = cc + 2 < c1 ? cc + 2 : cc + 1;
    conv_step(acc, f1, f0, abase - 64 * cn, bbase - 64 * cn);
  }
  conv_wait(f0);
}
__device__ __forceinline__ void conv_store(f32x4 y, unsigned cb, int c, int lane, float Dd, __amdgpu_buffer_rsrc_t rw, int d) {
  const int fr = lane & 15, g4 = lane >> 4;
  const u32x2 uu = lds_b64(cb + UOFF + 512 + 2 * (256 * c + 16 * fr + 4 * g4));
  const float r0 = fmaf(bflo(uu.x), Dd, y[0]), r1 = fmaf(bfhi(uu.x), Dd, y[1]);
  const float r2 = fmaf(bflo(uu.y), Dd, y[2]), r3 = fmaf(bfhi(uu.y), Dd, y[3]);
  __builtin_amdgcn_raw_buffer_store_b64((u32x2){pk2(r0, r1), pk2(r2, r3)}, rw, OFF_YT + d * 4096 + (256 * c + 16 * fr + 4 * g4) * 2, 0, SC1);
}

__global__ __launch_bounds__(512, 2) void fused(const u16* __restrict__ x, const u16* __restrict__ wqkv, const u16* __restrict__ wo,
                                                const u16* __restrict__ Bk, const u16* __restrict__ Bv, const u16* __restrict__ shk,
                                                const u16* __restrict__ hk, const u16* __restrict__ Dv, const u16* __restrict__ gw,
                                                u16* __restrict__ out, char* __restrict__ ws) {
  extern __shared__ __attribute__((aligned(16))) char sm[];
  const int tid = threadIdx.x, lane = tid & 63;
  const int w = __builtin_amdgcn_readfirstlane(tid >> 6);
  const int b = blockIdx.x, xcd = b & 7, r = b >> 3;
  const int fr = lane & 15, g4 = lane >> 4;
  const unsigned lb = (unsigned)(size_t)(LAS char*)sm;
  const __amdgpu_buffer_rsrc_t rw = __builtin_amdgcn_make_buffer_rsrc(ws, 0, WS_USED, 0x00020000);
  if (r < 24 && (w >> 1) == 2) {
    const bool kv_ = r < 16;
    gemm_first_x(w - 4, lane, lb, x, (xcd & 3) * 4 + (r & 3), kv_ ? (xcd >> 2) * 4 + (r >> 2) : (xcd >> 2) * 2 + ((r - 16) >> 2), kv_);
  }
  TS_DECL TS(0);
  unsigned e0 = 0;
#define PUT_TAG() do { if (w == 7 && lane == 0) { *(LAS unsigned*)(size_t)(lb + L_TAG) = e0 + 1u; *(LAS u32x4*)(size_t)(lb + L_CNT) = (u32x4){0u, 0u, 0u, 0u}; } } while (0)
#define TAG (*(const LAS unsigned*)(size_t)(lb + L_TAG))
  if (r < 24) {
    const bool isKV = r < 16;
    const int rt = (xcd & 3) * 4 + (r & 3);
    const int cg = isKV ? (xcd >> 2) * 4 + (r >> 2) : (xcd >> 2) * 2 + ((r - 16) >> 2);
    f32x4 acc[4][5];
    const int rp = (w >> 1) & 1, hf = w & 1;
    if (w < 4) {
      const unsigned xa0 = lb + (rp * 64 + fr) * 128 + ((g4 ^ ((fr >> 1) & 7)) << 4);
      const unsigned wl = lb + XS + (8 * g4 + (fr >> 2)) * 320 + ((lane & 3) >> 1) * 16 + (lane & 1) * 8 + hf * 160;
      const unsigned sgn = (g4 & 1) * 32;
      const unsigned wa0 = hf ? wl - sgn : wl + sgn, wa1 = hf ? wl + sgn : wl - sgn;
#pragma unroll
      for (int i = 0; i < 4; i++)
#pragma unroll
        for (int t = 0; t < 5; t++) acc[i][t] = (f32x4){0.f, 0.f, 0.f, 0.f};
      if (isKV) gemm_compute<true>(acc, xa0, wa0, wa1 TSARG); else gemm_compute<false>(acc, xa0, wa0, wa1 TSARG);
    } else if (w < 6) {
      gemm_loader(w - 4, lane, lb, x, wqkv, rt, cg, isKV);
    } else {
      const int w6 = w - 6;
      unsigned pf[5] = {0, 0, 0, 0, 0};
      SBAR();
      if (w == 7) e0 = flag_ld(ws, F_E);
      if (isKV) {
        const int l16 = lane & 15, row0 = rt * 128 + cg * 16 + w6 * 8;
        const u32x4 g0 = *(const u32x4*)(gw + lane * 8), g1 = *(const u32x4*)(gw + 512 + l16 * 8);
        u32x4 gx0[8], gx1[8];
#pragma unroll
        for (int i = 0; i < 8; i++) {
          const u16* xr = x + (size_t)(row0 + i) * D;
          gx0[i] = *(const u32x4*)(xr + lane * 8); gx1[i] = *(const u32x4*)(xr + 512 + l16 * 8);
        }
        SBAR();
        BAR(); BAR(); BAR();
        float a[8];
        const float m1 = lane < 16 ? 1.f : 0.f;
#pragma unroll
        for (int i = 0; i < 8; i++) {
          float a0 = 0.f, a1 = 0.f;
#pragma unroll
          for (int j = 0; j < 4; j++) {
            a0 = fmaf(bflo(gx0[i][j]), bflo(g0[j]), a0); a0 = fmaf(bfhi(gx0[i][j]), bfhi(g0[j]), a0);
            a1 = fmaf(bflo(gx1[i][j]), bflo(g1[j]), a1); a1 = fmaf(bfhi(gx1[i][j]), bfhi(g1[j]), a1);
          }
          a[i] = fmaf(a1, m1, a0);
          if (i == 3) { SBAR(); BAR(); SBAR(); }
        }
        SBAR(); BAR(); SBAR();
#pragma unroll
        for (int i = 0; i < 4; i++) { const float lo = a[i], hi = a[i + 4]; const bool up = lane & 4; a[i] = (up ? hi : lo) + __shfl_xor(up ? lo : hi, 4); }
#pragma unroll
        for (int i = 0; i < 2; i++) { const float lo = a[i], hi = a[i + 2]; const bool up = lane & 2; a[i] = (up ? hi : lo) + __shfl_xor(up ? lo : hi, 2); }
        { const float lo = a[0], hi = a[1]; const bool up = lane & 1; a[0] = (up ? hi : lo) + __shfl_xor(up ? lo : hi, 1); }
        a[0] += __shfl_xor(a[0], 8); a[0] += __shfl_xor(a[0], 16); a[0] += __shfl_xor(a[0], 32);
        if (lane < 8) __builtin_amdgcn_raw_buffer_store_b32(__float_as_uint(rbf(1.f / (1.f + __expf(-rbf(a[0]))))), rw, OFF_G + (row0 + lane) * 4, 0, SC1);
        PUT_TAG();
        asm volatile("" :: "v"(pf[0]), "v"(pf[1]), "v"(pf[2]), "v"(pf[3]), "v"(pf[4]));
        SBAR();
        BAR(); BAR(); BAR(); BAR(); BAR();
      } else {
        const int hw = (xcd * 8 + (r - 16)) * 2 + w6;
        const unsigned scr = lb + L_T + w6 * 6144;
#pragma unroll
        for (int bt = 0; bt < 2; bt++) {
          const int blk = bt == 0 ? hw : (hw < 32 ? 128 + hw : hw), ct = (blk * 3277) >> 16, ks = blk - 20 * ct;
          u32x4 v[5];
#pragma unroll
          for (int i = 0; i < 5; i++) {
            const int c = i * 64 + lane, row = (c * 6554) >> 16, ch = c - 10 * row;
            v[i] = *(const u32x4*)(wo + (size_t)(32 * ks + row) * D + 80 * ct + 8 * ch);
          }
          SBAR();
          BAR();
          if (bt == 0) PUT_TAG();
          BAR();
          if (bt == 0 || hw < 32) {
#pragma unroll
            for (int i = 0; i < 5; i++) *(LAS u32x4*)(size_t)(scr + (i * 64 + lane) * 16) = v[i];
            const unsigned ta = scr + (8 * g4 + (fr >> 2)) * 160 + (lane & 3) * 8;
#pragma unroll
            for (int t = 0; t < 5; t++) {
              const u32x2 lo = lds_tr(ta + 32 * t), hi = lds_tr(ta + 32 * t + 640);
              __builtin_amdgcn_raw_buffer_store_b128((u32x4){lo.x, lo.y, hi.x, hi.y}, rw, OFF_WOP + (blk * 5 + t) * 1024 + lane * 16, 0, SC1);
            }
          }
          SBAR();
          BAR(); BAR();
        }
        asm volatile("" :: "v"(pf[0]), "v"(pf[1]), "v"(pf[2]), "v"(pf[3]), "v"(pf[4]));
        DRAIN();
        if (lds_last(lb + L_CNT + 8, lane, 2) && lane == 0) flag_st(ws, F_W + (xcd * 8 + (r - 16)) * 128, TAG);
        BAR(); BAR();
      }
    }
    TS(1);
    BAR();
    TS(11);
    const unsigned tag = TAG;
    if (isKV) {
      if (w < 4) {
#pragma unroll
        for (int t = 0; t < 5; t++) {
          const bool isk = hf ? t < 2 : t < 3;
          const int slot = hf ? (t < 2 ? 3 + t : 5 + t) : (t < 3 ? t : 2 + t);
#pragma unroll
          for (int m = 0; m < 2; m++) {
            u32x4 f;
            if (isk) f = (u32x4){pk2(phi(acc[2 * m][t][0]), phi(acc[2 * m][t][1])), pk2(phi(acc[2 * m][t][2]), phi(acc[2 * m][t][3])),
                                 pk2(phi(acc[2 * m + 1][t][0]), phi(acc[2 * m + 1][t][1])), pk2(phi(acc[2 * m + 1][t][2]), phi(acc[2 * m + 1][t][3]))};
            else f = (u32x4){pk2(acc[2 * m][t][0], acc[2 * m][t][1]), pk2(acc[2 * m][t][2], acc[2 * m][t][3]),
                             pk2(acc[2 * m + 1][t][0], acc[2 * m + 1][t][1]), pk2(acc[2 * m + 1][t][2], acc[2 * m + 1][t][3])};
            *(LAS u32x4*)(size_t)(lb + ((rp * 2 + m) * 10 + slot) * 1024 + lane * 16) = f;
          }
        }
      } else {
        for (int i = tid - 256; i < 6048 / 16; i += 256) *(LAS u32x4*)(size_t)(lb + L_T + i * 16) = (u32x4){0, 0, 0, 0};
        if (w == 7) poll(ws, F_W + lane * 128, true, tag);
      }
      TS(12);
      BAR();
      TS(2);
      switch (w) {
        case 0: stat_tiles<0>(lb, lane, rw, OFF_P + (rt * 8 + cg) * 3360); break;
        case 1: stat_tiles<1>(lb, lane, rw, OFF_P + (rt * 8 + cg) * 3360); break;
        case 2: stat_tiles<2>(lb, lane, rw, OFF_P + (rt * 8 + cg) * 3360); break;
        case 3: stat_tiles<3>(lb, lane, rw, OFF_P + (rt * 8 + cg) * 3360); break;
        case 4: stat_tiles<4>(lb, lane, rw, OFF_P + (rt * 8 + cg) * 3360); break;
        case 5: stat_tiles<5>(lb, lane, rw, OFF_P + (rt * 8 + cg) * 3360); break;
        case 6: stat_tiles<6>(lb, lane, rw, OFF_P + (rt * 8 + cg) * 3360); break;
        default: stat_tiles<7>(lb, lane, rw, OFF_P + (rt * 8 + cg) * 3360); break;
      }
      TS(14);
      DRAIN();
      TS(15);
      BAR();
      TS(3);
      if (w == 0) {
        if (lane == 0) flag_st(ws, F_P + (rt * 8 + cg) * 128, tag);
        poll(ws, lane < 16 ? F_P + (lane * 8 + cg) * 128 : F_PQ + (rt * 4 + (cg >> 1)) * 128, lane <= 16, tag);
      } else if (w < 7) {
        const i32x4 sws = make_srd(ws, WS_USED);
        const unsigned vwv = OFF_WOP + ((xcd >> 2) * 4 + (r >> 3)) * 102400 + lane * 16;
        for (int i = w - 1; i < 100; i += 6) {
          int pc = 12 * (r & 7) + i; pc = __builtin_amdgcn_readfirstlane(pc >= 100 ? pc - 100 : pc);
          glds16_sc1(lb + pc * 1024, vwv, sws, pc * 1024);
        }
      }
      constexpr int PJ[9] = {0, 1, 1, 2, 2, 3, 3, 4, 4}, PK[9] = {0, 0, 1, 0, 1, 1, 2, 1, 2};
      unsigned an[9], ad[9];
#pragma unroll
      for (int p = 0; p < 9; p++) {
        const int E = 16 * PJ[p] + fr, h = (E * 205) >> 12, e = E - 20 * h;
        int o_ = 32 * PK[p] + 8 * g4 - 20 * h; o_ = o_ < -8 ? -8 : o_ > 20 ? 20 : o_;
        an[p] = lb + L_T + (h * 21 + e) * 72 + (8 + o_) * 2; ad[p] = lb + L_T + (h * 21 + 20) * 72 + (8 + o_) * 2;
      }
      BAR();
      TS(4);
      bf16x8 Bq[3];
      {
        const unsigned prow = OFF_PQ + ((rt * 8 + w) * 8 + cg) * 3072 + lane * 16;
#pragma unroll
        for (int ks = 0; ks < 3; ks++) Bq[ks] = __builtin_bit_cast(bf16x8, __builtin_amdgcn_raw_buffer_load_b128(rw, prow + 1024 * ks, 0, SC1));
      }
      if (tid < 210) {
        const unsigned pb = OFF_P + cg * 3360 + tid * 16;
        u32x4 v[16];
#pragma unroll
        for (int k = 0; k < 16; k++) v[k] = __builtin_amdgcn_raw_buffer_load_b128(rw, pb, (k ^ rt) * (8 * 3360), SC1);
        unsigned o4[4];
#pragma unroll
        for (int c = 0; c < 4; c++) {
          float lo[16], hi[16];
#pragma unroll
          for (int k = 0; k < 16; k++) { lo[k] = bflo(v[k][c]); hi[k] = bfhi(v[k][c]); }
#pragma unroll
          for (int st = 1; st < 16; st <<= 1)
#pragma unroll
            for (int k = 0; k < 16; k += 2 * st) { lo[k] += lo[k + st]; hi[k] += hi[k + st]; }
          o4[c] = pk2(lo[0], hi[0]);
        }
        TS(16);
#pragma unroll
        for (int q = 0; q < 2; q++) {
          const int i4 = 2 * tid + q, row = (i4 * 13108) >> 16, d = 4 * (i4 - row * 5);
          *(LAS u32x2*)(size_t)(lb + L_T + row * 72 + (8 + d) * 2) = (u32x2){o4[2 * q], o4[2 * q + 1]};
        }
      }
      BAR();
      TS(5);
      {
        if (g4 >= 2) Bq[2] = __builtin_bit_cast(bf16x8, (u32x4){0, 0, 0, 0});
        f32x4 nu[5], de[5];
#pragma unroll
        for (int j = 0; j < 5; j++) { nu[j] = (f32x4){0.f, 0.f, 0.f, 0.f}; de[j] = nu[j]; }
        bf16x8 An[9], Ad[9];
#pragma unroll
        for (int p = 0; p < 9; p++) {
          unsigned an8 = an[p] + 8, ad8 = ad[p] + 8;
          asm volatile("" : "+v"(an8), "+v"(ad8));
          An[p] = mk8(lds_b64(an[p]), lds_b64(an8)); Ad[p] = mk8(lds_b64(ad[p]), lds_b64(ad8));
        }
#pragma unroll
        for (int p = 0; p < 9; p++) { de[PJ[p]] = mfma16(Ad[p], Bq[PK[p]], de[PJ[p]]); nu[PJ[p]] = mfma16(An[p], Bq[PK[p]], nu[PJ[p]]); }
        u32x2 ov[5];
#pragma unroll
        for (int j = 0; j < 5; j++) {
          const float rd = __builtin_amdgcn_rcpf(de[j][0] + 1e-6f);
          ov[j] = (u32x2){pk2(nu[j][0] * rd, nu[j][1] * rd), pk2(nu[j][2] * rd, nu[j][3] * rd)};
        }
        store_frags(ov, rw, OFF_O + (rt * 8 + w) * 20480, 80 * cg, fr, g4);
      }
      TS(17);
      DRAIN();
      BAR();
      if (tid == 0) flag_st(ws, F_O + (rt * 8 + cg) * 128, tag);
      TS(6);
    } else {
      const int vb = xcd * 8 + (r - 16);
      const int d = 80 * (vb >> 3) + 48 + 4 * (vb & 7) + (w & 3);
      const unsigned cb = lb + (w & 3) * CHS;
      if (w < 4) {
#pragma unroll
        for (int i = 0; i < 4; i++) {
          u32x2 pv[5];
#pragma unroll
          for (int t = 0; t < 5; t++) pv[t] = (u32x2){pk2(phi(acc[i][t][0]), phi(acc[i][t][1])), pk2(phi(acc[i][t][2]), phi(acc[i][t][3]))};
          store_frags(pv, rw, OFF_PQ + ((rt * 8 + rp * 4 + i) * 8 + cg * 2 + hf) * 3072, 0, fr, g4);
        }
      } else {
        u32x4 hc[4], hp[4];
        conv_load_h(hc, hp, hk + (size_t)d * S, lane);
        poll(ws, F_U + lane * 128, true, tag);
        conv_load_u(rw, d, cb, lane, [&]() { conv_prep_h(hc, hp, cb, lane); });
      }
      TS(12);
      DRAIN();
      BAR();
      TS(2);
      if (tid == 0) flag_st(ws, F_PQ + (rt * 4 + cg) * 128, tag);
      if (w < 4) {
        const float Dd = bflo(Dv[d]);
        TS(3);
        f32x4 y[8];
        conv_main(y, cb, lane, 0, 8);
        TS(4);
#pragma unroll
        for (int c = 0; c < 8; c++) conv_store(y[c], cb, c, lane, Dd, rw, d);
        DRAIN();
      } else if (w == 7) {
        poll(ws, F_W + lane * 128, true, tag);
      }
      BAR();
      if (tid == 0) flag_st(ws, F_Y + (64 + vb) * 128, tag);
      TS(5); TS(6);
    }
  } else {
    const int pw = xcd * 8 + (r - 24);
    const int c6 = w < 4 ? w : 4 + ((w - 4) >> 1);
    const int d = 80 * (pw >> 3) + 6 * (pw & 7) + c6;
    const unsigned cb = lb + c6 * CHS;
    const bool prep = w < 4 || !(w & 1);
    u32x4 hc[4], hp[4];
    {
      const int rbase = xcd * 256 + ((r - 24) >> 1) * 64, cbase = ((r - 24) & 1) * 320;
      u32x4 kr[2][7], vr[2][4], sp[2][4];
#pragma unroll
      for (int q = 0; q < 2; q++) {
        const int it = w + 8 * q;
        if (q == 0 || w < 2) {
          const int ch = cbase + (it >> 1) * 64 + (lane & 7) * 8;
          const int s0 = rbase + (it & 1) * 32 + (lane >> 3) * 4;
#pragma unroll
          for (int j = 0; j < 7; j++) { int s = s0 - 3 + j; kr[q][j] = *(const u32x4*)(Bk + (size_t)(s < 0 ? 0 : s) * D + ch); }
#pragma unroll
          for (int j = 0; j < 4; j++) vr[q][j] = *(const u32x4*)(Bv + (size_t)(s0 + j) * D + ch);
#pragma unroll
          for (int j = 0; j < 4; j++) sp[q][j] = ((const u32x4*)(shk + ch * 4))[j];
        }
      }
      if (prep) conv_load_h(hc, hp, hk + (size_t)d * S, lane);
      if (w == 7) e0 = flag_ld(ws, F_E);
      SBAR();
#pragma unroll
      for (int q = 0; q < 2; q++) {
        const int it = w + 8 * q;
        if (q == 0 || w < 2) {
          const int s0 = rbase + (it & 1) * 32 + (lane >> 3) * 4;
          if (s0 == 0) { kr[q][0] = (u32x4){0, 0, 0, 0}; kr[q][1] = kr[q][0]; kr[q][2] = kr[q][0]; }
          const int rg = (it & 1) * 8 + (lane >> 3);
          const unsigned la = lb + L_UIMG + ((it >> 1) * 64 + (lane & 7) * 8) * 128 + (((rg >> 1) ^ (lane & 7)) << 4) + (rg & 1) * 8;
#pragma unroll
          for (int c = 0; c < 8; c++) {
            const unsigned s01 = sp[q][c >> 1][(c & 1) * 2], s23 = sp[q][c >> 1][(c & 1) * 2 + 1];
            const float w0 = bflo(s01), w1 = bfhi(s01), w2 = bflo(s23), w3 = bfhi(s23);
            float kk[7], u[4];
#pragma unroll
            for (int j = 0; j < 7; j++) { unsigned dd = kr[q][j][c >> 1]; kk[j] = (c & 1) ? bfhi(dd) : bflo(dd); }
#pragma unroll
            for (int j = 0; j < 4; j++) {
              unsigned dd = vr[q][j][c >> 1]; float v = (c & 1) ? bfhi(dd) : bflo(dd);
              float ks = kk[j] * w3; ks = fmaf(kk[j + 1], w2, ks); ks = fmaf(kk[j + 2], w1, ks); ks = fmaf(kk[j + 3], w0, ks);
              u[j] = ks * v;
            }
            *(LAS u32x2*)(size_t)(la + c * 128) = (u32x2){pk2(u[0], u[1]), pk2(u[2], u[3])};
          }
        }
      }
      PUT_TAG();
      BAR();
#pragma unroll
      for (int i = 0; i < 5; i++) {
        const int chl = (w * 5 + i) * 8 + (lane >> 3), k = lane & 7;
        const u32x4 v = *(const LAS u32x4*)(size_t)(lb + L_UIMG + chl * 128 + ((k ^ ((chl >> 3) & 7)) << 4));
        __builtin_amdgcn_raw_buffer_store_b128(v, rw, OFF_UT + (cbase + chl) * 4096 + rbase * 2 + k * 16, 0, SC1);
      }
      TS(1);
    }
    if (w < 5 && prep) conv_prep_h(hc, hp, cb, lane);
    DRAIN();
    BAR();
    TS(2);
    const unsigned tag = TAG;
    if (tid == 0) flag_st(ws, F_U + pw * 128, tag);
    const float Dd = bflo(Dv[d]);
    if (prep) {
      if (w == 6) conv_prep_h(hc, hp, cb, lane);
      poll(ws, F_U + lane * 128, true, tag);
      conv_load_u(rw, d, cb, lane, [&]() {});
    }
    BAR();
    TS(3);
    f32x4 y[8];
    conv_main(y, cb, lane, w < 4 ? 0 : (w & 1) * 4, w < 4 ? 8 : (w & 1) * 4 + 4);
    TS(4);
    if (w < 4) {
#pragma unroll
      for (int c = 0; c < 8; c++) conv_store(y[c], cb, c, lane, Dd, rw, d);
    }
    BAR();
    if (w >= 4) {
#pragma unroll
      for (int c = 0; c < 4; c++) { const f32x4 lo = y[c], hi = y[4 + c]; *(LAS f32x4*)(size_t)(cb + ((w & 1) * 4 + c) * 1024 + lane * 16) = (w & 1) ? lo : hi; }
    }
    BAR();
    if (w >= 4) {
#pragma unroll
      for (int c = 0; c < 4; c++) {
        const f32x4 o_ = *(const LAS f32x4*)(size_t)(cb + (((w & 1) ^ 1) * 4 + c) * 1024 + lane * 16);
        const f32x4 lo = y[c], hi = y[4 + c];
        const f32x4 mine = (w & 1) ? hi : lo;
        conv_store((w & 1) ? o_ + mine : mine + o_, cb, (w & 1) * 4 + c, lane, Dd, rw, d);
      }
    }
    if (w == 0) poll(ws, F_W + lane * 128, true, tag);
    DRAIN();
    BAR();
    if (tid == 0) flag_st(ws, F_Y + pw * 128, tag);
    TS(5); TS(6);
  }

  {
    const unsigned tag = TAG;
    const int rt = (xcd & 3) * 8 + (r & 7), ct = (xcd >> 2) * 4 + (r >> 3);
    const int sw_ = w & 3, kk = w >> 2;
    const i32x4 sws = make_srd(ws, WS_USED);
    TS(18);
    const int srow = rt * 64 + sw_ * 16 + fr;
    u32x2 xv[3];
#pragma unroll
    for (int t = 0; t < 3; t++) { const int tt = kk ? (t < 2 ? 3 + t : 4) : t; xv[t] = *(const u32x2*)(x + (size_t)srow * D + ct * 80 + 16 * tt + 4 * g4); }
    SBAR();
    const int rot = ((ct & 3) * 5) >> 1;
    const unsigned wa0 = lb + kk * 5120 + lane * 16;
    bf16x8 wf0[5][5];
    auto read_wf0 = [&]() {
#pragma unroll
      for (int s = 0; s < 5; s++) {
        int q = s + rot; q = q >= 10 ? q - 10 : q;
#pragma unroll
        for (int t = 0; t < 5; t++) wf0[s][t] = lds_b128(wa0 + q * 10240 + t * 1024);
      }
    };
    if (r < 16) read_wf0();
    SBAR();
    if (w == 0) {
      poll(ws, F_O + ((rt >> 1) * 8 + (lane & 7)) * 128, lane < 8, tag);
    } else if (w == 7) {
      poll(ws, F_Y + ((lane & 8 ? 64 : 0) + ct * 8 + (lane & 7)) * 128, lane < 16, tag);
    } else {
      if (r >= 16) {
        const unsigned vwv = OFF_WOP + ct * 102400 + lane * 16;
        for (int i = w - 1; i < 100; i += 6) { int pc = 12 * (r & 7) + i; pc = __builtin_amdgcn_readfirstlane(pc >= 100 ? pc - 100 : pc); glds16_sc1(lb + pc * 1024, vwv, sws, pc * 1024); }
      }
      if (w == 6 && b == 0) {
        poll(ws, F_U + lane * 128, true, tag); poll(ws, F_PQ + lane * 128, true, tag);
        poll(ws, F_P + lane * 128, true, tag); poll(ws, F_P + (64 + lane) * 128, true, tag);
        if (lane == 0) flag_st(ws, F_E, tag);
      }
      DRAIN();
    }
    BAR();
    TS(7);
    asm volatile("" :: "v"(xv[0]), "v"(xv[1]), "v"(xv[2]));
    bf16x8 of[10];
    {
      const unsigned vo = OFF_O + (rt * 4 + sw_) * 20480 + kk * 1024 + lane * 16;
#pragma unroll
      for (int s = 0; s < 10; s++) { int q = s + rot; q = __builtin_amdgcn_readfirstlane(q >= 10 ? q - 10 : q); bload16_sc1(of[s], vo, sws, q * 2048); }
    }
    if (w == 7) {
      for (int pc = 0; pc < 10; pc++)
        glds16_sc1(lb + L_Y + pc * 1024, OFF_YT + (ct * 80 + pc * 8 + (lane >> 3)) * 4096 + rt * 128 + (lane & 7) * 16, sws, 0);
    }
    float g;
    asm volatile("buffer_load_dword %0, %1, %2, 0 offen sc1" : "=v"(g) : "v"(OFF_G + srow * 4), "s"(sws) : "memory");
    TS(8);
    if (r >= 16) read_wf0();
    f32x4 acc[5];
#pragma unroll
    for (int t = 0; t < 5; t++) acc[t] = (f32x4){0.f, 0.f, 0.f, 0.f};
    {
      bf16x8 wf1[5][5];
#pragma unroll
      for (int s = 0; s < 5; s++) {
        int q = 5 + s + rot; q = q >= 10 ? q - 10 : q;
#pragma unroll
        for (int t = 0; t < 5; t++) wf1[s][t] = lds_b128(wa0 + q * 10240 + t * 1024);
      }
      SBAR();
      if (w == 7) asm volatile("s_waitcnt vmcnt(16)" ::: "memory"); else asm volatile("s_waitcnt vmcnt(6)" ::: "memory");
      asm volatile("" : "+v"(of[0]), "+v"(of[1]), "+v"(of[2]), "+v"(of[3]), "+v"(of[4]) :: "memory");
      SBAR();
#pragma unroll
      for (int s = 0; s < 5; s++)
#pragma unroll
        for (int t = 0; t < 5; t++) acc[t] = mfma16(wf0[s][t], of[s], acc[t]);
      SBAR();
      if (w == 7) asm volatile("s_waitcnt vmcnt(11)" ::: "memory"); else asm volatile("s_waitcnt vmcnt(1)" ::: "memory");
      asm volatile("" : "+v"(of[5]), "+v"(of[6]), "+v"(of[7]), "+v"(of[8]), "+v"(of[9]) :: "memory");
      SBAR();
#pragma unroll
      for (int s = 0; s < 5; s++)
#pragma unroll
        for (int t = 0; t < 5; t++) acc[t] = mfma16(wf1[s][t], of[5 + s], acc[t]);
      SBAR();
    }
    TS(9);
    const unsigned ra = lb + L_RED + (sw_ * 5) * 1024 + lane * 16;
    if (kk == 0) { *(LAS f32x4*)(size_t)(ra + 3 * 1024) = acc[3]; *(LAS f32x4*)(size_t)(ra + 4 * 1024) = acc[4]; }
    else { *(LAS f32x4*)(size_t)(ra) = acc[0]; *(LAS f32x4*)(size_t)(ra + 1024) = acc[1]; *(LAS f32x4*)(size_t)(ra + 2048) = acc[2]; }
    asm volatile("s_waitcnt vmcnt(0)" : "+v"(g) :: "memory");
    BAR();
    TS(20);
    const float omg = 1.f - g;
    const int nt = kk ? 2 : 3;
#pragma unroll
    for (int t = 0; t < 3; t++) {
      if (t < nt) {
        const int tt = kk ? 3 + t : t;
        const f32x4 a0 = acc[t], a1 = acc[3 + (t < 2 ? t : 1)];
        f32x4 a = *(const LAS f32x4*)(size_t)(ra + tt * 1024) + (kk ? a1 : a0);
        u32x2 yv = lds_tr(lb + L_Y + (16 * tt + 4 * g4 + (fr >> 2)) * 128 + (sw_ * 16 + 4 * (fr & 3)) * 2);
        float xs[4] = {bflo(xv[t].x), bfhi(xv[t].x), bflo(xv[t].y), bfhi(xv[t].y)};
        float ys[4] = {bflo(yv.x), bfhi(yv.x), bflo(yv.y), bfhi(yv.y)};
        float r_[4];
#pragma unroll
        for (int q = 0; q < 4; q++) r_[q] = fmaf(g, a[q], omg * (xs[q] * ys[q]));
        *(u32x2*)(out + (size_t)srow * D + ct * 80 + 16 * tt + 4 * g4) = (u32x2){pk2(r_[0], r_[1]), pk2(r_[2], r_[3])};
      }
    }
    TS(10); TS_OUT(21);
  }
}

extern "C" void kernel_launch(void* const* d_in, const int* in_sizes, int n_in, void* d_out, int out_size, void* d_ws, size_t ws_size,
                              hipStream_t stream) {
  const u16* x = (const u16*)d_in[0]; const u16* wqkv = (const u16*)d_in[1]; const u16* wo = (const u16*)d_in[2];
  const u16* Bk = (const u16*)d_in[4]; const u16* Bv = (const u16*)d_in[5]; const u16* shk = (const u16*)d_in[6];
  const u16* hk = (const u16*)d_in[7]; const u16* Dv = (const u16*)d_in[8]; const u16* gw = (const u16*)d_in[9];
  (void)hipFuncSetAttribute((const void*)fused, hipFuncAttributeMaxDynamicSharedMemorySize, LDS_TOTAL);
  fused<<<256, 512, LDS_TOTAL, stream>>>(x, wqkv, wo, Bk, Bv, shk, hk, Dv, gw, (u16*)d_out, (char*)d_ws);
}
```

```cpp
#include <hip/hip_runtime.h>
#include <hip/hip_bf16.h>
#include <type_traits>
#define NO_PF

typedef unsigned short u16;
typedef __bf16 bf16x2 __attribute__((ext_vector_type(2)));
typedef __bf16 bf16x8 __attribute__((ext_vector_type(8)));
typedef float f32x4 __attribute__((ext_vector_type(4)));
typedef unsigned u32x4 __attribute__((ext_vector_type(4)));
typedef unsigned u32x2 __attribute__((ext_vector_type(2)));
typedef short v4i16_t __attribute__((ext_vector_type(4)));
typedef int i32x4 __attribute__((ext_vector_type(4)));
typedef __attribute__((address_space(1))) unsigned gu32;
#define LAS __attribute__((address_space(3)))
#define SBAR() __builtin_amdgcn_sched_barrier(0)
#define SGB(mask, n) __builtin_amdgcn_sched_group_barrier(mask, n, 0)
#define BAR() asm volatile("s_waitcnt lgkmcnt(0)\n\ts_barrier" ::: "memory")
#define DRAIN() asm volatile("s_waitcnt vmcnt(0)" ::: "memory")
#define SC1 16

#define TS_DECL
#define TSARG
#define TSPAR
#define TS(i)
#define TS_OUT(n)

constexpr int S = 2048, D = 640;
constexpr unsigned SZ = S * D * 2;
constexpr unsigned F_E = 0, F_U = 128, F_PQ = F_U + 64 * 128, F_P = F_PQ + 64 * 128, F_O = F_P + 128 * 128, F_Y = F_O + 128 * 128, F_W = F_Y + 128 * 128;
constexpr unsigned OFF_PQ = 0x20000, OFF_P = OFF_PQ + 128 * 8 * 3 * 1024, OFF_UT = OFF_P + (1u << 19), OFF_G = OFF_UT + SZ, OFF_WOP = OFF_G + 8192,
                   OFF_O = OFF_WOP + (1u << 20), OFF_YT = OFF_O + SZ, WS_USED = OFF_YT + SZ;
constexpr int XS = 16384, WSB = 20480, STG = XS + WSB;
constexpr int L_EXTRA = 4 * STG;
constexpr int L_TAG = L_EXTRA, L_CNT = L_EXTRA + 16, L_T = L_EXTRA + 64;
constexpr int L_SCR = L_T + 6144 + 5120;
constexpr int LDS_TOTAL = L_SCR + 256;
constexpr int CPS = 4160, UOFF = 4 * CPS, CHS = UOFF + 4608;
constexpr int L_UIMG = 5 * CHS;
constexpr int L_Y = 102400, L_RED = L_Y + 10240;

__device__ __forceinline__ unsigned pk2(float a, float b) { bf16x2 r = {(__bf16)a, (__bf16)b}; return __builtin_bit_cast(unsigned, r); }
__device__ __forceinline__ float rbf(float a) { return (float)(__bf16)a; }
__device__ __forceinline__ float bflo(unsigned v) { return __uint_as_float(v << 16); }
__device__ __forceinline__ float bfhi(unsigned v) { return __uint_as_float(v & 0xffff0000u); }
__device__ __forceinline__ float phi(float t) {
  return __builtin_amdgcn_fmed3f(t, 0.f, 3.0e38f) + (__builtin_amdgcn_fmed3f(__builtin_amdgcn_exp2f(t * 1.44269504f), 0.f, 1.f) + 1e-6f);
}
__device__ __forceinline__ i32x4 make_srd(const void* p, unsigned bytes) {
  unsigned long long a = (unsigned long long)p; i32x4 r;
  r.x = __builtin_amdgcn_readfirstlane((int)(unsigned)a); r.y = __builtin_amdgcn_readfirstlane((int)(unsigned)(a >> 32));
  r.z = __builtin_amdgcn_readfirstlane((int)bytes); r.w = 0x00020000; return r;
}
__device__ __forceinline__ void glds16(unsigned ldsaddr, unsigned voff, i32x4 srd, unsigned soff) {
  asm volatile("s_mov_b32 m0, %0\n\ts_nop 0\n\tbuffer_load_dwordx4 %1, %2, %3 offen lds" :: "s"(ldsaddr), "v"(voff), "s"(srd), "s"(soff) : "memory");
}
__device__ __forceinline__ void glds16_sc1(unsigned ldsaddr, unsigned voff, i32x4 srd, unsigned soff) {
  asm volatile("s_mov_b32 m0, %0\n\ts_nop 0\n\tbuffer_load_dwordx4 %1, %2, %3 offen sc1 lds" :: "s"(ldsaddr), "v"(voff), "s"(srd), "s"(soff) : "memory");
}
__device__ __forceinline__ void bload16_sc1(bf16x8& dst, unsigned voff, i32x4 srd, unsigned soff) {
  asm volatile("buffer_load_dwordx4 %0, %1, %2, %3 offen sc1" : "=v"(dst) : "v"(voff), "s"(srd), "s"(soff) : "memory");
}
template <int N> __device__ __forceinline__ void wait_bar() { asm volatile("s_waitcnt vmcnt(%0) lgkmcnt(0)\n\ts_barrier" :: "n"(N) : "memory"); }
__device__ __forceinline__ bf16x8 lds_b128(unsigned addr) { return *(const LAS bf16x8*)(size_t)addr; }
__device__ __forceinline__ u32x2 lds_b64(unsigned addr) { return *(const LAS u32x2*)(size_t)addr; }
__device__ __forceinline__ u32x2 lds_tr(unsigned addr) { return __builtin_bit_cast(u32x2, __builtin_amdgcn_ds_read_tr16_b64_v4i16((LAS v4i16_t*)(size_t)addr)); }
__device__ __forceinline__ bf16x8 mk8(u32x2 lo, u32x2 hi) { u32x4 t = {lo.x, lo.y, hi.x, hi.y}; return __builtin_bit_cast(bf16x8, t); }
__device__ __forceinline__ f32x4 mfma16(bf16x8 a, bf16x8 b, f32x4 c) { return __builtin_amdgcn_mfma_f32_16x16x32_bf16(a, b, c, 0, 0, 0); }

__device__ __forceinline__ u32x4 pair16(u32x2 a, u32x2 b) {
  const auto r0 = __builtin_amdgcn_permlane16_swap(a.x, b.x, false, false);
  const auto r1 = __builtin_amdgcn_permlane16_swap(a.y, b.y, false, false);
  return (u32x4){r0[0], r1[0], r0[1], r1[1]};
}
__device__ __forceinline__ void store_frags(const u32x2 (&v)[5], __amdgpu_buffer_rsrc_t rw, unsigned base, int k0, int fr, int g4) {
#pragma unroll
  for (int j = 0; j < 4; j += 2) {
    const int k = k0 + 16 * (j + (g4 & 1)) + 8 * (g4 >> 1);
    __builtin_amdgcn_raw_buffer_store_b128(pair16(v[j], v[j + 1]), rw, base + (k >> 5) * 1024 + (((k >> 3) & 3) * 16 + fr) * 16, 0, SC1);
  }
  const int k = k0 + 64 + 4 * g4;
  __builtin_amdgcn_raw_buffer_store_b64(v[4], rw, base + (k >> 5) * 1024 + (((k >> 3) & 3) * 16 + fr) * 16 + ((k >> 2) & 1) * 8, 0, SC1);
}

__device__ __forceinline__ void lds_sync(unsigned cnt_addr, int lane, unsigned n) {
  asm volatile("s_waitcnt lgkmcnt(0)" ::: "memory");
  if (lane == 0) __hip_atomic_fetch_add((LAS unsigned*)(size_t)cnt_addr, 1u, __ATOMIC_RELAXED, __HIP_MEMORY_SCOPE_WORKGROUP);
  while (__builtin_amdgcn_readfirstlane(__hip_atomic_load((LAS unsigned*)(size_t)cnt_addr, __ATOMIC_RELAXED, __HIP_MEMORY_SCOPE_WORKGROUP)) < n) {}
  asm volatile("" ::: "memory");
}
__device__ __forceinline__ bool lds_last(unsigned cnt_addr, int lane, unsigned n) {
  unsigned old = 0;
  if (lane == 0) old = __hip_atomic_fetch_add((LAS unsigned*)(size_t)cnt_addr, 1u, __ATOMIC_RELAXED, __HIP_MEMORY_SCOPE_WORKGROUP);
  return __builtin_amdgcn_readfirstlane(old) == n - 1;
}

__device__ __forceinline__ unsigned flag_ld(const char* ws, unsigned off) { return __hip_atomic_load((const gu32*)(ws + off), __ATOMIC_RELAXED, __HIP_MEMORY_SCOPE_AGENT); }
__device__ __forceinline__ void flag_st(char* ws, unsigned off, unsigned v) { __hip_atomic_store((gu32*)(ws + off), v, __ATOMIC_RELAXED, __HIP_MEMORY_SCOPE_AGENT); }
__device__ __forceinline__ void poll(const char* ws, unsigned off, bool act, unsigned tag) {
  const unsigned o0 = __builtin_amdgcn_readfirstlane(off);
  off = act ? off : o0;
  unsigned a, b, n = 1u << 17;
  asm volatile(
      "global_load_dword %0, %3, %4 sc1\n\t"
      "s_sleep 6\n"
      "1:\n\t"
      "global_load_dword %1, %3, %4 sc1\n\t"
      "s_waitcnt vmcnt(1)\n\t"
      "v_cmp_ne_u32 vcc, %0, %5\n\t"
      "s_cbranch_vccz 2f\n\t"
      "global_load_dword %0, %3, %4 sc1\n\t"
      "s_waitcnt vmcnt(1)\n\t"
      "v_cmp_ne_u32 vcc, %1, %5\n\t"
      "s_cbranch_vccz 2f\n\t"
      "s_sub_u32 %2, %2, 1\n\t"
      "s_cbranch_scc0 1b\n"
      "2:\n\t"
      "s_waitcnt vmcnt(0)"
      : "=&v"(a), "=&v"(b), "+s"(n) : "v"(off), "s"(ws), "v"(tag) : "vcc", "scc", "memory");
}

template <bool KV>
__device__ __forceinline__ void gemm_compute(f32x4 (&acc)[4][5], unsigned xa0, unsigned wa0, unsigned wa1 TSPAR) {
  bf16x8 xa[4], xb[4], wa[5], wb[5];
  auto load = [&](bf16x8 (&xf)[4], bf16x8 (&wf)[5], int buf, int kk) {
    const unsigned bo = buf * STG;
    const unsigned xa = (xa0 + bo) ^ (kk * 64);
    const unsigned a0 = wa0 + bo + kk * 10240, a1 = wa1 + bo + kk * 10240;
#pragma unroll
    for (int i = 0; i < 4; i++) xf[i] = lds_b128(xa + 2048 * i);
#pragma unroll
    for (int t = 0; t < 5; t++) { const unsigned a = ((t & 1) ? a1 : a0) + 32 * t; wf[t] = mk8(lds_tr(a), lds_tr(a + 1280)); }
  };
  auto mma = [&](const bf16x8 (&xf)[4], const bf16x8 (&wf)[5]) {
#pragma unroll
    for (int t = 0; t < 5; t++)
#pragma unroll
      for (int i = 0; i < 4; i++) acc[i][t] = KV ? mfma16(xf[i], wf[t], acc[i][t]) : mfma16(wf[t], xf[i], acc[i][t]);
  };
  auto interleave = [&]() {
#pragma unroll
    for (int t = 0; t < 14; t++) { SGB(0x8, 1); SGB(0x100, 1); }
    SGB(0x8, 6);
  };
  BAR();
  TS(21);
  load(xa, wa, 0, 0);
  SBAR();
  for (int s = 0; s < 9; s++) {
    load(xb, wb, s & 3, 1); mma(xa, wa); interleave();
    SBAR();
    BAR();
    if (s == 0) TS(22);
    if (s == 4) TS(23);
    load(xa, wa, (s + 1) & 3, 0); mma(xb, wb); interleave();
    SBAR();
  }
  load(xb, wb, 1, 1); mma(xa, wa); interleave();
  SBAR();
  mma(xb, wb);
}

__device__ __forceinline__ int gemm_s0(int rt, int cg, bool isKV) { return (((rt & 3) * 5 + 1) / 2 + (isKV ? 2 * (cg & 3) : 8 + (cg & 1))) % 10; }
__device__ __forceinline__ void gemm_vx(unsigned (&vx)[2], int L, int lane, int rt) {
#pragma unroll
  for (int i = 0; i < 2; i++) vx[i] = (rt * 128 + L * 64 + i * 8 + (lane >> 3)) * 1280 + (((lane & 7) ^ ((4 * i + (lane >> 4)) & 7)) << 4);
}
__device__ __forceinline__ void gemm_first_x(int L, int lane, unsigned lb, const u16* x, int rt, int cg, bool isKV) {
  const i32x4 sx = make_srd(x, S * D * 2);
  unsigned vx[2];
  gemm_vx(vx, L, lane, rt);
  const int s0 = gemm_s0(rt, cg, isKV);
#pragma unroll
  for (int p = 0; p < 8; p++) glds16(lb + (L * 8 + p) * 1024, vx[p & 1], sx, s0 * 128 + (p >> 1) * (16 * 1280));
}
__device__ __forceinline__ void gemm_loader(int L, int lane, unsigned lb, const u16* x, const u16* wqkv, int rt, int cg, bool isKV) {
  const int s0 = gemm_s0(rt, cg, isKV);
  const i32x4 sx = make_srd(x, S * D * 2), sw = make_srd(wqkv, D * 3 * D * 2);
  unsigned vx[2], vw[5];
  gemm_vx(vx, L, lane, rt);
#pragma unroll
  for (int i = 0; i < 5; i++) {
    const int ci = i * 64 + lane, k = (ci * 3277) >> 16, pos = ci - k * 20, gc = pos ^ (((k >> 3) & 1) << 1), tl = gc >> 1;
    unsigned col;
    if (isKV) { const int isv = (tl >= 3 && tl < 5) || tl >= 7; const int tt = tl < 3 ? tl : tl < 5 ? tl - 3 : tl < 7 ? tl - 2 : tl - 5;
                col = (isv ? 2560u : 1280u) + 160u * cg + tt * 32; }
    else col = 320u * cg + tl * 32;
    vw[i] = (L * 32 + k) * 3840 + col + (gc & 1) * 16;
  }
  auto stage = [&](int st_, int buf) {
    const unsigned base = lb + buf * STG;
    int st = st_ + s0; st = st >= 10 ? st - 10 : st;
#pragma unroll
    for (int p = 0; p < 10; p++) {
      glds16(base + XS + (L * 10 + p) * 1024, vw[p % 5], sw, st * (64 * 3840) + (p / 5) * (16 * 3840));
      if (p < 8) glds16(base + (L * 8 + p) * 1024, vx[p & 1], sx, st * 128 + (p >> 1) * (16 * 1280));
    }
  };
#pragma unroll
  for (int p = 0; p < 10; p++) glds16(lb + XS + (L * 10 + p) * 1024, vw[p % 5], sw, s0 * (64 * 3840) + (p / 5) * (16 * 3840));
  stage(1, 1);
  wait_bar<18>();
  stage(2, 2);
  for (int s = 0; s < 9; s++) {
    if (s < 8) wait_bar<18>(); else wait_bar<0>();
    if (s < 7) stage(s + 3, (s + 3) & 3);
  }
}

template <int T> struct StatTile {
  static constexpr bool isz = T >= 13;
  static constexpr int i = isz ? T - 13 : (T + 1) / 3;
  static constexpr int j = isz ? 0 : T - (i == 0 ? 0 : 3 * i - 1) + (i == 0 ? 0 : i - 1);
};
template <int T> __device__ __forceinline__ f32x4 stat_mma(unsigned la) {
  const u32x4 o1 = {0x3F803F80u, 0x3F803F80u, 0x3F803F80u, 0x3F803F80u};
  bf16x8 A[4], B[4];
#pragma unroll
  for (int m = 0; m < 4; m++) {
    A[m] = lds_b128(la + StatTile<T>::i * 1024 + m * 10240);
    B[m] = StatTile<T>::isz ? __builtin_bit_cast(bf16x8, o1) : lds_b128(la + (5 + StatTile<T>::j) * 1024 + m * 10240);
  }
  f32x4 d4 = {0.f, 0.f, 0.f, 0.f};
#pragma unroll
  for (int m = 0; m < 4; m++) d4 = mfma16(A[m], B[m], d4);
  return d4;
}
template <int T> __device__ __forceinline__ void stat_store(f32x4 d4, int lane, __amdgpu_buffer_rsrc_t rw, unsigned pbase) {
  const int fr = lane & 15, g4 = lane >> 4;
  const int Dg = 16 * StatTile<T>::i + 4 * g4, h = (Dg * 205) >> 12, dd = Dg - 20 * h;
  const int ee = StatTile<T>::isz ? 20 : 16 * StatTile<T>::j + fr - 20 * h;
  if (StatTile<T>::isz ? fr == 0 : (ee >= 0 && ee < 20))
    __builtin_amdgcn_raw_buffer_store_b64((u32x2){pk2(d4[0], d4[1]), pk2(d4[2], d4[3])}, rw, pbase + (h * 420 + ee * 20 + dd) * 2, 0, SC1);
}
template <int W> __device__ __forceinline__ void stat_tiles(unsigned lb, int lane, __amdgpu_buffer_rsrc_t rw, unsigned pbase) {
  const unsigned la = lb + lane * 16;
  const f32x4 a = stat_mma<W>(la), b = stat_mma<W + 8>(la);
  if constexpr (W < 2) { const f32x4 c = stat_mma<W + 16>(la); stat_store<W + 16>(c, lane, rw, pbase); }
  stat_store<W>(a, lane, rw, pbase);
  stat_store<W + 8>(b, lane, rw, pbase);
}

__device__ __forceinline__ void conv_load_h(u32x4 (&cur)[4], u32x4 (&prv)[4], const u16* __restrict__ hrow, int lane) {
#pragma unroll
  for (int it = 0; it < 4; it++) {
    const int i = it * 64 + lane;
    cur[it] = *(const u32x4*)(hrow + i * 8); prv[it] = *(const u32x4*)(hrow + (i > 0 ? i * 8 - 8 : 0));
  }
}
__device__ __forceinline__ void conv_prep_h(const u32x4 (&cur)[4], const u32x4 (&prv_)[4], unsigned cb, int lane) {
#pragma unroll
  for (int it = 0; it < 4; it++) {
    u32x4 prv = prv_[it];
    if (it == 0 && lane == 0) prv = (u32x4){0, 0, 0, 0};
    const unsigned W_[8] = {prv.x, prv.y, prv.z, prv.w, cur[it].x, cur[it].y, cur[it].z, cur[it].w};
    const int c = 255 - (it * 64 + lane);
#pragma unroll
    for (int e = 0; e < 4; e++) {
      u32x4 ov;
#pragma unroll
      for (int q = 0; q < 4; q++) {
        const int i0 = 15 - e - 2 * q, i1 = i0 - 1;
        const unsigned sel = ((i0 & 1) ? 0x0706u : 0x0504u) | (((i1 & 1) ? 0x0302u : 0x0100u) << 16);
        ov[q] = __builtin_amdgcn_perm(W_[i0 >> 1], W_[i1 >> 1], sel);
      }
      *(LAS u32x4*)(size_t)(cb + e * CPS + c * 16) = ov;
    }
  }
  if (lane < 8) *(LAS u32x4*)(size_t)(cb + (lane >> 1) * CPS + (256 + (lane & 1)) * 16) = (u32x4){0, 0, 0, 0};
  if (lane < 32) *(LAS u32x4*)(size_t)(cb + UOFF + lane * 16) = (u32x4){0, 0, 0, 0};
}
template <class F>
__device__ __forceinline__ void conv_load_u(__amdgpu_buffer_rsrc_t rw, int d, unsigned cb, int lane, F&& between) {
  u32x4 uv[4];
#pragma unroll
  for (int it = 0; it < 4; it++) uv[it] = __builtin_amdgcn_raw_buffer_load_b128(rw, OFF_UT + d * 4096 + (it * 64 + lane) * 16, 0, SC1);
  SBAR();
  between();
  SBAR();
#pragma unroll
  for (int it = 0; it < 4; it++) *(LAS u32x4*)(size_t)(cb + UOFF + 512 + (it * 64 + lane) * 16) = uv[it];
}
struct ConvFrags { u32x2 al[8], ah[8]; u32x4 B[8]; };
__device__ __forceinline__ void conv_read(ConvFrags& f, unsigned aa, unsigned ba) {
  asm volatile(
      "ds_read_b128 %16, %25\n\tds_read_b64 %7, %24 offset:3584\n\tds_read_b64 %15, %24 offset:3592\n\t"
      "ds_read_b128 %17, %25 offset:512\n\tds_read_b128 %18, %25 offset:1024\n\tds_read_b128 %19, %25 offset:1536\n\t"
      "ds_read_b128 %20, %25 offset:2048\n\tds_read_b128 %21, %25 offset:2560\n\tds_read_b128 %22, %25 offset:3072\n\t"
      "ds_read_b128 %23, %25 offset:3584\n\t"
      "ds_read_b64 %6, %24 offset:3072\n\tds_read_b64 %14, %24 offset:3080\n\t"
      "ds_read_b64 %5, %24 offset:2560\n\tds_read_b64 %13, %24 offset:2568\n\t"
      "ds_read_b64 %4, %24 offset:2048\n\tds_read_b64 %12, %24 offset:2056\n\t"
      "ds_read_b64 %3, %24 offset:1536\n\tds_read_b64 %11, %24 offset:1544\n\t"
      "ds_read_b64 %2, %24 offset:1024\n\tds_read_b64 %10, %24 offset:1032\n\t"
      "ds_read_b64 %1, %24 offset:512\n\tds_read_b64 %9, %24 offset:520\n\t"
      "ds_read_b64 %0, %24\n\tds_read_b64 %8, %24 offset:8"
      : "=&v"(f.al[0]), "=&v"(f.al[1]), "=&v"(f.al[2]), "=&v"(f.al[3]), "=&v"(f.al[4]), "=&v"(f.al[5]), "=&v"(f.al[6]), "=&v"(f.al[7]),
        "=&v"(f.ah[0]), "=&v"(f.ah[1]), "=&v"(f.ah[2]), "=&v"(f.ah[3]), "=&v"(f.ah[4]), "=&v"(f.ah[5]), "=&v"(f.ah[6]), "=&v"(f.ah[7]),
        "=&v"(f.B[0]), "=&v"(f.B[1]), "=&v"(f.B[2]), "=&v"(f.B[3]), "=&v"(f.B[4]), "=&v"(f.B[5]), "=&v"(f.B[6]), "=&v"(f.B[7])
      : "v"(aa), "v"(ba) : "memory");
}
__device__ __forceinline__ void conv_wait(ConvFrags& f) {
  asm volatile("s_waitcnt lgkmcnt(0)"
      : "+v"(f.al[0]), "+v"(f.al[1]), "+v"(f.al[2]), "+v"(f.al[3]), "+v"(f.al[4]), "+v"(f.al[5]), "+v"(f.al[6]), "+v"(f.al[7]),
        "+v"(f.ah[0]), "+v"(f.ah[1]), "+v"(f.ah[2]), "+v"(f.ah[3]), "+v"(f.ah[4]), "+v"(f.ah[5]), "+v"(f.ah[6]), "+v"(f.ah[7]),
        "+v"(f.B[0]), "+v"(f.B[1]), "+v"(f.B[2]), "+v"(f.B[3]), "+v"(f.B[4]), "+v"(f.B[5]), "+v"(f.B[6]), "+v"(f.B[7]) :: "memory");
}
template <int K> __device__ __forceinline__ void conv_read1(ConvFrags& f, unsigned aa, unsigned ba) {
  if constexpr (K < 8) asm volatile("ds_read_b128 %0, %1 offset:%2" : "=&v"(f.B[K]) : "v"(ba), "n"(512 * K) : "memory");
  else if constexpr (K < 16) asm volatile("ds_read_b64 %0, %1 offset:%2" : "=&v"(f.al[K - 8]) : "v"(aa), "n"(512 * (K - 8)) : "memory");
  else if constexpr (K < 24) asm volatile("ds_read_b64 %0, %1 offset:%2" : "=&v"(f.ah[K - 16]) : "v"(aa), "n"(512 * (K - 16) + 8) : "memory");
}
template <int K = 0> __device__ __forceinline__ void conv_step(f32x4 (&acc)[8], const ConvFrags& f, ConvFrags& nx, unsigned aa, unsigned ba) {
  if constexpr (K < 36) {
    constexpr int a = K < 8 ? 0 : K < 15 ? 1 : K < 21 ? 2 : K < 26 ? 3 : K < 30 ? 4 : K < 33 ? 5 : K < 35 ? 6 : 7;
    constexpr int i = K - (a == 0 ? 0 : a == 1 ? 8 : a == 2 ? 15 : a == 3 ? 21 : a == 4 ? 26 : a == 5 ? 30 : a == 6 ? 33 : 35);
    acc[a + i] = mfma16(mk8(f.al[7 - a], f.ah[7 - a]), __builtin_bit_cast(bf16x8, f.B[i]), acc[a + i]);
    conv_read1<K>(nx, aa, ba);
    SBAR();
    conv_step<K + 1>(acc, f, nx, aa, ba);
  }
}
__device__ __forceinline__ void conv_main(f32x4 (&acc)[8], unsigned cb, int lane, int c0, int c1) {
  const int fr = lane & 15, g4 = lane >> 4;
  const unsigned abase = cb + (3 - (fr & 3)) * CPS + 2 * (2044 - 16 * 112 - 16 * (g4 >> 1) + 8 * (g4 & 1) - (fr & ~3));
  const unsigned bbase = cb + UOFF + 512 + 2 * (16 * fr - 16 * (g4 >> 1) + 8 * (g4 & 1));
#pragma unroll
  for (int c = 0; c < 8; c++) acc[c] = (f32x4){0.f, 0.f, 0.f, 0.f};
  ConvFrags f0, f1;
  asm volatile("s_waitcnt lgkmcnt(0)" ::: "memory");
  conv_read(f0, abase - 64 * c0, bbase - 64 * c0);
  for (int cc = c0; cc < c1; cc += 2) {
    conv_wait(f0);
    SBAR();
    conv_step(acc, f0, f1, abase - 64 * (cc + 1), bbase - 64 * (cc + 1));
    conv_wait(f1);
    SBAR();
    const int cn = cc + 2 < c1 ? cc + 2 : cc + 1;
    conv_step(acc, f1, f0, abase - 64 * cn, bbase - 64 * cn);
  }
  conv_wait(f0);
}
__device__ __forceinline__ void conv_store(f32x4 y, unsigned cb, int c, int lane, float Dd, __amdgpu_buffer_rsrc_t rw, int d) {
  const int fr = lane & 15, g4 = lane >> 4;
  const u32x2 uu = lds_b64(cb + UOFF + 512 + 2 * (256 * c + 16 * fr + 4 * g4));
  const float r0 = fmaf(bflo(uu.x), Dd, y[0]), r1 = fmaf(bfhi(uu.x), Dd, y[1]);
  const float r2 = fmaf(bflo(uu.y), Dd, y[2]), r3 = fmaf(bfhi(uu.y), Dd, y[3]);
  __builtin_amdgcn_raw_buffer_store_b64((u32x2){pk2(r0, r1), pk2(r2, r3)}, rw, OFF_YT + d * 4096 + (256 * c + 16 * fr + 4 * g4) * 2, 0, SC1);
}

__global__ __launch_bounds__(512, 2) void fused(const u16* __restrict__ x, const u16* __restrict__ wqkv, const u16* __restrict__ wo,
                                                const u16* __restrict__ Bk, const u16* __restrict__ Bv, const u16* __restrict__ shk,
                                                const u16* __restrict__ hk, const u16* __restrict__ Dv, const u16* __restrict__ gw,
                                                u16* __restrict__ out, char* __restrict__ ws) {
  extern __shared__ __attribute__((aligned(16))) char sm[];
  const int tid = threadIdx.x, lane = tid & 63;
  const int w = __builtin_amdgcn_readfirstlane(tid >> 6);
  const int b = blockIdx.x, xcd = b & 7, r = b >> 3;
  const int fr = lane & 15, g4 = lane >> 4;
  const unsigned lb = (unsigned)(size_t)(LAS char*)sm;
  const __amdgpu_buffer_rsrc_t rw = __builtin_amdgcn_make_buffer_rsrc(ws, 0, WS_USED, 0x00020000);
  if (r < 24 && (w >> 1) == 2) {
    const bool kv_ = r < 16;
    gemm_first_x(w - 4, lane, lb, x, (xcd & 3) * 4 + (r & 3), kv_ ? (xcd >> 2) * 4 + (r >> 2) : (xcd >> 2) * 2 + ((r - 16) >> 2), kv_);
  }
  TS_DECL TS(0);
  unsigned e0 = 0;
#define PUT_TAG() do { if (w == 7 && lane == 0) { *(LAS unsigned*)(size_t)(lb + L_TAG) = e0 + 1u; *(LAS u32x4*)(size_t)(lb + L_CNT) = (u32x4){0u, 0u, 0u, 0u}; } } while (0)
#define TAG (*(const LAS unsigned*)(size_t)(lb + L_TAG))
  if (r < 24) {
    const bool isKV = r < 16;
    const int rt = (xcd & 3) * 4 + (r & 3);
    const int cg = isKV ? (xcd >> 2) * 4 + (r >> 2) : (xcd >> 2) * 2 + ((r - 16) >> 2);
    f32x4 acc[4][5];
    const int rp = (w >> 1) & 1, hf = w & 1;
    if (w < 4) {
      const unsigned xa0 = lb + (rp * 64 + fr) * 128 + ((g4 ^ ((fr >> 1) & 7)) << 4);
      const unsigned wl = lb + XS + (8 * g4 + (fr >> 2)) * 320 + ((lane & 3) >> 1) * 16 + (lane & 1) * 8 + hf * 160;
      const unsigned sgn = (g4 & 1) * 32;
      const unsigned wa0 = hf ? wl - sgn : wl + sgn, wa1 = hf ? wl + sgn : wl - sgn;
#pragma unroll
      for (int i = 0; i < 4; i++)
#pragma unroll
        for (int t = 0; t < 5; t++) acc[i][t] = (f32x4){0.f, 0.f, 0.f, 0.f};
      if (isKV) gemm_compute<true>(acc, xa0, wa0, wa1 TSARG); else gemm_compute<false>(acc, xa0, wa0, wa1 TSARG);
    } else if (w < 6) {
      gemm_loader(w - 4, lane, lb, x, wqkv, rt, cg, isKV);
    } else {
      const int w6 = w - 6;
      unsigned pf[5] = {0, 0, 0, 0, 0};
      SBAR();
      if (w == 7) e0 = flag_ld(ws, F_E);
      if (isKV) {
        const int l16 = lane & 15, row0 = rt * 128 + cg * 16 + w6 * 8;
        const u32x4 g0 = *(const u32x4*)(gw + lane * 8), g1 = *(const u32x4*)(gw + 512 + l16 * 8);
        u32x4 gx0[8], gx1[8];
#pragma unroll
        for (int i = 0; i < 8; i++) {
          const u16* xr = x + (size_t)(row0 + i) * D;
          gx0[i] = *(const u32x4*)(xr + lane * 8); gx1[i] = *(const u32x4*)(xr + 512 + l16 * 8);
        }
        SBAR();
        BAR(); BAR(); BAR();
        float a[8];
        const float m1 = lane < 16 ? 1.f : 0.f;
#pragma unroll
        for (int i = 0; i < 8; i++) {
          float a0 = 0.f, a1 = 0.f;
#pragma unroll
          for (int j = 0; j < 4; j++) {
            a0 = fmaf(bflo(gx0[i][j]), bflo(g0[j]), a0); a0 = fmaf(bfhi(gx0[i][j]), bfhi(g0[j]), a0);
            a1 = fmaf(bflo(gx1[i][j]), bflo(g1[j]), a1); a1 = fmaf(bfhi(gx1[i][j]), bfhi(g1[j]), a1);
          }
          a[i] = fmaf(a1, m1, a0);
          if (i == 3) { SBAR(); BAR(); SBAR(); }
        }
        SBAR(); BAR(); SBAR();
#pragma unroll
        for (int i = 0; i < 4; i++) { const float lo = a[i], hi = a[i + 4]; const bool up = lane & 4; a[i] = (up ? hi : lo) + __shfl_xor(up ? lo : hi, 4); }
#pragma unroll
        for (int i = 0; i < 2; i++) { const float lo = a[i], hi = a[i + 2]; const bool up = lane & 2; a[i] = (up ? hi : lo) + __shfl_xor(up ? lo : hi, 2); }
        { const float lo = a[0], hi = a[1]; const bool up = lane & 1; a[0] = (up ? hi : lo) + __shfl_xor(up ? lo : hi, 1); }
        a[0] += __shfl_xor(a[0], 8); a[0] += __shfl_xor(a[0], 16); a[0] += __shfl_xor(a[0], 32);
        if (lane < 8) __builtin_amdgcn_raw_buffer_store_b32(__float_as_uint(rbf(1.f / (1.f + __expf(-rbf(a[0]))))), rw, OFF_G + (row0 + lane) * 4, 0, SC1);
        PUT_TAG();
        asm volatile("" :: "v"(pf[0]), "v"(pf[1]), "v"(pf[2]), "v"(pf[3]), "v"(pf[4]));
        SBAR();
        BAR(); BAR(); BAR(); BAR(); BAR();
      } else {
        const int hw = (xcd * 8 + (r - 16)) * 2 + w6;
        const unsigned scr = lb + L_T + w6 * 6144;
#pragma unroll
        for (int bt = 0; bt < 2; bt++) {
          const int blk = bt == 0 ? hw : (hw < 32 ? 128 + hw : hw), ct = (blk * 3277) >> 16, ks = blk - 20 * ct;
          u32x4 v[5];
#pragma unroll
          for (int i = 0; i < 5; i++) {
            const int c = i * 64 + lane, row = (c * 6554) >> 16, ch = c - 10 * row;
            v[i] = *(const u32x4*)(wo + (size_t)(32 * ks + row) * D + 80 * ct + 8 * ch);
          }
          SBAR();
          BAR();
          if (bt == 0) PUT_TAG();
          BAR();
          if (bt == 0 || hw < 32) {
#pragma unroll
            for (int i = 0; i < 5; i++) *(LAS u32x4*)(size_t)(scr + (i * 64 + lane) * 16) = v[i];
            const unsigned ta = scr + (8 * g4 + (fr >> 2)) * 160 + (lane & 3) * 8;
#pragma unroll
            for (int t = 0; t < 5; t++) {
              const u32x2 lo = lds_tr(ta + 32 * t), hi = lds_tr(ta + 32 * t + 640);
              __builtin_amdgcn_raw_buffer_store_b128((u32x4){lo.x, lo.y, hi.x, hi.y}, rw, OFF_WOP + (blk * 5 + t) * 1024 + lane * 16, 0, SC1);
            }
          }
          SBAR();
          BAR(); BAR();
        }
        asm volatile("" :: "v"(pf[0]), "v"(pf[1]), "v"(pf[2]), "v"(pf[3]), "v"(pf[4]));
        DRAIN();
        if (lds_last(lb + L_CNT + 8, lane, 2) && lane == 0) flag_st(ws, F_W + (xcd * 8 + (r - 16)) * 128, TAG);
        BAR(); BAR();
      }
    }
    TS(1);
    BAR();
    TS(11);
    const unsigned tag = TAG;
    if (isKV) {
      if (w < 4) {
#pragma unroll
        for (int t = 0; t < 5; t++) {
          const bool isk = hf ? t < 2 : t < 3;
          const int slot = hf ? (t < 2 ? 3 + t : 5 + t) : (t < 3 ? t : 2 + t);
#pragma unroll
          for (int m = 0; m < 2; m++) {
            u32x4 f;
            if (isk) f = (u32x4){pk2(phi(acc[2 * m][t][0]), phi(acc[2 * m][t][1])), pk2(phi(acc[2 * m][t][2]), phi(acc[2 * m][t][3])),
                                 pk2(phi(acc[2 * m + 1][t][0]), phi(acc[2 * m + 1][t][1])), pk2(phi(acc[2 * m + 1][t][2]), phi(acc[2 * m + 1][t][3]))};
            else f = (u32x4){pk2(acc[2 * m][t][0], acc[2 * m][t][1]), pk2(acc[2 * m][t][2], acc[2 * m][t][3]),
                             pk2(acc[2 * m + 1][t][0], acc[2 * m + 1][t][1]), pk2(acc[2 * m + 1][t][2], acc[2 * m + 1][t][3])};
            *(LAS u32x4*)(size_t)(lb + ((rp * 2 + m) * 10 + slot) * 1024 + lane * 16) = f;
          }
        }
      } else {
        for (int i = tid - 256; i < 6048 / 16; i += 256) *(LAS u32x4*)(size_t)(lb + L_T + i * 16) = (u32x4){0, 0, 0, 0};
        if (w == 7) poll(ws, F_W + lane * 128, true, tag);
      }
      TS(12);
      BAR();
      TS(2);
      switch (w) {
        case 0: stat_tiles<0>(lb, lane, rw, OFF_P + (rt * 8 + cg) * 3360); break;
        case 1: stat_tiles<1>(lb, lane, rw, OFF_P + (rt * 8 + cg) * 3360); break;
        case 2: stat_tiles<2>(lb, lane, rw, OFF_P + (rt * 8 + cg) * 3360); break;
        case 3: stat_tiles<3>(lb, lane, rw, OFF_P + (rt * 8 + cg) * 3360); break;
        case 4: stat_tiles<4>(lb, lane, rw, OFF_P + (rt * 8 + cg) * 3360); break;
        case 5: stat_tiles<5>(lb, lane, rw, OFF_P + (rt * 8 + cg) * 3360); break;
        case 6: stat_tiles<6>(lb, lane, rw, OFF_P + (rt * 8 + cg) * 3360); break;
        default: stat_tiles<7>(lb, lane, rw, OFF_P + (rt * 8 + cg) * 3360); break;
      }
      TS(14);
      DRAIN();
      TS(15);
      BAR();
      TS(3);
      if (w == 0) {
        if (lane == 0) flag_st(ws, F_P + (rt * 8 + cg) * 128, tag);
        poll(ws, lane < 16 ? F_P + (lane * 8 + cg) * 128 : F_PQ + (rt * 4 + (cg >> 1)) * 128, lane <= 16, tag);
      } else if (w < 7) {
        const i32x4 sws = make_srd(ws, WS_USED);
        const unsigned vwv = OFF_WOP + ((xcd >> 2) * 4 + (r >> 3)) * 102400 + lane * 16;
        for (int i = w - 1; i < 100; i += 6) {
          int pc = 12 * (r & 7) + i; pc = __builtin_amdgcn_readfirstlane(pc >= 100 ? pc - 100 : pc);
          glds16_sc1(lb + pc * 1024, vwv, sws, pc * 1024);
        }
      }
      constexpr int PJ[9] = {0, 1, 1, 2, 2, 3, 3, 4, 4}, PK[9] = {0, 0, 1, 0, 1, 1, 2, 1, 2};
      unsigned an[9], ad[9];
#pragma unroll
      for (int p = 0; p < 9; p++) {
        const int E = 16 * PJ[p] + fr, h = (E * 205) >> 12, e = E - 20 * h;
        int o_ = 32 * PK[p] + 8 * g4 - 20 * h; o_ = o_ < -8 ? -8 : o_ > 20 ? 20 : o_;
        an[p] = lb + L_T + (h * 21 + e) * 72 + (8 + o_) * 2; ad[p] = lb + L_T + (h * 21 + 20) * 72 + (8 + o_) * 2;
      }
      BAR();
      TS(4);
      bf16x8 Bq[3];
      {
        const unsigned prow = OFF_PQ + ((rt * 8 + w) * 8 + cg) * 3072 + lane * 16;
#pragma unroll
        for (int ks = 0; ks < 3; ks++) Bq[ks] = __builtin_bit_cast(bf16x8, __builtin_amdgcn_raw_buffer_load_b128(rw, prow + 1024 * ks, 0, SC1));
      }
      if (tid < 210) {
        const unsigned pb = OFF_P + cg * 3360 + tid * 16;
        u32x4 v[16];
#pragma unroll
        for (int k = 0; k < 16; k++) v[k] = __builtin_amdgcn_raw_buffer_load_b128(rw, pb, (k ^ rt) * (8 * 3360), SC1);
        unsigned o4[4];
#pragma unroll
        for (int c = 0; c < 4; c++) {
          float lo[16], hi[16];
#pragma unroll
          for (int k = 0; k < 16; k++) { lo[k] = bflo(v[k][c]); hi[k] = bfhi(v[k][c]); }
#pragma unroll
          for (int st = 1; st < 16; st <<= 1)
#pragma unroll
            for (int k = 0; k < 16; k += 2 * st) { lo[k] += lo[k + st]; hi[k] += hi[k + st]; }
          o4[c] = pk2(lo[0], hi[0]);
        }
        TS(16);
#pragma unroll
        for (int q = 0; q < 2; q++) {
          const int i4 = 2 * tid + q, row = (i4 * 13108) >> 16, d = 4 * (i4 - row * 5);
          *(LAS u32x2*)(size_t)(lb + L_T + row * 72 + (8 + d) * 2) = (u32x2){o4[2 * q], o4[2 * q + 1]};
        }
      }
      BAR();
      TS(5);
      {
        if (g4 >= 2) Bq[2] = __builtin_bit_cast(bf16x8, (u32x4){0, 0, 0, 0});
        f32x4 nu[5], de[5];
#pragma unroll
        for (int j = 0; j < 5; j++) { nu[j] = (f32x4){0.f, 0.f, 0.f, 0.f}; de[j] = nu[j]; }
        bf16x8 An[9], Ad[9];
#pragma unroll
        for (int p = 0; p < 9; p++) {
          unsigned an8 = an[p] + 8, ad8 = ad[p] + 8;
          asm volatile("" : "+v"(an8), "+v"(ad8));
          An[p] = mk8(lds_b64(an[p]), lds_b64(an8)); Ad[p] = mk8(lds_b64(ad[p]), lds_b64(ad8));
        }
#pragma unroll
        for (int p = 0; p < 9; p++) { de[PJ[p]] = mfma16(Ad[p], Bq[PK[p]], de[PJ[p]]); nu[PJ[p]] = mfma16(An[p], Bq[PK[p]], nu[PJ[p]]); }
        u32x2 ov[5];
#pragma unroll
        for (int j = 0; j < 5; j++) {
          const float rd = __builtin_amdgcn_rcpf(de[j][0] + 1e-6f);
          ov[j] = (u32x2){pk2(nu[j][0] * rd, nu[j][1] * rd), pk2(nu[j][2] * rd, nu[j][3] * rd)};
        }
        store_frags(ov, rw, OFF_O + (rt * 8 + w) * 20480, 80 * cg, fr, g4);
      }
      TS(17);
      DRAIN();
      BAR();
      if (tid == 0) flag_st(ws, F_O + (rt * 8 + cg) * 128, tag);
      TS(6);
    } else {
      const int vb = xcd * 8 + (r - 16);
      const int d = 80 * (vb >> 3) + 48 + 4 * (vb & 7) + (w & 3);
      const unsigned cb = lb + (w & 3) * CHS;
      if (w < 4) {
#pragma unroll
        for (int i = 0; i < 4; i++) {
          u32x2 pv[5];
#pragma unroll
          for (int t = 0; t < 5; t++) pv[t] = (u32x2){pk2(phi(acc[i][t][0]), phi(acc[i][t][1])), pk2(phi(acc[i][t][2]), phi(acc[i][t][3]))};
          store_frags(pv, rw, OFF_PQ + ((rt * 8 + rp * 4 + i) * 8 + cg * 2 + hf) * 3072, 0, fr, g4);
        }
      } else {
        u32x4 hc[4], hp[4];
        conv_load_h(hc, hp, hk + (size_t)d * S, lane);
        poll(ws, F_U + lane * 128, true, tag);
        conv_load_u(rw, d, cb, lane, [&]() { conv_prep_h(hc, hp, cb, lane); });
      }
      TS(12);
      DRAIN();
      BAR();
      TS(2);
      if (tid == 0) flag_st(ws, F_PQ + (rt * 4 + cg) * 128, tag);
      if (w < 4) {
        const float Dd = bflo(Dv[d]);
        TS(3);
        f32x4 y[8];
        conv_main(y, cb, lane, 0, 8);
        TS(4);
#pragma unroll
        for (int c = 0; c < 8; c++) conv_store(y[c], cb, c, lane, Dd, rw, d);
        DRAIN();
      } else if (w == 7) {
        poll(ws, F_W + lane * 128, true, tag);
      }
      BAR();
      if (tid == 0) flag_st(ws, F_Y + (64 + vb) * 128, tag);
      TS(5); TS(6);
    }
  } else {
    const int pw = xcd * 8 + (r - 24);
    const int c6 = w < 4 ? w : 4 + ((w - 4) >> 1);
    const int d = 80 * (pw >> 3) + 6 * (pw & 7) + c6;
    const unsigned cb = lb + c6 * CHS;
    const bool prep = w < 4 || !(w & 1);
    u32x4 hc[4], hp[4];
    {
      const int rbase = xcd * 256 + ((r - 24) >> 1) * 64, cbase = ((r - 24) & 1) * 320;
      u32x4 kr[2][7], vr[2][4], sp[2][4];
#pragma unroll
      for (int q = 0; q < 2; q++) {
        const int it = w + 8 * q;
        if (q == 0 || w < 2) {
          const int ch = cbase + (it >> 1) * 64 + (lane & 7) * 8;
          const int s0 = rbase + (it & 1) * 32 + (lane >> 3) * 4;
#pragma unroll
          for (int j = 0; j < 7; j++) { int s = s0 - 3 + j; kr[q][j] = *(const u32x4*)(Bk + (size_t)(s < 0 ? 0 : s) * D + ch); }
#pragma unroll
          for (int j = 0; j < 4; j++) vr[q][j] = *(const u32x4*)(Bv + (size_t)(s0 + j) * D + ch);
#pragma unroll
          for (int j = 0; j < 4; j++) sp[q][j] = ((const u32x4*)(shk + ch * 4))[j];
        }
      }
      if (prep) conv_load_h(hc, hp, hk + (size_t)d * S, lane);
      if (w == 7) e0 = flag_ld(ws, F_E);
      SBAR();
#pragma unroll
      for (int q = 0; q < 2; q++) {
        const int it = w + 8 * q;
        if (q == 0 || w < 2) {
          const int s0 = rbase + (it & 1) * 32 + (lane >> 3) * 4;
          if (s0 == 0) { kr[q][0] = (u32x4){0, 0, 0, 0}; kr[q][1] = kr[q][0]; kr[q][2] = kr[q][0]; }
          const int rg = (it & 1) * 8 + (lane >> 3);
          const unsigned la = lb + L_UIMG + ((it >> 1) * 64 + (lane & 7) * 8) * 128 + (((rg >> 1) ^ (lane & 7)) << 4) + (rg & 1) * 8;
#pragma unroll
          for (int c = 0; c < 8; c++) {
            const unsigned s01 = sp[q][c >> 1][(c & 1) * 2], s23 = sp[q][c >> 1][(c & 1) * 2 + 1];
            const float w0 = bflo(s01), w1 = bfhi(s01), w2 = bflo(s23), w3 = bfhi(s23);
            float kk[7], u[4];
#pragma unroll
            for (int j = 0; j < 7; j++) { unsigned dd = kr[q][j][c >> 1]; kk[j] = (c & 1) ? bfhi(dd) : bflo(dd); }
#pragma unroll
            for (int j = 0; j < 4; j++) {
              unsigned dd = vr[q][j][c >> 1]; float v = (c & 1) ? bfhi(dd) : bflo(dd);
              float ks = kk[j] * w3; ks = fmaf(kk[j + 1], w2, ks); ks = fmaf(kk[j + 2], w1, ks); ks = fmaf(kk[j + 3], w0, ks);
              u[j] = ks * v;
            }
            *(LAS u32x2*)(size_t)(la + c * 128) = (u32x2){pk2(u[0], u[1]), pk2(u[2], u[3])};
          }
        }
      }
      PUT_TAG();
      BAR();
#pragma unroll
      for (int i = 0; i < 5; i++) {
        const int chl = (w * 5 + i) * 8 + (lane >> 3), k = lane & 7;
        const u32x4 v = *(const LAS u32x4*)(size_t)(lb + L_UIMG + chl * 128 + ((k ^ ((chl >> 3) & 7)) << 4));
        __builtin_amdgcn_raw_buffer_store_b128(v, rw, OFF_UT + (cbase + chl) * 4096 + rbase * 2 + k * 16, 0, SC1);
      }
      TS(1);
    }
    if (w < 5 && prep) conv_prep_h(hc, hp, cb, lane);
    DRAIN();
    BAR();
    TS(2);
    const unsigned tag = TAG;
    if (tid == 0) flag_st(ws, F_U + pw * 128, tag);
    const float Dd = bflo(Dv[d]);
    if (prep) {
      if (w == 6) conv_prep_h(hc, hp, cb, lane);
      poll(ws, F_U + lane * 128, true, tag);
      conv_load_u(rw, d, cb, lane, [&]() {});
    }
    BAR();
    TS(3);
    f32x4 y[8];
    conv_main(y, cb, lane, w < 4 ? 0 : (w & 1) * 4, w < 4 ? 8 : (w & 1) * 4 + 4);
    TS(4);
    if (w < 4) {
#pragma unroll
      for (int c = 0; c < 8; c++) conv_store(y[c], cb, c, lane, Dd, rw, d);
    }
    BAR();
    if (w >= 4) {
#pragma unroll
      for (int c = 0; c < 4; c++) { const f32x4 lo = y[c], hi = y[4 + c]; *(LAS f32x4*)(size_t)(cb + ((w & 1) * 4 + c) * 1024 + lane * 16) = (w & 1) ? lo : hi; }
    }
    BAR();
    if (w >= 4) {
#pragma unroll
      for (int c = 0; c < 4; c++) {
        const f32x4 o_ = *(const LAS f32x4*)(size_t)(cb + (((w & 1) ^ 1) * 4 + c) * 1024 + lane * 16);
        const f32x4 lo = y[c], hi = y[4 + c];
        const f32x4 mine = (w & 1) ? hi : lo;
        conv_store((w & 1) ? o_ + mine : mine + o_, cb, (w & 1) * 4 + c, lane, Dd, rw, d);
      }
    }
    if (w == 0) poll(ws, F_W + lane * 128, true, tag);
    DRAIN();
    BAR();
    if (tid == 0) flag_st(ws, F_Y + pw * 128, tag);
    TS(5); TS(6);
  }

  {
    const unsigned tag = TAG;
    const int rt = (xcd & 3) * 8 + (r & 7), ct = (xcd >> 2) * 4 + (r >> 3);
    const int sw_ = w & 3, kk = w >> 2;
    const i32x4 sws = make_srd(ws, WS_USED);
    TS(18);
    const int srow = rt * 64 + sw_ * 16 + fr;
    u32x2 xv[3];
#pragma unroll
    for (int t = 0; t < 3; t++) { const int tt = kk ? (t < 2 ? 3 + t : 4) : t; xv[t] = *(const u32x2*)(x + (size_t)srow * D + ct * 80 + 16 * tt + 4 * g4); }
    SBAR();
    const int rot = ((ct & 3) * 5) >> 1;
    const unsigned wa0 = lb + kk * 5120 + lane * 16;
    bf16x8 wf0[5][5];
    auto read_wf0 = [&]() {
#pragma unroll
      for (int s = 0; s < 5; s++) {
        int q = s + rot; q = q >= 10 ? q - 10 : q;
#pragma unroll
        for (int t = 0; t < 5; t++) wf0[s][t] = lds_b128(wa0 + q * 10240 + t * 1024);
      }
    };
    if (r < 16) read_wf0();
    SBAR();
    if (w == 0) {
      poll(ws, F_O + ((rt >> 1) * 8 + (lane & 7)) * 128, lane < 8, tag);
    } else if (w == 7) {
      poll(ws, F_Y + ((lane & 8 ? 64 : 0) + ct * 8 + (lane & 7)) * 128, lane < 16, tag);
    } else {
      if (r >= 16) {
        const unsigned vwv = OFF_WOP + ct * 102400 + lane * 16;
        for (int i = w - 1; i < 100; i += 6) { int pc = 12 * (r & 7) + i; pc = __builtin_amdgcn_readfirstlane(pc >= 100 ? pc - 100 : pc); glds16_sc1(lb + pc * 1024, vwv, sws, pc * 1024); }
      }
      if (w == 6 && b == 255) {
        for (int spins = 0; spins < (1 << 17); spins++) {
          const unsigned a0 = flag_ld(ws, F_U + lane * 128), a1 = flag_ld(ws, F_U + (64 + lane) * 128);
          const unsigned a2 = flag_ld(ws, F_U + (128 + lane) * 128), a3 = flag_ld(ws, F_U + (192 + lane) * 128);
          if (__all(a0 == tag && a1 == tag && a2 == tag && a3 == tag)) break;
          __builtin_amdgcn_s_sleep(2);
        }
        if (lane == 0) flag_st(ws, F_E, tag);
      }
      DRAIN();
    }
    BAR();
    TS(7);
    asm volatile("" :: "v"(xv[0]), "v"(xv[1]), "v"(xv[2]));
    bf16x8 of[10];
    {
      const unsigned vo = OFF_O + (rt * 4 + sw_) * 20480 + kk * 1024 + lane * 16;
#pragma unroll
      for (int s = 0; s < 10; s++) { int q = s + rot; q = __builtin_amdgcn_readfirstlane(q >= 10 ? q - 10 : q); bload16_sc1(of[s], vo, sws, q * 2048); }
    }
    if (w == 7) {
      for (int pc = 0; pc < 10; pc++)
        glds16_sc1(lb + L_Y + pc * 1024, OFF_YT + (ct * 80 + pc * 8 + (lane >> 3)) * 4096 + rt * 128 + (lane & 7) * 16, sws, 0);
    }
    float g;
    asm volatile("buffer_load_dword %0, %1, %2, 0 offen sc1" : "=v"(g) : "v"(OFF_G + srow * 4), "s"(sws) : "memory");
    TS(8);
    if (r >= 16) read_wf0();
    f32x4 acc[5];
#pragma unroll
    for (int t = 0; t < 5; t++) acc[t] = (f32x4){0.f, 0.f, 0.f, 0.f};
    {
      bf16x8 wf1[5][5];
#pragma unroll
      for (int s = 0; s < 5; s++) {
        int q = 5 + s + rot; q = q >= 10 ? q - 10 : q;
#pragma unroll
        for (int t = 0; t < 5; t++) wf1[s][t] = lds_b128(wa0 + q * 10240 + t * 1024);
      }
      SBAR();
      if (w == 7) asm volatile("s_waitcnt vmcnt(16)" ::: "memory"); else asm volatile("s_waitcnt vmcnt(6)" ::: "memory");
      asm volatile("" : "+v"(of[0]), "+v"(of[1]), "+v"(of[2]), "+v"(of[3]), "+v"(of[4]) :: "memory");
      SBAR();
#pragma unroll
      for (int s = 0; s < 5; s++)
#pragma unroll
        for (int t = 0; t < 5; t++) acc[t] = mfma16(wf0[s][t], of[s], acc[t]);
      SBAR();
      if (w == 7) asm volatile("s_waitcnt vmcnt(11)" ::: "memory"); else asm volatile("s_waitcnt vmcnt(1)" ::: "memory");
      asm volatile("" : "+v"(of[5]), "+v"(of[6]), "+v"(of[7]), "+v"(of[8]), "+v"(of[9]) :: "memory");
      SBAR();
#pragma unroll
      for (int s = 0; s < 5; s++)
#pragma unroll
        for (int t = 0; t < 5; t++) acc[t] = mfma16(wf1[s][t], of[5 + s], acc[t]);
      SBAR();
    }
    TS(9);
    const unsigned ra = lb + L_RED + (sw_ * 5) * 1024 + lane * 16;
    if (kk == 0) { *(LAS f32x4*)(size_t)(ra + 3 * 1024) = acc[3]; *(LAS f32x4*)(size_t)(ra + 4 * 1024) = acc[4]; }
    else { *(LAS f32x4*)(size_t)(ra) = acc[0]; *(LAS f32x4*)(size_t)(ra + 1024) = acc[1]; *(LAS f32x4*)(size_t)(ra + 2048) = acc[2]; }
    asm volatile("s_waitcnt vmcnt(0)" : "+v"(g) :: "memory");
    BAR();
    TS(20);
    const float omg = 1.f - g;
    const int nt = kk ? 2 : 3;
#pragma unroll
    for (int t = 0; t < 3; t++) {
      if (t < nt) {
        const int tt = kk ? 3 + t : t;
        const f32x4 a0 = acc[t], a1 = acc[3 + (t < 2 ? t : 1)];
        f32x4 a = *(const LAS f32x4*)(size_t)(ra + tt * 1024) + (kk ? a1 : a0);
        u32x2 yv = lds_tr(lb + L_Y + (16 * tt + 4 * g4 + (fr >> 2)) * 128 + (sw_ * 16 + 4 * (fr & 3)) * 2);
        float xs[4] = {bflo(xv[t].x), bfhi(xv[t].x), bflo(xv[t].y), bfhi(xv[t].y)};
        float ys[4] = {bflo(yv.x), bfhi(yv.x), bflo(yv.y), bfhi(yv.y)};
        float r_[4];
#pragma unroll
        for (int q = 0; q < 4; q++) r_[q] = fmaf(g, a[q], omg * (xs[q] * ys[q]));
        *(u32x2*)(out + (size_t)srow * D + ct * 80 + 16 * tt + 4 * g4) = (u32x2){pk2(r_[0], r_[1]), pk2(r_[2], r_[3])};
      }
    }
    TS(10); TS_OUT(21);
  }
}

extern "C" void kernel_launch(void* const* d_in, const int* in_sizes, int n_in, void* d_out, int out_size, void* d_ws, size_t ws_size,
                              hipStream_t stream) {
  const u16* x = (const u16*)d_in[0]; const u16* wqkv = (const u16*)d_in[1]; const u16* wo = (const u16*)d_in[2];
  const u16* Bk = (const u16*)d_in[4]; const u16* Bv = (const u16*)d_in[5]; const u16* shk = (const u16*)d_in[6];
  const u16* hk = (const u16*)d_in[7]; const u16* Dv = (const u16*)d_in[8]; const u16* gw = (const u16*)d_in[9];
  (void)hipFuncSetAttribute((const void*)fused, hipFuncAttributeMaxDynamicSharedMemorySize, LDS_TOTAL);
  fused<<<256, 512, LDS_TOTAL, stream>>>(x, wqkv, wo, Bk, Bv, shk, hk, Dv, gw, (u16*)d_out, (char*)d_ws);
}
```

```cpp
#include <hip/hip_runtime.h>
#include <hip/hip_bf16.h>
#include <type_traits>
#define NO_PF

typedef unsigned short u16;
typedef __bf16 bf16x2 __attribute__((ext_vector_type(2)));
typedef __bf16 bf16x8 __attribute__((ext_vector_type(8)));
typedef float f32x4 __attribute__((ext_vector_type(4)));
typedef unsigned u32x4 __attribute__((ext_vector_type(4)));
typedef unsigned u32x2 __attribute__((ext_vector_type(2)));
typedef short v4i16_t __attribute__((ext_vector_type(4)));
typedef int i32x4 __attribute__((ext_vector_type(4)));
typedef __attribute__((address_space(1))) unsigned gu32;
#define LAS __attribute__((address_space(3)))
#define SBAR() __builtin_amdgcn_sched_barrier(0)
#define SGB(mask, n) __builtin_amdgcn_sched_group_barrier(mask, n, 0)
#define BAR() asm volatile("s_waitcnt lgkmcnt(0)\n\ts_barrier" ::: "memory")
#define DRAIN() asm volatile("s_waitcnt vmcnt(0)" ::: "memory")
#define SC1 16

#define TS_DECL unsigned long long tevt_ = 0;
#define TSARG , tevt_
#define TSPAR , unsigned long long& tevt_
#define TS(i) do { if ((i) == 0 && blockIdx.x == 0 && threadIdx.x == 0) tevt_ = __builtin_amdgcn_s_memrealtime(); } while (0)
#define TS_OUT(n) do { if (blockIdx.x == 0 && threadIdx.x == 0) { while (__builtin_amdgcn_s_memrealtime() < tevt_ + 3000ull) __builtin_amdgcn_s_sleep(1); } } while (0)

constexpr int S = 2048, D = 640;
constexpr unsigned SZ = S * D * 2;
constexpr unsigned F_E = 0, F_U = 128, F_PQ = F_U + 64 * 128, F_P = F_PQ + 64 * 128, F_O = F_P + 128 * 128, F_Y = F_O + 128 * 128, F_W = F_Y + 128 * 128;
constexpr unsigned OFF_PQ = 0x20000, OFF_P = OFF_PQ + 128 * 8 * 3 * 1024, OFF_UT = OFF_P + (1u << 19), OFF_G = OFF_UT + SZ, OFF_WOP = OFF_G + 8192,
                   OFF_O = OFF_WOP + (1u << 20), OFF_YT = OFF_O + SZ, WS_USED = OFF_YT + SZ;
constexpr int XS = 16384, WSB = 20480, STG = XS + WSB;
constexpr int L_EXTRA = 4 * STG;
constexpr int L_TAG = L_EXTRA, L_CNT = L_EXTRA + 16, L_T = L_EXTRA + 64;
constexpr int L_SCR = L_T + 6144 + 5120;
constexpr int LDS_TOTAL = L_SCR + 256;
constexpr int CPS = 4160, UOFF = 4 * CPS, CHS = UOFF + 4608;
constexpr int L_UIMG = 5 * CHS;
constexpr int L_Y = 102400, L_RED = L_Y + 10240;

__device__ __forceinline__ unsigned pk2(float a, float b) { bf16x2 r = {(__bf16)a, (__bf16)b}; return __builtin_bit_cast(unsigned, r); }
__device__ __forceinline__ float rbf(float a) { return (float)(__bf16)a; }
__device__ __forceinline__ float bflo(unsigned v) { return __uint_as_float(v << 16); }
__device__ __forceinline__ float bfhi(unsigned v) { return __uint_as_float(v & 0xffff0000u); }
__device__ __forceinline__ float phi(float t) {
  return __builtin_amdgcn_fmed3f(t, 0.f, 3.0e38f) + (__builtin_amdgcn_fmed3f(__builtin_amdgcn_exp2f(t * 1.44269504f), 0.f, 1.f) + 1e-6f);
}
__device__ __forceinline__ i32x4 make_srd(const void* p, unsigned bytes) {
  unsigned long long a = (unsigned long long)p; i32x4 r;
  r.x = __builtin_amdgcn_readfirstlane((int)(unsigned)a); r.y = __builtin_amdgcn_readfirstlane((int)(unsigned)(a >> 32));
  r.z = __builtin_amdgcn_readfirstlane((int)bytes); r.w = 0x00020000; return r;
}
__device__ __forceinline__ void glds16(unsigned ldsaddr, unsigned voff, i32x4 srd, unsigned soff) {
  asm volatile("s_mov_b32 m0, %0\n\ts_nop 0\n\tbuffer_load_dwordx4 %1, %2, %3 offen lds" :: "s"(ldsaddr), "v"(voff), "s"(srd), "s"(soff) : "memory");
}
__device__ __forceinline__ void glds16_sc1(unsigned ldsaddr, unsigned voff, i32x4 srd, unsigned soff) {
  asm volatile("s_mov_b32 m0, %0\n\ts_nop 0\n\tbuffer_load_dwordx4 %1, %2, %3 offen sc1 lds" :: "s"(ldsaddr), "v"(voff), "s"(srd), "s"(soff) : "memory");
}
__device__ __forceinline__ void bload16_sc1(bf16x8& dst, unsigned voff, i32x4 srd, unsigned soff) {
  asm volatile("buffer_load_dwordx4 %0, %1, %2, %3 offen sc1" : "=v"(dst) : "v"(voff), "s"(srd), "s"(soff) : "memory");
}
template <int N> __device__ __forceinline__ void wait_bar() { asm volatile("s_waitcnt vmcnt(%0) lgkmcnt(0)\n\ts_barrier" :: "n"(N) : "memory"); }
__device__ __forceinline__ bf16x8 lds_b128(unsigned addr) { return *(const LAS bf16x8*)(size_t)addr; }
__device__ __forceinline__ u32x2 lds_b64(unsigned addr) { return *(const LAS u32x2*)(size_t)addr; }
__device__ __forceinline__ u32x2 lds_tr(unsigned addr) { return __builtin_bit_cast(u32x2, __builtin_amdgcn_ds_read_tr16_b64_v4i16((LAS v4i16_t*)(size_t)addr)); }
__device__ __forceinline__ bf16x8 mk8(u32x2 lo, u32x2 hi) { u32x4 t = {lo.x, lo.y, hi.x, hi.y}; return __builtin_bit_cast(bf16x8, t); }
__device__ __forceinline__ f32x4 mfma16(bf16x8 a, bf16x8 b, f32x4 c) { return __builtin_amdgcn_mfma_f32_16x16x32_bf16(a, b, c, 0, 0, 0); }

__device__ __forceinline__ u32x4 pair16(u32x2 a, u32x2 b) {
  const auto r0 = __builtin_amdgcn_permlane16_swap(a.x, b.x, false, false);
  const auto r1 = __builtin_amdgcn_permlane16_swap(a.y, b.y, false, false);
  return (u32x4){r0[0], r1[0], r0[1], r1[1]};
}
__device__ __forceinline__ void store_frags(const u32x2 (&v)[5], __amdgpu_buffer_rsrc_t rw, unsigned base, int k0, int fr, int g4) {
#pragma unroll
  for (int j = 0; j < 4; j += 2) {
    const int k = k0 + 16 * (j + (g4 & 1)) + 8 * (g4 >> 1);
    __builtin_amdgcn_raw_buffer_store_b128(pair16(v[j], v[j + 1]), rw, base + (k >> 5) * 1024 + (((k >> 3) & 3) * 16 + fr) * 16, 0, SC1);
  }
  const int k = k0 + 64 + 4 * g4;
  __builtin_amdgcn_raw_buffer_store_b64(v[4], rw, base + (k >> 5) * 1024 + (((k >> 3) & 3) * 16 + fr) * 16 + ((k >> 2) & 1) * 8, 0, SC1);
}

__device__ __forceinline__ void lds_sync(unsigned cnt_addr, int lane, unsigned n) {
  asm volatile("s_waitcnt lgkmcnt(0)" ::: "memory");
  if (lane == 0) __hip_atomic_fetch_add((LAS unsigned*)(size_t)cnt_addr, 1u, __ATOMIC_RELAXED, __HIP_MEMORY_SCOPE_WORKGROUP);
  while (__builtin_amdgcn_readfirstlane(__hip_atomic_load((LAS unsigned*)(size_t)cnt_addr, __ATOMIC_RELAXED, __HIP_MEMORY_SCOPE_WORKGROUP)) < n) {}
  asm volatile("" ::: "memory");
}
__device__ __forceinline__ bool lds_last(unsigned cnt_addr, int lane, unsigned n) {
  unsigned old = 0;
  if (lane == 0) old = __hip_atomic_fetch_add((LAS unsigned*)(size_t)cnt_addr, 1u, __ATOMIC_RELAXED, __HIP_MEMORY_SCOPE_WORKGROUP);
  return __builtin_amdgcn_readfirstlane(old) == n - 1;
}

__device__ __forceinline__ unsigned flag_ld(const char* ws, unsigned off) { return __hip_atomic_load((const gu32*)(ws + off), __ATOMIC_RELAXED, __HIP_MEMORY_SCOPE_AGENT); }
__device__ __forceinline__ void flag_st(char* ws, unsigned off, unsigned v) { __hip_atomic_store((gu32*)(ws + off), v, __ATOMIC_RELAXED, __HIP_MEMORY_SCOPE_AGENT); }
__device__ __forceinline__ void poll(const char* ws, unsigned off, bool act, unsigned tag) {
  const unsigned o0 = __builtin_amdgcn_readfirstlane(off);
  off = act ? off : o0;
  unsigned a, b, n = 1u << 17;
  asm volatile(
      "global_load_dword %0, %3, %4 sc1\n\t"
      "s_sleep 6\n"
      "1:\n\t"
      "global_load_dword %1, %3, %4 sc1\n\t"
      "s_waitcnt vmcnt(1)\n\t"
      "v_cmp_ne_u32 vcc, %0, %5\n\t"
      "s_cbranch_vccz 2f\n\t"
      "global_load_dword %0, %3, %4 sc1\n\t"
      "s_waitcnt vmcnt(1)\n\t"
      "v_cmp_ne_u32 vcc, %1, %5\n\t"
      "s_cbranch_vccz 2f\n\t"
      "s_sub_u32 %2, %2, 1\n\t"
      "s_cbranch_scc0 1b\n"
      "2:\n\t"
      "s_waitcnt vmcnt(0)"
      : "=&v"(a), "=&v"(b), "+s"(n) : "v"(off), "s"(ws), "v"(tag) : "vcc", "scc", "memory");
}

template <bool KV>
__device__ __forceinline__ void gemm_compute(f32x4 (&acc)[4][5], unsigned xa0, unsigned wa0, unsigned wa1 TSPAR) {
  bf16x8 xa[4], xb[4], wa[5], wb[5];
  auto load = [&](bf16x8 (&xf)[4], bf16x8 (&wf)[5], int buf, int kk) {
    const unsigned bo = buf * STG;
    const unsigned xa = (xa0 + bo) ^ (kk * 64);
    const unsigned a0 = wa0 + bo + kk * 10240, a1 = wa1 + bo + kk * 10240;
#pragma unroll
    for (int i = 0; i < 4; i++) xf[i] = lds_b128(xa + 2048 * i);
#pragma unroll
    for (int t = 0; t < 5; t++) { const unsigned a = ((t & 1) ? a1 : a0) + 32 * t; wf[t] = mk8(lds_tr(a), lds_tr(a + 1280)); }
  };
  auto mma = [&](const bf16x8 (&xf)[4], const bf16x8 (&wf)[5]) {
#pragma unroll
    for (int t = 0; t < 5; t++)
#pragma unroll
      for (int i = 0; i < 4; i++) acc[i][t] = KV ? mfma16(xf[i], wf[t], acc[i][t]) : mfma16(wf[t], xf[i], acc[i][t]);
  };
  auto interleave = [&]() {
#pragma unroll
    for (int t = 0; t < 14; t++) { SGB(0x8, 1); SGB(0x100, 1); }
    SGB(0x8, 6);
  };
  BAR();
  TS(21);
  load(xa, wa, 0, 0);
  SBAR();
  for (int s = 0; s < 9; s++) {
    load(xb, wb, s & 3, 1); mma(xa, wa); interleave();
    SBAR();
    BAR();
    if (s == 0) TS(22);
    if (s == 4) TS(23);
    load(xa, wa, (s + 1) & 3, 0); mma(xb, wb); interleave();
    SBAR();
  }
  load(xb, wb, 1, 1); mma(xa, wa); interleave();
  SBAR();
  mma(xb, wb);
}

__device__ __forceinline__ int gemm_s0(int rt, int cg, bool isKV) { return (((rt & 3) * 5 + 1) / 2 + (isKV ? 2 * (cg & 3) : 8 + (cg & 1))) % 10; }
__device__ __forceinline__ void gemm_vx(unsigned (&vx)[2], int L, int lane, int rt) {
#pragma unroll
  for (int i = 0; i < 2; i++) vx[i] = (rt * 128 + L * 64 + i * 8 + (lane >> 3)) * 1280 + (((lane & 7) ^ ((4 * i + (lane >> 4)) & 7)) << 4);
}
__device__ __forceinline__ void gemm_first_x(int L, int lane, unsigned lb, const u16* x, int rt, int cg, bool isKV) {
  const i32x4 sx = make_srd(x, S * D * 2);
  unsigned vx[2];
  gemm_vx(vx, L, lane, rt);
  const int s0 = gemm_s0(rt, cg, isKV);
#pragma unroll
  for (int p = 0; p < 8; p++) glds16(lb + (L * 8 + p) * 1024, vx[p & 1], sx, s0 * 128 + (p >> 1) * (16 * 1280));
}
__device__ __forceinline__ void gemm_loader(int L, int lane, unsigned lb, const u16* x, const u16* wqkv, int rt, int cg, bool isKV) {
  const int s0 = gemm_s0(rt, cg, isKV);
  const i32x4 sx = make_srd(x, S * D * 2), sw = make_srd(wqkv, D * 3 * D * 2);
  unsigned vx[2], vw[5];
  gemm_vx(vx, L, lane, rt);
#pragma unroll
  for (int i = 0; i < 5; i++) {
    const int ci = i * 64 + lane, k = (ci * 3277) >> 16, pos = ci - k * 20, gc = pos ^ (((k >> 3) & 1) << 1), tl = gc >> 1;
    unsigned col;
    if (isKV) { const int isv = (tl >= 3 && tl < 5) || tl >= 7; const int tt = tl < 3 ? tl : tl < 5 ? tl - 3 : tl < 7 ? tl - 2 : tl - 5;
                col = (isv ? 2560u : 1280u) + 160u * cg + tt * 32; }
    else col = 320u * cg + tl * 32;
    vw[i] = (L * 32 + k) * 3840 + col + (gc & 1) * 16;
  }
  auto stage = [&](int st_, int buf) {
    const unsigned base = lb + buf * STG;
    int st = st_ + s0; st = st >= 10 ? st - 10 : st;
#pragma unroll
    for (int p = 0; p < 10; p++) {
      glds16(base + XS + (L * 10 + p) * 1024, vw[p % 5], sw, st * (64 * 3840) + (p / 5) * (16 * 3840));
      if (p < 8) glds16(base + (L * 8 + p) * 1024, vx[p & 1], sx, st * 128 + (p >> 1) * (16 * 1280));
    }
  };
#pragma unroll
  for (int p = 0; p < 10; p++) glds16(lb + XS + (L * 10 + p) * 1024, vw[p % 5], sw, s0 * (64 * 3840) + (p / 5) * (16 * 3840));
  stage(1, 1);
  wait_bar<18>();
  stage(2, 2);
  for (int s = 0; s < 9; s++) {
    if (s < 8) wait_bar<18>(); else wait_bar<0>();
    if (s < 7) stage(s + 3, (s + 3) & 3);
  }
}

template <int T> struct StatTile {
  static constexpr bool isz = T >= 13;
  static constexpr int i = isz ? T - 13 : (T + 1) / 3;
  static constexpr int j = isz ? 0 : T - (i == 0 ? 0 : 3 * i - 1) + (i == 0 ? 0 : i - 1);
};
template <int T> __device__ __forceinline__ f32x4 stat_mma(unsigned la) {
  const u32x4 o1 = {0x3F803F80u, 0x3F803F80u, 0x3F803F80u, 0x3F803F80u};
  bf16x8 A[4], B[4];
#pragma unroll
  for (int m = 0; m < 4; m++) {
    A[m] = lds_b128(la + StatTile<T>::i * 1024 + m * 10240);
    B[m] = StatTile<T>::isz ? __builtin_bit_cast(bf16x8, o1) : lds_b128(la + (5 + StatTile<T>::j) * 1024 + m * 10240);
  }
  f32x4 d4 = {0.f, 0.f, 0.f, 0.f};
#pragma unroll
  for (int m = 0; m < 4; m++) d4 = mfma16(A[m], B[m], d4);
  return d4;
}
template <int T> __device__ __forceinline__ void stat_store(f32x4 d4, int lane, __amdgpu_buffer_rsrc_t rw, unsigned pbase) {
  const int fr = lane & 15, g4 = lane >> 4;
  const int Dg = 16 * StatTile<T>::i + 4 * g4, h = (Dg * 205) >> 12, dd = Dg - 20 * h;
  const int ee = StatTile<T>::isz ? 20 : 16 * StatTile<T>::j + fr - 20 * h;
  if (StatTile<T>::isz ? fr == 0 : (ee >= 0 && ee < 20))
    __builtin_amdgcn_raw_buffer_store_b64((u32x2){pk2(d4[0], d4[1]), pk2(d4[2], d4[3])}, rw, pbase + (h * 420 + ee * 20 + dd) * 2, 0, SC1);
}
template <int W> __device__ __forceinline__ void stat_tiles(unsigned lb, int lane, __amdgpu_buffer_rsrc_t rw, unsigned pbase) {
  const unsigned la = lb + lane * 16;
  const f32x4 a = stat_mma<W>(la), b = stat_mma<W + 8>(la);
  if constexpr (W < 2) { const f32x4 c = stat_mma<W + 16>(la); stat_store<W + 16>(c, lane, rw, pbase); }
  stat_store<W>(a, lane, rw, pbase);
  stat_store<W + 8>(b, lane, rw, pbase);
}

__device__ __forceinline__ void conv_load_h(u32x4 (&cur)[4], u32x4 (&prv)[4], const u16* __restrict__ hrow, int lane) {
#pragma unroll
  for (int it = 0; it < 4; it++) {
    const int i = it * 64 + lane;
    cur[it] = *(const u32x4*)(hrow + i * 8); prv[it] = *(const u32x4*)(hrow + (i > 0 ? i * 8 - 8 : 0));
  }
}
__device__ __forceinline__ void conv_prep_h(const u32x4 (&cur)[4], const u32x4 (&prv_)[4], unsigned cb, int lane) {
#pragma unroll
  for (int it = 0; it < 4; it++) {
    u32x4 prv = prv_[it];
    if (it == 0 && lane == 0) prv = (u32x4){0, 0, 0, 0};
    const unsigned W_[8] = {prv.x, prv.y, prv.z, prv.w, cur[it].x, cur[it].y, cur[it].z, cur[it].w};
    const int c = 255 - (it * 64 + lane);
#pragma unroll
    for (int e = 0; e < 4; e++) {
      u32x4 ov;
#pragma unroll
      for (int q = 0; q < 4; q++) {
        const int i0 = 15 - e - 2 * q, i1 = i0 - 1;
        const unsigned sel = ((i0 & 1) ? 0x0706u : 0x0504u) | (((i1 & 1) ? 0x0302u : 0x0100u) << 16);
        ov[q] = __builtin_amdgcn_perm(W_[i0 >> 1], W_[i1 >> 1], sel);
      }
      *(LAS u32x4*)(size_t)(cb + e * CPS + c * 16) = ov;
    }
  }
  if (lane < 8) *(LAS u32x4*)(size_t)(cb + (lane >> 1) * CPS + (256 + (lane & 1)) * 16) = (u32x4){0, 0, 0, 0};
  if (lane < 32) *(LAS u32x4*)(size_t)(cb + UOFF + lane * 16) = (u32x4){0, 0, 0, 0};
}
template <class F>
__device__ __forceinline__ void conv_load_u(__amdgpu_buffer_rsrc_t rw, int d, unsigned cb, int lane, F&& between) {
  u32x4 uv[4];
#pragma unroll
  for (int it = 0; it < 4; it++) uv[it] = __builtin_amdgcn_raw_buffer_load_b128(rw, OFF_UT + d * 4096 + (it * 64 + lane) * 16, 0, SC1);
  SBAR();
  between();
  SBAR();
#pragma unroll
  for (int it = 0; it < 4; it++) *(LAS u32x4*)(size_t)(cb + UOFF + 512 + (it * 64 + lane) * 16) = uv[it];
}
struct ConvFrags { u32x2 al[8], ah[8]; u32x4 B[8]; };
__device__ __forceinline__ void conv_read(ConvFrags& f, unsigned aa, unsigned ba) {
  asm volatile(
      "ds_read_b128 %16, %25\n\tds_read_b64 %7, %24 offset:3584\n\tds_read_b64 %15, %24 offset:3592\n\t"
      "ds_read_b128 %17, %25 offset:512\n\tds_read_b128 %18, %25 offset:1024\n\tds_read_b128 %19, %25 offset:1536\n\t"
      "ds_read_b128 %20, %25 offset:2048\n\tds_read_b128 %21, %25 offset:2560\n\tds_read_b128 %22, %25 offset:3072\n\t"
      "ds_read_b128 %23, %25 offset:3584\n\t"
      "ds_read_b64 %6, %24 offset:3072\n\tds_read_b64 %14, %24 offset:3080\n\t"
      "ds_read_b64 %5, %24 offset:2560\n\tds_read_b64 %13, %24 offset:2568\n\t"
      "ds_read_b64 %4, %24 offset:2048\n\tds_read_b64 %12, %24 offset:2056\n\t"
      "ds_read_b64 %3, %24 offset:1536\n\tds_read_b64 %11, %24 offset:1544\n\t"
      "ds_read_b64 %2, %24 offset:1024\n\tds_read_b64 %10, %24 offset:1032\n\t"
      "ds_read_b64 %1, %24 offset:512\n\tds_read_b64 %9, %24 offset:520\n\t"
      "ds_read_b64 %0, %24\n\tds_read_b64 %8, %24 offset:8"
      : "=&v"(f.al[0]), "=&v"(f.al[1]), "=&v"(f.al[2]), "=&v"(f.al[3]), "=&v"(f.al[4]), "=&v"(f.al[5]), "=&v"(f.al[6]), "=&v"(f.al[7]),
        "=&v"(f.ah[0]), "=&v"(f.ah[1]), "=&v"(f.ah[2]), "=&v"(f.ah[3]), "=&v"(f.ah[4]), "=&v"(f.ah[5]), "=&v"(f.ah[6]), "=&v"(f.ah[7]),
        "=&v"(f.B[0]), "=&v"(f.B[1]), "=&v"(f.B[2]), "=&v"(f.B[3]), "=&v"(f.B[4]), "=&v"(f.B[5]), "=&v"(f.B[6]), "=&v"(f.B[7])
      : "v"(aa), "v"(ba) : "memory");
}
__device__ __forceinline__ void conv_wait(ConvFrags& f) {
  asm volatile("s_waitcnt lgkmcnt(0)"
      : "+v"(f.al[0]), "+v"(f.al[1]), "+v"(f.al[2]), "+v"(f.al[3]), "+v"(f.al[4]), "+v"(f.al[5]), "+v"(f.al[6]), "+v"(f.al[7]),
        "+v"(f.ah[0]), "+v"(f.ah[1]), "+v"(f.ah[2]), "+v"(f.ah[3]), "+v"(f.ah[4]), "+v"(f.ah[5]), "+v"(f.ah[6]), "+v"(f.ah[7]),
        "+v"(f.B[0]), "+v"(f.B[1]), "+v"(f.B[2]), "+v"(f.B[3]), "+v"(f.B[4]), "+v"(f.B[5]), "+v"(f.B[6]), "+v"(f.B[7]) :: "memory");
}
template <int K> __device__ __forceinline__ void conv_read1(ConvFrags& f, unsigned aa, unsigned ba) {
  if constexpr (K < 8) asm volatile("ds_read_b128 %0, %1 offset:%2" : "=&v"(f.B[K]) : "v"(ba), "n"(512 * K) : "memory");
  else if constexpr (K < 16) asm volatile("ds_read_b64 %0, %1 offset:%2" : "=&v"(f.al[K - 8]) : "v"(aa), "n"(512 * (K - 8)) : "memory");
  else if constexpr (K < 24) asm volatile("ds_read_b64 %0, %1 offset:%2" : "=&v"(f.ah[K - 16]) : "v"(aa), "n"(512 * (K - 16) + 8) : "memory");
}
template <int K = 0> __device__ __forceinline__ void conv_step(f32x4 (&acc)[8], const ConvFrags& f, ConvFrags& nx, unsigned aa, unsigned ba) {
  if constexpr (K < 36) {
    constexpr int a = K < 8 ? 0 : K < 15 ? 1 : K < 21 ? 2 : K < 26 ? 3 : K < 30 ? 4 : K < 33 ? 5 : K < 35 ? 6 : 7;
    constexpr int i = K - (a == 0 ? 0 : a == 1 ? 8 : a == 2 ? 15 : a == 3 ? 21 : a == 4 ? 26 : a == 5 ? 30 : a == 6 ? 33 : 35);
    acc[a + i] = mfma16(mk8(f.al[7 - a], f.ah[7 - a]), __builtin_bit_cast(bf16x8, f.B[i]), acc[a + i]);
    conv_read1<K>(nx, aa, ba);
    SBAR();
    conv_step<K + 1>(acc, f, nx, aa, ba);
  }
}
__device__ __forceinline__ void conv_main(f32x4 (&acc)[8], unsigned cb, int lane, int c0, int c1) {
  const int fr = lane & 15, g4 = lane >> 4;
  const unsigned abase = cb + (3 - (fr & 3)) * CPS + 2 * (2044 - 16 * 112 - 16 * (g4 >> 1) + 8 * (g4 & 1) - (fr & ~3));
  const unsigned bbase = cb + UOFF + 512 + 2 * (16 * fr - 16 * (g4 >> 1) + 8 * (g4 & 1));
#pragma unroll
  for (int c = 0; c < 8; c++) acc[c] = (f32x4){0.f, 0.f, 0.f, 0.f};
  ConvFrags f0, f1;
  asm volatile("s_waitcnt lgkmcnt(0)" ::: "memory");
  conv_read(f0, abase - 64 * c0, bbase - 64 * c0);
  for (int cc = c0; cc < c1; cc += 2) {
    conv_wait(f0);
    SBAR();
    conv_step(acc, f0, f1, abase - 64 * (cc + 1), bbase - 64 * (cc + 1));
    conv_wait(f1);
    SBAR();
    const int cn = cc + 2 < c1 ? cc + 2 : cc + 1;
    conv_step(acc, f1, f0, abase - 64 * cn, bbase - 64 * cn);
  }
  conv_wait(f0);
}
__device__ __forceinline__ void conv_store(f32x4 y, unsigned cb, int c, int lane, float Dd, __amdgpu_buffer_rsrc_t rw, int d) {
  const int fr = lane & 15, g4 = lane >> 4;
  const u32x2 uu = lds_b64(cb + UOFF + 512 + 2 * (256 * c + 16 * fr + 4 * g4));
  const float r0 = fmaf(bflo(uu.x), Dd, y[0]), r1 = fmaf(bfhi(uu.x), Dd, y[1]);
  const float r2 = fmaf(bflo(uu.y), Dd, y[2]), r3 = fmaf(bfhi(uu.y), Dd, y[3]);
  __builtin_amdgcn_raw_buffer_store_b64((u32x2){pk2(r0, r1), pk2(r2, r3)}, rw, OFF_YT + d * 4096 + (256 * c + 16 * fr + 4 * g4) * 2, 0, SC1);
}

__global__ __launch_bounds__(512, 2) void fused(const u16* __restrict__ x, const u16* __restrict__ wqkv, const u16* __restrict__ wo,
                                                const u16* __restrict__ Bk, const u16* __restrict__ Bv, const u16* __restrict__ shk,
                                                const u16* __restrict__ hk, const u16* __restrict__ Dv, const u16* __restrict__ gw,
                                                u16* __restrict__ out, char* __restrict__ ws) {
  extern __shared__ __attribute__((aligned(16))) char sm[];
  const int tid = threadIdx.x, lane = tid & 63;
  const int w = __builtin_amdgcn_readfirstlane(tid >> 6);
  const int b = blockIdx.x, xcd = b & 7, r = b >> 3;
  const int fr = lane & 15, g4 = lane >> 4;
  const unsigned lb = (unsigned)(size_t)(LAS char*)sm;
  const __amdgpu_buffer_rsrc_t rw = __builtin_amdgcn_make_buffer_rsrc(ws, 0, WS_USED, 0x00020000);
  if (r < 24 && (w >> 1) == 2) {
    const bool kv_ = r < 16;
    gemm_first_x(w - 4, lane, lb, x, (xcd & 3) * 4 + (r & 3), kv_ ? (xcd >> 2) * 4 + (r >> 2) : (xcd >> 2) * 2 + ((r - 16) >> 2), kv_);
  }
  TS_DECL TS(0);
  unsigned e0 = 0;
#define PUT_TAG() do { if (w == 7 && lane == 0) { *(LAS unsigned*)(size_t)(lb + L_TAG) = e0 + 1u; *(LAS u32x4*)(size_t)(lb + L_CNT) = (u32x4){0u, 0u, 0u, 0u}; } } while (0)
#define TAG (*(const LAS unsigned*)(size_t)(lb + L_TAG))
  if (r < 24) {
    const bool isKV = r < 16;
    const int rt = (xcd & 3) * 4 + (r & 3);
    const int cg = isKV ? (xcd >> 2) * 4 + (r >> 2) : (xcd >> 2) * 2 + ((r - 16) >> 2);
    f32x4 acc[4][5];
    const int rp = (w >> 1) & 1, hf = w & 1;
    if (w < 4) {
      const unsigned xa0 = lb + (rp * 64 + fr) * 128 + ((g4 ^ ((fr >> 1) & 7)) << 4);
      const unsigned wl = lb + XS + (8 * g4 + (fr >> 2)) * 320 + ((lane & 3) >> 1) * 16 + (lane & 1) * 8 + hf * 160;
      const unsigned sgn = (g4 & 1) * 32;
      const unsigned wa0 = hf ? wl - sgn : wl + sgn, wa1 = hf ? wl + sgn : wl - sgn;
#pragma unroll
      for (int i = 0; i < 4; i++)
#pragma unroll
        for (int t = 0; t < 5; t++) acc[i][t] = (f32x4){0.f, 0.f, 0.f, 0.f};
      if (isKV) gemm_compute<true>(acc, xa0, wa0, wa1 TSARG); else gemm_compute<false>(acc, xa0, wa0, wa1 TSARG);
    } else if (w < 6) {
      gemm_loader(w - 4, lane, lb, x, wqkv, rt, cg, isKV);
    } else {
      const int w6 = w - 6;
      unsigned pf[5] = {0, 0, 0, 0, 0};
      SBAR();
      if (w == 7) e0 = flag_ld(ws, F_E);
      if (isKV) {
        const int l16 = lane & 15, row0 = rt * 128 + cg * 16 + w6 * 8;
        const u32x4 g0 = *(const u32x4*)(gw + lane * 8), g1 = *(const u32x4*)(gw + 512 + l16 * 8);
        u32x4 gx0[8], gx1[8];
#pragma unroll
        for (int i = 0; i < 8; i++) {
          const u16* xr = x + (size_t)(row0 + i) * D;
          gx0[i] = *(const u32x4*)(xr + lane * 8); gx1[i] = *(const u32x4*)(xr + 512 + l16 * 8);
        }
        SBAR();
        BAR(); BAR(); BAR();
        float a[8];
        const float m1 = lane < 16 ? 1.f : 0.f;
#pragma unroll
        for (int i = 0; i < 8; i++) {
          float a0 = 0.f, a1 = 0.f;
#pragma unroll
          for (int j = 0; j < 4; j++) {
            a0 = fmaf(bflo(gx0[i][j]), bflo(g0[j]), a0); a0 = fmaf(bfhi(gx0[i][j]), bfhi(g0[j]), a0);
            a1 = fmaf(bflo(gx1[i][j]), bflo(g1[j]), a1); a1 = fmaf(bfhi(gx1[i][j]), bfhi(g1[j]), a1);
          }
          a[i] = fmaf(a1, m1, a0);
          if (i == 3) { SBAR(); BAR(); SBAR(); }
        }
        SBAR(); BAR(); SBAR();
#pragma unroll
        for (int i = 0; i < 4; i++) { const float lo = a[i], hi = a[i + 4]; const bool up = lane & 4; a[i] = (up ? hi : lo) + __shfl_xor(up ? lo : hi, 4); }
#pragma unroll
        for (int i = 0; i < 2; i++) { const float lo = a[i], hi = a[i + 2]; const bool up = lane & 2; a[i] = (up ? hi : lo) + __shfl_xor(up ? lo : hi, 2); }
        { const float lo = a[0], hi = a[1]; const bool up = lane & 1; a[0] = (up ? hi : lo) + __shfl_xor(up ? lo : hi, 1); }
        a[0] += __shfl_xor(a[0], 8); a[0] += __shfl_xor(a[0], 16); a[0] += __shfl_xor(a[0], 32);
        if (lane < 8) __builtin_amdgcn_raw_buffer_store_b32(__float_as_uint(rbf(1.f / (1.f + __expf(-rbf(a[0]))))), rw, OFF_G + (row0 + lane) * 4, 0, SC1);
        PUT_TAG();
        asm volatile("" :: "v"(pf[0]), "v"(pf[1]), "v"(pf[2]), "v"(pf[3]), "v"(pf[4]));
        SBAR();
        BAR(); BAR(); BAR(); BAR(); BAR();
      } else {
        const int hw = (xcd * 8 + (r - 16)) * 2 + w6;
        const unsigned scr = lb + L_T + w6 * 6144;
#pragma unroll
        for (int bt = 0; bt < 2; bt++) {
          const int blk = bt == 0 ? hw : (hw < 32 ? 128 + hw : hw), ct = (blk * 3277) >> 16, ks = blk - 20 * ct;
          u32x4 v[5];
#pragma unroll
          for (int i = 0; i < 5; i++) {
            const int c = i * 64 + lane, row = (c * 6554) >> 16, ch = c - 10 * row;
            v[i] = *(const u32x4*)(wo + (size_t)(32 * ks + row) * D + 80 * ct + 8 * ch);
          }
          SBAR();
          BAR();
          if (bt == 0) PUT_TAG();
          BAR();
          if (bt == 0 || hw < 32) {
#pragma unroll
            for (int i = 0; i < 5; i++) *(LAS u32x4*)(size_t)(scr + (i * 64 + lane) * 16) = v[i];
            const unsigned ta = scr + (8 * g4 + (fr >> 2)) * 160 + (lane & 3) * 8;
#pragma unroll
            for (int t = 0; t < 5; t++) {
              const u32x2 lo = lds_tr(ta + 32 * t), hi = lds_tr(ta + 32 * t + 640);
              __builtin_amdgcn_raw_buffer_store_b128((u32x4){lo.x, lo.y, hi.x, hi.y}, rw, OFF_WOP + (blk * 5 + t) * 1024 + lane * 16, 0, SC1);
            }
          }
          SBAR();
          BAR(); BAR();
        }
        asm volatile("" :: "v"(pf[0]), "v"(pf[1]), "v"(pf[2]), "v"(pf[3]), "v"(pf[4]));
        DRAIN();
        if (lds_last(lb + L_CNT + 8, lane, 2) && lane == 0) flag_st(ws, F_W + (xcd * 8 + (r - 16)) * 128, TAG);
        BAR(); BAR();
      }
    }
    TS(1);
    BAR();
    TS(11);
    const unsigned tag = TAG;
    if (isKV) {
      if (w < 4) {
#pragma unroll
        for (int t = 0; t < 5; t++) {
          const bool isk = hf ? t < 2 : t < 3;
          const int slot = hf ? (t < 2 ? 3 + t : 5 + t) : (t < 3 ? t : 2 + t);
#pragma unroll
          for (int m = 0; m < 2; m++) {
            u32x4 f;
            if (isk) f = (u32x4){pk2(phi(acc[2 * m][t][0]), phi(acc[2 * m][t][1])), pk2(phi(acc[2 * m][t][2]), phi(acc[2 * m][t][3])),
                                 pk2(phi(acc[2 * m + 1][t][0]), phi(acc[2 * m + 1][t][1])), pk2(phi(acc[2 * m + 1][t][2]), phi(acc[2 * m + 1][t][3]))};
            else f = (u32x4){pk2(acc[2 * m][t][0], acc[2 * m][t][1]), pk2(acc[2 * m][t][2], acc[2 * m][t][3]),
                             pk2(acc[2 * m + 1][t][0], acc[2 * m + 1][t][1]), pk2(acc[2 * m + 1][t][2], acc[2 * m + 1][t][3])};
            *(LAS u32x4*)(size_t)(lb + ((rp * 2 + m) * 10 + slot) * 1024 + lane * 16) = f;
          }
        }
      } else {
        for (int i = tid - 256; i < 6048 / 16; i += 256) *(LAS u32x4*)(size_t)(lb + L_T + i * 16) = (u32x4){0, 0, 0, 0};
        if (w == 7) poll(ws, F_W + lane * 128, true, tag);
      }
      TS(12);
      BAR();
      TS(2);
      switch (w) {
        case 0: stat_tiles<0>(lb, lane, rw, OFF_P + (rt * 8 + cg) * 3360); break;
        case 1: stat_tiles<1>(lb, lane, rw, OFF_P + (rt * 8 + cg) * 3360); break;
        case 2: stat_tiles<2>(lb, lane, rw, OFF_P + (rt * 8 + cg) * 3360); break;
        case 3: stat_tiles<3>(lb, lane, rw, OFF_P + (rt * 8 + cg) * 3360); break;
        case 4: stat_tiles<4>(lb, lane, rw, OFF_P + (rt * 8 + cg) * 3360); break;
        case 5: stat_tiles<5>(lb, lane, rw, OFF_P + (rt * 8 + cg) * 3360); break;
        case 6: stat_tiles<6>(lb, lane, rw, OFF_P + (rt * 8 + cg) * 3360); break;
        default: stat_tiles<7>(lb, lane, rw, OFF_P + (rt * 8 + cg) * 3360); break;
      }
      TS(14);
      DRAIN();
      TS(15);
      BAR();
      TS(3);
      if (w == 0) {
        if (lane == 0) flag_st(ws, F_P + (rt * 8 + cg) * 128, tag);
        poll(ws, lane < 16 ? F_P + (lane * 8 + cg) * 128 : F_PQ + (rt * 4 + (cg >> 1)) * 128, lane <= 16, tag);
      } else if (w < 7) {
        const i32x4 sws = make_srd(ws, WS_USED);
        const unsigned vwv = OFF_WOP + ((xcd >> 2) * 4 + (r >> 3)) * 102400 + lane * 16;
        for (int i = w - 1; i < 100; i += 6) {
          int pc = 12 * (r & 7) + i; pc = __builtin_amdgcn_readfirstlane(pc >= 100 ? pc - 100 : pc);
          glds16_sc1(lb + pc * 1024, vwv, sws, pc * 1024);
        }
      }
      constexpr int PJ[9] = {0, 1, 1, 2, 2, 3, 3, 4, 4}, PK[9] = {0, 0, 1, 0, 1, 1, 2, 1, 2};
      unsigned an[9], ad[9];
#pragma unroll
      for (int p = 0; p < 9; p++) {
        const int E = 16 * PJ[p] + fr, h = (E * 205) >> 12, e = E - 20 * h;
        int o_ = 32 * PK[p] + 8 * g4 - 20 * h; o_ = o_ < -8 ? -8 : o_ > 20 ? 20 : o_;
        an[p] = lb + L_T + (h * 21 + e) * 72 + (8 + o_) * 2; ad[p] = lb + L_T + (h * 21 + 20) * 72 + (8 + o_) * 2;
      }
      BAR();
      TS(4);
      bf16x8 Bq[3];
      {
        const unsigned prow = OFF_PQ + ((rt * 8 + w) * 8 + cg) * 3072 + lane * 16;
#pragma unroll
        for (int ks = 0; ks < 3; ks++) Bq[ks] = __builtin_bit_cast(bf16x8, __builtin_amdgcn_raw_buffer_load_b128(rw, prow + 1024 * ks, 0, SC1));
      }
      if (tid < 210) {
        const unsigned pb = OFF_P + cg * 3360 + tid * 16;
        u32x4 v[16];
#pragma unroll
        for (int k = 0; k < 16; k++) v[k] = __builtin_amdgcn_raw_buffer_load_b128(rw, pb, (k ^ rt) * (8 * 3360), SC1);
        unsigned o4[4];
#pragma unroll
        for (int c = 0; c < 4; c++) {
          float lo[16], hi[16];
#pragma unroll
          for (int k = 0; k < 16; k++) { lo[k] = bflo(v[k][c]); hi[k] = bfhi(v[k][c]); }
#pragma unroll
          for (int st = 1; st < 16; st <<= 1)
#pragma unroll
            for (int k = 0; k < 16; k += 2 * st) { lo[k] += lo[k + st]; hi[k] += hi[k + st]; }
          o4[c] = pk2(lo[0], hi[0]);
        }
        TS(16);
#pragma unroll
        for (int q = 0; q < 2; q++) {
          const int i4 = 2 * tid + q, row = (i4 * 13108) >> 16, d = 4 * (i4 - row * 5);
          *(LAS u32x2*)(size_t)(lb + L_T + row * 72 + (8 + d) * 2) = (u32x2){o4[2 * q], o4[2 * q + 1]};
        }
      }
      BAR();
      TS(5);
      {
        if (g4 >= 2) Bq[2] = __builtin_bit_cast(bf16x8, (u32x4){0, 0, 0, 0});
        f32x4 nu[5], de[5];
#pragma unroll
        for (int j = 0; j < 5; j++) { nu[j] = (f32x4){0.f, 0.f, 0.f, 0.f}; de[j] = nu[j]; }
        bf16x8 An[9], Ad[9];
#pragma unroll
        for (int p = 0; p < 9; p++) {
          unsigned an8 = an[p] + 8, ad8 = ad[p] + 8;
          asm volatile("" : "+v"(an8), "+v"(ad8));
          An[p] = mk8(lds_b64(an[p]), lds_b64(an8)); Ad[p] = mk8(lds_b64(ad[p]), lds_b64(ad8));
        }
#pragma unroll
        for (int p = 0; p < 9; p++) { de[PJ[p]] = mfma16(Ad[p], Bq[PK[p]], de[PJ[p]]); nu[PJ[p]] = mfma16(An[p], Bq[PK[p]], nu[PJ[p]]); }
        u32x2 ov[5];
#pragma unroll
        for (int j = 0; j < 5; j++) {
          const float rd = __builtin_amdgcn_rcpf(de[j][0] + 1e-6f);
          ov[j] = (u32x2){pk2(nu[j][0] * rd, nu[j][1] * rd), pk2(nu[j][2] * rd, nu[j][3] * rd)};
        }
        store_frags(ov, rw, OFF_O + (rt * 8 + w) * 20480, 80 * cg, fr, g4);
      }
      TS(17);
      DRAIN();
      BAR();
      if (tid == 0) flag_st(ws, F_O + (rt * 8 + cg) * 128, tag);
      TS(6);
    } else {
      const int vb = xcd * 8 + (r - 16);
      const int d = 80 * (vb >> 3) + 48 + 4 * (vb & 7) + (w & 3);
      const unsigned cb = lb + (w & 3) * CHS;
      if (w < 4) {
#pragma unroll
        for (int i = 0; i < 4; i++) {
          u32x2 pv[5];
#pragma unroll
          for (int t = 0; t < 5; t++) pv[t] = (u32x2){pk2(phi(acc[i][t][0]), phi(acc[i][t][1])), pk2(phi(acc[i][t][2]), phi(acc[i][t][3]))};
          store_frags(pv, rw, OFF_PQ + ((rt * 8 + rp * 4 + i) * 8 + cg * 2 + hf) * 3072, 0, fr, g4);
        }
      } else {
        u32x4 hc[4], hp[4];
        conv_load_h(hc, hp, hk + (size_t)d * S, lane);
        poll(ws, F_U + lane * 128, true, tag);
        conv_load_u(rw, d, cb, lane, [&]() { conv_prep_h(hc, hp, cb, lane); });
      }
      TS(12);
      DRAIN();
      BAR();
      TS(2);
      if (tid == 0) flag_st(ws, F_PQ + (rt * 4 + cg) * 128, tag);
      if (w < 4) {
        const float Dd = bflo(Dv[d]);
        TS(3);
        f32x4 y[8];
        conv_main(y, cb, lane, 0, 8);
        TS(4);
#pragma unroll
        for (int c = 0; c < 8; c++) conv_store(y[c], cb, c, lane, Dd, rw, d);
        DRAIN();
      } else if (w == 7) {
        poll(ws, F_W + lane * 128, true, tag);
      }
      BAR();
      if (tid == 0) flag_st(ws, F_Y + (64 + vb) * 128, tag);
      TS(5); TS(6);
    }
  } else {
    const int pw = xcd * 8 + (r - 24);
    const int c6 = w < 4 ? w : 4 + ((w - 4) >> 1);
    const int d = 80 * (pw >> 3) + 6 * (pw & 7) + c6;
    const unsigned cb = lb + c6 * CHS;
    const bool prep = w < 4 || !(w & 1);
    u32x4 hc[4], hp[4];
    {
      const int rbase = xcd * 256 + ((r - 24) >> 1) * 64, cbase = ((r - 24) & 1) * 320;
      u32x4 kr[2][7], vr[2][4], sp[2][4];
#pragma unroll
      for (int q = 0; q < 2; q++) {
        const int it = w + 8 * q;
        if (q == 0 || w < 2) {
          const int ch = cbase + (it >> 1) * 64 + (lane & 7) * 8;
          const int s0 = rbase + (it & 1) * 32 + (lane >> 3) * 4;
#pragma unroll
          for (int j = 0; j < 7; j++) { int s = s0 - 3 + j; kr[q][j] = *(const u32x4*)(Bk + (size_t)(s < 0 ? 0 : s) * D + ch); }
#pragma unroll
          for (int j = 0; j < 4; j++) vr[q][j] = *(const u32x4*)(Bv + (size_t)(s0 + j) * D + ch);
#pragma unroll
          for (int j = 0; j < 4; j++) sp[q][j] = ((const u32x4*)(shk + ch * 4))[j];
        }
      }
      if (prep) conv_load_h(hc, hp, hk + (size_t)d * S, lane);
      if (w == 7) e0 = flag_ld(ws, F_E);
      SBAR();
#pragma unroll
      for (int q = 0; q < 2; q++) {
        const int it = w + 8 * q;
        if (q == 0 || w < 2) {
          const int s0 = rbase + (it & 1) * 32 + (lane >> 3) * 4;
          if (s0 == 0) { kr[q][0] = (u32x4){0, 0, 0, 0}; kr[q][1] = kr[q][0]; kr[q][2] = kr[q][0]; }
          const int rg = (it & 1) * 8 + (lane >> 3);
          const unsigned la = lb + L_UIMG + ((it >> 1) * 64 + (lane & 7) * 8) * 128 + (((rg >> 1) ^ (lane & 7)) << 4) + (rg & 1) * 8;
#pragma unroll
          for (int c = 0; c < 8; c++) {
            const unsigned s01 = sp[q][c >> 1][(c & 1) * 2], s23 = sp[q][c >> 1][(c & 1) * 2 + 1];
            const float w0 = bflo(s01), w1 = bfhi(s01), w2 = bflo(s23), w3 = bfhi(s23);
            float kk[7], u[4];
#pragma unroll
            for (int j = 0; j < 7; j++) { unsigned dd = kr[q][j][c >> 1]; kk[j] = (c & 1) ? bfhi(dd) : bflo(dd); }
#pragma unroll
            for (int j = 0; j < 4; j++) {
              unsigned dd = vr[q][j][c >> 1]; float v = (c & 1) ? bfhi(dd) : bflo(dd);
              float ks = kk[j] * w3; ks = fmaf(kk[j + 1], w2, ks); ks = fmaf(kk[j + 2], w1, ks); ks = fmaf(kk[j + 3], w0, ks);
              u[j] = ks * v;
            }
            *(LAS u32x2*)(size_t)(la + c * 128) = (u32x2){pk2(u[0], u[1]), pk2(u[2], u[3])};
          }
        }
      }
      PUT_TAG();
      BAR();
#pragma unroll
      for (int i = 0; i < 5; i++) {
        const int chl = (w * 5 + i) * 8 + (lane >> 3), k = lane & 7;
        const u32x4 v = *(const LAS u32x4*)(size_t)(lb + L_UIMG + chl * 128 + ((k ^ ((chl >> 3) & 7)) << 4));
        __builtin_amdgcn_raw_buffer_store_b128(v, rw, OFF_UT + (cbase + chl) * 4096 + rbase * 2 + k * 16, 0, SC1);
      }
      TS(1);
    }
    if (w < 5 && prep) conv_prep_h(hc, hp, cb, lane);
    DRAIN();
    BAR();
    TS(2);
    const unsigned tag = TAG;
    if (tid == 0) flag_st(ws, F_U + pw * 128, tag);
    const float Dd = bflo(Dv[d]);
    if (prep) {
      if (w == 6) conv_prep_h(hc, hp, cb, lane);
      poll(ws, F_U + lane * 128, true, tag);
      conv_load_u(rw, d, cb, lane, [&]() {});
    }
    BAR();
    TS(3);
    f32x4 y[8];
    conv_main(y, cb, lane, w < 4 ? 0 : (w & 1) * 4, w < 4 ? 8 : (w & 1) * 4 + 4);
    TS(4);
    if (w < 4) {
#pragma unroll
      for (int c = 0; c < 8; c++) conv_store(y[c], cb, c, lane, Dd, rw, d);
    }
    BAR();
    if (w >= 4) {
#pragma unroll
      for (int c = 0; c < 4; c++) { const f32x4 lo = y[c], hi = y[4 + c]; *(LAS f32x4*)(size_t)(cb + ((w & 1) * 4 + c) * 1024 + lane * 16) = (w & 1) ? lo : hi; }
    }
    BAR();
    if (w >= 4) {
#pragma unroll
      for (int c = 0; c < 4; c++) {
        const f32x4 o_ = *(const LAS f32x4*)(size_t)(cb + (((w & 1) ^ 1) * 4 + c) * 1024 + lane * 16);
        const f32x4 lo = y[c], hi = y[4 + c];
        const f32x4 mine = (w & 1) ? hi : lo;
        conv_store((w & 1) ? o_ + mine : mine + o_, cb, (w & 1) * 4 + c, lane, Dd, rw, d);
      }
    }
    if (w == 0) poll(ws, F_W + lane * 128, true, tag);
    DRAIN();
    BAR();
    if (tid == 0) flag_st(ws, F_Y + pw * 128, tag);
    TS(5); TS(6);
  }

  {
    const unsigned tag = TAG;
    const int rt = (xcd & 3) * 8 + (r & 7), ct = (xcd >> 2) * 4 + (r >> 3);
    const int sw_ = w & 3, kk = w >> 2;
    const i32x4 sws = make_srd(ws, WS_USED);
    TS(18);
    const int srow = rt * 64 + sw_ * 16 + fr;
    u32x2 xv[3];
#pragma unroll
    for (int t = 0; t < 3; t++) { const int tt = kk ? (t < 2 ? 3 + t : 4) : t; xv[t] = *(const u32x2*)(x + (size_t)srow * D + ct * 80 + 16 * tt + 4 * g4); }
    SBAR();
    const int rot = ((ct & 3) * 5) >> 1;
    const unsigned wa0 = lb + kk * 5120 + lane * 16;
    bf16x8 wf0[5][5];
    auto read_wf0 = [&]() {
#pragma unroll
      for (int s = 0; s < 5; s++) {
        int q = s + rot; q = q >= 10 ? q - 10 : q;
#pragma unroll
        for (int t = 0; t < 5; t++) wf0[s][t] = lds_b128(wa0 + q * 10240 + t * 1024);
      }
    };
    if (r < 16) read_wf0();
    SBAR();
    if (w == 0) {
      poll(ws, F_O + ((rt >> 1) * 8 + (lane & 7)) * 128, lane < 8, tag);
    } else if (w == 7) {
      poll(ws, F_Y + ((lane & 8 ? 64 : 0) + ct * 8 + (lane & 7)) * 128, lane < 16, tag);
    } else {
      if (r >= 16) {
        const unsigned vwv = OFF_WOP + ct * 102400 + lane * 16;
        for (int i = w - 1; i < 100; i += 6) { int pc = 12 * (r & 7) + i; pc = __builtin_amdgcn_readfirstlane(pc >= 100 ? pc - 100 : pc); glds16_sc1(lb + pc * 1024, vwv, sws, pc * 1024); }
      }
      if (w == 6 && b == 255) {
        for (int spins = 0; spins < (1 << 17); spins++) {
          const unsigned a0 = flag_ld(ws, F_U + lane * 128), a1 = flag_ld(ws, F_U + (64 + lane) * 128);
          const unsigned a2 = flag_ld(ws, F_U + (128 + lane) * 128), a3 = flag_ld(ws, F_U + (192 + lane) * 128);
          if (__all(a0 == tag && a1 == tag && a2 == tag && a3 == tag)) break;
          __builtin_amdgcn_s_sleep(2);
        }
        if (lane == 0) flag_st(ws, F_E, tag);
      }
      DRAIN();
    }
    BAR();
    TS(7);
    asm volatile("" :: "v"(xv[0]), "v"(xv[1]), "v"(xv[2]));
    bf16x8 of[10];
    {
      const unsigned vo = OFF_O + (rt * 4 + sw_) * 20480 + kk * 1024 + lane * 16;
#pragma unroll
      for (int s = 0; s < 10; s++) { int q = s + rot; q = __builtin_amdgcn_readfirstlane(q >= 10 ? q - 10 : q); bload16_sc1(of[s], vo, sws, q * 2048); }
    }
    if (w == 7) {
      for (int pc = 0; pc < 10; pc++)
        glds16_sc1(lb + L_Y + pc * 1024, OFF_YT + (ct * 80 + pc * 8 + (lane >> 3)) * 4096 + rt * 128 + (lane & 7) * 16, sws, 0);
    }
    float g;
    asm volatile("buffer_load_dword %0, %1, %2, 0 offen sc1" : "=v"(g) : "v"(OFF_G + srow * 4), "s"(sws) : "memory");
    TS(8);
    if (r >= 16) read_wf0();
    f32x4 acc[5];
#pragma unroll
    for (int t = 0; t < 5; t++) acc[t] = (f32x4){0.f, 0.f, 0.f, 0.f};
    {
      bf16x8 wf1[5][5];
#pragma unroll
      for (int s = 0; s < 5; s++) {
        int q = 5 + s + rot; q = q >= 10 ? q - 10 : q;
#pragma unroll
        for (int t = 0; t < 5; t++) wf1[s][t] = lds_b128(wa0 + q * 10240 + t * 1024);
      }
      SBAR();
      if (w == 7) asm volatile("s_waitcnt vmcnt(16)" ::: "memory"); else asm volatile("s_waitcnt vmcnt(6)" ::: "memory");
      asm volatile("" : "+v"(of[0]), "+v"(of[1]), "+v"(of[2]), "+v"(of[3]), "+v"(of[4]) :: "memory");
      SBAR();
#pragma unroll
      for (int s = 0; s < 5; s++)
#pragma unroll
        for (int t = 0; t < 5; t++) acc[t] = mfma16(wf0[s][t], of[s], acc[t]);
      SBAR();
      if (w == 7) asm volatile("s_waitcnt vmcnt(11)" ::: "memory"); else asm volatile("s_waitcnt vmcnt(1)" ::: "memory");
      asm volatile("" : "+v"(of[5]), "+v"(of[6]), "+v"(of[7]), "+v"(of[8]), "+v"(of[9]) :: "memory");
      SBAR();
#pragma unroll
      for (int s = 0; s < 5; s++)
#pragma unroll
        for (int t = 0; t < 5; t++) acc[t] = mfma16(wf1[s][t], of[5 + s], acc[t]);
      SBAR();
    }
    TS(9);
    const unsigned ra = lb + L_RED + (sw_ * 5) * 1024 + lane * 16;
    if (kk == 0) { *(LAS f32x4*)(size_t)(ra + 3 * 1024) = acc[3]; *(LAS f32x4*)(size_t)(ra + 4 * 1024) = acc[4]; }
    else { *(LAS f32x4*)(size_t)(ra) = acc[0]; *(LAS f32x4*)(size_t)(ra + 1024) = acc[1]; *(LAS f32x4*)(size_t)(ra + 2048) = acc[2]; }
    asm volatile("s_waitcnt vmcnt(0)" : "+v"(g) :: "memory");
    BAR();
    TS(20);
    const float omg = 1.f - g;
    const int nt = kk ? 2 : 3;
#pragma unroll
    for (int t = 0; t < 3; t++) {
      if (t < nt) {
        const int tt = kk ? 3 + t : t;
        const f32x4 a0 = acc[t], a1 = acc[3 + (t < 2 ? t : 1)];
        f32x4 a = *(const LAS f32x4*)(size_t)(ra + tt * 1024) + (kk ? a1 : a0);
        u32x2 yv = lds_tr(lb + L_Y + (16 * tt + 4 * g4 + (fr >> 2)) * 128 + (sw_ * 16 + 4 * (fr & 3)) * 2);
        float xs[4] = {bflo(xv[t].x), bfhi(xv[t].x), bflo(xv[t].y), bfhi(xv[t].y)};
        float ys[4] = {bflo(yv.x), bfhi(yv.x), bflo(yv.y), bfhi(yv.y)};
        float r_[4];
#pragma unroll
        for (int q = 0; q < 4; q++) r_[q] = fmaf(g, a[q], omg * (xs[q] * ys[q]));
        *(u32x2*)(out + (size_t)srow * D + ct * 80 + 16 * tt + 4 * g4) = (u32x2){pk2(r_[0], r_[1]), pk2(r_[2], r_[3])};
      }
    }
    TS(10); TS_OUT(21);
  }
}

extern "C" void kernel_launch(void* const* d_in, const int* in_sizes, int n_in, void* d_out, int out_size, void* d_ws, size_t ws_size,
                              hipStream_t stream) {
  const u16* x = (const u16*)d_in[0]; const u16* wqkv = (const u16*)d_in[1]; const u16* wo = (const u16*)d_in[2];
  const u16* Bk = (const u16*)d_in[4]; const u16* Bv = (const u16*)d_in[5]; const u16* shk = (const u16*)d_in[6];
  const u16* hk = (const u16*)d_in[7]; const u16* Dv = (const u16*)d_in[8]; const u16* gw = (const u16*)d_in[9];
  (void)hipFuncSetAttribute((const void*)fused, hipFuncAttributeMaxDynamicSharedMemorySize, LDS_TOTAL);
  fused<<<256, 512, LDS_TOTAL, stream>>>(x, wqkv, wo, Bk, Bv, shk, hk, Dv, gw, (u16*)d_out, (char*)d_ws);
}
```

```cpp
#include <hip/hip_runtime.h>
#include <hip/hip_bf16.h>
#include <type_traits>
#define NO_PF

typedef unsigned short u16;
typedef __bf16 bf16x2 __attribute__((ext_vector_type(2)));
typedef __bf16 bf16x8 __attribute__((ext_vector_type(8)));
typedef float f32x4 __attribute__((ext_vector_type(4)));
typedef unsigned u32x4 __attribute__((ext_vector_type(4)));
typedef unsigned u32x2 __attribute__((ext_vector_type(2)));
typedef short v4i16_t __attribute__((ext_vector_type(4)));
typedef int i32x4 __attribute__((ext_vector_type(4)));
typedef __attribute__((address_space(1))) unsigned gu32;
#define LAS __attribute__((address_space(3)))
#define SBAR() __builtin_amdgcn_sched_barrier(0)
#define SGB(mask, n) __builtin_amdgcn_sched_group_barrier(mask, n, 0)
#define BAR() asm volatile("s_waitcnt lgkmcnt(0)\n\ts_barrier" ::: "memory")
#define DRAIN() asm volatile("s_waitcnt vmcnt(0)" ::: "memory")
#define SC1 16

#define TS_DECL
#define TSARG
#define TSPAR
#define TS(i)
#define TS_OUT(n)

constexpr int S = 2048, D = 640;
constexpr unsigned SZ = S * D * 2;
constexpr unsigned F_E = 0, F_U = 128, F_PQ = F_U + 64 * 128, F_P = F_PQ + 64 * 128, F_O = F_P + 128 * 128, F_Y = F_O + 128 * 128, F_W = F_Y + 128 * 128;
constexpr unsigned OFF_PQ = 0x20000, OFF_P = OFF_PQ + 128 * 8 * 3 * 1024, OFF_UT = OFF_P + (1u << 19), OFF_G = OFF_UT + SZ, OFF_WOP = OFF_G + 8192,
                   OFF_O = OFF_WOP + (1u << 20), OFF_YT = OFF_O + SZ, WS_USED = OFF_YT + SZ;
constexpr int XS = 16384, WSB = 20480, STG = XS + WSB;
constexpr int L_EXTRA = 4 * STG;
constexpr int L_TAG = L_EXTRA, L_CNT = L_EXTRA + 16, L_T = L_EXTRA + 64;
constexpr int L_SCR = L_T + 6144 + 5120;
constexpr int LDS_TOTAL = L_SCR + 256;
constexpr int CPS = 4160, UOFF = 4 * CPS, CHS = UOFF + 4608;
constexpr int L_UIMG = 5 * CHS;
constexpr int L_Y = 102400, L_RED = L_Y + 10240;

__device__ __forceinline__ unsigned pk2(float a, float b) { bf16x2 r = {(__bf16)a, (__bf16)b}; return __builtin_bit_cast(unsigned, r); }
__device__ __forceinline__ float rbf(float a) { return (float)(__bf16)a; }
__device__ __forceinline__ float bflo(unsigned v) { return __uint_as_float(v << 16); }
__device__ __forceinline__ float bfhi(unsigned v) { return __uint_as_float(v & 0xffff0000u); }
__device__ __forceinline__ float phi(float t) {
  return __builtin_amdgcn_fmed3f(t, 0.f, 3.0e38f) + (__builtin_amdgcn_fmed3f(__builtin_amdgcn_exp2f(t * 1.44269504f), 0.f, 1.f) + 1e-6f);
}
__device__ __forceinline__ i32x4 make_srd(const void* p, unsigned bytes) {
  unsigned long long a = (unsigned long long)p; i32x4 r;
  r.x = __builtin_amdgcn_readfirstlane((int)(unsigned)a); r.y = __builtin_amdgcn_readfirstlane((int)(unsigned)(a >> 32));
  r.z = __builtin_amdgcn_readfirstlane((int)bytes); r.w = 0x00020000; return r;
}
__device__ __forceinline__ void glds16(unsigned ldsaddr, unsigned voff, i32x4 srd, unsigned soff) {
  asm volatile("s_mov_b32 m0, %0\n\ts_nop 0\n\tbuffer_load_dwordx4 %1, %2, %3 offen lds" :: "s"(ldsaddr), "v"(voff), "s"(srd), "s"(soff) : "memory");
}
__device__ __forceinline__ void glds16_sc1(unsigned ldsaddr, unsigned voff, i32x4 srd, unsigned soff) {
  asm volatile("s_mov_b32 m0, %0\n\ts_nop 0\n\tbuffer_load_dwordx4 %1, %2, %3 offen sc1 lds" :: "s"(ldsaddr), "v"(voff), "s"(srd), "s"(soff) : "memory");
}
__device__ __forceinline__ void bload16_sc1(bf16x8& dst, unsigned voff, i32x4 srd, unsigned soff) {
  asm volatile("buffer_load_dwordx4 %0, %1, %2, %3 offen sc1" : "=v"(dst) : "v"(voff), "s"(srd), "s"(soff) : "memory");
}
template <int N> __device__ __forceinline__ void wait_bar() { asm volatile("s_waitcnt vmcnt(%0) lgkmcnt(0)\n\ts_barrier" :: "n"(N) : "memory"); }
__device__ __forceinline__ bf16x8 lds_b128(unsigned addr) { return *(const LAS bf16x8*)(size_t)addr; }
__device__ __forceinline__ u32x2 lds_b64(unsigned addr) { return *(const LAS u32x2*)(size_t)addr; }
__device__ __forceinline__ u32x2 lds_tr(unsigned addr) { return __builtin_bit_cast(u32x2, __builtin_amdgcn_ds_read_tr16_b64_v4i16((LAS v4i16_t*)(size_t)addr)); }
__device__ __forceinline__ bf16x8 mk8(u32x2 lo, u32x2 hi) { u32x4 t = {lo.x, lo.y, hi.x, hi.y}; return __builtin_bit_cast(bf16x8, t); }
__device__ __forceinline__ f32x4 mfma16(bf16x8 a, bf16x8 b, f32x4 c) { return __builtin_amdgcn_mfma_f32_16x16x32_bf16(a, b, c, 0, 0, 0); }

__device__ __forceinline__ u32x4 pair16(u32x2 a, u32x2 b) {
  const auto r0 = __builtin_amdgcn_permlane16_swap(a.x, b.x, false, false);
  const auto r1 = __builtin_amdgcn_permlane16_swap(a.y, b.y, false, false);
  return (u32x4){r0[0], r1[0], r0[1], r1[1]};
}
__device__ __forceinline__ void store_frags(const u32x2 (&v)[5], __amdgpu_buffer_rsrc_t rw, unsigned base, int k0, int fr, int g4) {
#pragma unroll
  for (int j = 0; j < 4; j += 2) {
    const int k = k0 + 16 * (j + (g4 & 1)) + 8 * (g4 >> 1);
    __builtin_amdgcn_raw_buffer_store_b128(pair16(v[j], v[j + 1]), rw, base + (k >> 5) * 1024 + (((k >> 3) & 3) * 16 + fr) * 16, 0, SC1);
  }
  const int k = k0 + 64 + 4 * g4;
  __builtin_amdgcn_raw_buffer_store_b64(v[4], rw, base + (k >> 5) * 1024 + (((k >> 3) & 3) * 16 + fr) * 16 + ((k >> 2) & 1) * 8, 0, SC1);
}

__device__ __forceinline__ void lds_sync(unsigned cnt_addr, int lane, unsigned n) {
  asm volatile("s_waitcnt lgkmcnt(0)" ::: "memory");
  if (lane == 0) __hip_atomic_fetch_add((LAS unsigned*)(size_t)cnt_addr, 1u, __ATOMIC_RELAXED, __HIP_MEMORY_SCOPE_WORKGROUP);
  while (__builtin_amdgcn_readfirstlane(__hip_atomic_load((LAS unsigned*)(size_t)cnt_addr, __ATOMIC_RELAXED, __HIP_MEMORY_SCOPE_WORKGROUP)) < n) {}
  asm volatile("" ::: "memory");
}
__device__ __forceinline__ bool lds_last(unsigned cnt_addr, int lane, unsigned n) {
  unsigned old = 0;
  if (lane == 0) old = __hip_atomic_fetch_add((LAS unsigned*)(size_t)cnt_addr, 1u, __ATOMIC_RELAXED, __HIP_MEMORY_SCOPE_WORKGROUP);
  return __builtin_amdgcn_readfirstlane(old) == n - 1;
}

__device__ __forceinline__ unsigned flag_ld(const char* ws, unsigned off) { return __hip_atomic_load((const gu32*)(ws + off), __ATOMIC_RELAXED, __HIP_MEMORY_SCOPE_AGENT); }
__device__ __forceinline__ void flag_st(char* ws, unsigned off, unsigned v) { __hip_atomic_store((gu32*)(ws + off), v, __ATOMIC_RELAXED, __HIP_MEMORY_SCOPE_AGENT); }
__device__ __forceinline__ void poll(const char* ws, unsigned off, bool act, unsigned tag) {
  const unsigned o0 = __builtin_amdgcn_readfirstlane(off);
  off = act ? off : o0;
  unsigned a, b, n = 1u << 17;
  asm volatile(
      "global_load_dword %0, %3, %4 sc1\n\t"
      "s_sleep 6\n"
      "1:\n\t"
      "global_load_dword %1, %3, %4 sc1\n\t"
      "s_waitcnt vmcnt(1)\n\t"
      "v_cmp_ne_u32 vcc, %0, %5\n\t"
      "s_cbranch_vccz 2f\n\t"
      "global_load_dword %0, %3, %4 sc1\n\t"
      "s_waitcnt vmcnt(1)\n\t"
      "v_cmp_ne_u32 vcc, %1, %5\n\t"
      "s_cbranch_vccz 2f\n\t"
      "s_sub_u32 %2, %2, 1\n\t"
      "s_cbranch_scc0 1b\n"
      "2:\n\t"
      "s_waitcnt vmcnt(0)"
      : "=&v"(a), "=&v"(b), "+s"(n) : "v"(off), "s"(ws), "v"(tag) : "vcc", "scc", "memory");
}

template <bool KV>
__device__ __forceinline__ void gemm_compute(f32x4 (&acc)[4][5], unsigned xa0, unsigned wa0, unsigned wa1 TSPAR) {
  bf16x8 xa[4], xb[4], wa[5], wb[5];
  auto load = [&](bf16x8 (&xf)[4], bf16x8 (&wf)[5], int buf, int kk) {
    const unsigned bo = buf * STG;
    const unsigned xa = (xa0 + bo) ^ (kk * 64);
    const unsigned a0 = wa0 + bo + kk * 10240, a1 = wa1 + bo + kk * 10240;
#pragma unroll
    for (int i = 0; i < 4; i++) xf[i] = lds_b128(xa + 2048 * i);
#pragma unroll
    for (int t = 0; t < 5; t++) { const unsigned a = ((t & 1) ? a1 : a0) + 32 * t; wf[t] = mk8(lds_tr(a), lds_tr(a + 1280)); }
  };
  auto mma = [&](const bf16x8 (&xf)[4], const bf16x8 (&wf)[5]) {
#pragma unroll
    for (int t = 0; t < 5; t++)
#pragma unroll
      for (int i = 0; i < 4; i++) acc[i][t] = KV ? mfma16(xf[i], wf[t], acc[i][t]) : mfma16(wf[t], xf[i], acc[i][t]);
  };
  auto interleave = [&]() {
#pragma unroll
    for (int t = 0; t < 14; t++) { SGB(0x8, 1); SGB(0x100, 1); }
    SGB(0x8, 6);
  };
  BAR();
  TS(21);
  load(xa, wa, 0, 0);
  SBAR();
  for (int s = 0; s < 9; s++) {
    load(xb, wb, s & 3, 1); mma(xa, wa); interleave();
    SBAR();
    BAR();
    if (s == 0) TS(22);
    if (s == 4) TS(23);
    load(xa, wa, (s + 1) & 3, 0); mma(xb, wb); interleave();
    SBAR();
  }
  load(xb, wb, 1, 1); mma(xa, wa); interleave();
  SBAR();
  mma(xb, wb);
}

__device__ __forceinline__ int gemm_s0(int rt, int cg, bool isKV) { return (((rt & 3) * 5 + 1) / 2 + (isKV ? 2 * (cg & 3) : 8 + (cg & 1))) % 10; }
__device__ __forceinline__ void gemm_vx(unsigned (&vx)[2], int L, int lane, int rt) {
#pragma unroll
  for (int i = 0; i < 2; i++) vx[i] = (rt * 128 + L * 64 + i * 8 + (lane >> 3)) * 1280 + (((lane & 7) ^ ((4 * i + (lane >> 4)) & 7)) << 4);
}
__device__ __forceinline__ void gemm_first_x(int L, int lane, unsigned lb, const u16* x, int rt, int cg, bool isKV) {
  const i32x4 sx = make_srd(x, S * D * 2);
  unsigned vx[2];
  gemm_vx(vx, L, lane, rt);
  const int s0 = gemm_s0(rt, cg, isKV);
#pragma unroll
  for (int p = 0; p < 8; p++) glds16(lb + (L * 8 + p) * 1024, vx[p & 1], sx, s0 * 128 + (p >> 1) * (16 * 1280));
}
__device__ __forceinline__ void gemm_loader(int L, int lane, unsigned lb, const u16* x, const u16* wqkv, int rt, int cg, bool isKV) {
  const int s0 = gemm_s0(rt, cg, isKV);
  const i32x4 sx = make_srd(x, S * D * 2), sw = make_srd(wqkv, D * 3 * D * 2);
  unsigned vx[2], vw[5];
  gemm_vx(vx, L, lane, rt);
#pragma unroll
  for (int i = 0; i < 5; i++) {
    const int ci = i * 64 + lane, k = (ci * 3277) >> 16, pos = ci - k * 20, gc = pos ^ (((k >> 3) & 1) << 1), tl = gc >> 1;
    unsigned col;
    if (isKV) { const int isv = (tl >= 3 && tl < 5) || tl >= 7; const int tt = tl < 3 ? tl : tl < 5 ? tl - 3 : tl < 7 ? tl - 2 : tl - 5;
                col = (isv ? 2560u : 1280u) + 160u * cg + tt * 32; }
    else col = 320u * cg + tl * 32;
    vw[i] = (L * 32 + k) * 3840 + col + (gc & 1) * 16;
  }
  auto stage = [&](int st_, int buf) {
    const unsigned base = lb + buf * STG;
    int st = st_ + s0; st = st >= 10 ? st - 10 : st;
#pragma unroll
    for (int p = 0; p < 10; p++) {
      glds16(base + XS + (L * 10 + p) * 1024, vw[p % 5], sw, st * (64 * 3840) + (p / 5) * (16 * 3840));
      if (p < 8) glds16(base + (L * 8 + p) * 1024, vx[p & 1], sx, st * 128 + (p >> 1) * (16 * 1280));
    }
  };
#pragma unroll
  for (int p = 0; p < 10; p++) glds16(lb + XS + (L * 10 + p) * 1024, vw[p % 5], sw, s0 * (64 * 3840) + (p / 5) * (16 * 3840));
  stage(1, 1); stage(2, 2);
  wait_bar<36>();
  stage(3, 3);
  for (int s = 0; s < 9; s++) {
    if (s < 7) wait_bar<36>(); else if (s == 7) wait_bar<18>(); else wait_bar<0>();
    if (s < 6) stage(s + 4, s & 3);
  }
}

template <int T> struct StatTile {
  static constexpr bool isz = T >= 13;
  static constexpr int i = isz ? T - 13 : (T + 1) / 3;
  static constexpr int j = isz ? 0 : T - (i == 0 ? 0 : 3 * i - 1) + (i == 0 ? 0 : i - 1);
};
template <int T> __device__ __forceinline__ f32x4 stat_mma(unsigned la) {
  const u32x4 o1 = {0x3F803F80u, 0x3F803F80u, 0x3F803F80u, 0x3F803F80u};
  bf16x8 A[4], B[4];
#pragma unroll
  for (int m = 0; m < 4; m++) {
    A[m] = lds_b128(la + StatTile<T>::i * 1024 + m * 10240);
    B[m] = StatTile<T>::isz ? __builtin_bit_cast(bf16x8, o1) : lds_b128(la + (5 + StatTile<T>::j) * 1024 + m * 10240);
  }
  f32x4 d4 = {0.f, 0.f, 0.f, 0.f};
#pragma unroll
  for (int m = 0; m < 4; m++) d4 = mfma16(A[m], B[m], d4);
  return d4;
}
template <int T> __device__ __forceinline__ void stat_store(f32x4 d4, int lane, __amdgpu_buffer_rsrc_t rw, unsigned pbase) {
  const int fr = lane & 15, g4 = lane >> 4;
  const int Dg = 16 * StatTile<T>::i + 4 * g4, h = (Dg * 205) >> 12, dd = Dg - 20 * h;
  const int ee = StatTile<T>::isz ? 20 : 16 * StatTile<T>::j + fr - 20 * h;
  if (StatTile<T>::isz ? fr == 0 : (ee >= 0 && ee < 20))
    __builtin_amdgcn_raw_buffer_store_b64((u32x2){pk2(d4[0], d4[1]), pk2(d4[2], d4[3])}, rw, pbase + (h * 420 + ee * 20 + dd) * 2, 0, SC1);
}
template <int W> __device__ __forceinline__ void stat_tiles(unsigned lb, int lane, __amdgpu_buffer_rsrc_t rw, unsigned pbase) {
  const unsigned la = lb + lane * 16;
  const f32x4 a = stat_mma<W>(la), b = stat_mma<W + 8>(la);
  if constexpr (W < 2) { const f32x4 c = stat_mma<W + 16>(la); stat_store<W + 16>(c, lane, rw, pbase); }
  stat_store<W>(a, lane, rw, pbase);
  stat_store<W + 8>(b, lane, rw, pbase);
}

__device__ __forceinline__ void conv_load_h(u32x4 (&cur)[4], u32x4 (&prv)[4], const u16* __restrict__ hrow, int lane) {
#pragma unroll
  for (int it = 0; it < 4; it++) {
    const int i = it * 64 + lane;
    cur[it] = *(const u32x4*)(hrow + i * 8); prv[it] = *(const u32x4*)(hrow + (i > 0 ? i * 8 - 8 : 0));
  }
}
__device__ __forceinline__ void conv_prep_h(const u32x4 (&cur)[4], const u32x4 (&prv_)[4], unsigned cb, int lane) {
#pragma unroll
  for (int it = 0; it < 4; it++) {
    u32x4 prv = prv_[it];
    if (it == 0 && lane == 0) prv = (u32x4){0, 0, 0, 0};
    const unsigned W_[8] = {prv.x, prv.y, prv.z, prv.w, cur[it].x, cur[it].y, cur[it].z, cur[it].w};
    const int c = 255 - (it * 64 + lane);
#pragma unroll
    for (int e = 0; e < 4; e++) {
      u32x4 ov;
#pragma unroll
      for (int q = 0; q < 4; q++) {
        const int i0 = 15 - e - 2 * q, i1 = i0 - 1;
        const unsigned sel = ((i0 & 1) ? 0x0706u : 0x0504u) | (((i1 & 1) ? 0x0302u : 0x0100u) << 16);
        ov[q] = __builtin_amdgcn_perm(W_[i0 >> 1], W_[i1 >> 1], sel);
      }
      *(LAS u32x4*)(size_t)(cb + e * CPS + c * 16) = ov;
    }
  }
  if (lane < 8) *(LAS u32x4*)(size_t)(cb + (lane >> 1) * CPS + (256 + (lane & 1)) * 16) = (u32x4){0, 0, 0, 0};
  if (lane < 32) *(LAS u32x4*)(size_t)(cb + UOFF + lane * 16) = (u32x4){0, 0, 0, 0};
}
template <class F>
__device__ __forceinline__ void conv_load_u(__amdgpu_buffer_rsrc_t rw, int d, unsigned cb, int lane, F&& between) {
  u32x4 uv[4];
#pragma unroll
  for (int it = 0; it < 4; it++) uv[it] = __builtin_amdgcn_raw_buffer_load_b128(rw, OFF_UT + d * 4096 + (it * 64 + lane) * 16, 0, SC1);
  SBAR();
  between();
  SBAR();
#pragma unroll
  for (int it = 0; it < 4; it++) *(LAS u32x4*)(size_t)(cb + UOFF + 512 + (it * 64 + lane) * 16) = uv[it];
}
struct ConvFrags { u32x2 al[8], ah[8]; u32x4 B[8]; };
__device__ __forceinline__ void conv_read(ConvFrags& f, unsigned aa, unsigned ba) {
  asm volatile(
      "ds_read_b128 %16, %25\n\tds_read_b64 %7, %24 offset:3584\n\tds_read_b64 %15, %24 offset:3592\n\t"
      "ds_read_b128 %17, %25 offset:512\n\tds_read_b128 %18, %25 offset:1024\n\tds_read_b128 %19, %25 offset:1536\n\t"
      "ds_read_b128 %20, %25 offset:2048\n\tds_read_b128 %21, %25 offset:2560\n\tds_read_b128 %22, %25 offset:3072\n\t"
      "ds_read_b128 %23, %25 offset:3584\n\t"
      "ds_read_b64 %6, %24 offset:3072\n\tds_read_b64 %14, %24 offset:3080\n\t"
      "ds_read_b64 %5, %24 offset:2560\n\tds_read_b64 %13, %24 offset:2568\n\t"
      "ds_read_b64 %4, %24 offset:2048\n\tds_read_b64 %12, %24 offset:2056\n\t"
      "ds_read_b64 %3, %24 offset:1536\n\tds_read_b64 %11, %24 offset:1544\n\t"
      "ds_read_b64 %2, %24 offset:1024\n\tds_read_b64 %10, %24 offset:1032\n\t"
      "ds_read_b64 %1, %24 offset:512\n\tds_read_b64 %9, %24 offset:520\n\t"
      "ds_read_b64 %0, %24\n\tds_read_b64 %8, %24 offset:8"
      : "=&v"(f.al[0]), "=&v"(f.al[1]), "=&v"(f.al[2]), "=&v"(f.al[3]), "=&v"(f.al[4]), "=&v"(f.al[5]), "=&v"(f.al[6]), "=&v"(f.al[7]),
        "=&v"(f.ah[0]), "=&v"(f.ah[1]), "=&v"(f.ah[2]), "=&v"(f.ah[3]), "=&v"(f.ah[4]), "=&v"(f.ah[5]), "=&v"(f.ah[6]), "=&v"(f.ah[7]),
        "=&v"(f.B[0]), "=&v"(f.B[1]), "=&v"(f.B[2]), "=&v"(f.B[3]), "=&v"(f.B[4]), "=&v"(f.B[5]), "=&v"(f.B[6]), "=&v"(f.B[7])
      : "v"(aa), "v"(ba) : "memory");
}
__device__ __forceinline__ void conv_wait(ConvFrags& f) {
  asm volatile("s_waitcnt lgkmcnt(0)"
      : "+v"(f.al[0]), "+v"(f.al[1]), "+v"(f.al[2]), "+v"(f.al[3]), "+v"(f.al[4]), "+v"(f.al[5]), "+v"(f.al[6]), "+v"(f.al[7]),
        "+v"(f.ah[0]), "+v"(f.ah[1]), "+v"(f.ah[2]), "+v"(f.ah[3]), "+v"(f.ah[4]), "+v"(f.ah[5]), "+v"(f.ah[6]), "+v"(f.ah[7]),
        "+v"(f.B[0]), "+v"(f.B[1]), "+v"(f.B[2]), "+v"(f.B[3]), "+v"(f.B[4]), "+v"(f.B[5]), "+v"(f.B[6]), "+v"(f.B[7]) :: "memory");
}
template <int K> __device__ __forceinline__ void conv_read1(ConvFrags& f, unsigned aa, unsigned ba) {
  if constexpr (K < 8) asm volatile("ds_read_b128 %0, %1 offset:%2" : "=&v"(f.B[K]) : "v"(ba), "n"(512 * K) : "memory");
  else if constexpr (K < 16) asm volatile("ds_read_b64 %0, %1 offset:%2" : "=&v"(f.al[K - 8]) : "v"(aa), "n"(512 * (K - 8)) : "memory");
  else if constexpr (K < 24) asm volatile("ds_read_b64 %0, %1 offset:%2" : "=&v"(f.ah[K - 16]) : "v"(aa), "n"(512 * (K - 16) + 8) : "memory");
}
template <int K = 0> __device__ __forceinline__ void conv_step(f32x4 (&acc)[8], const ConvFrags& f, ConvFrags& nx, unsigned aa, unsigned ba) {
  if constexpr (K < 36) {
    constexpr int a = K < 8 ? 0 : K < 15 ? 1 : K < 21 ? 2 : K < 26 ? 3 : K < 30 ? 4 : K < 33 ? 5 : K < 35 ? 6 : 7;
    constexpr int i = K - (a == 0 ? 0 : a == 1 ? 8 : a == 2 ? 15 : a == 3 ? 21 : a == 4 ? 26 : a == 5 ? 30 : a == 6 ? 33 : 35);
    acc[a + i] = mfma16(mk8(f.al[7 - a], f.ah[7 - a]), __builtin_bit_cast(bf16x8, f.B[i]), acc[a + i]);
    conv_read1<K>(nx, aa, ba);
    SBAR();
    conv_step<K + 1>(acc, f, nx, aa, ba);
  }
}
__device__ __forceinline__ void conv_main(f32x4 (&acc)[8], unsigned cb, int lane, int c0, int c1) {
  const int fr = lane & 15, g4 = lane >> 4;
  const unsigned abase = cb + (3 - (fr & 3)) * CPS + 2 * (2044 - 16 * 112 - 16 * (g4 >> 1) + 8 * (g4 & 1) - (fr & ~3));
  const unsigned bbase = cb + UOFF + 512 + 2 * (16 * fr - 16 * (g4 >> 1) + 8 * (g4 & 1));
#pragma unroll
  for (int c = 0; c < 8; c++) acc[c] = (f32x4){0.f, 0.f, 0.f, 0.f};
  ConvFrags f0, f1;
  asm volatile("s_waitcnt lgkmcnt(0)" ::: "memory");
  conv_read(f0, abase - 64 * c0, bbase - 64 * c0);
  for (int cc = c0; cc < c1; cc += 2) {
    conv_wait(f0);
    SBAR();
    conv_step(acc, f0, f1, abase - 64 * (cc + 1), bbase - 64 * (cc + 1));
    conv_wait(f1);
    SBAR();
    const int cn = cc + 2 < c1 ? cc + 2 : cc + 1;
    conv_step(acc, f1, f0, abase - 64 * cn, bbase - 64 * cn);
  }
  conv_wait(f0);
}
__device__ __forceinline__ void conv_store(f32x4 y, unsigned cb, int c, int lane, float Dd, __amdgpu_buffer_rsrc_t rw, int d) {
  const int fr = lane & 15, g4 = lane >> 4;
  const u32x2 uu = lds_b64(cb + UOFF + 512 + 2 * (256 * c + 16 * fr + 4 * g4));
  const float r0 = fmaf(bflo(uu.x), Dd, y[0]), r1 = fmaf(bfhi(uu.x), Dd, y[1]);
  const float r2 = fmaf(bflo(uu.y), Dd, y[2]), r3 = fmaf(bfhi(uu.y), Dd, y[3]);
  __builtin_amdgcn_raw_buffer_store_b64((u32x2){pk2(r0, r1), pk2(r2, r3)}, rw, OFF_YT + d * 4096 + (256 * c + 16 * fr + 4 * g4) * 2, 0, SC1);
}

__global__ __launch_bounds__(512, 2) void fused(const u16* __restrict__ x, const u16* __restrict__ wqkv, const u16* __restrict__ wo,
                                                const u16* __restrict__ Bk, const u16* __restrict__ Bv, const u16* __restrict__ shk,
                                                const u16* __restrict__ hk, const u16* __restrict__ Dv, const u16* __restrict__ gw,
                                                u16* __restrict__ out, char* __restrict__ ws) {
  extern __shared__ __attribute__((aligned(16))) char sm[];
  const int tid = threadIdx.x, lane = tid & 63;
  const int w = __builtin_amdgcn_readfirstlane(tid >> 6);
  const int b = blockIdx.x, xcd = b & 7, r = b >> 3;
  const int fr = lane & 15, g4 = lane >> 4;
  const unsigned lb = (unsigned)(size_t)(LAS char*)sm;
  const __amdgpu_buffer_rsrc_t rw = __builtin_amdgcn_make_buffer_rsrc(ws, 0, WS_USED, 0x00020000);
  if (r < 24 && (w >> 1) == 2) {
    const bool kv_ = r < 16;
    gemm_first_x(w - 4, lane, lb, x, (xcd & 3) * 4 + (r & 3), kv_ ? (xcd >> 2) * 4 + (r >> 2) : (xcd >> 2) * 2 + ((r - 16) >> 2), kv_);
  }
  TS_DECL TS(0);
  unsigned e0 = 0;
#define PUT_TAG() do { if (w == 7 && lane == 0) { *(LAS unsigned*)(size_t)(lb + L_TAG) = e0 + 1u; *(LAS u32x4*)(size_t)(lb + L_CNT) = (u32x4){0u, 0u, 0u, 0u}; } } while (0)
#define TAG (*(const LAS unsigned*)(size_t)(lb + L_TAG))
  if (r < 24) {
    const bool isKV = r < 16;
    const int rt = (xcd & 3) * 4 + (r & 3);
    const int cg = isKV ? (xcd >> 2) * 4 + (r >> 2) : (xcd >> 2) * 2 + ((r - 16) >> 2);
    f32x4 acc[4][5];
    const int rp = (w >> 1) & 1, hf = w & 1;
    if (w < 4) {
      const unsigned xa0 = lb + (rp * 64 + fr) * 128 + ((g4 ^ ((fr >> 1) & 7)) << 4);
      const unsigned wl = lb + XS + (8 * g4 + (fr >> 2)) * 320 + ((lane & 3) >> 1) * 16 + (lane & 1) * 8 + hf * 160;
      const unsigned sgn = (g4 & 1) * 32;
      const unsigned wa0 = hf ? wl - sgn : wl + sgn, wa1 = hf ? wl + sgn : wl - sgn;
#pragma unroll
      for (int i = 0; i < 4; i++)
#pragma unroll
        for (int t = 0; t < 5; t++) acc[i][t] = (f32x4){0.f, 0.f, 0.f, 0.f};
      if (isKV) gemm_compute<true>(acc, xa0, wa0, wa1 TSARG); else gemm_compute<false>(acc, xa0, wa0, wa1 TSARG);
    } else if (w < 6) {
      gemm_loader(w - 4, lane, lb, x, wqkv, rt, cg, isKV);
    } else {
      const int w6 = w - 6;
      const char* pfa;
      {
        const int q = rt & 3, u = isKV ? (cg & 3) : 4 + (cg & 1);
        if (lane < 32) {
          const int row = 16 * q + 8 * w6 + (lane >> 2);
          const unsigned col = isKV ? ((lane & 2) ? 2560u : 1280u) + 160u * cg : 320u * cg;
          const unsigned ln = isKV ? (lane & 1) : ((lane & 3) == 3 ? 2 : (lane & 3));
          pfa = (const char*)wqkv + row * 3840 + ((col >> 7) + ln) * 128;
        } else {
          int row = u * 22 + w6 * 11 + (lane - 32); row = (lane < 43 && row < 128) ? row : u * 22;
          pfa = (const char*)x + (rt * 128 + row) * 1280;
        }
      }
      const int pfs0 = gemm_s0(rt, cg, isKV);
      auto prefetch = [&](int j) {
      };
      SBAR();
      if (w == 7) e0 = flag_ld(ws, F_E);
      if (isKV) {
        const int l16 = lane & 15, row0 = rt * 128 + cg * 16 + w6 * 8;
        const u32x4 g0 = *(const u32x4*)(gw + lane * 8), g1 = *(const u32x4*)(gw + 512 + l16 * 8);
        u32x4 gx0[8], gx1[8];
#pragma unroll
        for (int i = 0; i < 8; i++) {
          const u16* xr = x + (size_t)(row0 + i) * D;
          gx0[i] = *(const u32x4*)(xr + lane * 8); gx1[i] = *(const u32x4*)(xr + 512 + l16 * 8);
        }
        SBAR();
        BAR(); prefetch(4); prefetch(5); BAR(); prefetch(6); prefetch(7); BAR(); prefetch(8); prefetch(9);
        float a[8];
        const float m1 = lane < 16 ? 1.f : 0.f;
#pragma unroll
        for (int i = 0; i < 8; i++) {
          float a0 = 0.f, a1 = 0.f;
#pragma unroll
          for (int j = 0; j < 4; j++) {
            a0 = fmaf(bflo(gx0[i][j]), bflo(g0[j]), a0); a0 = fmaf(bfhi(gx0[i][j]), bfhi(g0[j]), a0);
            a1 = fmaf(bflo(gx1[i][j]), bflo(g1[j]), a1); a1 = fmaf(bfhi(gx1[i][j]), bfhi(g1[j]), a1);
          }
          a[i] = fmaf(a1, m1, a0);
          if (i == 3) { SBAR(); BAR(); SBAR(); }
        }
        SBAR(); BAR(); SBAR();
#pragma unroll
        for (int i = 0; i < 4; i++) { const float lo = a[i], hi = a[i + 4]; const bool up = lane & 4; a[i] = (up ? hi : lo) + __shfl_xor(up ? lo : hi, 4); }
#pragma unroll
        for (int i = 0; i < 2; i++) { const float lo = a[i], hi = a[i + 2]; const bool up = lane & 2; a[i] = (up ? hi : lo) + __shfl_xor(up ? lo : hi, 2); }
        { const float lo = a[0], hi = a[1]; const bool up = lane & 1; a[0] = (up ? hi : lo) + __shfl_xor(up ? lo : hi, 1); }
        a[0] += __shfl_xor(a[0], 8); a[0] += __shfl_xor(a[0], 16); a[0] += __shfl_xor(a[0], 32);
        if (lane < 8) __builtin_amdgcn_raw_buffer_store_b32(__float_as_uint(rbf(1.f / (1.f + __expf(-rbf(a[0]))))), rw, OFF_G + (row0 + lane) * 4, 0, SC1);
        PUT_TAG();
        SBAR();
        BAR(); BAR(); BAR(); BAR(); BAR();
      } else {
        const int hw = (xcd * 8 + (r - 16)) * 2 + w6;
        const unsigned scr = lb + L_T + w6 * 6144;
#pragma unroll
        for (int bt = 0; bt < 2; bt++) {
          const int blk = bt == 0 ? hw : (hw < 32 ? 128 + hw : hw), ct = (blk * 3277) >> 16, ks = blk - 20 * ct;
          u32x4 v[5];
#pragma unroll
          for (int i = 0; i < 5; i++) {
            const int c = i * 64 + lane, row = (c * 6554) >> 16, ch = c - 10 * row;
            v[i] = *(const u32x4*)(wo + (size_t)(32 * ks + row) * D + 80 * ct + 8 * ch);
          }
          SBAR();
          BAR();
          if (bt == 0) { PUT_TAG(); prefetch(4); prefetch(5); }
          BAR();
          if (bt == 0) { prefetch(6); prefetch(7); }
          if (bt == 0 || hw < 32) {
#pragma unroll
            for (int i = 0; i < 5; i++) *(LAS u32x4*)(size_t)(scr + (i * 64 + lane) * 16) = v[i];
            const unsigned ta = scr + (8 * g4 + (fr >> 2)) * 160 + (lane & 3) * 8;
#pragma unroll
            for (int t = 0; t < 5; t++) {
              const u32x2 lo = lds_tr(ta + 32 * t), hi = lds_tr(ta + 32 * t + 640);
              __builtin_amdgcn_raw_buffer_store_b128((u32x4){lo.x, lo.y, hi.x, hi.y}, rw, OFF_WOP + (blk * 5 + t) * 1024 + lane * 16, 0, SC1);
            }
          }
          SBAR();
          BAR();
          if (bt == 0) { prefetch(8); prefetch(9); }
          BAR();
        }
        DRAIN();
        if (lds_last(lb + L_CNT + 8, lane, 2) && lane == 0) flag_st(ws, F_W + (xcd * 8 + (r - 16)) * 128, TAG);
        BAR(); BAR();
      }
    }
    TS(1);
    BAR();
    TS(11);
    const unsigned tag = TAG;
    if (isKV) {
      if (w < 4) {
#pragma unroll
        for (int t = 0; t < 5; t++) {
          const bool isk = hf ? t < 2 : t < 3;
          const int slot = hf ? (t < 2 ? 3 + t : 5 + t) : (t < 3 ? t : 2 + t);
#pragma unroll
          for (int m = 0; m < 2; m++) {
            u32x4 f;
            if (isk) f = (u32x4){pk2(phi(acc[2 * m][t][0]), phi(acc[2 * m][t][1])), pk2(phi(acc[2 * m][t][2]), phi(acc[2 * m][t][3])),
                                 pk2(phi(acc[2 * m + 1][t][0]), phi(acc[2 * m + 1][t][1])), pk2(phi(acc[2 * m + 1][t][2]), phi(acc[2 * m + 1][t][3]))};
            else f = (u32x4){pk2(acc[2 * m][t][0], acc[2 * m][t][1]), pk2(acc[2 * m][t][2], acc[2 * m][t][3]),
                             pk2(acc[2 * m + 1][t][0], acc[2 * m + 1][t][1]), pk2(acc[2 * m + 1][t][2], acc[2 * m + 1][t][3])};
            *(LAS u32x4*)(size_t)(lb + ((rp * 2 + m) * 10 + slot) * 1024 + lane * 16) = f;
          }
        }
      } else {
        for (int i = tid - 256; i < 6048 / 16; i += 256) *(LAS u32x4*)(size_t)(lb + L_T + i * 16) = (u32x4){0, 0, 0, 0};
        if (w == 7) poll(ws, F_W + lane * 128, true, tag);
      }
      TS(12);
      BAR();
      TS(2);
      switch (w) {
        case 0: stat_tiles<0>(lb, lane, rw, OFF_P + (rt * 8 + cg) * 3360); break;
        case 1: stat_tiles<1>(lb, lane, rw, OFF_P + (rt * 8 + cg) * 3360); break;
        case 2: stat_tiles<2>(lb, lane, rw, OFF_P + (rt * 8 + cg) * 3360); break;
        case 3: stat_tiles<3>(lb, lane, rw, OFF_P + (rt * 8 + cg) * 3360); break;
        case 4: stat_tiles<4>(lb, lane, rw, OFF_P + (rt * 8 + cg) * 3360); break;
        case 5: stat_tiles<5>(lb, lane, rw, OFF_P + (rt * 8 + cg) * 3360); break;
        case 6: stat_tiles<6>(lb, lane, rw, OFF_P + (rt * 8 + cg) * 3360); break;
        default: stat_tiles<7>(lb, lane, rw, OFF_P + (rt * 8 + cg) * 3360); break;
      }
      TS(14);
      DRAIN();
      TS(15);
      BAR();
      TS(3);
      if (w == 0) {
        if (lane == 0) flag_st(ws, F_P + (rt * 8 + cg) * 128, tag);
        poll(ws, lane < 16 ? F_P + (lane * 8 + cg) * 128 : F_PQ + (rt * 4 + (cg >> 1)) * 128, lane <= 16, tag);
      } else if (w < 7) {
        const i32x4 sws = make_srd(ws, WS_USED);
        const unsigned vwv = OFF_WOP + ((xcd >> 2) * 4 + (r >> 3)) * 102400 + lane * 16;
        for (int i = w - 1; i < 100; i += 6) {
          int pc = 12 * (r & 7) + i; pc = __builtin_amdgcn_readfirstlane(pc >= 100 ? pc - 100 : pc);
          glds16_sc1(lb + pc * 1024, vwv, sws, pc * 1024);
        }
      }
      constexpr int PJ[9] = {0, 1, 1, 2, 2, 3, 3, 4, 4}, PK[9] = {0, 0, 1, 0, 1, 1, 2, 1, 2};
      unsigned an[9], ad[9];
#pragma unroll
      for (int p = 0; p < 9; p++) {
        const int E = 16 * PJ[p] + fr, h = (E * 205) >> 12, e = E - 20 * h;
        int o_ = 32 * PK[p] + 8 * g4 - 20 * h; o_ = o_ < -8 ? -8 : o_ > 20 ? 20 : o_;
        an[p] = lb + L_T + (h * 21 + e) * 72 + (8 + o_) * 2; ad[p] = lb + L_T + (h * 21 + 20) * 72 + (8 + o_) * 2;
      }
      BAR();
      TS(4);
      bf16x8 Bq[3];
      {
        const unsigned prow = OFF_PQ + ((rt * 8 + w) * 8 + cg) * 3072 + lane * 16;
#pragma unroll
        for (int ks = 0; ks < 3; ks++) Bq[ks] = __builtin_bit_cast(bf16x8, __builtin_amdgcn_raw_buffer_load_b128(rw, prow + 1024 * ks, 0, SC1));
      }
      if (tid < 210) {
        const unsigned pb = OFF_P + cg * 3360 + tid * 16;
        u32x4 v[16];
#pragma unroll
        for (int k = 0; k < 16; k++) v[k] = __builtin_amdgcn_raw_buffer_load_b128(rw, pb, (k ^ rt) * (8 * 3360), SC1);
        unsigned o4[4];
#pragma unroll
        for (int c = 0; c < 4; c++) {
          float lo[16], hi[16];
#pragma unroll
          for (int k = 0; k < 16; k++) { lo[k] = bflo(v[k][c]); hi[k] = bfhi(v[k][c]); }
#pragma unroll
          for (int st = 1; st < 16; st <<= 1)
#pragma unroll
            for (int k = 0; k < 16; k += 2 * st) { lo[k] += lo[k + st]; hi[k] += hi[k + st]; }
          o4[c] = pk2(lo[0], hi[0]);
        }
        TS(16);
#pragma unroll
        for (int q = 0; q < 2; q++) {
          const int i4 = 2 * tid + q, row = (i4 * 13108) >> 16, d = 4 * (i4 - row * 5);
          *(LAS u32x2*)(size_t)(lb + L_T + row * 72 + (8 + d) * 2) = (u32x2){o4[2 * q], o4[2 * q + 1]};
        }
      }
      BAR();
      TS(5);
      {
        if (g4 >= 2) Bq[2] = __builtin_bit_cast(bf16x8, (u32x4){0, 0, 0, 0});
        f32x4 nu[5], de[5];
#pragma unroll
        for (int j = 0; j < 5; j++) { nu[j] = (f32x4){0.f, 0.f, 0.f, 0.f}; de[j] = nu[j]; }
        bf16x8 An[9], Ad[9];
#pragma unroll
        for (int p = 0; p < 9; p++) {
          unsigned an8 = an[p] + 8, ad8 = ad[p] + 8;
          asm volatile("" : "+v"(an8), "+v"(ad8));
          An[p] = mk8(lds_b64(an[p]), lds_b64(an8)); Ad[p] = mk8(lds_b64(ad[p]), lds_b64(ad8));
        }
#pragma unroll
        for (int p = 0; p < 9; p++) { de[PJ[p]] = mfma16(Ad[p], Bq[PK[p]], de[PJ[p]]); nu[PJ[p]] = mfma16(An[p], Bq[PK[p]], nu[PJ[p]]); }
        u32x2 ov[5];
#pragma unroll
        for (int j = 0; j < 5; j++) {
          const float rd = __builtin_amdgcn_rcpf(de[j][0] + 1e-6f);
          ov[j] = (u32x2){pk2(nu[j][0] * rd, nu[j][1] * rd), pk2(nu[j][2] * rd, nu[j][3] * rd)};
        }
        store_frags(ov, rw, OFF_O + (rt * 8 + w) * 20480, 80 * cg, fr, g4);
      }
      TS(17);
      DRAIN();
      BAR();
      if (tid == 0) flag_st(ws, F_O + (rt * 8 + cg) * 128, tag);
      TS(6);
    } else {
      const int vb = xcd * 8 + (r - 16);
      const int d = 80 * (vb >> 3) + 48 + 4 * (vb & 7) + (w & 3);
      const unsigned cb = lb + (w & 3) * CHS;
      if (w < 4) {
#pragma unroll
        for (int i = 0; i < 4; i++) {
          u32x2 pv[5];
#pragma unroll
          for (int t = 0; t < 5; t++) pv[t] = (u32x2){pk2(phi(acc[i][t][0]), phi(acc[i][t][1])), pk2(phi(acc[i][t][2]), phi(acc[i][t][3]))};
          store_frags(pv, rw, OFF_PQ + ((rt * 8 + rp * 4 + i) * 8 + cg * 2 + hf) * 3072, 0, fr, g4);
        }
      } else {
        u32x4 hc[4], hp[4];
        conv_load_h(hc, hp, hk + (size_t)d * S, lane);
        poll(ws, F_U + lane * 128, true, tag);
        conv_load_u(rw, d, cb, lane, [&]() { conv_prep_h(hc, hp, cb, lane); });
      }
      TS(12);
      DRAIN();
      BAR();
      TS(2);
      if (tid == 0) flag_st(ws, F_PQ + (rt * 4 + cg) * 128, tag);
      if (w < 4) {
        const float Dd = bflo(Dv[d]);
        TS(3);
        f32x4 y[8];
        conv_main(y, cb, lane, 0, 8);
        TS(4);
#pragma unroll
        for (int c = 0; c < 8; c++) conv_store(y[c], cb, c, lane, Dd, rw, d);
        DRAIN();
      } else if (w == 7) {
        poll(ws, F_W + lane * 128, true, tag);
      }
      BAR();
      if (tid == 0) flag_st(ws, F_Y + (64 + vb) * 128, tag);
      TS(5); TS(6);
    }
  } else {
    const int pw = xcd * 8 + (r - 24);
    const int c6 = w < 4 ? w : 4 + ((w - 4) >> 1);
    const int d = 80 * (pw >> 3) + 6 * (pw & 7) + c6;
    const unsigned cb = lb + c6 * CHS;
    const bool prep = w < 4 || !(w & 1);
    u32x4 hc[4], hp[4];
    {
      const int rbase = xcd * 256 + ((r - 24) >> 1) * 64, cbase = ((r - 24) & 1) * 320;
      u32x4 kr[2][7], vr[2][4], sp[2][4];
#pragma unroll
      for (int q = 0; q < 2; q++) {
        const int it = w + 8 * q;
        if (q == 0 || w < 2) {
          const int ch = cbase + (it >> 1) * 64 + (lane & 7) * 8;
          const int s0 = rbase + (it & 1) * 32 + (lane >> 3) * 4;
#pragma unroll
          for (int j = 0; j < 7; j++) { int s = s0 - 3 + j; kr[q][j] = *(const u32x4*)(Bk + (size_t)(s < 0 ? 0 : s) * D + ch); }
#pragma unroll
          for (int j = 0; j < 4; j++) vr[q][j] = *(const u32x4*)(Bv + (size_t)(s0 + j) * D + ch);
#pragma unroll
          for (int j = 0; j < 4; j++) sp[q][j] = ((const u32x4*)(shk + ch * 4))[j];
        }
      }
      if (prep) conv_load_h(hc, hp, hk + (size_t)d * S, lane);
      if (w == 7) e0 = flag_ld(ws, F_E);
      SBAR();
#pragma unroll
      for (int q = 0; q < 2; q++) {
        const int it = w + 8 * q;
        if (q == 0 || w < 2) {
          const int s0 = rbase + (it & 1) * 32 + (lane >> 3) * 4;
          if (s0 == 0) { kr[q][0] = (u32x4){0, 0, 0, 0}; kr[q][1] = kr[q][0]; kr[q][2] = kr[q][0]; }
          const int rg = (it & 1) * 8 + (lane >> 3);
          const unsigned la = lb + L_UIMG + ((it >> 1) * 64 + (lane & 7) * 8) * 128 + (((rg >> 1) ^ (lane & 7)) << 4) + (rg & 1) * 8;
#pragma unroll
          for (int c = 0; c < 8; c++) {
            const unsigned s01 = sp[q][c >> 1][(c & 1) * 2], s23 = sp[q][c >> 1][(c & 1) * 2 + 1];
            const float w0 = bflo(s01), w1 = bfhi(s01), w2 = bflo(s23), w3 = bfhi(s23);
            float kk[7], u[4];
#pragma unroll
            for (int j = 0; j < 7; j++) { unsigned dd = kr[q][j][c >> 1]; kk[j] = (c & 1) ? bfhi(dd) : bflo(dd); }
#pragma unroll
            for (int j = 0; j < 4; j++) {
              unsigned dd = vr[q][j][c >> 1]; float v = (c & 1) ? bfhi(dd) : bflo(dd);
              float ks = kk[j] * w3; ks = fmaf(kk[j + 1], w2, ks); ks = fmaf(kk[j + 2], w1, ks); ks = fmaf(kk[j + 3], w0, ks);
              u[j] = ks * v;
            }
            *(LAS u32x2*)(size_t)(la + c * 128) = (u32x2){pk2(u[0], u[1]), pk2(u[2], u[3])};
          }
        }
      }
      PUT_TAG();
      BAR();
#pragma unroll
      for (int i = 0; i < 5; i++) {
        const int chl = (w * 5 + i) * 8 + (lane >> 3), k = lane & 7;
        const u32x4 v = *(const LAS u32x4*)(size_t)(lb + L_UIMG + chl * 128 + ((k ^ ((chl >> 3) & 7)) << 4));
        __builtin_amdgcn_raw_buffer_store_b128(v, rw, OFF_UT + (cbase + chl) * 4096 + rbase * 2 + k * 16, 0, SC1);
      }
      TS(1);
    }
    if (w < 5 && prep) conv_prep_h(hc, hp, cb, lane);
    DRAIN();
    BAR();
    TS(2);
    const unsigned tag = TAG;
    if (tid == 0) flag_st(ws, F_U + pw * 128, tag);
    const float Dd = bflo(Dv[d]);
    if (prep) {
      if (w == 6) conv_prep_h(hc, hp, cb, lane);
      poll(ws, F_U + lane * 128, true, tag);
      conv_load_u(rw, d, cb, lane, [&]() {});
    }
    BAR();
    TS(3);
    f32x4 y[8];
    conv_main(y, cb, lane, w < 4 ? 0 : (w & 1) * 4, w < 4 ? 8 : (w & 1) * 4 + 4);
    TS(4);
    if (w < 4) {
#pragma unroll
      for (int c = 0; c < 8; c++) conv_store(y[c], cb, c, lane, Dd, rw, d);
    }
    BAR();
    if (w >= 4) {
#pragma unroll
      for (int c = 0; c < 4; c++) { const f32x4 lo = y[c], hi = y[4 + c]; *(LAS f32x4*)(size_t)(cb + ((w & 1) * 4 + c) * 1024 + lane * 16) = (w & 1) ? lo : hi; }
    }
    BAR();
    if (w >= 4) {
#pragma unroll
      for (int c = 0; c < 4; c++) {
        const f32x4 o_ = *(const LAS f32x4*)(size_t)(cb + (((w & 1) ^ 1) * 4 + c) * 1024 + lane * 16);
        const f32x4 lo = y[c], hi = y[4 + c];
        const f32x4 mine = (w & 1) ? hi : lo;
        conv_store((w & 1) ? o_ + mine : mine + o_, cb, (w & 1) * 4 + c, lane, Dd, rw, d);
      }
    }
    if (w == 0) poll(ws, F_W + lane * 128, true, tag);
    DRAIN();
    BAR();
    if (tid == 0) flag_st(ws, F_Y + pw * 128, tag);
    TS(5); TS(6);
  }

  {
    const unsigned tag = TAG;
    const int rt = (xcd & 3) * 8 + (r & 7), ct = (xcd >> 2) * 4 + (r >> 3);
    const int sw_ = w & 3, kk = w >> 2;
    const i32x4 sws = make_srd(ws, WS_USED);
    TS(18);
    const int srow = rt * 64 + sw_ * 16 + fr;
    u32x2 xv[3];
#pragma unroll
    for (int t = 0; t < 3; t++) { const int tt = kk ? (t < 2 ? 3 + t : 4) : t; xv[t] = *(const u32x2*)(x + (size_t)srow * D + ct * 80 + 16 * tt + 4 * g4); }
    SBAR();
    const int rot = ((ct & 3) * 5) >> 1;
    const unsigned wa0 = lb + kk * 5120 + lane * 16;
    bf16x8 wf0[5][5];
    auto read_wf0 = [&]() {
#pragma unroll
      for (int s = 0; s < 5; s++) {
        int q = s + rot; q = q >= 10 ? q - 10 : q;
#pragma unroll
        for (int t = 0; t < 5; t++) wf0[s][t] = lds_b128(wa0 + q * 10240 + t * 1024);
      }
    };
    if (r < 16) read_wf0();
    SBAR();
    if (w == 0) {
      poll(ws, F_O + ((rt >> 1) * 8 + (lane & 7)) * 128, lane < 8, tag);
    } else if (w == 7) {
      poll(ws, F_Y + ((lane & 8 ? 64 : 0) + ct * 8 + (lane & 7)) * 128, lane < 16, tag);
    } else {
      if (r >= 16) {
        const unsigned vwv = OFF_WOP + ct * 102400 + lane * 16;
        for (int i = w - 1; i < 100; i += 6) { int pc = 12 * (r & 7) + i; pc = __builtin_amdgcn_readfirstlane(pc >= 100 ? pc - 100 : pc); glds16_sc1(lb + pc * 1024, vwv, sws, pc * 1024); }
      }
      if (w == 6 && b == 255) {
        for (int spins = 0; spins < (1 << 17); spins++) {
          const unsigned a0 = flag_ld(ws, F_U + lane * 128), a1 = flag_ld(ws, F_U + (64 + lane) * 128);
          const unsigned a2 = flag_ld(ws, F_U + (128 + lane) * 128), a3 = flag_ld(ws, F_U + (192 + lane) * 128);
          if (__all(a0 == tag && a1 == tag && a2 == tag && a3 == tag)) break;
          __builtin_amdgcn_s_sleep(2);
        }
        if (lane == 0) flag_st(ws, F_E, tag);
      }
      DRAIN();
    }
    BAR();
    TS(7);
    asm volatile("" :: "v"(xv[0]), "v"(xv[1]), "v"(xv[2]));
    bf16x8 of[10];
    {
      const unsigned vo = OFF_O + (rt * 4 + sw_) * 20480 + kk * 1024 + lane * 16;
#pragma unroll
      for (int s = 0; s < 10; s++) { int q = s + rot; q = __builtin_amdgcn_readfirstlane(q >= 10 ? q - 10 : q); bload16_sc1(of[s], vo, sws, q * 2048); }
    }
    if (w == 7) {
      for (int pc = 0; pc < 10; pc++)
        glds16_sc1(lb + L_Y + pc * 1024, OFF_YT + (ct * 80 + pc * 8 + (lane >> 3)) * 4096 + rt * 128 + (lane & 7) * 16, sws, 0);
    }
    float g;
    asm volatile("buffer_load_dword %0, %1, %2, 0 offen sc1" : "=v"(g) : "v"(OFF_G + srow * 4), "s"(sws) : "memory");
    TS(8);
    if (r >= 16) read_wf0();
    f32x4 acc[5];
#pragma unroll
    for (int t = 0; t < 5; t++) acc[t] = (f32x4){0.f, 0.f, 0.f, 0.f};
    {
      bf16x8 wf1[5][5];
#pragma unroll
      for (int s = 0; s < 5; s++) {
        int q = 5 + s + rot; q = q >= 10 ? q - 10 : q;
#pragma unroll
        for (int t = 0; t < 5; t++) wf1[s][t] = lds_b128(wa0 + q * 10240 + t * 1024);
      }
      SBAR();
      if (w == 7) asm volatile("s_waitcnt vmcnt(16)" ::: "memory"); else asm volatile("s_waitcnt vmcnt(6)" ::: "memory");
      asm volatile("" : "+v"(of[0]), "+v"(of[1]), "+v"(of[2]), "+v"(of[3]), "+v"(of[4]) :: "memory");
      SBAR();
#pragma unroll
      for (int s = 0; s < 5; s++)
#pragma unroll
        for (int t = 0; t < 5; t++) acc[t] = mfma16(wf0[s][t], of[s], acc[t]);
      SBAR();
      if (w == 7) asm volatile("s_waitcnt vmcnt(11)" ::: "memory"); else asm volatile("s_waitcnt vmcnt(1)" ::: "memory");
      asm volatile("" : "+v"(of[5]), "+v"(of[6]), "+v"(of[7]), "+v"(of[8]), "+v"(of[9]) :: "memory");
      SBAR();
#pragma unroll
      for (int s = 0; s < 5; s++)
#pragma unroll
        for (int t = 0; t < 5; t++) acc[t] = mfma16(wf1[s][t], of[5 + s], acc[t]);
      SBAR();
    }
    TS(9);
    const unsigned ra = lb + L_RED + (sw_ * 5) * 1024 + lane * 16;
    if (kk == 0) { *(LAS f32x4*)(size_t)(ra + 3 * 1024) = acc[3]; *(LAS f32x4*)(size_t)(ra + 4 * 1024) = acc[4]; }
    else { *(LAS f32x4*)(size_t)(ra) = acc[0]; *(LAS f32x4*)(size_t)(ra + 1024) = acc[1]; *(LAS f32x4*)(size_t)(ra + 2048) = acc[2]; }
    asm volatile("s_waitcnt vmcnt(0)" : "+v"(g) :: "memory");
    BAR();
    TS(20);
    const float omg = 1.f - g;
    const int nt = kk ? 2 : 3;
#pragma unroll
    for (int t = 0; t < 3; t++) {
      if (t < nt) {
        const int tt = kk ? 3 + t : t;
        const f32x4 a0 = acc[t], a1 = acc[3 + (t < 2 ? t : 1)];
        f32x4 a = *(const LAS f32x4*)(size_t)(ra + tt * 1024) + (kk ? a1 : a0);
        u32x2 yv = lds_tr(lb + L_Y + (16 * tt + 4 * g4 + (fr >> 2)) * 128 + (sw_ * 16 + 4 * (fr & 3)) * 2);
        float xs[4] = {bflo(xv[t].x), bfhi(xv[t].x), bflo(xv[t].y), bfhi(xv[t].y)};
        float ys[4] = {bflo(yv.x), bfhi(yv.x), bflo(yv.y), bfhi(yv.y)};
        float r_[4];
#pragma unroll
        for (int q = 0; q < 4; q++) r_[q] = fmaf(g, a[q], omg * (xs[q] * ys[q]));
        *(u32x2*)(out + (size_t)srow * D + ct * 80 + 16 * tt + 4 * g4) = (u32x2){pk2(r_[0], r_[1]), pk2(r_[2], r_[3])};
      }
    }
    TS(10); TS_OUT(21);
  }
}

extern "C" void kernel_launch(void* const* d_in, const int* in_sizes, int n_in, void* d_out, int out_size, void* d_ws, size_t ws_size,
                              hipStream_t stream) {
  const u16* x = (const u16*)d_in[0]; const u16* wqkv = (const u16*)d_in[1]; const u16* wo = (const u16*)d_in[2];
  const u16* Bk = (const u16*)d_in[4]; const u16* Bv = (const u16*)d_in[5]; const u16* shk = (const u16*)d_in[6];
  const u16* hk = (const u16*)d_in[7]; const u16* Dv = (const u16*)d_in[8]; const u16* gw = (const u16*)d_in[9];
  (void)hipFuncSetAttribute((const void*)fused, hipFuncAttributeMaxDynamicSharedMemorySize, LDS_TOTAL);
  fused<<<256, 512, LDS_TOTAL, stream>>>(x, wqkv, wo, Bk, Bv, shk, hk, Dv, gw, (u16*)d_out, (char*)d_ws);
}
```

```cpp
#include <hip/hip_runtime.h>
#include <hip/hip_bf16.h>
#include <type_traits>
#define NO_PF

typedef unsigned short u16;
typedef __bf16 bf16x2 __attribute__((ext_vector_type(2)));
typedef __bf16 bf16x8 __attribute__((ext_vector_type(8)));
typedef float f32x4 __attribute__((ext_vector_type(4)));
typedef unsigned u32x4 __attribute__((ext_vector_type(4)));
typedef unsigned u32x2 __attribute__((ext_vector_type(2)));
typedef short v4i16_t __attribute__((ext_vector_type(4)));
typedef int i32x4 __attribute__((ext_vector_type(4)));
typedef __attribute__((address_space(1))) unsigned gu32;
#define LAS __attribute__((address_space(3)))
#define SBAR() __builtin_amdgcn_sched_barrier(0)
#define SGB(mask, n) __builtin_amdgcn_sched_group_barrier(mask, n, 0)
#define BAR() asm volatile("s_waitcnt lgkmcnt(0)\n\ts_barrier" ::: "memory")
#define DRAIN() asm volatile("s_waitcnt vmcnt(0)" ::: "memory")
#define SC1 16

#define TS_DECL unsigned long long tevt_ = 0;
#define TSARG , tevt_
#define TSPAR , unsigned long long& tevt_
#define TS(i) do { if ((i) == 0 && blockIdx.x == 0 && threadIdx.x == 0) tevt_ = __builtin_amdgcn_s_memrealtime(); } while (0)
#define TS_OUT(n) do { if (blockIdx.x == 0 && threadIdx.x == 0) { while (__builtin_amdgcn_s_memrealtime() < tevt_ + 3000ull) __builtin_amdgcn_s_sleep(1); } } while (0)

constexpr int S = 2048, D = 640;
constexpr unsigned SZ = S * D * 2;
constexpr unsigned F_E = 0, F_U = 128, F_PQ = F_U + 64 * 128, F_P = F_PQ + 64 * 128, F_O = F_P + 128 * 128, F_Y = F_O + 128 * 128, F_W = F_Y + 128 * 128;
constexpr unsigned OFF_PQ = 0x20000, OFF_P = OFF_PQ + 128 * 8 * 3 * 1024, OFF_UT = OFF_P + (1u << 19), OFF_G = OFF_UT + SZ, OFF_WOP = OFF_G + 8192,
                   OFF_O = OFF_WOP + (1u << 20), OFF_YT = OFF_O + SZ, WS_USED = OFF_YT + SZ;
constexpr int XS = 16384, WSB = 20480, STG = XS + WSB;
constexpr int L_EXTRA = 4 * STG;
constexpr int L_TAG = L_EXTRA, L_CNT = L_EXTRA + 16, L_T = L_EXTRA + 64;
constexpr int L_SCR = L_T + 6144 + 5120;
constexpr int LDS_TOTAL = L_SCR + 256;
constexpr int CPS = 4160, UOFF = 4 * CPS, CHS = UOFF + 4608;
constexpr int L_UIMG = 5 * CHS;
constexpr int L_Y = 102400, L_RED = L_Y + 10240;

__device__ __forceinline__ unsigned pk2(float a, float b) { bf16x2 r = {(__bf16)a, (__bf16)b}; return __builtin_bit_cast(unsigned, r); }
__device__ __forceinline__ float rbf(float a) { return (float)(__bf16)a; }
__device__ __forceinline__ float bflo(unsigned v) { return __uint_as_float(v << 16); }
__device__ __forceinline__ float bfhi(unsigned v) { return __uint_as_float(v & 0xffff0000u); }
__device__ __forceinline__ float phi(float t) {
  return __builtin_amdgcn_fmed3f(t, 0.f, 3.0e38f) + (__builtin_amdgcn_fmed3f(__builtin_amdgcn_exp2f(t * 1.44269504f), 0.f, 1.f) + 1e-6f);
}
__device__ __forceinline__ i32x4 make_srd(const void* p, unsigned bytes) {
  unsigned long long a = (unsigned long long)p; i32x4 r;
  r.x = __builtin_amdgcn_readfirstlane((int)(unsigned)a); r.y = __builtin_amdgcn_readfirstlane((int)(unsigned)(a >> 32));
  r.z = __builtin_amdgcn_readfirstlane((int)bytes); r.w = 0x00020000; return r;
}
__device__ __forceinline__ void glds16(unsigned ldsaddr, unsigned voff, i32x4 srd, unsigned soff) {
  asm volatile("s_mov_b32 m0, %0\n\ts_nop 0\n\tbuffer_load_dwordx4 %1, %2, %3 offen lds" :: "s"(ldsaddr), "v"(voff), "s"(srd), "s"(soff) : "memory");
}
__device__ __forceinline__ void glds16_sc1(unsigned ldsaddr, unsigned voff, i32x4 srd, unsigned soff) {
  asm volatile("s_mov_b32 m0, %0\n\ts_nop 0\n\tbuffer_load_dwordx4 %1, %2, %3 offen sc1 lds" :: "s"(ldsaddr), "v"(voff), "s"(srd), "s"(soff) : "memory");
}
__device__ __forceinline__ void bload16_sc1(bf16x8& dst, unsigned voff, i32x4 srd, unsigned soff) {
  asm volatile("buffer_load_dwordx4 %0, %1, %2, %3 offen sc1" : "=v"(dst) : "v"(voff), "s"(srd), "s"(soff) : "memory");
}
template <int N> __device__ __forceinline__ void wait_bar() { asm volatile("s_waitcnt vmcnt(%0) lgkmcnt(0)\n\ts_barrier" :: "n"(N) : "memory"); }
__device__ __forceinline__ bf16x8 lds_b128(unsigned addr) { return *(const LAS bf16x8*)(size_t)addr; }
__device__ __forceinline__ u32x2 lds_b64(unsigned addr) { return *(const LAS u32x2*)(size_t)addr; }
__device__ __forceinline__ u32x2 lds_tr(unsigned addr) { return __builtin_bit_cast(u32x2, __builtin_amdgcn_ds_read_tr16_b64_v4i16((LAS v4i16_t*)(size_t)addr)); }
__device__ __forceinline__ bf16x8 mk8(u32x2 lo, u32x2 hi) { u32x4 t = {lo.x, lo.y, hi.x, hi.y}; return __builtin_bit_cast(bf16x8, t); }
__device__ __forceinline__ f32x4 mfma16(bf16x8 a, bf16x8 b, f32x4 c) { return __builtin_amdgcn_mfma_f32_16x16x32_bf16(a, b, c, 0, 0, 0); }

__device__ __forceinline__ u32x4 pair16(u32x2 a, u32x2 b) {
  const auto r0 = __builtin_amdgcn_permlane16_swap(a.x, b.x, false, false);
  const auto r1 = __builtin_amdgcn_permlane16_swap(a.y, b.y, false, false);
  return (u32x4){r0[0], r1[0], r0[1], r1[1]};
}
__device__ __forceinline__ void store_frags(const u32x2 (&v)[5], __amdgpu_buffer_rsrc_t rw, unsigned base, int k0, int fr, int g4) {
#pragma unroll
  for (int j = 0; j < 4; j += 2) {
    const int k = k0 + 16 * (j + (g4 & 1)) + 8 * (g4 >> 1);
    __builtin_amdgcn_raw_buffer_store_b128(pair16(v[j], v[j + 1]), rw, base + (k >> 5) * 1024 + (((k >> 3) & 3) * 16 + fr) * 16, 0, SC1);
  }
  const int k = k0 + 64 + 4 * g4;
  __builtin_amdgcn_raw_buffer_store_b64(v[4], rw, base + (k >> 5) * 1024 + (((k >> 3) & 3) * 16 + fr) * 16 + ((k >> 2) & 1) * 8, 0, SC1);
}

__device__ __forceinline__ void lds_sync(unsigned cnt_addr, int lane, unsigned n) {
  asm volatile("s_waitcnt lgkmcnt(0)" ::: "memory");
  if (lane == 0) __hip_atomic_fetch_add((LAS unsigned*)(size_t)cnt_addr, 1u, __ATOMIC_RELAXED, __HIP_MEMORY_SCOPE_WORKGROUP);
  while (__builtin_amdgcn_readfirstlane(__hip_atomic_load((LAS unsigned*)(size_t)cnt_addr, __ATOMIC_RELAXED, __HIP_MEMORY_SCOPE_WORKGROUP)) < n) {}
  asm volatile("" ::: "memory");
}
__device__ __forceinline__ bool lds_last(unsigned cnt_addr, int lane, unsigned n) {
  unsigned old = 0;
  if (lane == 0) old = __hip_atomic_fetch_add((LAS unsigned*)(size_t)cnt_addr, 1u, __ATOMIC_RELAXED, __HIP_MEMORY_SCOPE_WORKGROUP);
  return __builtin_amdgcn_readfirstlane(old) == n - 1;
}

__device__ __forceinline__ unsigned flag_ld(const char* ws, unsigned off) { return __hip_atomic_load((const gu32*)(ws + off), __ATOMIC_RELAXED, __HIP_MEMORY_SCOPE_AGENT); }
__device__ __forceinline__ void flag_st(char* ws, unsigned off, unsigned v) { __hip_atomic_store((gu32*)(ws + off), v, __ATOMIC_RELAXED, __HIP_MEMORY_SCOPE_AGENT); }
__device__ __forceinline__ void poll(const char* ws, unsigned off, bool act, unsigned tag) {
  const unsigned o0 = __builtin_amdgcn_readfirstlane(off);
  off = act ? off : o0;
  unsigned a, b, n = 1u << 17;
  asm volatile(
      "global_load_dword %0, %3, %4 sc1\n\t"
      "s_sleep 6\n"
      "1:\n\t"
      "global_load_dword %1, %3, %4 sc1\n\t"
      "s_waitcnt vmcnt(1)\n\t"
      "v_cmp_ne_u32 vcc, %0, %5\n\t"
      "s_cbranch_vccz 2f\n\t"
      "global_load_dword %0, %3, %4 sc1\n\t"
      "s_waitcnt vmcnt(1)\n\t"
      "v_cmp_ne_u32 vcc, %1, %5\n\t"
      "s_cbranch_vccz 2f\n\t"
      "s_sub_u32 %2, %2, 1\n\t"
      "s_cbranch_scc0 1b\n"
      "2:\n\t"
      "s_waitcnt vmcnt(0)"
      : "=&v"(a), "=&v"(b), "+s"(n) : "v"(off), "s"(ws), "v"(tag) : "vcc", "scc", "memory");
}

template <bool KV>
__device__ __forceinline__ void gemm_compute(f32x4 (&acc)[4][5], unsigned xa0, unsigned wa0, unsigned wa1 TSPAR) {
  bf16x8 xa[4], xb[4], wa[5], wb[5];
  auto load = [&](bf16x8 (&xf)[4], bf16x8 (&wf)[5], int buf, int kk) {
    const unsigned bo = buf * STG;
    const unsigned xa = (xa0 + bo) ^ (kk * 64);
    const unsigned a0 = wa0 + bo + kk * 10240, a1 = wa1 + bo + kk * 10240;
#pragma unroll
    for (int i = 0; i < 4; i++) xf[i] = lds_b128(xa + 2048 * i);
#pragma unroll
    for (int t = 0; t < 5; t++) { const unsigned a = ((t & 1) ? a1 : a0) + 32 * t; wf[t] = mk8(lds_tr(a), lds_tr(a + 1280)); }
  };
  auto mma = [&](const bf16x8 (&xf)[4], const bf16x8 (&wf)[5]) {
#pragma unroll
    for (int t = 0; t < 5; t++)
#pragma unroll
      for (int i = 0; i < 4; i++) acc[i][t] = KV ? mfma16(xf[i], wf[t], acc[i][t]) : mfma16(wf[t], xf[i], acc[i][t]);
  };
  auto interleave = [&]() {
#pragma unroll
    for (int t = 0; t < 14; t++) { SGB(0x8, 1); SGB(0x100, 1); }
    SGB(0x8, 6);
  };
  BAR();
  TS(21);
  load(xa, wa, 0, 0);
  SBAR();
  for (int s = 0; s < 9; s++) {
    load(xb, wb, s & 3, 1); mma(xa, wa); interleave();
    SBAR();
    BAR();
    if (s == 0) TS(22);
    if (s == 4) TS(23);
    load(xa, wa, (s + 1) & 3, 0); mma(xb, wb); interleave();
    SBAR();
  }
  load(xb, wb, 1, 1); mma(xa, wa); interleave();
  SBAR();
  mma(xb, wb);
}

__device__ __forceinline__ int gemm_s0(int rt, int cg, bool isKV) { return (((rt & 3) * 5 + 1) / 2 + (isKV ? 2 * (cg & 3) : 8 + (cg & 1))) % 10; }
__device__ __forceinline__ void gemm_vx(unsigned (&vx)[2], int L, int lane, int rt) {
#pragma unroll
  for (int i = 0; i < 2; i++) vx[i] = (rt * 128 + L * 64 + i * 8 + (lane >> 3)) * 1280 + (((lane & 7) ^ ((4 * i + (lane >> 4)) & 7)) << 4);
}
__device__ __forceinline__ void gemm_first_x(int L, int lane, unsigned lb, const u16* x, int rt, int cg, bool isKV) {
  const i32x4 sx = make_srd(x, S * D * 2);
  unsigned vx[2];
  gemm_vx(vx, L, lane, rt);
  const int s0 = gemm_s0(rt, cg, isKV);
#pragma unroll
  for (int p = 0; p < 8; p++) glds16(lb + (L * 8 + p) * 1024, vx[p & 1], sx, s0 * 128 + (p >> 1) * (16 * 1280));
}
__device__ __forceinline__ void gemm_loader(int L, int lane, unsigned lb, const u16* x, const u16* wqkv, int rt, int cg, bool isKV) {
  const int s0 = gemm_s0(rt, cg, isKV);
  const i32x4 sx = make_srd(x, S * D * 2), sw = make_srd(wqkv, D * 3 * D * 2);
  unsigned vx[2], vw[5];
  gemm_vx(vx, L, lane, rt);
#pragma unroll
  for (int i = 0; i < 5; i++) {
    const int ci = i * 64 + lane, k = (ci * 3277) >> 16, pos = ci - k * 20, gc = pos ^ (((k >> 3) & 1) << 1), tl = gc >> 1;
    unsigned col;
    if (isKV) { const int isv = (tl >= 3 && tl < 5) || tl >= 7; const int tt = tl < 3 ? tl : tl < 5 ? tl - 3 : tl < 7 ? tl - 2 : tl - 5;
                col = (isv ? 2560u : 1280u) + 160u * cg + tt * 32; }
    else col = 320u * cg + tl * 32;
    vw[i] = (L * 32 + k) * 3840 + col + (gc & 1) * 16;
  }
  auto stage = [&](int st_, int buf) {
    const unsigned base = lb + buf * STG;
    int st = st_ + s0; st = st >= 10 ? st - 10 : st;
#pragma unroll
    for (int p = 0; p < 10; p++) {
      glds16(base + XS + (L * 10 + p) * 1024, vw[p % 5], sw, st * (64 * 3840) + (p / 5) * (16 * 3840));
      if (p < 8) glds16(base + (L * 8 + p) * 1024, vx[p & 1], sx, st * 128 + (p >> 1) * (16 * 1280));
    }
  };
#pragma unroll
  for (int p = 0; p < 10; p++) glds16(lb + XS + (L * 10 + p) * 1024, vw[p % 5], sw, s0 * (64 * 3840) + (p / 5) * (16 * 3840));
  stage(1, 1); stage(2, 2);
  wait_bar<36>();
  stage(3, 3);
  for (int s = 0; s < 9; s++) {
    if (s < 7) wait_bar<36>(); else if (s == 7) wait_bar<18>(); else wait_bar<0>();
    if (s < 6) stage(s + 4, s & 3);
  }
}

template <int T> struct StatTile {
  static constexpr bool isz = T >= 13;
  static constexpr int i = isz ? T - 13 : (T + 1) / 3;
  static constexpr int j = isz ? 0 : T - (i == 0 ? 0 : 3 * i - 1) + (i == 0 ? 0 : i - 1);
};
template <int T> __device__ __forceinline__ f32x4 stat_mma(unsigned la) {
  const u32x4 o1 = {0x3F803F80u, 0x3F803F80u, 0x3F803F80u, 0x3F803F80u};
  bf16x8 A[4], B[4];
#pragma unroll
  for (int m = 0; m < 4; m++) {
    A[m] = lds_b128(la + StatTile<T>::i * 1024 + m * 10240);
    B[m] = StatTile<T>::isz ? __builtin_bit_cast(bf16x8, o1) : lds_b128(la + (5 + StatTile<T>::j) * 1024 + m * 10240);
  }
  f32x4 d4 = {0.f, 0.f, 0.f, 0.f};
#pragma unroll
  for (int m = 0; m < 4; m++) d4 = mfma16(A[m], B[m], d4);
  return d4;
}
template <int T> __device__ __forceinline__ void stat_store(f32x4 d4, int lane, __amdgpu_buffer_rsrc_t rw, unsigned pbase) {
  const int fr = lane & 15, g4 = lane >> 4;
  const int Dg = 16 * StatTile<T>::i + 4 * g4, h = (Dg * 205) >> 12, dd = Dg - 20 * h;
  const int ee = StatTile<T>::isz ? 20 : 16 * StatTile<T>::j + fr - 20 * h;
  if (StatTile<T>::isz ? fr == 0 : (ee >= 0 && ee < 20))
    __builtin_amdgcn_raw_buffer_store_b64((u32x2){pk2(d4[0], d4[1]), pk2(d4[2], d4[3])}, rw, pbase + (h * 420 + ee * 20 + dd) * 2, 0, SC1);
}
template <int W> __device__ __forceinline__ void stat_tiles(unsigned lb, int lane, __amdgpu_buffer_rsrc_t rw, unsigned pbase) {
  const unsigned la = lb + lane * 16;
  const f32x4 a = stat_mma<W>(la), b = stat_mma<W + 8>(la);
  if constexpr (W < 2) { const f32x4 c = stat_mma<W + 16>(la); stat_store<W + 16>(c, lane, rw, pbase); }
  stat_store<W>(a, lane, rw, pbase);
  stat_store<W + 8>(b, lane, rw, pbase);
}

__device__ __forceinline__ void conv_load_h(u32x4 (&cur)[4], u32x4 (&prv)[4], const u16* __restrict__ hrow, int lane) {
#pragma unroll
  for (int it = 0; it < 4; it++) {
    const int i = it * 64 + lane;
    cur[it] = *(const u32x4*)(hrow + i * 8); prv[it] = *(const u32x4*)(hrow + (i > 0 ? i * 8 - 8 : 0));
  }
}
__device__ __forceinline__ void conv_prep_h(const u32x4 (&cur)[4], const u32x4 (&prv_)[4], unsigned cb, int lane) {
#pragma unroll
  for (int it = 0; it < 4; it++) {
    u32x4 prv = prv_[it];
    if (it == 0 && lane == 0) prv = (u32x4){0, 0, 0, 0};
    const unsigned W_[8] = {prv.x, prv.y, prv.z, prv.w, cur[it].x, cur[it].y, cur[it].z, cur[it].w};
    const int c = 255 - (it * 64 + lane);
#pragma unroll
    for (int e = 0; e < 4; e++) {
      u32x4 ov;
#pragma unroll
      for (int q = 0; q < 4; q++) {
        const int i0 = 15 - e - 2 * q, i1 = i0 - 1;
        const unsigned sel = ((i0 & 1) ? 0x0706u : 0x0504u) | (((i1 & 1) ? 0x0302u : 0x0100u) << 16);
        ov[q] = __builtin_amdgcn_perm(W_[i0 >> 1], W_[i1 >> 1], sel);
      }
      *(LAS u32x4*)(size_t)(cb + e * CPS + c * 16) = ov;
    }
  }
  if (lane < 8) *(LAS u32x4*)(size_t)(cb + (lane >> 1) * CPS + (256 + (lane & 1)) * 16) = (u32x4){0, 0, 0, 0};
  if (lane < 32) *(LAS u32x4*)(size_t)(cb + UOFF + lane * 16) = (u32x4){0, 0, 0, 0};
}
template <class F>
__device__ __forceinline__ void conv_load_u(__amdgpu_buffer_rsrc_t rw, int d, unsigned cb, int lane, F&& between) {
  u32x4 uv[4];
#pragma unroll
  for (int it = 0; it < 4; it++) uv[it] = __builtin_amdgcn_raw_buffer_load_b128(rw, OFF_UT + d * 4096 + (it * 64 + lane) * 16, 0, SC1);
  SBAR();
  between();
  SBAR();
#pragma unroll
  for (int it = 0; it < 4; it++) *(LAS u32x4*)(size_t)(cb + UOFF + 512 + (it * 64 + lane) * 16) = uv[it];
}
struct ConvFrags { u32x2 al[8], ah[8]; u32x4 B[8]; };
__device__ __forceinline__ void conv_read(ConvFrags& f, unsigned aa, unsigned ba) {
  asm volatile(
      "ds_read_b128 %16, %25\n\tds_read_b64 %7, %24 offset:3584\n\tds_read_b64 %15, %24 offset:3592\n\t"
      "ds_read_b128 %17, %25 offset:512\n\tds_read_b128 %18, %25 offset:1024\n\tds_read_b128 %19, %25 offset:1536\n\t"
      "ds_read_b128 %20, %25 offset:2048\n\tds_read_b128 %21, %25 offset:2560\n\tds_read_b128 %22, %25 offset:3072\n\t"
      "ds_read_b128 %23, %25 offset:3584\n\t"
      "ds_read_b64 %6, %24 offset:3072\n\tds_read_b64 %14, %24 offset:3080\n\t"
      "ds_read_b64 %5, %24 offset:2560\n\tds_read_b64 %13, %24 offset:2568\n\t"
      "ds_read_b64 %4, %24 offset:2048\n\tds_read_b64 %12, %24 offset:2056\n\t"
      "ds_read_b64 %3, %24 offset:1536\n\tds_read_b64 %11, %24 offset:1544\n\t"
      "ds_read_b64 %2, %24 offset:1024\n\tds_read_b64 %10, %24 offset:1032\n\t"
      "ds_read_b64 %1, %24 offset:512\n\tds_read_b64 %9, %24 offset:520\n\t"
      "ds_read_b64 %0, %24\n\tds_read_b64 %8, %24 offset:8"
      : "=&v"(f.al[0]), "=&v"(f.al[1]), "=&v"(f.al[2]), "=&v"(f.al[3]), "=&v"(f.al[4]), "=&v"(f.al[5]), "=&v"(f.al[6]), "=&v"(f.al[7]),
        "=&v"(f.ah[0]), "=&v"(f.ah[1]), "=&v"(f.ah[2]), "=&v"(f.ah[3]), "=&v"(f.ah[4]), "=&v"(f.ah[5]), "=&v"(f.ah[6]), "=&v"(f.ah[7]),
        "=&v"(f.B[0]), "=&v"(f.B[1]), "=&v"(f.B[2]), "=&v"(f.B[3]), "=&v"(f.B[4]), "=&v"(f.B[5]), "=&v"(f.B[6]), "=&v"(f.B[7])
      : "v"(aa), "v"(ba) : "memory");
}
__device__ __forceinline__ void conv_wait(ConvFrags& f) {
  asm volatile("s_waitcnt lgkmcnt(0)"
      : "+v"(f.al[0]), "+v"(f.al[1]), "+v"(f.al[2]), "+v"(f.al[3]), "+v"(f.al[4]), "+v"(f.al[5]), "+v"(f.al[6]), "+v"(f.al[7]),
        "+v"(f.ah[0]), "+v"(f.ah[1]), "+v"(f.ah[2]), "+v"(f.ah[3]), "+v"(f.ah[4]), "+v"(f.ah[5]), "+v"(f.ah[6]), "+v"(f.ah[7]),
        "+v"(f.B[0]), "+v"(f.B[1]), "+v"(f.B[2]), "+v"(f.B[3]), "+v"(f.B[4]), "+v"(f.B[5]), "+v"(f.B[6]), "+v"(f.B[7]) :: "memory");
}
template <int K> __device__ __forceinline__ void conv_read1(ConvFrags& f, unsigned aa, unsigned ba) {
  if constexpr (K < 8) asm volatile("ds_read_b128 %0, %1 offset:%2" : "=&v"(f.B[K]) : "v"(ba), "n"(512 * K) : "memory");
  else if constexpr (K < 16) asm volatile("ds_read_b64 %0, %1 offset:%2" : "=&v"(f.al[K - 8]) : "v"(aa), "n"(512 * (K - 8)) : "memory");
  else if constexpr (K < 24) asm volatile("ds_read_b64 %0, %1 offset:%2" : "=&v"(f.ah[K - 16]) : "v"(aa), "n"(512 * (K - 16) + 8) : "memory");
}
template <int K = 0> __device__ __forceinline__ void conv_step(f32x4 (&acc)[8], const ConvFrags& f, ConvFrags& nx, unsigned aa, unsigned ba) {
  if constexpr (K < 36) {
    constexpr int a = K < 8 ? 0 : K < 15 ? 1 : K < 21 ? 2 : K < 26 ? 3 : K < 30 ? 4 : K < 33 ? 5 : K < 35 ? 6 : 7;
    constexpr int i = K - (a == 0 ? 0 : a == 1 ? 8 : a == 2 ? 15 : a == 3 ? 21 : a == 4 ? 26 : a == 5 ? 30 : a == 6 ? 33 : 35);
    acc[a + i] = mfma16(mk8(f.al[7 - a], f.ah[7 - a]), __builtin_bit_cast(bf16x8, f.B[i]), acc[a + i]);
    conv_read1<K>(nx, aa, ba);
    SBAR();
    conv_step<K + 1>(acc, f, nx, aa, ba);
  }
}
__device__ __forceinline__ void conv_main(f32x4 (&acc)[8], unsigned cb, int lane, int c0, int c1) {
  const int fr = lane & 15, g4 = lane >> 4;
  const unsigned abase = cb + (3 - (fr & 3)) * CPS + 2 * (2044 - 16 * 112 - 16 * (g4 >> 1) + 8 * (g4 & 1) - (fr & ~3));
  const unsigned bbase = cb + UOFF + 512 + 2 * (16 * fr - 16 * (g4 >> 1) + 8 * (g4 & 1));
#pragma unroll
  for (int c = 0; c < 8; c++) acc[c] = (f32x4){0.f, 0.f, 0.f, 0.f};
  ConvFrags f0, f1;
  asm volatile("s_waitcnt lgkmcnt(0)" ::: "memory");
  conv_read(f0, abase - 64 * c0, bbase - 64 * c0);
  for (int cc = c0; cc < c1; cc += 2) {
    conv_wait(f0);
    SBAR();
    conv_step(acc, f0, f1, abase - 64 * (cc + 1), bbase - 64 * (cc + 1));
    conv_wait(f1);
    SBAR();
    const int cn = cc + 2 < c1 ? cc + 2 : cc + 1;
    conv_step(acc, f1, f0, abase - 64 * cn, bbase - 64 * cn);
  }
  conv_wait(f0);
}
__device__ __forceinline__ void conv_store(f32x4 y, unsigned cb, int c, int lane, float Dd, __amdgpu_buffer_rsrc_t rw, int d) {
  const int fr = lane & 15, g4 = lane >> 4;
  const u32x2 uu = lds_b64(cb + UOFF + 512 + 2 * (256 * c + 16 * fr + 4 * g4));
  const float r0 = fmaf(bflo(uu.x), Dd, y[0]), r1 = fmaf(bfhi(uu.x), Dd, y[1]);
  const float r2 = fmaf(bflo(uu.y), Dd, y[2]), r3 = fmaf(bfhi(uu.y), Dd, y[3]);
  __builtin_amdgcn_raw_buffer_store_b64((u32x2){pk2(r0, r1), pk2(r2, r3)}, rw, OFF_YT + d * 4096 + (256 * c + 16 * fr + 4 * g4) * 2, 0, SC1);
}

__global__ __launch_bounds__(512, 2) void fused(const u16* __restrict__ x, const u16* __restrict__ wqkv, const u16* __restrict__ wo,
                                                const u16* __restrict__ Bk, const u16* __restrict__ Bv, const u16* __restrict__ shk,
                                                const u16* __restrict__ hk, const u16* __restrict__ Dv, const u16* __restrict__ gw,
                                                u16* __restrict__ out, char* __restrict__ ws) {
  extern __shared__ __attribute__((aligned(16))) char sm[];
  const int tid = threadIdx.x, lane = tid & 63;
  const int w = __builtin_amdgcn_readfirstlane(tid >> 6);
  const int b = blockIdx.x, xcd = b & 7, r = b >> 3;
  const int fr = lane & 15, g4 = lane >> 4;
  const unsigned lb = (unsigned)(size_t)(LAS char*)sm;
  const __amdgpu_buffer_rsrc_t rw = __builtin_amdgcn_make_buffer_rsrc(ws, 0, WS_USED, 0x00020000);
  if (r < 24 && (w >> 1) == 2) {
    const bool kv_ = r < 16;
    gemm_first_x(w - 4, lane, lb, x, (xcd & 3) * 4 + (r & 3), kv_ ? (xcd >> 2) * 4 + (r >> 2) : (xcd >> 2) * 2 + ((r - 16) >> 2), kv_);
  }
  TS_DECL TS(0);
  unsigned e0 = 0;
#define PUT_TAG() do { if (w == 7 && lane == 0) { *(LAS unsigned*)(size_t)(lb + L_TAG) = e0 + 1u; *(LAS u32x4*)(size_t)(lb + L_CNT) = (u32x4){0u, 0u, 0u, 0u}; } } while (0)
#define TAG (*(const LAS unsigned*)(size_t)(lb + L_TAG))
  if (r < 24) {
    const bool isKV = r < 16;
    const int rt = (xcd & 3) * 4 + (r & 3);
    const int cg = isKV ? (xcd >> 2) * 4 + (r >> 2) : (xcd >> 2) * 2 + ((r - 16) >> 2);
    f32x4 acc[4][5];
    const int rp = (w >> 1) & 1, hf = w & 1;
    if (w < 4) {
      const unsigned xa0 = lb + (rp * 64 + fr) * 128 + ((g4 ^ ((fr >> 1) & 7)) << 4);
      const unsigned wl = lb + XS + (8 * g4 + (fr >> 2)) * 320 + ((lane & 3) >> 1) * 16 + (lane & 1) * 8 + hf * 160;
      const unsigned sgn = (g4 & 1) * 32;
      const unsigned wa0 = hf ? wl - sgn : wl + sgn, wa1 = hf ? wl + sgn : wl - sgn;
#pragma unroll
      for (int i = 0; i < 4; i++)
#pragma unroll
        for (int t = 0; t < 5; t++) acc[i][t] = (f32x4){0.f, 0.f, 0.f, 0.f};
      if (isKV) gemm_compute<true>(acc, xa0, wa0, wa1 TSARG); else gemm_compute<false>(acc, xa0, wa0, wa1 TSARG);
    } else if (w < 6) {
      gemm_loader(w - 4, lane, lb, x, wqkv, rt, cg, isKV);
    } else {
      const int w6 = w - 6;
      const char* pfa;
      {
        const int q = rt & 3, u = isKV ? (cg & 3) : 4 + (cg & 1);
        if (lane < 32) {
          const int row = 16 * q + 8 * w6 + (lane >> 2);
          const unsigned col = isKV ? ((lane & 2) ? 2560u : 1280u) + 160u * cg : 320u * cg;
          const unsigned ln = isKV ? (lane & 1) : ((lane & 3) == 3 ? 2 : (lane & 3));
          pfa = (const char*)wqkv + row * 3840 + ((col >> 7) + ln) * 128;
        } else {
          int row = u * 22 + w6 * 11 + (lane - 32); row = (lane < 43 && row < 128) ? row : u * 22;
          pfa = (const char*)x + (rt * 128 + row) * 1280;
        }
      }
      const int pfs0 = gemm_s0(rt, cg, isKV);
      auto prefetch = [&](int j) {
      };
      SBAR();
      if (w == 7) e0 = flag_ld(ws, F_E);
      if (isKV) {
        const int l16 = lane & 15, row0 = rt * 128 + cg * 16 + w6 * 8;
        const u32x4 g0 = *(const u32x4*)(gw + lane * 8), g1 = *(const u32x4*)(gw + 512 + l16 * 8);
        u32x4 gx0[8], gx1[8];
#pragma unroll
        for (int i = 0; i < 8; i++) {
          const u16* xr = x + (size_t)(row0 + i) * D;
          gx0[i] = *(const u32x4*)(xr + lane * 8); gx1[i] = *(const u32x4*)(xr + 512 + l16 * 8);
        }
        SBAR();
        BAR(); prefetch(4); prefetch(5); BAR(); prefetch(6); prefetch(7); BAR(); prefetch(8); prefetch(9);
        float a[8];
        const float m1 = lane < 16 ? 1.f : 0.f;
#pragma unroll
        for (int i = 0; i < 8; i++) {
          float a0 = 0.f, a1 = 0.f;
#pragma unroll
          for (int j = 0; j < 4; j++) {
            a0 = fmaf(bflo(gx0[i][j]), bflo(g0[j]), a0); a0 = fmaf(bfhi(gx0[i][j]), bfhi(g0[j]), a0);
            a1 = fmaf(bflo(gx1[i][j]), bflo(g1[j]), a1); a1 = fmaf(bfhi(gx1[i][j]), bfhi(g1[j]), a1);
          }
          a[i] = fmaf(a1, m1, a0);
          if (i == 3) { SBAR(); BAR(); SBAR(); }
        }
        SBAR(); BAR(); SBAR();
#pragma unroll
        for (int i = 0; i < 4; i++) { const float lo = a[i], hi = a[i + 4]; const bool up = lane & 4; a[i] = (up ? hi : lo) + __shfl_xor(up ? lo : hi, 4); }
#pragma unroll
        for (int i = 0; i < 2; i++) { const float lo = a[i], hi = a[i + 2]; const bool up = lane & 2; a[i] = (up ? hi : lo) + __shfl_xor(up ? lo : hi, 2); }
        { const float lo = a[0], hi = a[1]; const bool up = lane & 1; a[0] = (up ? hi : lo) + __shfl_xor(up ? lo : hi, 1); }
        a[0] += __shfl_xor(a[0], 8); a[0] += __shfl_xor(a[0], 16); a[0] += __shfl_xor(a[0], 32);
        if (lane < 8) __builtin_amdgcn_raw_buffer_store_b32(__float_as_uint(rbf(1.f / (1.f + __expf(-rbf(a[0]))))), rw, OFF_G + (row0 + lane) * 4, 0, SC1);
        PUT_TAG();
        SBAR();
        BAR(); BAR(); BAR(); BAR(); BAR();
      } else {
        const int hw = (xcd * 8 + (r - 16)) * 2 + w6;
        const unsigned scr = lb + L_T + w6 * 6144;
#pragma unroll
        for (int bt = 0; bt < 2; bt++) {
          const int blk = bt == 0 ? hw : (hw < 32 ? 128 + hw : hw), ct = (blk * 3277) >> 16, ks = blk - 20 * ct;
          u32x4 v[5];
#pragma unroll
          for (int i = 0; i < 5; i++) {
            const int c = i * 64 + lane, row = (c * 6554) >> 16, ch = c - 10 * row;
            v[i] = *(const u32x4*)(wo + (size_t)(32 * ks + row) * D + 80 * ct + 8 * ch);
          }
          SBAR();
          BAR();
          if (bt == 0) { PUT_TAG(); prefetch(4); prefetch(5); }
          BAR();
          if (bt == 0) { prefetch(6); prefetch(7); }
          if (bt == 0 || hw < 32) {
#pragma unroll
            for (int i = 0; i < 5; i++) *(LAS u32x4*)(size_t)(scr + (i * 64 + lane) * 16) = v[i];
            const unsigned ta = scr + (8 * g4 + (fr >> 2)) * 160 + (lane & 3) * 8;
#pragma unroll
            for (int t = 0; t < 5; t++) {
              const u32x2 lo = lds_tr(ta + 32 * t), hi = lds_tr(ta + 32 * t + 640);
              __builtin_amdgcn_raw_buffer_store_b128((u32x4){lo.x, lo.y, hi.x, hi.y}, rw, OFF_WOP + (blk * 5 + t) * 1024 + lane * 16, 0, SC1);
            }
          }
          SBAR();
          BAR();
          if (bt == 0) { prefetch(8); prefetch(9); }
          BAR();
        }
        DRAIN();
        if (lds_last(lb + L_CNT + 8, lane, 2) && lane == 0) flag_st(ws, F_W + (xcd * 8 + (r - 16)) * 128, TAG);
        BAR(); BAR();
      }
    }
    TS(1);
    BAR();
    TS(11);
    const unsigned tag = TAG;
    if (isKV) {
      if (w < 4) {
#pragma unroll
        for (int t = 0; t < 5; t++) {
          const bool isk = hf ? t < 2 : t < 3;
          const int slot = hf ? (t < 2 ? 3 + t : 5 + t) : (t < 3 ? t : 2 + t);
#pragma unroll
          for (int m = 0; m < 2; m++) {
            u32x4 f;
            if (isk) f = (u32x4){pk2(phi(acc[2 * m][t][0]), phi(acc[2 * m][t][1])), pk2(phi(acc[2 * m][t][2]), phi(acc[2 * m][t][3])),
                                 pk2(phi(acc[2 * m + 1][t][0]), phi(acc[2 * m + 1][t][1])), pk2(phi(acc[2 * m + 1][t][2]), phi(acc[2 * m + 1][t][3]))};
            else f = (u32x4){pk2(acc[2 * m][t][0], acc[2 * m][t][1]), pk2(acc[2 * m][t][2], acc[2 * m][t][3]),
                             pk2(acc[2 * m + 1][t][0], acc[2 * m + 1][t][1]), pk2(acc[2 * m + 1][t][2], acc[2 * m + 1][t][3])};
            *(LAS u32x4*)(size_t)(lb + ((rp * 2 + m) * 10 + slot) * 1024 + lane * 16) = f;
          }
        }
      } else {
        for (int i = tid - 256; i < 6048 / 16; i += 256) *(LAS u32x4*)(size_t)(lb + L_T + i * 16) = (u32x4){0, 0, 0, 0};
        if (w == 7) poll(ws, F_W + lane * 128, true, tag);
      }
      TS(12);
      BAR();
      TS(2);
      switch (w) {
        case 0: stat_tiles<0>(lb, lane, rw, OFF_P + (rt * 8 + cg) * 3360); break;
        case 1: stat_tiles<1>(lb, lane, rw, OFF_P + (rt * 8 + cg) * 3360); break;
        case 2: stat_tiles<2>(lb, lane, rw, OFF_P + (rt * 8 + cg) * 3360); break;
        case 3: stat_tiles<3>(lb, lane, rw, OFF_P + (rt * 8 + cg) * 3360); break;
        case 4: stat_tiles<4>(lb, lane, rw, OFF_P + (rt * 8 + cg) * 3360); break;
        case 5: stat_tiles<5>(lb, lane, rw, OFF_P + (rt * 8 + cg) * 3360); break;
        case 6: stat_tiles<6>(lb, lane, rw, OFF_P + (rt * 8 + cg) * 3360); break;
        default: stat_tiles<7>(lb, lane, rw, OFF_P + (rt * 8 + cg) * 3360); break;
      }
      TS(14);
      DRAIN();
      TS(15);
      BAR();
      TS(3);
      if (w == 0) {
        if (lane == 0) flag_st(ws, F_P + (rt * 8 + cg) * 128, tag);
        poll(ws, lane < 16 ? F_P + (lane * 8 + cg) * 128 : F_PQ + (rt * 4 + (cg >> 1)) * 128, lane <= 16, tag);
      } else if (w < 7) {
        const i32x4 sws = make_srd(ws, WS_USED);
        const unsigned vwv = OFF_WOP + ((xcd >> 2) * 4 + (r >> 3)) * 102400 + lane * 16;
        for (int i = w - 1; i < 100; i += 6) {
          int pc = 12 * (r & 7) + i; pc = __builtin_amdgcn_readfirstlane(pc >= 100 ? pc - 100 : pc);
          glds16_sc1(lb + pc * 1024, vwv, sws, pc * 1024);
        }
      }
      constexpr int PJ[9] = {0, 1, 1, 2, 2, 3, 3, 4, 4}, PK[9] = {0, 0, 1, 0, 1, 1, 2, 1, 2};
      unsigned an[9], ad[9];
#pragma unroll
      for (int p = 0; p < 9; p++) {
        const int E = 16 * PJ[p] + fr, h = (E * 205) >> 12, e = E - 20 * h;
        int o_ = 32 * PK[p] + 8 * g4 - 20 * h; o_ = o_ < -8 ? -8 : o_ > 20 ? 20 : o_;
        an[p] = lb + L_T + (h * 21 + e) * 72 + (8 + o_) * 2; ad[p] = lb + L_T + (h * 21 + 20) * 72 + (8 + o_) * 2;
      }
      BAR();
      TS(4);
      bf16x8 Bq[3];
      {
        const unsigned prow = OFF_PQ + ((rt * 8 + w) * 8 + cg) * 3072 + lane * 16;
#pragma unroll
        for (int ks = 0; ks < 3; ks++) Bq[ks] = __builtin_bit_cast(bf16x8, __builtin_amdgcn_raw_buffer_load_b128(rw, prow + 1024 * ks, 0, SC1));
      }
      if (tid < 210) {
        const unsigned pb = OFF_P + cg * 3360 + tid * 16;
        u32x4 v[16];
#pragma unroll
        for (int k = 0; k < 16; k++) v[k] = __builtin_amdgcn_raw_buffer_load_b128(rw, pb, (k ^ rt) * (8 * 3360), SC1);
        unsigned o4[4];
#pragma unroll
        for (int c = 0; c < 4; c++) {
          float lo[16], hi[16];
#pragma unroll
          for (int k = 0; k < 16; k++) { lo[k] = bflo(v[k][c]); hi[k] = bfhi(v[k][c]); }
#pragma unroll
          for (int st = 1; st < 16; st <<= 1)
#pragma unroll
            for (int k = 0; k < 16; k += 2 * st) { lo[k] += lo[k + st]; hi[k] += hi[k + st]; }
          o4[c] = pk2(lo[0], hi[0]);
        }
        TS(16);
#pragma unroll
        for (int q = 0; q < 2; q++) {
          const int i4 = 2 * tid + q, row = (i4 * 13108) >> 16, d = 4 * (i4 - row * 5);
          *(LAS u32x2*)(size_t)(lb + L_T + row * 72 + (8 + d) * 2) = (u32x2){o4[2 * q], o4[2 * q + 1]};
        }
      }
      BAR();
      TS(5);
      {
        if (g4 >= 2) Bq[2] = __builtin_bit_cast(bf16x8, (u32x4){0, 0, 0, 0});
        f32x4 nu[5], de[5];
#pragma unroll
        for (int j = 0; j < 5; j++) { nu[j] = (f32x4){0.f, 0.f, 0.f, 0.f}; de[j] = nu[j]; }
        bf16x8 An[9], Ad[9];
#pragma unroll
        for (int p = 0; p < 9; p++) {
          unsigned an8 = an[p] + 8, ad8 = ad[p] + 8;
          asm volatile("" : "+v"(an8), "+v"(ad8));
          An[p] = mk8(lds_b64(an[p]), lds_b64(an8)); Ad[p] = mk8(lds_b64(ad[p]), lds_b64(ad8));
        }
#pragma unroll
        for (int p = 0; p < 9; p++) { de[PJ[p]] = mfma16(Ad[p], Bq[PK[p]], de[PJ[p]]); nu[PJ[p]] = mfma16(An[p], Bq[PK[p]], nu[PJ[p]]); }
        u32x2 ov[5];
#pragma unroll
        for (int j = 0; j < 5; j++) {
          const float rd = __builtin_amdgcn_rcpf(de[j][0] + 1e-6f);
          ov[j] = (u32x2){pk2(nu[j][0] * rd, nu[j][1] * rd), pk2(nu[j][2] * rd, nu[j][3] * rd)};
        }
        store_frags(ov, rw, OFF_O + (rt * 8 + w) * 20480, 80 * cg, fr, g4);
      }
      TS(17);
      DRAIN();
      BAR();
      if (tid == 0) flag_st(ws, F_O + (rt * 8 + cg) * 128, tag);
      TS(6);
    } else {
      const int vb = xcd * 8 + (r - 16);
      const int d = 80 * (vb >> 3) + 48 + 4 * (vb & 7) + (w & 3);
      const unsigned cb = lb + (w & 3) * CHS;
      if (w < 4) {
#pragma unroll
        for (int i = 0; i < 4; i++) {
          u32x2 pv[5];
#pragma unroll
          for (int t = 0; t < 5; t++) pv[t] = (u32x2){pk2(phi(acc[i][t][0]), phi(acc[i][t][1])), pk2(phi(acc[i][t][2]), phi(acc[i][t][3]))};
          store_frags(pv, rw, OFF_PQ + ((rt * 8 + rp * 4 + i) * 8 + cg * 2 + hf) * 3072, 0, fr, g4);
        }
      } else {
        u32x4 hc[4], hp[4];
        conv_load_h(hc, hp, hk + (size_t)d * S, lane);
        poll(ws, F_U + lane * 128, true, tag);
        conv_load_u(rw, d, cb, lane, [&]() { conv_prep_h(hc, hp, cb, lane); });
      }
      TS(12);
      DRAIN();
      BAR();
      TS(2);
      if (tid == 0) flag_st(ws, F_PQ + (rt * 4 + cg) * 128, tag);
      if (w < 4) {
        const float Dd = bflo(Dv[d]);
        TS(3);
        f32x4 y[8];
        conv_main(y, cb, lane, 0, 8);
        TS(4);
#pragma unroll
        for (int c = 0; c < 8; c++) conv_store(y[c], cb, c, lane, Dd, rw, d);
        DRAIN();
      } else if (w == 7) {
        poll(ws, F_W + lane * 128, true, tag);
      }
      BAR();
      if (tid == 0) flag_st(ws, F_Y + (64 + vb) * 128, tag);
      TS(5); TS(6);
    }
  } else {
    const int pw = xcd * 8 + (r - 24);
    const int c6 = w < 4 ? w : 4 + ((w - 4) >> 1);
    const int d = 80 * (pw >> 3) + 6 * (pw & 7) + c6;
    const unsigned cb = lb + c6 * CHS;
    const bool prep = w < 4 || !(w & 1);
    u32x4 hc[4], hp[4];
    {
      const int rbase = xcd * 256 + ((r - 24) >> 1) * 64, cbase = ((r - 24) & 1) * 320;
      u32x4 kr[2][7], vr[2][4], sp[2][4];
#pragma unroll
      for (int q = 0; q < 2; q++) {
        const int it = w + 8 * q;
        if (q == 0 || w < 2) {
          const int ch = cbase + (it >> 1) * 64 + (lane & 7) * 8;
          const int s0 = rbase + (it & 1) * 32 + (lane >> 3) * 4;
#pragma unroll
          for (int j = 0; j < 7; j++) { int s = s0 - 3 + j; kr[q][j] = *(const u32x4*)(Bk + (size_t)(s < 0 ? 0 : s) * D + ch); }
#pragma unroll
          for (int j = 0; j < 4; j++) vr[q][j] = *(const u32x4*)(Bv + (size_t)(s0 + j) * D + ch);
#pragma unroll
          for (int j = 0; j < 4; j++) sp[q][j] = ((const u32x4*)(shk + ch * 4))[j];
        }
      }
      if (prep) conv_load_h(hc, hp, hk + (size_t)d * S, lane);
      if (w == 7) e0 = flag_ld(ws, F_E);
      SBAR();
#pragma unroll
      for (int q = 0; q < 2; q++) {
        const int it = w + 8 * q;
        if (q == 0 || w < 2) {
          const int s0 = rbase + (it & 1) * 32 + (lane >> 3) * 4;
          if (s0 == 0) { kr[q][0] = (u32x4){0, 0, 0, 0}; kr[q][1] = kr[q][0]; kr[q][2] = kr[q][0]; }
          const int rg = (it & 1) * 8 + (lane >> 3);
          const unsigned la = lb + L_UIMG + ((it >> 1) * 64 + (lane & 7) * 8) * 128 + (((rg >> 1) ^ (lane & 7)) << 4) + (rg & 1) * 8;
#pragma unroll
          for (int c = 0; c < 8; c++) {
            const unsigned s01 = sp[q][c >> 1][(c & 1) * 2], s23 = sp[q][c >> 1][(c & 1) * 2 + 1];
            const float w0 = bflo(s01), w1 = bfhi(s01), w2 = bflo(s23), w3 = bfhi(s23);
            float kk[7], u[4];
#pragma unroll
            for (int j = 0; j < 7; j++) { unsigned dd = kr[q][j][c >> 1]; kk[j] = (c & 1) ? bfhi(dd) : bflo(dd); }
#pragma unroll
            for (int j = 0; j < 4; j++) {
              unsigned dd = vr[q][j][c >> 1]; float v = (c & 1) ? bfhi(dd) : bflo(dd);
              float ks = kk[j] * w3; ks = fmaf(kk[j + 1], w2, ks); ks = fmaf(kk[j + 2], w1, ks); ks = fmaf(kk[j + 3], w0, ks);
              u[j] = ks * v;
            }
            *(LAS u32x2*)(size_t)(la + c * 128) = (u32x2){pk2(u[0], u[1]), pk2(u[2], u[3])};
          }
        }
      }
      PUT_TAG();
      BAR();
#pragma unroll
      for (int i = 0; i < 5; i++) {
        const int chl = (w * 5 + i) * 8 + (lane >> 3), k = lane & 7;
        const u32x4 v = *(const LAS u32x4*)(size_t)(lb + L_UIMG + chl * 128 + ((k ^ ((chl >> 3) & 7)) << 4));
        __builtin_amdgcn_raw_buffer_store_b128(v, rw, OFF_UT + (cbase + chl) * 4096 + rbase * 2 + k * 16, 0, SC1);
      }
      TS(1);
    }
    if (w < 5 && prep) conv_prep_h(hc, hp, cb, lane);
    DRAIN();
    BAR();
    TS(2);
    const unsigned tag = TAG;
    if (tid == 0) flag_st(ws, F_U + pw * 128, tag);
    const float Dd = bflo(Dv[d]);
    if (prep) {
      if (w == 6) conv_prep_h(hc, hp, cb, lane);
      poll(ws, F_U + lane * 128, true, tag);
      conv_load_u(rw, d, cb, lane, [&]() {});
    }
    BAR();
    TS(3);
    f32x4 y[8];
    conv_main(y, cb, lane, w < 4 ? 0 : (w & 1) * 4, w < 4 ? 8 : (w & 1) * 4 + 4);
    TS(4);
    if (w < 4) {
#pragma unroll
      for (int c = 0; c < 8; c++) conv_store(y[c], cb, c, lane, Dd, rw, d);
    }
    BAR();
    if (w >= 4) {
#pragma unroll
      for (int c = 0; c < 4; c++) { const f32x4 lo = y[c], hi = y[4 + c]; *(LAS f32x4*)(size_t)(cb + ((w & 1) * 4 + c) * 1024 + lane * 16) = (w & 1) ? lo : hi; }
    }
    BAR();
    if (w >= 4) {
#pragma unroll
      for (int c = 0; c < 4; c++) {
        const f32x4 o_ = *(const LAS f32x4*)(size_t)(cb + (((w & 1) ^ 1) * 4 + c) * 1024 + lane * 16);
        const f32x4 lo = y[c], hi = y[4 + c];
        const f32x4 mine = (w & 1) ? hi : lo;
        conv_store((w & 1) ? o_ + mine : mine + o_, cb, (w & 1) * 4 + c, lane, Dd, rw, d);
      }
    }
    if (w == 0) poll(ws, F_W + lane * 128, true, tag);
    DRAIN();
    BAR();
    if (tid == 0) flag_st(ws, F_Y + pw * 128, tag);
    TS(5); TS(6);
  }

  {
    const unsigned tag = TAG;
    const int rt = (xcd & 3) * 8 + (r & 7), ct = (xcd >> 2) * 4 + (r >> 3);
    const int sw_ = w & 3, kk = w >> 2;
    const i32x4 sws = make_srd(ws, WS_USED);
    TS(18);
    const int srow = rt * 64 + sw_ * 16 + fr;
    u32x2 xv[3];
#pragma unroll
    for (int t = 0; t < 3; t++) { const int tt = kk ? (t < 2 ? 3 + t : 4) : t; xv[t] = *(const u32x2*)(x + (size_t)srow * D + ct * 80 + 16 * tt + 4 * g4); }
    SBAR();
    const int rot = ((ct & 3) * 5) >> 1;
    const unsigned wa0 = lb + kk * 5120 + lane * 16;
    bf16x8 wf0[5][5];
    auto read_wf0 = [&]() {
#pragma unroll
      for (int s = 0; s < 5; s++) {
        int q = s + rot; q = q >= 10 ? q - 10 : q;
#pragma unroll
        for (int t = 0; t < 5; t++) wf0[s][t] = lds_b128(wa0 + q * 10240 + t * 1024);
      }
    };
    if (r < 16) read_wf0();
    SBAR();
    if (w == 0) {
      poll(ws, F_O + ((rt >> 1) * 8 + (lane & 7)) * 128, lane < 8, tag);
    } else if (w == 7) {
      poll(ws, F_Y + ((lane & 8 ? 64 : 0) + ct * 8 + (lane & 7)) * 128, lane < 16, tag);
    } else {
      if (r >= 16) {
        const unsigned vwv = OFF_WOP + ct * 102400 + lane * 16;
        for (int i = w - 1; i < 100; i += 6) { int pc = 12 * (r & 7) + i; pc = __builtin_amdgcn_readfirstlane(pc >= 100 ? pc - 100 : pc); glds16_sc1(lb + pc * 1024, vwv, sws, pc * 1024); }
      }
      if (w == 6 && b == 255) {
        for (int spins = 0; spins < (1 << 17); spins++) {
          const unsigned a0 = flag_ld(ws, F_U + lane * 128), a1 = flag_ld(ws, F_U + (64 + lane) * 128);
          const unsigned a2 = flag_ld(ws, F_U + (128 + lane) * 128), a3 = flag_ld(ws, F_U + (192 + lane) * 128);
          if (__all(a0 == tag && a1 == tag && a2 == tag && a3 == tag)) break;
          __builtin_amdgcn_s_sleep(2);
        }
        if (lane == 0) flag_st(ws, F_E, tag);
      }
      DRAIN();
    }
    BAR();
    TS(7);
    asm volatile("" :: "v"(xv[0]), "v"(xv[1]), "v"(xv[2]));
    bf16x8 of[10];
    {
      const unsigned vo = OFF_O + (rt * 4 + sw_) * 20480 + kk * 1024 + lane * 16;
#pragma unroll
      for (int s = 0; s < 10; s++) { int q = s + rot; q = __builtin_amdgcn_readfirstlane(q >= 10 ? q - 10 : q); bload16_sc1(of[s], vo, sws, q * 2048); }
    }
    if (w == 7) {
      for (int pc = 0; pc < 10; pc++)
        glds16_sc1(lb + L_Y + pc * 1024, OFF_YT + (ct * 80 + pc * 8 + (lane >> 3)) * 4096 + rt * 128 + (lane & 7) * 16, sws, 0);
    }
    float g;
    asm volatile("buffer_load_dword %0, %1, %2, 0 offen sc1" : "=v"(g) : "v"(OFF_G + srow * 4), "s"(sws) : "memory");
    TS(8);
    if (r >= 16) read_wf0();
    f32x4 acc[5];
#pragma unroll
    for (int t = 0; t < 5; t++) acc[t] = (f32x4){0.f, 0.f, 0.f, 0.f};
    {
      bf16x8 wf1[5][5];
#pragma unroll
      for (int s = 0; s < 5; s++) {
        int q = 5 + s + rot; q = q >= 10 ? q - 10 : q;
#pragma unroll
        for (int t = 0; t < 5; t++) wf1[s][t] = lds_b128(wa0 + q * 10240 + t * 1024);
      }
      SBAR();
      if (w == 7) asm volatile("s_waitcnt vmcnt(16)" ::: "memory"); else asm volatile("s_waitcnt vmcnt(6)" ::: "memory");
      asm volatile("" : "+v"(of[0]), "+v"(of[1]), "+v"(of[2]), "+v"(of[3]), "+v"(of[4]) :: "memory");
      SBAR();
#pragma unroll
      for (int s = 0; s < 5; s++)
#pragma unroll
        for (int t = 0; t < 5; t++) acc[t] = mfma16(wf0[s][t], of[s], acc[t]);
      SBAR();
      if (w == 7) asm volatile("s_waitcnt vmcnt(11)" ::: "memory"); else asm volatile("s_waitcnt vmcnt(1)" ::: "memory");
      asm volatile("" : "+v"(of[5]), "+v"(of[6]), "+v"(of[7]), "+v"(of[8]), "+v"(of[9]) :: "memory");
      SBAR();
#pragma unroll
      for (int s = 0; s < 5; s++)
#pragma unroll
        for (int t = 0; t < 5; t++) acc[t] = mfma16(wf1[s][t], of[5 + s], acc[t]);
      SBAR();
    }
    TS(9);
    const unsigned ra = lb + L_RED + (sw_ * 5) * 1024 + lane * 16;
    if (kk == 0) { *(LAS f32x4*)(size_t)(ra + 3 * 1024) = acc[3]; *(LAS f32x4*)(size_t)(ra + 4 * 1024) = acc[4]; }
    else { *(LAS f32x4*)(size_t)(ra) = acc[0]; *(LAS f32x4*)(size_t)(ra + 1024) = acc[1]; *(LAS f32x4*)(size_t)(ra + 2048) = acc[2]; }
    asm volatile("s_waitcnt vmcnt(0)" : "+v"(g) :: "memory");
    BAR();
    TS(20);
    const float omg = 1.f - g;
    const int nt = kk ? 2 : 3;
#pragma unroll
    for (int t = 0; t < 3; t++) {
      if (t < nt) {
        const int tt = kk ? 3 + t : t;
        const f32x4 a0 = acc[t], a1 = acc[3 + (t < 2 ? t : 1)];
        f32x4 a = *(const LAS f32x4*)(size_t)(ra + tt * 1024) + (kk ? a1 : a0);
        u32x2 yv = lds_tr(lb + L_Y + (16 * tt + 4 * g4 + (fr >> 2)) * 128 + (sw_ * 16 + 4 * (fr & 3)) * 2);
        float xs[4] = {bflo(xv[t].x), bfhi(xv[t].x), bflo(xv[t].y), bfhi(xv[t].y)};
        float ys[4] = {bflo(yv.x), bfhi(yv.x), bflo(yv.y), bfhi(yv.y)};
        float r_[4];
#pragma unroll
        for (int q = 0; q < 4; q++) r_[q] = fmaf(g, a[q], omg * (xs[q] * ys[q]));
        *(u32x2*)(out + (size_t)srow * D + ct * 80 + 16 * tt + 4 * g4) = (u32x2){pk2(r_[0], r_[1]), pk2(r_[2], r_[3])};
      }
    }
    TS(10); TS_OUT(21);
  }
}

extern "C" void kernel_launch(void* const* d_in, const int* in_sizes, int n_in, void* d_out, int out_size, void* d_ws, size_t ws_size,
                              hipStream_t stream) {
  const u16* x = (const u16*)d_in[0]; const u16* wqkv = (const u16*)d_in[1]; const u16* wo = (const u16*)d_in[2];
  const u16* Bk = (const u16*)d_in[4]; const u16* Bv = (const u16*)d_in[5]; const u16* shk = (const u16*)d_in[6];
  const u16* hk = (const u16*)d_in[7]; const u16* Dv = (const u16*)d_in[8]; const u16* gw = (const u16*)d_in[9];
  (void)hipFuncSetAttribute((const void*)fused, hipFuncAttributeMaxDynamicSharedMemorySize, LDS_TOTAL);
  fused<<<256, 512, LDS_TOTAL, stream>>>(x, wqkv, wo, Bk, Bv, shk, hk, Dv, gw, (u16*)d_out, (char*)d_ws);
}
```

```cpp
#include <hip/hip_runtime.h>
#include <hip/hip_bf16.h>
#include <type_traits>
#define NO_PF

typedef unsigned short u16;
typedef __bf16 bf16x2 __attribute__((ext_vector_type(2)));
typedef __bf16 bf16x8 __attribute__((ext_vector_type(8)));
typedef float f32x4 __attribute__((ext_vector_type(4)));
typedef unsigned u32x4 __attribute__((ext_vector_type(4)));
typedef unsigned u32x2 __attribute__((ext_vector_type(2)));
typedef short v4i16_t __attribute__((ext_vector_type(4)));
typedef int i32x4 __attribute__((ext_vector_type(4)));
typedef __attribute__((address_space(1))) unsigned gu32;
#define LAS __attribute__((address_space(3)))
#define SBAR() __builtin_amdgcn_sched_barrier(0)
#define SGB(mask, n) __builtin_amdgcn_sched_group_barrier(mask, n, 0)
#define BAR() asm volatile("s_waitcnt lgkmcnt(0)\n\ts_barrier" ::: "memory")
#define DRAIN() asm volatile("s_waitcnt vmcnt(0)" ::: "memory")
#define SC1 16

#define TS_DECL
#define TSARG
#define TSPAR
#define TS(i)
#define TS_OUT(n)

constexpr int S = 2048, D = 640;
constexpr unsigned SZ = S * D * 2;
constexpr unsigned F_E = 0, F_U = 128, F_PQ = F_U + 64 * 128, F_P = F_PQ + 64 * 128, F_O = F_P + 128 * 128, F_Y = F_O + 128 * 128, F_W = F_Y + 128 * 128;
constexpr unsigned OFF_PQ = 0x20000, OFF_P = OFF_PQ + 128 * 8 * 3 * 1024, OFF_UT = OFF_P + (1u << 19), OFF_G = OFF_UT + SZ, OFF_WOP = OFF_G + 8192,
                   OFF_O = OFF_WOP + (1u << 20), OFF_YT = OFF_O + SZ, WS_USED = OFF_YT + SZ;
constexpr int XS = 16384, WSB = 20480, STG = XS + WSB;
constexpr int L_EXTRA = 4 * STG;
constexpr int L_TAG = L_EXTRA, L_CNT = L_EXTRA + 16, L_T = L_EXTRA + 64;
constexpr int L_SCR = L_T + 6144 + 5120;
constexpr int LDS_TOTAL = L_SCR + 256;
constexpr int CPS = 4160, UOFF = 4 * CPS, CHS = UOFF + 4608;
constexpr int L_UIMG = 5 * CHS;
constexpr int L_Y = 102400, L_RED = L_Y + 10240;

__device__ __forceinline__ unsigned pk2(float a, float b) { bf16x2 r = {(__bf16)a, (__bf16)b}; return __builtin_bit_cast(unsigned, r); }
__device__ __forceinline__ float rbf(float a) { return (float)(__bf16)a; }
__device__ __forceinline__ float bflo(unsigned v) { return __uint_as_float(v << 16); }
__device__ __forceinline__ float bfhi(unsigned v) { return __uint_as_float(v & 0xffff0000u); }
__device__ __forceinline__ float phi(float t) {
  return __builtin_amdgcn_fmed3f(t, 0.f, 3.0e38f) + (__builtin_amdgcn_fmed3f(__builtin_amdgcn_exp2f(t * 1.44269504f), 0.f, 1.f) + 1e-6f);
}
__device__ __forceinline__ i32x4 make_srd(const void* p, unsigned bytes) {
  unsigned long long a = (unsigned long long)p; i32x4 r;
  r.x = __builtin_amdgcn_readfirstlane((int)(unsigned)a); r.y = __builtin_amdgcn_readfirstlane((int)(unsigned)(a >> 32));
  r.z = __builtin_amdgcn_readfirstlane((int)bytes); r.w = 0x00020000; return r;
}
__device__ __forceinline__ void glds16(unsigned ldsaddr, unsigned voff, i32x4 srd, unsigned soff) {
  asm volatile("s_mov_b32 m0, %0\n\ts_nop 0\n\tbuffer_load_dwordx4 %1, %2, %3 offen lds" :: "s"(ldsaddr), "v"(voff), "s"(srd), "s"(soff) : "memory");
}
__device__ __forceinline__ void glds16_sc1(unsigned ldsaddr, unsigned voff, i32x4 srd, unsigned soff) {
  asm volatile("s_mov_b32 m0, %0\n\ts_nop 0\n\tbuffer_load_dwordx4 %1, %2, %3 offen sc1 lds" :: "s"(ldsaddr), "v"(voff), "s"(srd), "s"(soff) : "memory");
}
__device__ __forceinline__ void bload16_sc1(bf16x8& dst, unsigned voff, i32x4 srd, unsigned soff) {
  asm volatile("buffer_load_dwordx4 %0, %1, %2, %3 offen sc1" : "=v"(dst) : "v"(voff), "s"(srd), "s"(soff) : "memory");
}
template <int N> __device__ __forceinline__ void wait_bar() { asm volatile("s_waitcnt vmcnt(%0) lgkmcnt(0)\n\ts_barrier" :: "n"(N) : "memory"); }
__device__ __forceinline__ bf16x8 lds_b128(unsigned addr) { return *(const LAS bf16x8*)(size_t)addr; }
__device__ __forceinline__ u32x2 lds_b64(unsigned addr) { return *(const LAS u32x2*)(size_t)addr; }
__device__ __forceinline__ u32x2 lds_tr(unsigned addr) { return __builtin_bit_cast(u32x2, __builtin_amdgcn_ds_read_tr16_b64_v4i16((LAS v4i16_t*)(size_t)addr)); }
__device__ __forceinline__ bf16x8 mk8(u32x2 lo, u32x2 hi) { u32x4 t = {lo.x, lo.y, hi.x, hi.y}; return __builtin_bit_cast(bf16x8, t); }
__device__ __forceinline__ f32x4 mfma16(bf16x8 a, bf16x8 b, f32x4 c) { return __builtin_amdgcn_mfma_f32_16x16x32_bf16(a, b, c, 0, 0, 0); }

__device__ __forceinline__ u32x4 pair16(u32x2 a, u32x2 b) {
  const auto r0 = __builtin_amdgcn_permlane16_swap(a.x, b.x, false, false);
  const auto r1 = __builtin_amdgcn_permlane16_swap(a.y, b.y, false, false);
  return (u32x4){r0[0], r1[0], r0[1], r1[1]};
}
__device__ __forceinline__ void store_frags(const u32x2 (&v)[5], __amdgpu_buffer_rsrc_t rw, unsigned base, int k0, int fr, int g4) {
#pragma unroll
  for (int j = 0; j < 4; j += 2) {
    const int k = k0 + 16 * (j + (g4 & 1)) + 8 * (g4 >> 1);
    __builtin_amdgcn_raw_buffer_store_b128(pair16(v[j], v[j + 1]), rw, base + (k >> 5) * 1024 + (((k >> 3) & 3) * 16 + fr) * 16, 0, SC1);
  }
  const int k = k0 + 64 + 4 * g4;
  __builtin_amdgcn_raw_buffer_store_b64(v[4], rw, base + (k >> 5) * 1024 + (((k >> 3) & 3) * 16 + fr) * 16 + ((k >> 2) & 1) * 8, 0, SC1);
}

__device__ __forceinline__ void lds_sync(unsigned cnt_addr, int lane, unsigned n) {
  asm volatile("s_waitcnt lgkmcnt(0)" ::: "memory");
  if (lane == 0) __hip_atomic_fetch_add((LAS unsigned*)(size_t)cnt_addr, 1u, __ATOMIC_RELAXED, __HIP_MEMORY_SCOPE_WORKGROUP);
  while (__builtin_amdgcn_readfirstlane(__hip_atomic_load((LAS unsigned*)(size_t)cnt_addr, __ATOMIC_RELAXED, __HIP_MEMORY_SCOPE_WORKGROUP)) < n) {}
  asm volatile("" ::: "memory");
}
__device__ __forceinline__ bool lds_last(unsigned cnt_addr, int lane, unsigned n) {
  unsigned old = 0;
  if (lane == 0) old = __hip_atomic_fetch_add((LAS unsigned*)(size_t)cnt_addr, 1u, __ATOMIC_RELAXED, __HIP_MEMORY_SCOPE_WORKGROUP);
  return __builtin_amdgcn_readfirstlane(old) == n - 1;
}

__device__ __forceinline__ unsigned flag_ld(const char* ws, unsigned off) { return __hip_atomic_load((const gu32*)(ws + off), __ATOMIC_RELAXED, __HIP_MEMORY_SCOPE_AGENT); }
__device__ __forceinline__ void flag_st(char* ws, unsigned off, unsigned v) { __hip_atomic_store((gu32*)(ws + off), v, __ATOMIC_RELAXED, __HIP_MEMORY_SCOPE_AGENT); }
__device__ __forceinline__ void poll(const char* ws, unsigned off, bool act, unsigned tag) {
  const unsigned o0 = __builtin_amdgcn_readfirstlane(off);
  off = act ? off : o0;
  unsigned a, b, n = 1u << 17;
  asm volatile(
      "global_load_dword %0, %3, %4 sc1\n\t"
      "s_sleep 6\n"
      "1:\n\t"
      "global_load_dword %1, %3, %4 sc1\n\t"
      "s_waitcnt vmcnt(1)\n\t"
      "v_cmp_ne_u32 vcc, %0, %5\n\t"
      "s_cbranch_vccz 2f\n\t"
      "global_load_dword %0, %3, %4 sc1\n\t"
      "s_waitcnt vmcnt(1)\n\t"
      "v_cmp_ne_u32 vcc, %1, %5\n\t"
      "s_cbranch_vccz 2f\n\t"
      "s_sub_u32 %2, %2, 1\n\t"
      "s_cbranch_scc0 1b\n"
      "2:\n\t"
      "s_waitcnt vmcnt(0)"
      : "=&v"(a), "=&v"(b), "+s"(n) : "v"(off), "s"(ws), "v"(tag) : "vcc", "scc", "memory");
}

template <bool KV>
__device__ __forceinline__ void gemm_compute(f32x4 (&acc)[4][5], unsigned xa0, unsigned wa0, unsigned wa1 TSPAR) {
  bf16x8 xa[4], xb[4], wa[5], wb[5];
  auto load = [&](bf16x8 (&xf)[4], bf16x8 (&wf)[5], int buf, int kk) {
    const unsigned bo = buf * STG;
    const unsigned xa = (xa0 + bo) ^ (kk * 64);
    const unsigned a0 = wa0 + bo + kk * 10240, a1 = wa1 + bo + kk * 10240;
#pragma unroll
    for (int i = 0; i < 4; i++) xf[i] = lds_b128(xa + 2048 * i);
#pragma unroll
    for (int t = 0; t < 5; t++) { const unsigned a = ((t & 1) ? a1 : a0) + 32 * t; wf[t] = mk8(lds_tr(a), lds_tr(a + 1280)); }
  };
  auto mma = [&](const bf16x8 (&xf)[4], const bf16x8 (&wf)[5]) {
#pragma unroll
    for (int t = 0; t < 5; t++)
#pragma unroll
      for (int i = 0; i < 4; i++) acc[i][t] = KV ? mfma16(xf[i], wf[t], acc[i][t]) : mfma16(wf[t], xf[i], acc[i][t]);
  };
  auto interleave = [&]() {
#pragma unroll
    for (int t = 0; t < 14; t++) { SGB(0x8, 1); SGB(0x100, 1); }
    SGB(0x8, 6);
  };
  BAR();
  TS(21);
  load(xa, wa, 0, 0);
  SBAR();
  for (int s = 0; s < 9; s++) {
    load(xb, wb, s & 3, 1); mma(xa, wa); interleave();
    SBAR();
    BAR();
    if (s == 0) TS(22);
    if (s == 4) TS(23);
    load(xa, wa, (s + 1) & 3, 0); mma(xb, wb); interleave();
    SBAR();
  }
  load(xb, wb, 1, 1); mma(xa, wa); interleave();
  SBAR();
  mma(xb, wb);
}

__device__ __forceinline__ int gemm_s0(int rt, int cg, bool isKV) { return (((rt & 3) * 5 + 1) / 2 + (isKV ? 2 * (cg & 3) : 8 + (cg & 1))) % 10; }
__device__ __forceinline__ void gemm_vx(unsigned (&vx)[2], int L, int lane, int rt) {
#pragma unroll
  for (int i = 0; i < 2; i++) vx[i] = (rt * 128 + L * 64 + i * 8 + (lane >> 3)) * 1280 + (((lane & 7) ^ ((4 * i + (lane >> 4)) & 7)) << 4);
}
__device__ __forceinline__ void gemm_first_x(int L, int lane, unsigned lb, const u16* x, int rt, int cg, bool isKV) {
  const i32x4 sx = make_srd(x, S * D * 2);
  unsigned vx[2];
  gemm_vx(vx, L, lane, rt);
  const int s0 = gemm_s0(rt, cg, isKV);
#pragma unroll
  for (int p = 0; p < 8; p++) glds16(lb + (L * 8 + p) * 1024, vx[p & 1], sx, s0 * 128 + (p >> 1) * (16 * 1280));
}
__device__ __forceinline__ void gemm_loader(int L, int lane, unsigned lb, const u16* x, const u16* wqkv, int rt, int cg, bool isKV) {
  const int s0 = gemm_s0(rt, cg, isKV);
  const i32x4 sx = make_srd(x, S * D * 2), sw = make_srd(wqkv, D * 3 * D * 2);
  unsigned vx[2], vw[5];
  gemm_vx(vx, L, lane, rt);
#pragma unroll
  for (int i = 0; i < 5; i++) {
    const int ci = i * 64 + lane, k = (ci * 3277) >> 16, pos = ci - k * 20, gc = pos ^ (((k >> 3) & 1) << 1), tl = gc >> 1;
    unsigned col;
    if (isKV) { const int isv = (tl >= 3 && tl < 5) || tl >= 7; const int tt = tl < 3 ? tl : tl < 5 ? tl - 3 : tl < 7 ? tl - 2 : tl - 5;
                col = (isv ? 2560u : 1280u) + 160u * cg + tt * 32; }
    else col = 320u * cg + tl * 32;
    vw[i] = (L * 32 + k) * 3840 + col + (gc & 1) * 16;
  }
  auto stage = [&](int st_, int buf) {
    const unsigned base = lb + buf * STG;
    int st = st_ + s0; st = st >= 10 ? st - 10 : st;
#pragma unroll
    for (int p = 0; p < 10; p++) {
      glds16(base + XS + (L * 10 + p) * 1024, vw[p % 5], sw, st * (64 * 3840) + (p / 5) * (16 * 3840));
      if (p < 8) glds16(base + (L * 8 + p) * 1024, vx[p & 1], sx, st * 128 + (p >> 1) * (16 * 1280));
    }
  };
#pragma unroll
  for (int p = 0; p < 10; p++) glds16(lb + XS + (L * 10 + p) * 1024, vw[p % 5], sw, s0 * (64 * 3840) + (p / 5) * (16 * 3840));
  stage(1, 1); stage(2, 2);
  wait_bar<36>();
  stage(3, 3);
  for (int s = 0; s < 9; s++) {
    if (s < 7) wait_bar<36>(); else if (s == 7) wait_bar<18>(); else wait_bar<0>();
    if (s < 6) stage(s + 4, s & 3);
  }
}

template <int T> struct StatTile {
  static constexpr bool isz = T >= 13;
  static constexpr int i = isz ? T - 13 : (T + 1) / 3;
  static constexpr int j = isz ? 0 : T - (i == 0 ? 0 : 3 * i - 1) + (i == 0 ? 0 : i - 1);
};
template <int T> __device__ __forceinline__ f32x4 stat_mma(unsigned la) {
  const u32x4 o1 = {0x3F803F80u, 0x3F803F80u, 0x3F803F80u, 0x3F803F80u};
  bf16x8 A[4], B[4];
#pragma unroll
  for (int m = 0; m < 4; m++) {
    A[m] = lds_b128(la + StatTile<T>::i * 1024 + m * 10240);
    B[m] = StatTile<T>::isz ? __builtin_bit_cast(bf16x8, o1) : lds_b128(la + (5 + StatTile<T>::j) * 1024 + m * 10240);
  }
  f32x4 d4 = {0.f, 0.f, 0.f, 0.f};
#pragma unroll
  for (int m = 0; m < 4; m++) d4 = mfma16(A[m], B[m], d4);
  return d4;
}
template <int T> __device__ __forceinline__ void stat_store(f32x4 d4, int lane, __amdgpu_buffer_rsrc_t rw, unsigned pbase) {
  const int fr = lane & 15, g4 = lane >> 4;
  const int Dg = 16 * StatTile<T>::i + 4 * g4, h = (Dg * 205) >> 12, dd = Dg - 20 * h;
  const int ee = StatTile<T>::isz ? 20 : 16 * StatTile<T>::j + fr - 20 * h;
  if (StatTile<T>::isz ? fr == 0 : (ee >= 0 && ee < 20))
    __builtin_amdgcn_raw_buffer_store_b64((u32x2){pk2(d4[0], d4[1]), pk2(d4[2], d4[3])}, rw, pbase + (h * 420 + ee * 20 + dd) * 2, 0, SC1);
}
template <int W> __device__ __forceinline__ void stat_tiles(unsigned lb, int lane, __amdgpu_buffer_rsrc_t rw, unsigned pbase) {
  const unsigned la = lb + lane * 16;
  const f32x4 a = stat_mma<W>(la), b = stat_mma<W + 8>(la);
  if constexpr (W < 2) { const f32x4 c = stat_mma<W + 16>(la); stat_store<W + 16>(c, lane, rw, pbase); }
  stat_store<W>(a, lane, rw, pbase);
  stat_store<W + 8>(b, lane, rw, pbase);
}

__device__ __forceinline__ void conv_load_h(u32x4 (&cur)[4], u32x4 (&prv)[4], const u16* __restrict__ hrow, int lane) {
#pragma unroll
  for (int it = 0; it < 4; it++) {
    const int i = it * 64 + lane;
    cur[it] = *(const u32x4*)(hrow + i * 8); prv[it] = *(const u32x4*)(hrow + (i > 0 ? i * 8 - 8 : 0));
  }
}
__device__ __forceinline__ void conv_prep_h(const u32x4 (&cur)[4], const u32x4 (&prv_)[4], unsigned cb, int lane) {
#pragma unroll
  for (int it = 0; it < 4; it++) {
    u32x4 prv = prv_[it];
    if (it == 0 && lane == 0) prv = (u32x4){0, 0, 0, 0};
    const unsigned W_[8] = {prv.x, prv.y, prv.z, prv.w, cur[it].x, cur[it].y, cur[it].z, cur[it].w};
    const int c = 255 - (it * 64 + lane);
#pragma unroll
    for (int e = 0; e < 4; e++) {
      u32x4 ov;
#pragma unroll
      for (int q = 0; q < 4; q++) {
        const int i0 = 15 - e - 2 * q, i1 = i0 - 1;
        const unsigned sel = ((i0 & 1) ? 0x0706u : 0x0504u) | (((i1 & 1) ? 0x0302u : 0x0100u) << 16);
        ov[q] = __builtin_amdgcn_perm(W_[i0 >> 1], W_[i1 >> 1], sel);
      }
      *(LAS u32x4*)(size_t)(cb + e * CPS + c * 16) = ov;
    }
  }
  if (lane < 8) *(LAS u32x4*)(size_t)(cb + (lane >> 1) * CPS + (256 + (lane & 1)) * 16) = (u32x4){0, 0, 0, 0};
  if (lane < 32) *(LAS u32x4*)(size_t)(cb + UOFF + lane * 16) = (u32x4){0, 0, 0, 0};
}
template <class F>
__device__ __forceinline__ void conv_load_u(__amdgpu_buffer_rsrc_t rw, int d, unsigned cb, int lane, F&& between) {
  u32x4 uv[4];
#pragma unroll
  for (int it = 0; it < 4; it++) uv[it] = __builtin_amdgcn_raw_buffer_load_b128(rw, OFF_UT + d * 4096 + (it * 64 + lane) * 16, 0, SC1);
  SBAR();
  between();
  SBAR();
#pragma unroll
  for (int it = 0; it < 4; it++) *(LAS u32x4*)(size_t)(cb + UOFF + 512 + (it * 64 + lane) * 16) = uv[it];
}
struct ConvFrags { u32x2 al[8], ah[8]; u32x4 B[8]; };
__device__ __forceinline__ void conv_read(ConvFrags& f, unsigned aa, unsigned ba) {
  asm volatile(
      "ds_read_b128 %16, %25\n\tds_read_b64 %7, %24 offset:3584\n\tds_read_b64 %15, %24 offset:3592\n\t"
      "ds_read_b128 %17, %25 offset:512\n\tds_read_b128 %18, %25 offset:1024\n\tds_read_b128 %19, %25 offset:1536\n\t"
      "ds_read_b128 %20, %25 offset:2048\n\tds_read_b128 %21, %25 offset:2560\n\tds_read_b128 %22, %25 offset:3072\n\t"
      "ds_read_b128 %23, %25 offset:3584\n\t"
      "ds_read_b64 %6, %24 offset:3072\n\tds_read_b64 %14, %24 offset:3080\n\t"
      "ds_read_b64 %5, %24 offset:2560\n\tds_read_b64 %13, %24 offset:2568\n\t"
      "ds_read_b64 %4, %24 offset:2048\n\tds_read_b64 %12, %24 offset:2056\n\t"
      "ds_read_b64 %3, %24 offset:1536\n\tds_read_b64 %11, %24 offset:1544\n\t"
      "ds_read_b64 %2, %24 offset:1024\n\tds_read_b64 %10, %24 offset:1032\n\t"
      "ds_read_b64 %1, %24 offset:512\n\tds_read_b64 %9, %24 offset:520\n\t"
      "ds_read_b64 %0, %24\n\tds_read_b64 %8, %24 offset:8"
      : "=&v"(f.al[0]), "=&v"(f.al[1]), "=&v"(f.al[2]), "=&v"(f.al[3]), "=&v"(f.al[4]), "=&v"(f.al[5]), "=&v"(f.al[6]), "=&v"(f.al[7]),
        "=&v"(f.ah[0]), "=&v"(f.ah[1]), "=&v"(f.ah[2]), "=&v"(f.ah[3]), "=&v"(f.ah[4]), "=&v"(f.ah[5]), "=&v"(f.ah[6]), "=&v"(f.ah[7]),
        "=&v"(f.B[0]), "=&v"(f.B[1]), "=&v"(f.B[2]), "=&v"(f.B[3]), "=&v"(f.B[4]), "=&v"(f.B[5]), "=&v"(f.B[6]), "=&v"(f.B[7])
      : "v"(aa), "v"(ba) : "memory");
}
__device__ __forceinline__ void conv_wait(ConvFrags& f) {
  asm volatile("s_waitcnt lgkmcnt(0)"
      : "+v"(f.al[0]), "+v"(f.al[1]), "+v"(f.al[2]), "+v"(f.al[3]), "+v"(f.al[4]), "+v"(f.al[5]), "+v"(f.al[6]), "+v"(f.al[7]),
        "+v"(f.ah[0]), "+v"(f.ah[1]), "+v"(f.ah[2]), "+v"(f.ah[3]), "+v"(f.ah[4]), "+v"(f.ah[5]), "+v"(f.ah[6]), "+v"(f.ah[7]),
        "+v"(f.B[0]), "+v"(f.B[1]), "+v"(f.B[2]), "+v"(f.B[3]), "+v"(f.B[4]), "+v"(f.B[5]), "+v"(f.B[6]), "+v"(f.B[7]) :: "memory");
}
template <int K> __device__ __forceinline__ void conv_read1(ConvFrags& f, unsigned aa, unsigned ba) {
  if constexpr (K < 8) asm volatile("ds_read_b128 %0, %1 offset:%2" : "=&v"(f.B[K]) : "v"(ba), "n"(512 * K) : "memory");
  else if constexpr (K < 16) asm volatile("ds_read_b64 %0, %1 offset:%2" : "=&v"(f.al[K - 8]) : "v"(aa), "n"(512 * (K - 8)) : "memory");
  else if constexpr (K < 24) asm volatile("ds_read_b64 %0, %1 offset:%2" : "=&v"(f.ah[K - 16]) : "v"(aa), "n"(512 * (K - 16) + 8) : "memory");
}
template <int K = 0> __device__ __forceinline__ void conv_step(f32x4 (&acc)[8], const ConvFrags& f, ConvFrags& nx, unsigned aa, unsigned ba) {
  if constexpr (K < 36) {
    constexpr int a = K < 8 ? 0 : K < 15 ? 1 : K < 21 ? 2 : K < 26 ? 3 : K < 30 ? 4 : K < 33 ? 5 : K < 35 ? 6 : 7;
    constexpr int i = K - (a == 0 ? 0 : a == 1 ? 8 : a == 2 ? 15 : a == 3 ? 21 : a == 4 ? 26 : a == 5 ? 30 : a == 6 ? 33 : 35);
    acc[a + i] = mfma16(mk8(f.al[7 - a], f.ah[7 - a]), __builtin_bit_cast(bf16x8, f.B[i]), acc[a + i]);
    conv_read1<K>(nx, aa, ba);
    SBAR();
    conv_step<K + 1>(acc, f, nx, aa, ba);
  }
}
__device__ __forceinline__ void conv_main(f32x4 (&acc)[8], unsigned cb, int lane, int c0, int c1) {
  const int fr = lane & 15, g4 = lane >> 4;
  const unsigned abase = cb + (3 - (fr & 3)) * CPS + 2 * (2044 - 16 * 112 - 16 * (g4 >> 1) + 8 * (g4 & 1) - (fr & ~3));
  const unsigned bbase = cb + UOFF + 512 + 2 * (16 * fr - 16 * (g4 >> 1) + 8 * (g4 & 1));
#pragma unroll
  for (int c = 0; c < 8; c++) acc[c] = (f32x4){0.f, 0.f, 0.f, 0.f};
  ConvFrags f0, f1;
  asm volatile("s_waitcnt lgkmcnt(0)" ::: "memory");
  conv_read(f0, abase - 64 * c0, bbase - 64 * c0);
  for (int cc = c0; cc < c1; cc += 2) {
    conv_wait(f0);
    SBAR();
    conv_step(acc, f0, f1, abase - 64 * (cc + 1), bbase - 64 * (cc + 1));
    conv_wait(f1);
    SBAR();
    const int cn = cc + 2 < c1 ? cc + 2 : cc + 1;
    conv_step(acc, f1, f0, abase - 64 * cn, bbase - 64 * cn);
  }
  conv_wait(f0);
}
__device__ __forceinline__ void conv_store(f32x4 y, unsigned cb, int c, int lane, float Dd, __amdgpu_buffer_rsrc_t rw, int d) {
  const int fr = lane & 15, g4 = lane >> 4;
  const u32x2 uu = lds_b64(cb + UOFF + 512 + 2 * (256 * c + 16 * fr + 4 * g4));
  const float r0 = fmaf(bflo(uu.x), Dd, y[0]), r1 = fmaf(bfhi(uu.x), Dd, y[1]);
  const float r2 = fmaf(bflo(uu.y), Dd, y[2]), r3 = fmaf(bfhi(uu.y), Dd, y[3]);
  __builtin_amdgcn_raw_buffer_store_b64((u32x2){pk2(r0, r1), pk2(r2, r3)}, rw, OFF_YT + d * 4096 + (256 * c + 16 * fr + 4 * g4) * 2, 0, SC1);
}

__global__ __launch_bounds__(512, 2) void fused(const u16* __restrict__ x, const u16* __restrict__ wqkv, const u16* __restrict__ wo,
                                                const u16* __restrict__ Bk, const u16* __restrict__ Bv, const u16* __restrict__ shk,
                                                const u16* __restrict__ hk, const u16* __restrict__ Dv, const u16* __restrict__ gw,
                                                u16* __restrict__ out, char* __restrict__ ws) {
  extern __shared__ __attribute__((aligned(16))) char sm[];
  const int tid = threadIdx.x, lane = tid & 63;
  const int w = __builtin_amdgcn_readfirstlane(tid >> 6);
  const int b = blockIdx.x, xcd = b & 7, r = b >> 3;
  const int fr = lane & 15, g4 = lane >> 4;
  const unsigned lb = (unsigned)(size_t)(LAS char*)sm;
  const __amdgpu_buffer_rsrc_t rw = __builtin_amdgcn_make_buffer_rsrc(ws, 0, WS_USED, 0x00020000);
  if (r < 24 && (w >> 1) == 2) {
    const bool kv_ = r < 16;
    gemm_first_x(w - 4, lane, lb, x, (xcd & 3) * 4 + (r & 3), kv_ ? (xcd >> 2) * 4 + (r >> 2) : (xcd >> 2) * 2 + ((r - 16) >> 2), kv_);
  }
  TS_DECL TS(0);
  unsigned e0 = 0;
#define PUT_TAG() do { if (w == 7 && lane == 0) { *(LAS unsigned*)(size_t)(lb + L_TAG) = e0 + 1u; *(LAS u32x4*)(size_t)(lb + L_CNT) = (u32x4){0u, 0u, 0u, 0u}; } } while (0)
#define TAG (*(const LAS unsigned*)(size_t)(lb + L_TAG))
  if (r < 24) {
    const bool isKV = r < 16;
    const int rt = (xcd & 3) * 4 + (r & 3);
    const int cg = isKV ? (xcd >> 2) * 4 + (r >> 2) : (xcd >> 2) * 2 + ((r - 16) >> 2);
    f32x4 acc[4][5];
    const int rp = (w >> 1) & 1, hf = w & 1;
    if (w < 4) {
      const unsigned xa0 = lb + (rp * 64 + fr) * 128 + ((g4 ^ ((fr >> 1) & 7)) << 4);
      const unsigned wl = lb + XS + (8 * g4 + (fr >> 2)) * 320 + ((lane & 3) >> 1) * 16 + (lane & 1) * 8 + hf * 160;
      const unsigned sgn = (g4 & 1) * 32;
      const unsigned wa0 = hf ? wl - sgn : wl + sgn, wa1 = hf ? wl + sgn : wl - sgn;
#pragma unroll
      for (int i = 0; i < 4; i++)
#pragma unroll
        for (int t = 0; t < 5; t++) acc[i][t] = (f32x4){0.f, 0.f, 0.f, 0.f};
      if (isKV) gemm_compute<true>(acc, xa0, wa0, wa1 TSARG); else gemm_compute<false>(acc, xa0, wa0, wa1 TSARG);
    } else if (w < 6) {
      gemm_loader(w - 4, lane, lb, x, wqkv, rt, cg, isKV);
    } else {
      const int w6 = w - 6;
      const char* pfa;
      {
        const int q = rt & 3, u = isKV ? (cg & 3) : 4 + (cg & 1);
        if (lane < 32) {
          const int row = 16 * q + 8 * w6 + (lane >> 2);
          const unsigned col = isKV ? ((lane & 2) ? 2560u : 1280u) + 160u * cg : 320u * cg;
          const unsigned ln = isKV ? (lane & 1) : ((lane & 3) == 3 ? 2 : (lane & 3));
          pfa = (const char*)wqkv + row * 3840 + ((col >> 7) + ln) * 128;
        } else {
          int row = u * 22 + w6 * 11 + (lane - 32); row = (lane < 43 && row < 128) ? row : u * 22;
          pfa = (const char*)x + (rt * 128 + row) * 1280;
        }
      }
      const int pfs0 = gemm_s0(rt, cg, isKV);
      auto prefetch = [&](int j) {
      };
      SBAR();
      if (w == 7) e0 = flag_ld(ws, F_E);
      if (isKV) {
        const int l16 = lane & 15, row0 = rt * 128 + cg * 16 + w6 * 8;
        const u32x4 g0 = *(const u32x4*)(gw + lane * 8), g1 = *(const u32x4*)(gw + 512 + l16 * 8);
        u32x4 gx0[8], gx1[8];
#pragma unroll
        for (int i = 0; i < 8; i++) {
          const u16* xr = x + (size_t)(row0 + i) * D;
          gx0[i] = *(const u32x4*)(xr + lane * 8); gx1[i] = *(const u32x4*)(xr + 512 + l16 * 8);
        }
        SBAR();
        BAR(); prefetch(4); prefetch(5); BAR(); prefetch(6); prefetch(7); BAR(); prefetch(8); prefetch(9);
        float a[8];
        const float m1 = lane < 16 ? 1.f : 0.f;
#pragma unroll
        for (int i = 0; i < 8; i++) {
          float a0 = 0.f, a1 = 0.f;
#pragma unroll
          for (int j = 0; j < 4; j++) {
            a0 = fmaf(bflo(gx0[i][j]), bflo(g0[j]), a0); a0 = fmaf(bfhi(gx0[i][j]), bfhi(g0[j]), a0);
            a1 = fmaf(bflo(gx1[i][j]), bflo(g1[j]), a1); a1 = fmaf(bfhi(gx1[i][j]), bfhi(g1[j]), a1);
          }
          a[i] = fmaf(a1, m1, a0);
          if (i == 3) { SBAR(); BAR(); SBAR(); }
        }
        SBAR(); BAR(); SBAR();
#pragma unroll
        for (int i = 0; i < 4; i++) { const float lo = a[i], hi = a[i + 4]; const bool up = lane & 4; a[i] = (up ? hi : lo) + __shfl_xor(up ? lo : hi, 4); }
#pragma unroll
        for (int i = 0; i < 2; i++) { const float lo = a[i], hi = a[i + 2]; const bool up = lane & 2; a[i] = (up ? hi : lo) + __shfl_xor(up ? lo : hi, 2); }
        { const float lo = a[0], hi = a[1]; const bool up = lane & 1; a[0] = (up ? hi : lo) + __shfl_xor(up ? lo : hi, 1); }
        a[0] += __shfl_xor(a[0], 8); a[0] += __shfl_xor(a[0], 16); a[0] += __shfl_xor(a[0], 32);
        if (lane < 8) __builtin_amdgcn_raw_buffer_store_b32(__float_as_uint(rbf(1.f / (1.f + __expf(-rbf(a[0]))))), rw, OFF_G + (row0 + lane) * 4, 0, SC1);
        PUT_TAG();
        SBAR();
        BAR(); BAR(); BAR(); BAR(); BAR();
      } else {
        const int hw = (xcd * 8 + (r - 16)) * 2 + w6;
        const unsigned scr = lb + L_T + w6 * 6144;
#pragma unroll
        for (int bt = 0; bt < 2; bt++) {
          const int blk = bt == 0 ? hw : (hw < 32 ? 128 + hw : hw), ct = (blk * 3277) >> 16, ks = blk - 20 * ct;
          u32x4 v[5];
#pragma unroll
          for (int i = 0; i < 5; i++) {
            const int c = i * 64 + lane, row = (c * 6554) >> 16, ch = c - 10 * row;
            v[i] = *(const u32x4*)(wo + (size_t)(32 * ks + row) * D + 80 * ct + 8 * ch);
          }
          SBAR();
          BAR();
          if (bt == 0) { PUT_TAG(); prefetch(4); prefetch(5); }
          BAR();
          if (bt == 0) { prefetch(6); prefetch(7); }
          if (bt == 0 || hw < 32) {
#pragma unroll
            for (int i = 0; i < 5; i++) *(LAS u32x4*)(size_t)(scr + (i * 64 + lane) * 16) = v[i];
            const unsigned ta = scr + (8 * g4 + (fr >> 2)) * 160 + (lane & 3) * 8;
#pragma unroll
            for (int t = 0; t < 5; t++) {
              const u32x2 lo = lds_tr(ta + 32 * t), hi = lds_tr(ta + 32 * t + 640);
              __builtin_amdgcn_raw_buffer_store_b128((u32x4){lo.x, lo.y, hi.x, hi.y}, rw, OFF_WOP + (blk * 5 + t) * 1024 + lane * 16, 0, SC1);
            }
          }
          SBAR();
          BAR();
          if (bt == 0) { prefetch(8); prefetch(9); }
          BAR();
        }
        DRAIN();
        if (lds_last(lb + L_CNT + 8, lane, 2) && lane == 0) flag_st(ws, F_W + (xcd * 8 + (r - 16)) * 128, TAG);
        BAR(); BAR();
      }
    }
    TS(1);
    BAR();
    TS(11);
    const unsigned tag = TAG;
    if (isKV) {
      if (w < 4) {
#pragma unroll
        for (int t = 0; t < 5; t++) {
          const bool isk = hf ? t < 2 : t < 3;
          const int slot = hf ? (t < 2 ? 3 + t : 5 + t) : (t < 3 ? t : 2 + t);
#pragma unroll
          for (int m = 0; m < 2; m++) {
            u32x4 f;
            if (isk) f = (u32x4){pk2(phi(acc[2 * m][t][0]), phi(acc[2 * m][t][1])), pk2(phi(acc[2 * m][t][2]), phi(acc[2 * m][t][3])),
                                 pk2(phi(acc[2 * m + 1][t][0]), phi(acc[2 * m + 1][t][1])), pk2(phi(acc[2 * m + 1][t][2]), phi(acc[2 * m + 1][t][3]))};
            else f = (u32x4){pk2(acc[2 * m][t][0], acc[2 * m][t][1]), pk2(acc[2 * m][t][2], acc[2 * m][t][3]),
                             pk2(acc[2 * m + 1][t][0], acc[2 * m + 1][t][1]), pk2(acc[2 * m + 1][t][2], acc[2 * m + 1][t][3])};
            *(LAS u32x4*)(size_t)(lb + ((rp * 2 + m) * 10 + slot) * 1024 + lane * 16) = f;
          }
        }
      } else {
        for (int i = tid - 256; i < 6048 / 16; i += 256) *(LAS u32x4*)(size_t)(lb + L_T + i * 16) = (u32x4){0, 0, 0, 0};
        if (w == 7) poll(ws, F_W + lane * 128, true, tag);
      }
      TS(12);
      BAR();
      TS(2);
      switch (w) {
        case 0: stat_tiles<0>(lb, lane, rw, OFF_P + (rt * 8 + cg) * 3360); break;
        case 1: stat_tiles<1>(lb, lane, rw, OFF_P + (rt * 8 + cg) * 3360); break;
        case 2: stat_tiles<2>(lb, lane, rw, OFF_P + (rt * 8 + cg) * 3360); break;
        case 3: stat_tiles<3>(lb, lane, rw, OFF_P + (rt * 8 + cg) * 3360); break;
        case 4: stat_tiles<4>(lb, lane, rw, OFF_P + (rt * 8 + cg) * 3360); break;
        case 5: stat_tiles<5>(lb, lane, rw, OFF_P + (rt * 8 + cg) * 3360); break;
        case 6: stat_tiles<6>(lb, lane, rw, OFF_P + (rt * 8 + cg) * 3360); break;
        default: stat_tiles<7>(lb, lane, rw, OFF_P + (rt * 8 + cg) * 3360); break;
      }
      TS(14);
      DRAIN();
      TS(15);
      BAR();
      TS(3);
      if (w == 0) {
        if (lane == 0) flag_st(ws, F_P + (rt * 8 + cg) * 128, tag);
        poll(ws, lane < 16 ? F_P + (lane * 8 + cg) * 128 : F_PQ + (rt * 4 + (cg >> 1)) * 128, lane <= 16, tag);
      } else if (w >= 4) {
        const i32x4 sws = make_srd(ws, WS_USED);
        const unsigned vwv = OFF_WOP + ((xcd >> 2) * 4 + (r >> 3)) * 102400 + lane * 16;
        for (int i = w - 4; i < 100; i += 4) {
          int pc = 12 * (r & 7) + i; pc = __builtin_amdgcn_readfirstlane(pc >= 100 ? pc - 100 : pc);
          glds16_sc1(lb + pc * 1024, vwv, sws, pc * 1024);
        }
      }
      constexpr int PJ[9] = {0, 1, 1, 2, 2, 3, 3, 4, 4}, PK[9] = {0, 0, 1, 0, 1, 1, 2, 1, 2};
      unsigned an[9], ad[9];
#pragma unroll
      for (int p = 0; p < 9; p++) {
        const int E = 16 * PJ[p] + fr, h = (E * 205) >> 12, e = E - 20 * h;
        int o_ = 32 * PK[p] + 8 * g4 - 20 * h; o_ = o_ < -8 ? -8 : o_ > 20 ? 20 : o_;
        an[p] = lb + L_T + (h * 21 + e) * 72 + (8 + o_) * 2; ad[p] = lb + L_T + (h * 21 + 20) * 72 + (8 + o_) * 2;
      }
      BAR();
      TS(4);
      bf16x8 Bq[3];
      {
        const unsigned prow = OFF_PQ + ((rt * 8 + w) * 8 + cg) * 3072 + lane * 16;
#pragma unroll
        for (int ks = 0; ks < 3; ks++) Bq[ks] = __builtin_bit_cast(bf16x8, __builtin_amdgcn_raw_buffer_load_b128(rw, prow + 1024 * ks, 0, SC1));
      }
      if (tid < 210) {
        const unsigned pb = OFF_P + cg * 3360 + tid * 16;
        u32x4 v[16];
#pragma unroll
        for (int k = 0; k < 16; k++) v[k] = __builtin_amdgcn_raw_buffer_load_b128(rw, pb, (k ^ rt) * (8 * 3360), SC1);
        unsigned o4[4];
#pragma unroll
        for (int c = 0; c < 4; c++) {
          float lo[16], hi[16];
#pragma unroll
          for (int k = 0; k < 16; k++) { lo[k] = bflo(v[k][c]); hi[k] = bfhi(v[k][c]); }
#pragma unroll
          for (int st = 1; st < 16; st <<= 1)
#pragma unroll
            for (int k = 0; k < 16; k += 2 * st) { lo[k] += lo[k + st]; hi[k] += hi[k + st]; }
          o4[c] = pk2(lo[0], hi[0]);
        }
        TS(16);
#pragma unroll
        for (int q = 0; q < 2; q++) {
          const int i4 = 2 * tid + q, row = (i4 * 13108) >> 16, d = 4 * (i4 - row * 5);
          *(LAS u32x2*)(size_t)(lb + L_T + row * 72 + (8 + d) * 2) = (u32x2){o4[2 * q], o4[2 * q + 1]};
        }
      }
      BAR();
      TS(5);
      {
        if (g4 >= 2) Bq[2] = __builtin_bit_cast(bf16x8, (u32x4){0, 0, 0, 0});
        f32x4 nu[5], de[5];
#pragma unroll
        for (int j = 0; j < 5; j++) { nu[j] = (f32x4){0.f, 0.f, 0.f, 0.f}; de[j] = nu[j]; }
        bf16x8 An[9], Ad[9];
#pragma unroll
        for (int p = 0; p < 9; p++) {
          unsigned an8 = an[p] + 8, ad8 = ad[p] + 8;
          asm volatile("" : "+v"(an8), "+v"(ad8));
          An[p] = mk8(lds_b64(an[p]), lds_b64(an8)); Ad[p] = mk8(lds_b64(ad[p]), lds_b64(ad8));
        }
#pragma unroll
        for (int p = 0; p < 9; p++) { de[PJ[p]] = mfma16(Ad[p], Bq[PK[p]], de[PJ[p]]); nu[PJ[p]] = mfma16(An[p], Bq[PK[p]], nu[PJ[p]]); }
        u32x2 ov[5];
#pragma unroll
        for (int j = 0; j < 5; j++) {
          const float rd = __builtin_amdgcn_rcpf(de[j][0] + 1e-6f);
          ov[j] = (u32x2){pk2(nu[j][0] * rd, nu[j][1] * rd), pk2(nu[j][2] * rd, nu[j][3] * rd)};
        }
        store_frags(ov, rw, OFF_O + (rt * 8 + w) * 20480, 80 * cg, fr, g4);
      }
      TS(17);
      DRAIN();
      BAR();
      if (tid == 0) flag_st(ws, F_O + (rt * 8 + cg) * 128, tag);
      TS(6);
    } else {
      const int vb = xcd * 8 + (r - 16);
      const int d = 80 * (vb >> 3) + 48 + 4 * (vb & 7) + (w & 3);
      const unsigned cb = lb + (w & 3) * CHS;
      if (w < 4) {
#pragma unroll
        for (int i = 0; i < 4; i++) {
          u32x2 pv[5];
#pragma unroll
          for (int t = 0; t < 5; t++) pv[t] = (u32x2){pk2(phi(acc[i][t][0]), phi(acc[i][t][1])), pk2(phi(acc[i][t][2]), phi(acc[i][t][3]))};
          store_frags(pv, rw, OFF_PQ + ((rt * 8 + rp * 4 + i) * 8 + cg * 2 + hf) * 3072, 0, fr, g4);
        }
      } else {
        u32x4 hc[4], hp[4];
        conv_load_h(hc, hp, hk + (size_t)d * S, lane);
        poll(ws, F_U + lane * 128, true, tag);
        conv_load_u(rw, d, cb, lane, [&]() { conv_prep_h(hc, hp, cb, lane); });
      }
      TS(12);
      DRAIN();
      BAR();
      TS(2);
      if (tid == 0) flag_st(ws, F_PQ + (rt * 4 + cg) * 128, tag);
      if (w < 4) {
        const float Dd = bflo(Dv[d]);
        TS(3);
        f32x4 y[8];
        conv_main(y, cb, lane, 0, 8);
        TS(4);
#pragma unroll
        for (int c = 0; c < 8; c++) conv_store(y[c], cb, c, lane, Dd, rw, d);
        DRAIN();
      } else if (w == 7) {
        poll(ws, F_W + lane * 128, true, tag);
      }
      BAR();
      if (tid == 0) flag_st(ws, F_Y + (64 + vb) * 128, tag);
      TS(5); TS(6);
    }
  } else {
    const int pw = xcd * 8 + (r - 24);
    const int c6 = w < 4 ? w : 4 + ((w - 4) >> 1);
    const int d = 80 * (pw >> 3) + 6 * (pw & 7) + c6;
    const unsigned cb = lb + c6 * CHS;
    const bool prep = w < 4 || !(w & 1);
    u32x4 hc[4], hp[4];
    {
      const int rbase = xcd * 256 + ((r - 24) >> 1) * 64, cbase = ((r - 24) & 1) * 320;
      u32x4 kr[2][7], vr[2][4], sp[2][4];
#pragma unroll
      for (int q = 0; q < 2; q++) {
        const int it = w + 8 * q;
        if (q == 0 || w < 2) {
          const int ch = cbase + (it >> 1) * 64 + (lane & 7) * 8;
          const int s0 = rbase + (it & 1) * 32 + (lane >> 3) * 4;
#pragma unroll
          for (int j = 0; j < 7; j++) { int s = s0 - 3 + j; kr[q][j] = *(const u32x4*)(Bk + (size_t)(s < 0 ? 0 : s) * D + ch); }
#pragma unroll
          for (int j = 0; j < 4; j++) vr[q][j] = *(const u32x4*)(Bv + (size_t)(s0 + j) * D + ch);
#pragma unroll
          for (int j = 0; j < 4; j++) sp[q][j] = ((const u32x4*)(shk + ch * 4))[j];
        }
      }
      if (prep) conv_load_h(hc, hp, hk + (size_t)d * S, lane);
      if (w == 7) e0 = flag_ld(ws, F_E);
      SBAR();
#pragma unroll
      for (int q = 0; q < 2; q++) {
        const int it = w + 8 * q;
        if (q == 0 || w < 2) {
          const int s0 = rbase + (it & 1) * 32 + (lane >> 3) * 4;
          if (s0 == 0) { kr[q][0] = (u32x4){0, 0, 0, 0}; kr[q][1] = kr[q][0]; kr[q][2] = kr[q][0]; }
          const int rg = (it & 1) * 8 + (lane >> 3);
          const unsigned la = lb + L_UIMG + ((it >> 1) * 64 + (lane & 7) * 8) * 128 + (((rg >> 1) ^ (lane & 7)) << 4) + (rg & 1) * 8;
#pragma unroll
          for (int c = 0; c < 8; c++) {
            const unsigned s01 = sp[q][c >> 1][(c & 1) * 2], s23 = sp[q][c >> 1][(c & 1) * 2 + 1];
            const float w0 = bflo(s01), w1 = bfhi(s01), w2 = bflo(s23), w3 = bfhi(s23);
            float kk[7], u[4];
#pragma unroll
            for (int j = 0; j < 7; j++) { unsigned dd = kr[q][j][c >> 1]; kk[j] = (c & 1) ? bfhi(dd) : bflo(dd); }
#pragma unroll
            for (int j = 0; j < 4; j++) {
              unsigned dd = vr[q][j][c >> 1]; float v = (c & 1) ? bfhi(dd) : bflo(dd);
              float ks = kk[j] * w3; ks = fmaf(kk[j + 1], w2, ks); ks = fmaf(kk[j + 2], w1, ks); ks = fmaf(kk[j + 3], w0, ks);
              u[j] = ks * v;
            }
            *(LAS u32x2*)(size_t)(la + c * 128) = (u32x2){pk2(u[0], u[1]), pk2(u[2], u[3])};
          }
        }
      }
      PUT_TAG();
      BAR();
#pragma unroll
      for (int i = 0; i < 5; i++) {
        const int chl = (w * 5 + i) * 8 + (lane >> 3), k = lane & 7;
        const u32x4 v = *(const LAS u32x4*)(size_t)(lb + L_UIMG + chl * 128 + ((k ^ ((chl >> 3) & 7)) << 4));
        __builtin_amdgcn_raw_buffer_store_b128(v, rw, OFF_UT + (cbase + chl) * 4096 + rbase * 2 + k * 16, 0, SC1);
      }
      TS(1);
    }
    if (w < 5 && prep) conv_prep_h(hc, hp, cb, lane);
    DRAIN();
    BAR();
    TS(2);
    const unsigned tag = TAG;
    if (tid == 0) flag_st(ws, F_U + pw * 128, tag);
    const float Dd = bflo(Dv[d]);
    if (prep) {
      if (w == 6) conv_prep_h(hc, hp, cb, lane);
      poll(ws, F_U + lane * 128, true, tag);
      conv_load_u(rw, d, cb, lane, [&]() {});
    }
    BAR();
    TS(3);
    f32x4 y[8];
    conv_main(y, cb, lane, w < 4 ? 0 : (w & 1) * 4, w < 4 ? 8 : (w & 1) * 4 + 4);
    TS(4);
    if (w < 4) {
#pragma unroll
      for (int c = 0; c < 8; c++) conv_store(y[c], cb, c, lane, Dd, rw, d);
    }
    BAR();
    if (w >= 4) {
#pragma unroll
      for (int c = 0; c < 4; c++) { const f32x4 lo = y[c], hi = y[4 + c]; *(LAS f32x4*)(size_t)(cb + ((w & 1) * 4 + c) * 1024 + lane * 16) = (w & 1) ? lo : hi; }
    }
    BAR();
    if (w >= 4) {
#pragma unroll
      for (int c = 0; c < 4; c++) {
        const f32x4 o_ = *(const LAS f32x4*)(size_t)(cb + (((w & 1) ^ 1) * 4 + c) * 1024 + lane * 16);
        const f32x4 lo = y[c], hi = y[4 + c];
        const f32x4 mine = (w & 1) ? hi : lo;
        conv_store((w & 1) ? o_ + mine : mine + o_, cb, (w & 1) * 4 + c, lane, Dd, rw, d);
      }
    }
    if (w == 0) poll(ws, F_W + lane * 128, true, tag);
    DRAIN();
    BAR();
    if (tid == 0) flag_st(ws, F_Y + pw * 128, tag);
    TS(5); TS(6);
  }

  {
    const unsigned tag = TAG;
    const int rt = (xcd & 3) * 8 + (r & 7), ct = (xcd >> 2) * 4 + (r >> 3);
    const int sw_ = w & 3, kk = w >> 2;
    const i32x4 sws = make_srd(ws, WS_USED);
    TS(18);
    const int srow = rt * 64 + sw_ * 16 + fr;
    u32x2 xv[3];
#pragma unroll
    for (int t = 0; t < 3; t++) { const int tt = kk ? (t < 2 ? 3 + t : 4) : t; xv[t] = *(const u32x2*)(x + (size_t)srow * D + ct * 80 + 16 * tt + 4 * g4); }
    SBAR();
    const int rot = ((ct & 3) * 5) >> 1;
    const unsigned wa0 = lb + kk * 5120 + lane * 16;
    bf16x8 wf0[5][5];
    auto read_wf0 = [&]() {
#pragma unroll
      for (int s = 0; s < 5; s++) {
        int q = s + rot; q = q >= 10 ? q - 10 : q;
#pragma unroll
        for (int t = 0; t < 5; t++) wf0[s][t] = lds_b128(wa0 + q * 10240 + t * 1024);
      }
    };
    if (r < 16) read_wf0();
    SBAR();
    if (w == 0) {
      poll(ws, F_O + ((rt >> 1) * 8 + (lane & 7)) * 128, lane < 8, tag);
    } else if (w == 7) {
      poll(ws, F_Y + ((lane & 8 ? 64 : 0) + ct * 8 + (lane & 7)) * 128, lane < 16, tag);
    } else {
      if (r >= 16) {
        const unsigned vwv = OFF_WOP + ct * 102400 + lane * 16;
        for (int i = w - 1; i < 100; i += 6) { int pc = 12 * (r & 7) + i; pc = __builtin_amdgcn_readfirstlane(pc >= 100 ? pc - 100 : pc); glds16_sc1(lb + pc * 1024, vwv, sws, pc * 1024); }
      }
      if (w == 6 && b == 255) {
        for (int spins = 0; spins < (1 << 17); spins++) {
          const unsigned a0 = flag_ld(ws, F_U + lane * 128), a1 = flag_ld(ws, F_U + (64 + lane) * 128);
          const unsigned a2 = flag_ld(ws, F_U + (128 + lane) * 128), a3 = flag_ld(ws, F_U + (192 + lane) * 128);
          if (__all(a0 == tag && a1 == tag && a2 == tag && a3 == tag)) break;
          __builtin_amdgcn_s_sleep(2);
        }
        if (lane == 0) flag_st(ws, F_E, tag);
      }
      DRAIN();
    }
    BAR();
    TS(7);
    asm volatile("" :: "v"(xv[0]), "v"(xv[1]), "v"(xv[2]));
    bf16x8 of[10];
    {
      const unsigned vo = OFF_O + (rt * 4 + sw_) * 20480 + kk * 1024 + lane * 16;
#pragma unroll
      for (int s = 0; s < 10; s++) { int q = s + rot; q = __builtin_amdgcn_readfirstlane(q >= 10 ? q - 10 : q); bload16_sc1(of[s], vo, sws, q * 2048); }
    }
    if (w == 7) {
      for (int pc = 0; pc < 10; pc++)
        glds16_sc1(lb + L_Y + pc * 1024, OFF_YT + (ct * 80 + pc * 8 + (lane >> 3)) * 4096 + rt * 128 + (lane & 7) * 16, sws, 0);
    }
    float g;
    asm volatile("buffer_load_dword %0, %1, %2, 0 offen sc1" : "=v"(g) : "v"(OFF_G + srow * 4), "s"(sws) : "memory");
    TS(8);
    if (r >= 16) read_wf0();
    f32x4 acc[5];
#pragma unroll
    for (int t = 0; t < 5; t++) acc[t] = (f32x4){0.f, 0.f, 0.f, 0.f};
    {
      bf16x8 wf1[5][5];
#pragma unroll
      for (int s = 0; s < 5; s++) {
        int q = 5 + s + rot; q = q >= 10 ? q - 10 : q;
#pragma unroll
        for (int t = 0; t < 5; t++) wf1[s][t] = lds_b128(wa0 + q * 10240 + t * 1024);
      }
      SBAR();
      if (w == 7) asm volatile("s_waitcnt vmcnt(16)" ::: "memory"); else asm volatile("s_waitcnt vmcnt(6)" ::: "memory");
      asm volatile("" : "+v"(of[0]), "+v"(of[1]), "+v"(of[2]), "+v"(of[3]), "+v"(of[4]) :: "memory");
      SBAR();
#pragma unroll
      for (int s = 0; s < 5; s++)
#pragma unroll
        for (int t = 0; t < 5; t++) acc[t] = mfma16(wf0[s][t], of[s], acc[t]);
      SBAR();
      if (w == 7) asm volatile("s_waitcnt vmcnt(11)" ::: "memory"); else asm volatile("s_waitcnt vmcnt(1)" ::: "memory");
      asm volatile("" : "+v"(of[5]), "+v"(of[6]), "+v"(of[7]), "+v"(of[8]), "+v"(of[9]) :: "memory");
      SBAR();
#pragma unroll
      for (int s = 0; s < 5; s++)
#pragma unroll
        for (int t = 0; t < 5; t++) acc[t] = mfma16(wf1[s][t], of[5 + s], acc[t]);
      SBAR();
    }
    TS(9);
    const unsigned ra = lb + L_RED + (sw_ * 5) * 1024 + lane * 16;
    if (kk == 0) { *(LAS f32x4*)(size_t)(ra + 3 * 1024) = acc[3]; *(LAS f32x4*)(size_t)(ra + 4 * 1024) = acc[4]; }
    else { *(LAS f32x4*)(size_t)(ra) = acc[0]; *(LAS f32x4*)(size_t)(ra + 1024) = acc[1]; *(LAS f32x4*)(size_t)(ra + 2048) = acc[2]; }
    asm volatile("s_waitcnt vmcnt(0)" : "+v"(g) :: "memory");
    BAR();
    TS(20);
    const float omg = 1.f - g;
    const int nt = kk ? 2 : 3;
#pragma unroll
    for (int t = 0; t < 3; t++) {
      if (t < nt) {
        const int tt = kk ? 3 + t : t;
        const f32x4 a0 = acc[t], a1 = acc[3 + (t < 2 ? t : 1)];
        f32x4 a = *(const LAS f32x4*)(size_t)(ra + tt * 1024) + (kk ? a1 : a0);
        u32x2 yv = lds_tr(lb + L_Y + (16 * tt + 4 * g4 + (fr >> 2)) * 128 + (sw_ * 16 + 4 * (fr & 3)) * 2);
        float xs[4] = {bflo(xv[t].x), bfhi(xv[t].x), bflo(xv[t].y), bfhi(xv[t].y)};
        float ys[4] = {bflo(yv.x), bfhi(yv.x), bflo(yv.y), bfhi(yv.y)};
        float r_[4];
#pragma unroll
        for (int q = 0; q < 4; q++) r_[q] = fmaf(g, a[q], omg * (xs[q] * ys[q]));
        *(u32x2*)(out + (size_t)srow * D + ct * 80 + 16 * tt + 4 * g4) = (u32x2){pk2(r_[0], r_[1]), pk2(r_[2], r_[3])};
      }
    }
    TS(10); TS_OUT(21);
  }
}

extern "C" void kernel_launch(void* const* d_in, const int* in_sizes, int n_in, void* d_out, int out_size, void* d_ws, size_t ws_size,
                              hipStream_t stream) {
  const u16* x = (const u16*)d_in[0]; const u16* wqkv = (const u16*)d_in[1]; const u16* wo = (const u16*)d_in[2];
  const u16* Bk = (const u16*)d_in[4]; const u16* Bv = (const u16*)d_in[5]; const u16* shk = (const u16*)d_in[6];
  const u16* hk = (const u16*)d_in[7]; const u16* Dv = (const u16*)d_in[8]; const u16* gw = (const u16*)d_in[9];
  (void)hipFuncSetAttribute((const void*)fused, hipFuncAttributeMaxDynamicSharedMemorySize, LDS_TOTAL);
  fused<<<256, 512, LDS_TOTAL, stream>>>(x, wqkv, wo, Bk, Bv, shk, hk, Dv, gw, (u16*)d_out, (char*)d_ws);
}
```

```cpp
#include <hip/hip_runtime.h>
#include <hip/hip_bf16.h>
#include <type_traits>
#define NO_PF

typedef unsigned short u16;
typedef __bf16 bf16x2 __attribute__((ext_vector_type(2)));
typedef __bf16 bf16x8 __attribute__((ext_vector_type(8)));
typedef float f32x4 __attribute__((ext_vector_type(4)));
typedef unsigned u32x4 __attribute__((ext_vector_type(4)));
typedef unsigned u32x2 __attribute__((ext_vector_type(2)));
typedef short v4i16_t __attribute__((ext_vector_type(4)));
typedef int i32x4 __attribute__((ext_vector_type(4)));
typedef __attribute__((address_space(1))) unsigned gu32;
#define LAS __attribute__((address_space(3)))
#define SBAR() __builtin_amdgcn_sched_barrier(0)
#define SGB(mask, n) __builtin_amdgcn_sched_group_barrier(mask, n, 0)
#define BAR() asm volatile("s_waitcnt lgkmcnt(0)\n\ts_barrier" ::: "memory")
#define DRAIN() asm volatile("s_waitcnt vmcnt(0)" ::: "memory")
#define SC1 16

#define TS_DECL
#define TSARG
#define TSPAR
#define TS(i)
#define TS_OUT(n)

constexpr int S = 2048, D = 640;
constexpr unsigned SZ = S * D * 2;
constexpr unsigned F_E = 0, F_U = 128, F_PQ = F_U + 64 * 128, F_P = F_PQ + 64 * 128, F_O = F_P + 128 * 128, F_Y = F_O + 128 * 128, F_W = F_Y + 128 * 128;
constexpr unsigned OFF_PQ = 0x20000, OFF_P = OFF_PQ + 128 * 8 * 3 * 1024, OFF_UT = OFF_P + (1u << 19), OFF_G = OFF_UT + SZ, OFF_WOP = OFF_G + 8192,
                   OFF_O = OFF_WOP + (1u << 20), OFF_YT = OFF_O + SZ, WS_USED = OFF_YT + SZ;
constexpr int XS = 16384, WSB = 20480, STG = XS + WSB;
constexpr int L_EXTRA = 4 * STG;
constexpr int L_TAG = L_EXTRA, L_CNT = L_EXTRA + 16, L_T = L_EXTRA + 64;
constexpr int L_SCR = L_T + 6144 + 5120;
constexpr int LDS_TOTAL = L_SCR + 256;
constexpr int CPS = 4160, UOFF = 4 * CPS, CHS = UOFF + 4608;
constexpr int L_UIMG = 5 * CHS;
constexpr int L_Y = 102400, L_RED = L_Y + 10240;

__device__ __forceinline__ unsigned pk2(float a, float b) { bf16x2 r = {(__bf16)a, (__bf16)b}; return __builtin_bit_cast(unsigned, r); }
__device__ __forceinline__ float rbf(float a) { return (float)(__bf16)a; }
__device__ __forceinline__ float bflo(unsigned v) { return __uint_as_float(v << 16); }
__device__ __forceinline__ float bfhi(unsigned v) { return __uint_as_float(v & 0xffff0000u); }
__device__ __forceinline__ float phi(float t) {
  return __builtin_amdgcn_fmed3f(t, 0.f, 3.0e38f) + (__builtin_amdgcn_fmed3f(__builtin_amdgcn_exp2f(t * 1.44269504f), 0.f, 1.f) + 1e-6f);
}
typedef float f32x2 __attribute__((ext_vector_type(2)));
__device__ __forceinline__ unsigned phi_pk(float a, float b) {
  const f32x2 t = {a, b};
  const f32x2 y = t * (f32x2){1.44269504f, 1.44269504f};
  const f32x2 e = {__builtin_amdgcn_fmed3f(__builtin_amdgcn_exp2f(y.x), 0.f, 1.f), __builtin_amdgcn_fmed3f(__builtin_amdgcn_exp2f(y.y), 0.f, 1.f)};
  const f32x2 m = {__builtin_amdgcn_fmed3f(a, 0.f, 3.0e38f), __builtin_amdgcn_fmed3f(b, 0.f, 3.0e38f)};
  const f32x2 r = m + (e + (f32x2){1e-6f, 1e-6f});
  return pk2(r.x, r.y);
}
__device__ __forceinline__ i32x4 make_srd(const void* p, unsigned bytes) {
  unsigned long long a = (unsigned long long)p; i32x4 r;
  r.x = __builtin_amdgcn_readfirstlane((int)(unsigned)a); r.y = __builtin_amdgcn_readfirstlane((int)(unsigned)(a >> 32));
  r.z = __builtin_amdgcn_readfirstlane((int)bytes); r.w = 0x00020000; return r;
}
__device__ __forceinline__ void glds16(unsigned ldsaddr, unsigned voff, i32x4 srd, unsigned soff) {
  asm volatile("s_mov_b32 m0, %0\n\ts_nop 0\n\tbuffer_load_dwordx4 %1, %2, %3 offen lds" :: "s"(ldsaddr), "v"(voff), "s"(srd), "s"(soff) : "memory");
}
__device__ __forceinline__ void glds16_sc1(unsigned ldsaddr, unsigned voff, i32x4 srd, unsigned soff) {
  asm volatile("s_mov_b32 m0, %0\n\ts_nop 0\n\tbuffer_load_dwordx4 %1, %2, %3 offen sc1 lds" :: "s"(ldsaddr), "v"(voff), "s"(srd), "s"(soff) : "memory");
}
__device__ __forceinline__ void bload16_sc1(bf16x8& dst, unsigned voff, i32x4 srd, unsigned soff) {
  asm volatile("buffer_load_dwordx4 %0, %1, %2, %3 offen sc1" : "=v"(dst) : "v"(voff), "s"(srd), "s"(soff) : "memory");
}
template <int N> __device__ __forceinline__ void wait_bar() { asm volatile("s_waitcnt vmcnt(%0) lgkmcnt(0)\n\ts_barrier" :: "n"(N) : "memory"); }
__device__ __forceinline__ bf16x8 lds_b128(unsigned addr) { return *(const LAS bf16x8*)(size_t)addr; }
__device__ __forceinline__ u32x2 lds_b64(unsigned addr) { return *(const LAS u32x2*)(size_t)addr; }
__device__ __forceinline__ u32x2 lds_tr(unsigned addr) { return __builtin_bit_cast(u32x2, __builtin_amdgcn_ds_read_tr16_b64_v4i16((LAS v4i16_t*)(size_t)addr)); }
__device__ __forceinline__ bf16x8 mk8(u32x2 lo, u32x2 hi) { u32x4 t = {lo.x, lo.y, hi.x, hi.y}; return __builtin_bit_cast(bf16x8, t); }
__device__ __forceinline__ f32x4 mfma16(bf16x8 a, bf16x8 b, f32x4 c) { return __builtin_amdgcn_mfma_f32_16x16x32_bf16(a, b, c, 0, 0, 0); }

__device__ __forceinline__ u32x4 pair16(u32x2 a, u32x2 b) {
  const auto r0 = __builtin_amdgcn_permlane16_swap(a.x, b.x, false, false);
  const auto r1 = __builtin_amdgcn_permlane16_swap(a.y, b.y, false, false);
  return (u32x4){r0[0], r1[0], r0[1], r1[1]};
}
__device__ __forceinline__ void store_frags(const u32x2 (&v)[5], __amdgpu_buffer_rsrc_t rw, unsigned base, int k0, int fr, int g4) {
#pragma unroll
  for (int j = 0; j < 4; j += 2) {
    const int k = k0 + 16 * (j + (g4 & 1)) + 8 * (g4 >> 1);
    __builtin_amdgcn_raw_buffer_store_b128(pair16(v[j], v[j + 1]), rw, base + (k >> 5) * 1024 + (((k >> 3) & 3) * 16 + fr) * 16, 0, SC1);
  }
  const int k = k0 + 64 + 4 * g4;
  __builtin_amdgcn_raw_buffer_store_b64(v[4], rw, base + (k >> 5) * 1024 + (((k >> 3) & 3) * 16 + fr) * 16 + ((k >> 2) & 1) * 8, 0, SC1);
}

__device__ __forceinline__ void lds_sync(unsigned cnt_addr, int lane, unsigned n) {
  asm volatile("s_waitcnt lgkmcnt(0)" ::: "memory");
  if (lane == 0) __hip_atomic_fetch_add((LAS unsigned*)(size_t)cnt_addr, 1u, __ATOMIC_RELAXED, __HIP_MEMORY_SCOPE_WORKGROUP);
  while (__builtin_amdgcn_readfirstlane(__hip_atomic_load((LAS unsigned*)(size_t)cnt_addr, __ATOMIC_RELAXED, __HIP_MEMORY_SCOPE_WORKGROUP)) < n) {}
  asm volatile("" ::: "memory");
}
__device__ __forceinline__ bool lds_last(unsigned cnt_addr, int lane, unsigned n) {
  unsigned old = 0;
  if (lane == 0) old = __hip_atomic_fetch_add((LAS unsigned*)(size_t)cnt_addr, 1u, __ATOMIC_RELAXED, __HIP_MEMORY_SCOPE_WORKGROUP);
  return __builtin_amdgcn_readfirstlane(old) == n - 1;
}

__device__ __forceinline__ unsigned flag_ld(const char* ws, unsigned off) { return __hip_atomic_load((const gu32*)(ws + off), __ATOMIC_RELAXED, __HIP_MEMORY_SCOPE_AGENT); }
__device__ __forceinline__ void flag_st(char* ws, unsigned off, unsigned v) { __hip_atomic_store((gu32*)(ws + off), v, __ATOMIC_RELAXED, __HIP_MEMORY_SCOPE_AGENT); }
__device__ __forceinline__ void poll(const char* ws, unsigned off, bool act, unsigned tag) {
  const unsigned o0 = __builtin_amdgcn_readfirstlane(off);
  off = act ? off : o0;
  unsigned a, b, n = 1u << 17;
  asm volatile(
      "global_load_dword %0, %3, %4 sc1\n\t"
      "s_sleep 6\n"
      "1:\n\t"
      "global_load_dword %1, %3, %4 sc1\n\t"
      "s_waitcnt vmcnt(1)\n\t"
      "v_cmp_ne_u32 vcc, %0, %5\n\t"
      "s_cbranch_vccz 2f\n\t"
      "global_load_dword %0, %3, %4 sc1\n\t"
      "s_waitcnt vmcnt(1)\n\t"
      "v_cmp_ne_u32 vcc, %1, %5\n\t"
      "s_cbranch_vccz 2f\n\t"
      "s_sub_u32 %2, %2, 1\n\t"
      "s_cbranch_scc0 1b\n"
      "2:\n\t"
      "s_waitcnt vmcnt(0)"
      : "=&v"(a), "=&v"(b), "+s"(n) : "v"(off), "s"(ws), "v"(tag) : "vcc", "scc", "memory");
}

template <bool KV>
__device__ __forceinline__ void gemm_compute(f32x4 (&acc)[4][5], unsigned xa0, unsigned wa0, unsigned wa1 TSPAR) {
  bf16x8 xa[4], xb[4], wa[5], wb[5];
  auto load = [&](bf16x8 (&xf)[4], bf16x8 (&wf)[5], int buf, int kk) {
    const unsigned bo = buf * STG;
    const unsigned xa = (xa0 + bo) ^ (kk * 64);
    const unsigned a0 = wa0 + bo + kk * 10240, a1 = wa1 + bo + kk * 10240;
#pragma unroll
    for (int i = 0; i < 4; i++) xf[i] = lds_b128(xa + 2048 * i);
#pragma unroll
    for (int t = 0; t < 5; t++) { const unsigned a = ((t & 1) ? a1 : a0) + 32 * t; wf[t] = mk8(lds_tr(a), lds_tr(a + 1280)); }
  };
  auto mma = [&](const bf16x8 (&xf)[4], const bf16x8 (&wf)[5]) {
#pragma unroll
    for (int t = 0; t < 5; t++)
#pragma unroll
      for (int i = 0; i < 4; i++) acc[i][t] = KV ? mfma16(xf[i], wf[t], acc[i][t]) : mfma16(wf[t], xf[i], acc[i][t]);
  };
  auto interleave = [&]() {
#pragma unroll
    for (int t = 0; t < 14; t++) { SGB(0x8, 1); SGB(0x100, 1); }
    SGB(0x8, 6);
  };
  BAR();
  TS(21);
  load(xa, wa, 0, 0);
  SBAR();
  for (int s = 0; s < 9; s++) {
    load(xb, wb, s & 3, 1); mma(xa, wa); interleave();
    SBAR();
    BAR();
    if (s == 0) TS(22);
    if (s == 4) TS(23);
    load(xa, wa, (s + 1) & 3, 0); mma(xb, wb); interleave();
    SBAR();
  }
  load(xb, wb, 1, 1); mma(xa, wa); interleave();
  SBAR();
  mma(xb, wb);
}

__device__ __forceinline__ int gemm_s0(int rt, int cg, bool isKV) { return (((rt & 3) * 5 + 1) / 2 + (isKV ? 2 * (cg & 3) : 8 + (cg & 1))) % 10; }
__device__ __forceinline__ void gemm_vx(unsigned (&vx)[2], int L, int lane, int rt) {
#pragma unroll
  for (int i = 0; i < 2; i++) vx[i] = (rt * 128 + L * 64 + i * 8 + (lane >> 3)) * 1280 + (((lane & 7) ^ ((4 * i + (lane >> 4)) & 7)) << 4);
}
__device__ __forceinline__ void gemm_first_x(int L, int lane, unsigned lb, const u16* x, int rt, int cg, bool isKV) {
  const i32x4 sx = make_srd(x, S * D * 2);
  unsigned vx[2];
  gemm_vx(vx, L, lane, rt);
  const int s0 = gemm_s0(rt, cg, isKV);
#pragma unroll
  for (int p = 0; p < 8; p++) glds16(lb + (L * 8 + p) * 1024, vx[p & 1], sx, s0 * 128 + (p >> 1) * (16 * 1280));
}
__device__ __forceinline__ void gemm_loader(int L, int lane, unsigned lb, const u16* x, const u16* wqkv, int rt, int cg, bool isKV) {
  const int s0 = gemm_s0(rt, cg, isKV);
  const i32x4 sx = make_srd(x, S * D * 2), sw = make_srd(wqkv, D * 3 * D * 2);
  unsigned vx[2], vw[5];
  gemm_vx(vx, L, lane, rt);
#pragma unroll
  for (int i = 0; i < 5; i++) {
    const int ci = i * 64 + lane, k = (ci * 3277) >> 16, pos = ci - k * 20, gc = pos ^ (((k >> 3) & 1) << 1), tl = gc >> 1;
    unsigned col;
    if (isKV) { const int isv = (tl >= 3 && tl < 5) || tl >= 7; const int tt = tl < 3 ? tl : tl < 5 ? tl - 3 : tl < 7 ? tl - 2 : tl - 5;
                col = (isv ? 2560u : 1280u) + 160u * cg + tt * 32; }
    else col = 320u * cg + tl * 32;
    vw[i] = (L * 32 + k) * 3840 + col + (gc & 1) * 16;
  }
  auto stage = [&](int st_, int buf) {
    const unsigned base = lb + buf * STG;
    int st = st_ + s0; st = st >= 10 ? st - 10 : st;
#pragma unroll
    for (int p = 0; p < 10; p++) {
      glds16(base + XS + (L * 10 + p) * 1024, vw[p % 5], sw, st * (64 * 3840) + (p / 5) * (16 * 3840));
      if (p < 8) glds16(base + (L * 8 + p) * 1024, vx[p & 1], sx, st * 128 + (p >> 1) * (16 * 1280));
    }
  };
#pragma unroll
  for (int p = 0; p < 10; p++) glds16(lb + XS + (L * 10 + p) * 1024, vw[p % 5], sw, s0 * (64 * 3840) + (p / 5) * (16 * 3840));
  stage(1, 1); stage(2, 2);
  wait_bar<36>();
  stage(3, 3);
  for (int s = 0; s < 9; s++) {
    if (s < 7) wait_bar<36>(); else if (s == 7) wait_bar<18>(); else wait_bar<0>();
    if (s < 6) stage(s + 4, s & 3);
  }
}

template <int T> struct StatTile {
  static constexpr bool isz = T >= 13;
  static constexpr int i = isz ? T - 13 : (T + 1) / 3;
  static constexpr int j = isz ? 0 : T - (i == 0 ? 0 : 3 * i - 1) + (i == 0 ? 0 : i - 1);
};
template <int T> __device__ __forceinline__ f32x4 stat_mma(unsigned la) {
  const u32x4 o1 = {0x3F803F80u, 0x3F803F80u, 0x3F803F80u, 0x3F803F80u};
  bf16x8 A[4], B[4];
#pragma unroll
  for (int m = 0; m < 4; m++) {
    A[m] = lds_b128(la + StatTile<T>::i * 1024 + m * 10240);
    B[m] = StatTile<T>::isz ? __builtin_bit_cast(bf16x8, o1) : lds_b128(la + (5 + StatTile<T>::j) * 1024 + m * 10240);
  }
  f32x4 d4 = {0.f, 0.f, 0.f, 0.f};
#pragma unroll
  for (int m = 0; m < 4; m++) d4 = mfma16(A[m], B[m], d4);
  return d4;
}
template <int T> __device__ __forceinline__ void stat_store(f32x4 d4, int lane, __amdgpu_buffer_rsrc_t rw, unsigned pbase) {
  const int fr = lane & 15, g4 = lane >> 4;
  const int Dg = 16 * StatTile<T>::i + 4 * g4, h = (Dg * 205) >> 12, dd = Dg - 20 * h;
  const int ee = StatTile<T>::isz ? 20 : 16 * StatTile<T>::j + fr - 20 * h;
  if (StatTile<T>::isz ? fr == 0 : (ee >= 0 && ee < 20))
    __builtin_amdgcn_raw_buffer_store_b64((u32x2){pk2(d4[0], d4[1]), pk2(d4[2], d4[3])}, rw, pbase + (h * 420 + ee * 20 + dd) * 2, 0, SC1);
}
template <int W> __device__ __forceinline__ void stat_tiles(unsigned lb, int lane, __amdgpu_buffer_rsrc_t rw, unsigned pbase) {
  const unsigned la = lb + lane * 16;
  const f32x4 a = stat_mma<W>(la), b = stat_mma<W + 8>(la);
  if constexpr (W < 2) { const f32x4 c = stat_mma<W + 16>(la); stat_store<W + 16>(c, lane, rw, pbase); }
  stat_store<W>(a, lane, rw, pbase);
  stat_store<W + 8>(b, lane, rw, pbase);
}

__device__ __forceinline__ void conv_load_h(u32x4 (&cur)[4], u32x4 (&prv)[4], const u16* __restrict__ hrow, int lane) {
#pragma unroll
  for (int it = 0; it < 4; it++) {
    const int i = it * 64 + lane;
    cur[it] = *(const u32x4*)(hrow + i * 8); prv[it] = *(const u32x4*)(hrow + (i > 0 ? i * 8 - 8 : 0));
  }
}
__device__ __forceinline__ void conv_prep_h(const u32x4 (&cur)[4], const u32x4 (&prv_)[4], unsigned cb, int lane) {
#pragma unroll
  for (int it = 0; it < 4; it++) {
    u32x4 prv = prv_[it];
    if (it == 0 && lane == 0) prv = (u32x4){0, 0, 0, 0};
    const unsigned W_[8] = {prv.x, prv.y, prv.z, prv.w, cur[it].x, cur[it].y, cur[it].z, cur[it].w};
    const int c = 255 - (it * 64 + lane);
#pragma unroll
    for (int e = 0; e < 4; e++) {
      u32x4 ov;
#pragma unroll
      for (int q = 0; q < 4; q++) {
        const int i0 = 15 - e - 2 * q, i1 = i0 - 1;
        const unsigned sel = ((i0 & 1) ? 0x0706u : 0x0504u) | (((i1 & 1) ? 0x0302u : 0x0100u) << 16);
        ov[q] = __builtin_amdgcn_perm(W_[i0 >> 1], W_[i1 >> 1], sel);
      }
      *(LAS u32x4*)(size_t)(cb + e * CPS + c * 16) = ov;
    }
  }
  if (lane < 8) *(LAS u32x4*)(size_t)(cb + (lane >> 1) * CPS + (256 + (lane & 1)) * 16) = (u32x4){0, 0, 0, 0};
  if (lane < 32) *(LAS u32x4*)(size_t)(cb + UOFF + lane * 16) = (u32x4){0, 0, 0, 0};
}
template <class F>
__device__ __forceinline__ void conv_load_u(__amdgpu_buffer_rsrc_t rw, int d, unsigned cb, int lane, F&& between) {
  u32x4 uv[4];
#pragma unroll
  for (int it = 0; it < 4; it++) uv[it] = __builtin_amdgcn_raw_buffer_load_b128(rw, OFF_UT + d * 4096 + (it * 64 + lane) * 16, 0, SC1);
  SBAR();
  between();
  SBAR();
#pragma unroll
  for (int it = 0; it < 4; it++) *(LAS u32x4*)(size_t)(cb + UOFF + 512 + (it * 64 + lane) * 16) = uv[it];
}
struct ConvFrags { u32x2 al[8], ah[8]; u32x4 B[8]; };
__device__ __forceinline__ void conv_read(ConvFrags& f, unsigned aa, unsigned ba) {
  asm volatile(
      "ds_read_b128 %16, %25\n\tds_read_b64 %7, %24 offset:3584\n\tds_read_b64 %15, %24 offset:3592\n\t"
      "ds_read_b128 %17, %25 offset:512\n\tds_read_b128 %18, %25 offset:1024\n\tds_read_b128 %19, %25 offset:1536\n\t"
      "ds_read_b128 %20, %25 offset:2048\n\tds_read_b128 %21, %25 offset:2560\n\tds_read_b128 %22, %25 offset:3072\n\t"
      "ds_read_b128 %23, %25 offset:3584\n\t"
      "ds_read_b64 %6, %24 offset:3072\n\tds_read_b64 %14, %24 offset:3080\n\t"
      "ds_read_b64 %5, %24 offset:2560\n\tds_read_b64 %13, %24 offset:2568\n\t"
      "ds_read_b64 %4, %24 offset:2048\n\tds_read_b64 %12, %24 offset:2056\n\t"
      "ds_read_b64 %3, %24 offset:1536\n\tds_read_b64 %11, %24 offset:1544\n\t"
      "ds_read_b64 %2, %24 offset:1024\n\tds_read_b64 %10, %24 offset:1032\n\t"
      "ds_read_b64 %1, %24 offset:512\n\tds_read_b64 %9, %24 offset:520\n\t"
      "ds_read_b64 %0, %24\n\tds_read_b64 %8, %24 offset:8"
      : "=&v"(f.al[0]), "=&v"(f.al[1]), "=&v"(f.al[2]), "=&v"(f.al[3]), "=&v"(f.al[4]), "=&v"(f.al[5]), "=&v"(f.al[6]), "=&v"(f.al[7]),
        "=&v"(f.ah[0]), "=&v"(f.ah[1]), "=&v"(f.ah[2]), "=&v"(f.ah[3]), "=&v"(f.ah[4]), "=&v"(f.ah[5]), "=&v"(f.ah[6]), "=&v"(f.ah[7]),
        "=&v"(f.B[0]), "=&v"(f.B[1]), "=&v"(f.B[2]), "=&v"(f.B[3]), "=&v"(f.B[4]), "=&v"(f.B[5]), "=&v"(f.B[6]), "=&v"(f.B[7])
      : "v"(aa), "v"(ba) : "memory");
}
__device__ __forceinline__ void conv_wait(ConvFrags& f) {
  asm volatile("s_waitcnt lgkmcnt(0)"
      : "+v"(f.al[0]), "+v"(f.al[1]), "+v"(f.al[2]), "+v"(f.al[3]), "+v"(f.al[4]), "+v"(f.al[5]), "+v"(f.al[6]), "+v"(f.al[7]),
        "+v"(f.ah[0]), "+v"(f.ah[1]), "+v"(f.ah[2]), "+v"(f.ah[3]), "+v"(f.ah[4]), "+v"(f.ah[5]), "+v"(f.ah[6]), "+v"(f.ah[7]),
        "+v"(f.B[0]), "+v"(f.B[1]), "+v"(f.B[2]), "+v"(f.B[3]), "+v"(f.B[4]), "+v"(f.B[5]), "+v"(f.B[6]), "+v"(f.B[7]) :: "memory");
}
template <int K> __device__ __forceinline__ void conv_read1(ConvFrags& f, unsigned aa, unsigned ba) {
  if constexpr (K < 8) asm volatile("ds_read_b128 %0, %1 offset:%2" : "=&v"(f.B[K]) : "v"(ba), "n"(512 * K) : "memory");
  else if constexpr (K < 16) asm volatile("ds_read_b64 %0, %1 offset:%2" : "=&v"(f.al[K - 8]) : "v"(aa), "n"(512 * (K - 8)) : "memory");
  else if constexpr (K < 24) asm volatile("ds_read_b64 %0, %1 offset:%2" : "=&v"(f.ah[K - 16]) : "v"(aa), "n"(512 * (K - 16) + 8) : "memory");
}
template <int K = 0> __device__ __forceinline__ void conv_step(f32x4 (&acc)[8], const ConvFrags& f, ConvFrags& nx, unsigned aa, unsigned ba) {
  if constexpr (K < 36) {
    constexpr int a = K < 8 ? 0 : K < 15 ? 1 : K < 21 ? 2 : K < 26 ? 3 : K < 30 ? 4 : K < 33 ? 5 : K < 35 ? 6 : 7;
    constexpr int i = K - (a == 0 ? 0 : a == 1 ? 8 : a == 2 ? 15 : a == 3 ? 21 : a == 4 ? 26 : a == 5 ? 30 : a == 6 ? 33 : 35);
    acc[a + i] = mfma16(mk8(f.al[7 - a], f.ah[7 - a]), __builtin_bit_cast(bf16x8, f.B[i]), acc[a + i]);
    conv_read1<K>(nx, aa, ba);
    SBAR();
    conv_step<K + 1>(acc, f, nx, aa, ba);
  }
}
__device__ __forceinline__ void conv_main(f32x4 (&acc)[8], unsigned cb, int lane, int c0, int c1) {
  const int fr = lane & 15, g4 = lane >> 4;
  const unsigned abase = cb + (3 - (fr & 3)) * CPS + 2 * (2044 - 16 * 112 - 16 * (g4 >> 1) + 8 * (g4 & 1) - (fr & ~3));
  const unsigned bbase = cb + UOFF + 512 + 2 * (16 * fr - 16 * (g4 >> 1) + 8 * (g4 & 1));
#pragma unroll
  for (int c = 0; c < 8; c++) acc[c] = (f32x4){0.f, 0.f, 0.f, 0.f};
  ConvFrags f0, f1;
  asm volatile("s_waitcnt lgkmcnt(0)" ::: "memory");
  conv_read(f0, abase - 64 * c0, bbase - 64 * c0);
  for (int cc = c0; cc < c1; cc += 2) {
    conv_wait(f0);
    SBAR();
    conv_step(acc, f0, f1, abase - 64 * (cc + 1), bbase - 64 * (cc + 1));
    conv_wait(f1);
    SBAR();
    const int cn = cc + 2 < c1 ? cc + 2 : cc + 1;
    conv_step(acc, f1, f0, abase - 64 * cn, bbase - 64 * cn);
  }
  conv_wait(f0);
}
__device__ __forceinline__ void conv_store(f32x4 y, unsigned cb, int c, int lane, float Dd, __amdgpu_buffer_rsrc_t rw, int d) {
  const int fr = lane & 15, g4 = lane >> 4;
  const u32x2 uu = lds_b64(cb + UOFF + 512 + 2 * (256 * c + 16 * fr + 4 * g4));
  const float r0 = fmaf(bflo(uu.x), Dd, y[0]), r1 = fmaf(bfhi(uu.x), Dd, y[1]);
  const float r2 = fmaf(bflo(uu.y), Dd, y[2]), r3 = fmaf(bfhi(uu.y), Dd, y[3]);
  __builtin_amdgcn_raw_buffer_store_b64((u32x2){pk2(r0, r1), pk2(r2, r3)}, rw, OFF_YT + d * 4096 + (256 * c + 16 * fr + 4 * g4) * 2, 0, SC1);
}

__global__ __launch_bounds__(512, 2) void fused(const u16* __restrict__ x, const u16* __restrict__ wqkv, const u16* __restrict__ wo,
                                                const u16* __restrict__ Bk, const u16* __restrict__ Bv, const u16* __restrict__ shk,
                                                const u16* __restrict__ hk, const u16* __restrict__ Dv, const u16* __restrict__ gw,
                                                u16* __restrict__ out, char* __restrict__ ws) {
  extern __shared__ __attribute__((aligned(16))) char sm[];
  const int tid = threadIdx.x, lane = tid & 63;
  const int w = __builtin_amdgcn_readfirstlane(tid >> 6);
  const int b = blockIdx.x, xcd = b & 7, r = b >> 3;
  const int fr = lane & 15, g4 = lane >> 4;
  const unsigned lb = (unsigned)(size_t)(LAS char*)sm;
  const __amdgpu_buffer_rsrc_t rw = __builtin_amdgcn_make_buffer_rsrc(ws, 0, WS_USED, 0x00020000);
  if (r < 24 && (w >> 1) == 2) {
    const bool kv_ = r < 16;
    gemm_first_x(w - 4, lane, lb, x, (xcd & 3) * 4 + (r & 3), kv_ ? (xcd >> 2) * 4 + (r >> 2) : (xcd >> 2) * 2 + ((r - 16) >> 2), kv_);
  }
  TS_DECL TS(0);
  unsigned e0 = 0;
#define PUT_TAG() do { if (w == 7 && lane == 0) { *(LAS unsigned*)(size_t)(lb + L_TAG) = e0 + 1u; *(LAS u32x4*)(size_t)(lb + L_CNT) = (u32x4){0u, 0u, 0u, 0u}; } } while (0)
#define TAG (*(const LAS unsigned*)(size_t)(lb + L_TAG))
  if (r < 24) {
    const bool isKV = r < 16;
    const int rt = (xcd & 3) * 4 + (r & 3);
    const int cg = isKV ? (xcd >> 2) * 4 + (r >> 2) : (xcd >> 2) * 2 + ((r - 16) >> 2);
    f32x4 acc[4][5];
    const int rp = (w >> 1) & 1, hf = w & 1;
    if (w < 4) {
      const unsigned xa0 = lb + (rp * 64 + fr) * 128 + ((g4 ^ ((fr >> 1) & 7)) << 4);
      const unsigned wl = lb + XS + (8 * g4 + (fr >> 2)) * 320 + ((lane & 3) >> 1) * 16 + (lane & 1) * 8 + hf * 160;
      const unsigned sgn = (g4 & 1) * 32;
      const unsigned wa0 = hf ? wl - sgn : wl + sgn, wa1 = hf ? wl + sgn : wl - sgn;
#pragma unroll
      for (int i = 0; i < 4; i++)
#pragma unroll
        for (int t = 0; t < 5; t++) acc[i][t] = (f32x4){0.f, 0.f, 0.f, 0.f};
      if (isKV) gemm_compute<true>(acc, xa0, wa0, wa1 TSARG); else gemm_compute<false>(acc, xa0, wa0, wa1 TSARG);
    } else if (w < 6) {
      gemm_loader(w - 4, lane, lb, x, wqkv, rt, cg, isKV);
    } else {
      const int w6 = w - 6;
      const char* pfa;
      {
        const int q = rt & 3, u = isKV ? (cg & 3) : 4 + (cg & 1);
        if (lane < 32) {
          const int row = 16 * q + 8 * w6 + (lane >> 2);
          const unsigned col = isKV ? ((lane & 2) ? 2560u : 1280u) + 160u * cg : 320u * cg;
          const unsigned ln = isKV ? (lane & 1) : ((lane & 3) == 3 ? 2 : (lane & 3));
          pfa = (const char*)wqkv + row * 3840 + ((col >> 7) + ln) * 128;
        } else {
          int row = u * 22 + w6 * 11 + (lane - 32); row = (lane < 43 && row < 128) ? row : u * 22;
          pfa = (const char*)x + (rt * 128 + row) * 1280;
        }
      }
      const int pfs0 = gemm_s0(rt, cg, isKV);
      auto prefetch = [&](int j) {
      };
      SBAR();
      if (w == 7) e0 = flag_ld(ws, F_E);
      if (isKV) {
        const int l16 = lane & 15, row0 = rt * 128 + cg * 16 + w6 * 8;
        const u32x4 g0 = *(const u32x4*)(gw + lane * 8), g1 = *(const u32x4*)(gw + 512 + l16 * 8);
        u32x4 gx0[8], gx1[8];
#pragma unroll
        for (int i = 0; i < 8; i++) {
          const u16* xr = x + (size_t)(row0 + i) * D;
          gx0[i] = *(const u32x4*)(xr + lane * 8); gx1[i] = *(const u32x4*)(xr + 512 + l16 * 8);
        }
        SBAR();
        BAR(); prefetch(4); prefetch(5); BAR(); prefetch(6); prefetch(7); BAR(); prefetch(8); prefetch(9);
        float a[8];
        const float m1 = lane < 16 ? 1.f : 0.f;
#pragma unroll
        for (int i = 0; i < 8; i++) {
          float a0 = 0.f, a1 = 0.f;
#pragma unroll
          for (int j = 0; j < 4; j++) {
            a0 = fmaf(bflo(gx0[i][j]), bflo(g0[j]), a0); a0 = fmaf(bfhi(gx0[i][j]), bfhi(g0[j]), a0);
            a1 = fmaf(bflo(gx1[i][j]), bflo(g1[j]), a1); a1 = fmaf(bfhi(gx1[i][j]), bfhi(g1[j]), a1);
          }
          a[i] = fmaf(a1, m1, a0);
          if (i == 3) { SBAR(); BAR(); SBAR(); }
        }
        SBAR(); BAR(); SBAR();
#pragma unroll
        for (int i = 0; i < 4; i++) { const float lo = a[i], hi = a[i + 4]; const bool up = lane & 4; a[i] = (up ? hi : lo) + __shfl_xor(up ? lo : hi, 4); }
#pragma unroll
        for (int i = 0; i < 2; i++) { const float lo = a[i], hi = a[i + 2]; const bool up = lane & 2; a[i] = (up ? hi : lo) + __shfl_xor(up ? lo : hi, 2); }
        { const float lo = a[0], hi = a[1]; const bool up = lane & 1; a[0] = (up ? hi : lo) + __shfl_xor(up ? lo : hi, 1); }
        a[0] += __shfl_xor(a[0], 8); a[0] += __shfl_xor(a[0], 16); a[0] += __shfl_xor(a[0], 32);
        if (lane < 8) __builtin_amdgcn_raw_buffer_store_b32(__float_as_uint(rbf(1.f / (1.f + __expf(-rbf(a[0]))))), rw, OFF_G + (row0 + lane) * 4, 0, SC1);
        PUT_TAG();
        SBAR();
        BAR(); BAR(); BAR(); BAR(); BAR();
      } else {
        const int hw = (xcd * 8 + (r - 16)) * 2 + w6;
        const unsigned scr = lb + L_T + w6 * 6144;
#pragma unroll
        for (int bt = 0; bt < 2; bt++) {
          const int blk = bt == 0 ? hw : (hw < 32 ? 128 + hw : hw), ct = (blk * 3277) >> 16, ks = blk - 20 * ct;
          u32x4 v[5];
#pragma unroll
          for (int i = 0; i < 5; i++) {
            const int c = i * 64 + lane, row = (c * 6554) >> 16, ch = c - 10 * row;
            v[i] = *(const u32x4*)(wo + (size_t)(32 * ks + row) * D + 80 * ct + 8 * ch);
          }
          SBAR();
          BAR();
          if (bt == 0) { PUT_TAG(); prefetch(4); prefetch(5); }
          BAR();
          if (bt == 0) { prefetch(6); prefetch(7); }
          if (bt == 0 || hw < 32) {
#pragma unroll
            for (int i = 0; i < 5; i++) *(LAS u32x4*)(size_t)(scr + (i * 64 + lane) * 16) = v[i];
            const unsigned ta = scr + (8 * g4 + (fr >> 2)) * 160 + (lane & 3) * 8;
#pragma unroll
            for (int t = 0; t < 5; t++) {
              const u32x2 lo = lds_tr(ta + 32 * t), hi = lds_tr(ta + 32 * t + 640);
              __builtin_amdgcn_raw_buffer_store_b128((u32x4){lo.x, lo.y, hi.x, hi.y}, rw, OFF_WOP + (blk * 5 + t) * 1024 + lane * 16, 0, SC1);
            }
          }
          SBAR();
          BAR();
          if (bt == 0) { prefetch(8); prefetch(9); }
          BAR();
        }
        DRAIN();
        if (lds_last(lb + L_CNT + 8, lane, 2) && lane == 0) flag_st(ws, F_W + (xcd * 8 + (r - 16)) * 128, TAG);
        BAR(); BAR();
      }
    }
    TS(1);
    BAR();
    TS(11);
    const unsigned tag = TAG;
    if (isKV) {
      if (w < 4) {
#pragma unroll
        for (int t = 0; t < 5; t++) {
          const bool isk = hf ? t < 2 : t < 3;
          const int slot = hf ? (t < 2 ? 3 + t : 5 + t) : (t < 3 ? t : 2 + t);
#pragma unroll
          for (int m = 0; m < 2; m++) {
            u32x4 f;
            if (isk) f = (u32x4){phi_pk(acc[2 * m][t][0], acc[2 * m][t][1]), phi_pk(acc[2 * m][t][2], acc[2 * m][t][3]),
                                 phi_pk(acc[2 * m + 1][t][0], acc[2 * m + 1][t][1]), phi_pk(acc[2 * m + 1][t][2], acc[2 * m + 1][t][3])};
            else f = (u32x4){pk2(acc[2 * m][t][0], acc[2 * m][t][1]), pk2(acc[2 * m][t][2], acc[2 * m][t][3]),
                             pk2(acc[2 * m + 1][t][0], acc[2 * m + 1][t][1]), pk2(acc[2 * m + 1][t][2], acc[2 * m + 1][t][3])};
            *(LAS u32x4*)(size_t)(lb + ((rp * 2 + m) * 10 + slot) * 1024 + lane * 16) = f;
          }
        }
      } else {
        for (int i = tid - 256; i < 6048 / 16; i += 256) *(LAS u32x4*)(size_t)(lb + L_T + i * 16) = (u32x4){0, 0, 0, 0};
        if (w == 7) poll(ws, F_W + lane * 128, true, tag);
      }
      TS(12);
      BAR();
      TS(2);
      switch (w) {
        case 0: stat_tiles<0>(lb, lane, rw, OFF_P + (rt * 8 + cg) * 3360); break;
        case 1: stat_tiles<1>(lb, lane, rw, OFF_P + (rt * 8 + cg) * 3360); break;
        case 2: stat_tiles<2>(lb, lane, rw, OFF_P + (rt * 8 + cg) * 3360); break;
        case 3: stat_tiles<3>(lb, lane, rw, OFF_P + (rt * 8 + cg) * 3360); break;
        case 4: stat_tiles<4>(lb, lane, rw, OFF_P + (rt * 8 + cg) * 3360); break;
        case 5: stat_tiles<5>(lb, lane, rw, OFF_P + (rt * 8 + cg) * 3360); break;
        case 6: stat_tiles<6>(lb, lane, rw, OFF_P + (rt * 8 + cg) * 3360); break;
        default: stat_tiles<7>(lb, lane, rw, OFF_P + (rt * 8 + cg) * 3360); break;
      }
      TS(14);
      DRAIN();
      TS(15);
      BAR();
      TS(3);
      if (w == 0) {
        if (lane == 0) flag_st(ws, F_P + (rt * 8 + cg) * 128, tag);
        poll(ws, lane < 16 ? F_P + (lane * 8 + cg) * 128 : F_PQ + (rt * 4 + (cg >> 1)) * 128, lane <= 16, tag);
      } else if (w >= 4) {
        const i32x4 sws = make_srd(ws, WS_USED);
        const unsigned vwv = OFF_WOP + ((xcd >> 2) * 4 + (r >> 3)) * 102400 + lane * 16;
        for (int i = w - 4; i < 100; i += 4) {
          int pc = 12 * (r & 7) + i; pc = __builtin_amdgcn_readfirstlane(pc >= 100 ? pc - 100 : pc);
          glds16_sc1(lb + pc * 1024, vwv, sws, pc * 1024);
        }
      }
      constexpr int PJ[9] = {0, 1, 1, 2, 2, 3, 3, 4, 4}, PK[9] = {0, 0, 1, 0, 1, 1, 2, 1, 2};
      unsigned an[9], ad[9];
#pragma unroll
      for (int p = 0; p < 9; p++) {
        const int E = 16 * PJ[p] + fr, h = (E * 205) >> 12, e = E - 20 * h;
        int o_ = 32 * PK[p] + 8 * g4 - 20 * h; o_ = o_ < -8 ? -8 : o_ > 20 ? 20 : o_;
        an[p] = lb + L_T + (h * 21 + e) * 72 + (8 + o_) * 2; ad[p] = lb + L_T + (h * 21 + 20) * 72 + (8 + o_) * 2;
      }
      BAR();
      TS(4);
      bf16x8 Bq[3];
      {
        const unsigned prow = OFF_PQ + ((rt * 8 + w) * 8 + cg) * 3072 + lane * 16;
#pragma unroll
        for (int ks = 0; ks < 3; ks++) Bq[ks] = __builtin_bit_cast(bf16x8, __builtin_amdgcn_raw_buffer_load_b128(rw, prow + 1024 * ks, 0, SC1));
      }
      if (tid < 210) {
        const unsigned pb = OFF_P + cg * 3360 + tid * 16;
        u32x4 v[16];
#pragma unroll
        for (int k = 0; k < 16; k++) v[k] = __builtin_amdgcn_raw_buffer_load_b128(rw, pb, (k ^ rt) * (8 * 3360), SC1);
        unsigned o4[4];
#pragma unroll
        for (int c = 0; c < 4; c++) {
          float lo[16], hi[16];
#pragma unroll
          for (int k = 0; k < 16; k++) { lo[k] = bflo(v[k][c]); hi[k] = bfhi(v[k][c]); }
#pragma unroll
          for (int st = 1; st < 16; st <<= 1)
#pragma unroll
            for (int k = 0; k < 16; k += 2 * st) { lo[k] += lo[k + st]; hi[k] += hi[k + st]; }
          o4[c] = pk2(lo[0], hi[0]);
        }
        TS(16);
#pragma unroll
        for (int q = 0; q < 2; q++) {
          const int i4 = 2 * tid + q, row = (i4 * 13108) >> 16, d = 4 * (i4 - row * 5);
          *(LAS u32x2*)(size_t)(lb + L_T + row * 72 + (8 + d) * 2) = (u32x2){o4[2 * q], o4[2 * q + 1]};
        }
      }
      BAR();
      TS(5);
      {
        if (g4 >= 2) Bq[2] = __builtin_bit_cast(bf16x8, (u32x4){0, 0, 0, 0});
        f32x4 nu[5], de[5];
#pragma unroll
        for (int j = 0; j < 5; j++) { nu[j] = (f32x4){0.f, 0.f, 0.f, 0.f}; de[j] = nu[j]; }
        bf16x8 An[9], Ad[9];
#pragma unroll
        for (int p = 0; p < 9; p++) {
          unsigned an8 = an[p] + 8, ad8 = ad[p] + 8;
          asm volatile("" : "+v"(an8), "+v"(ad8));
          An[p] = mk8(lds_b64(an[p]), lds_b64(an8)); Ad[p] = mk8(lds_b64(ad[p]), lds_b64(ad8));
        }
#pragma unroll
        for (int p = 0; p < 9; p++) { de[PJ[p]] = mfma16(Ad[p], Bq[PK[p]], de[PJ[p]]); nu[PJ[p]] = mfma16(An[p], Bq[PK[p]], nu[PJ[p]]); }
        u32x2 ov[5];
#pragma unroll
        for (int j = 0; j < 5; j++) {
          const float rd = __builtin_amdgcn_rcpf(de[j][0] + 1e-6f);
          ov[j] = (u32x2){pk2(nu[j][0] * rd, nu[j][1] * rd), pk2(nu[j][2] * rd, nu[j][3] * rd)};
        }
        store_frags(ov, rw, OFF_O + (rt * 8 + w) * 20480, 80 * cg, fr, g4);
      }
      TS(17);
      DRAIN();
      BAR();
      if (tid == 0) flag_st(ws, F_O + (rt * 8 + cg) * 128, tag);
      TS(6);
    } else {
      const int vb = xcd * 8 + (r - 16);
      const int d = 80 * (vb >> 3) + 48 + 4 * (vb & 7) + (w & 3);
      const unsigned cb = lb + (w & 3) * CHS;
      if (w < 4) {
#pragma unroll
        for (int i = 0; i < 4; i++) {
          u32x2 pv[5];
#pragma unroll
          for (int t = 0; t < 5; t++) pv[t] = (u32x2){phi_pk(acc[i][t][0], acc[i][t][1]), phi_pk(acc[i][t][2], acc[i][t][3])};
          store_frags(pv, rw, OFF_PQ + ((rt * 8 + rp * 4 + i) * 8 + cg * 2 + hf) * 3072, 0, fr, g4);
        }
      } else {
        u32x4 hc[4], hp[4];
        conv_load_h(hc, hp, hk + (size_t)d * S, lane);
        poll(ws, F_U + lane * 128, true, tag);
        conv_load_u(rw, d, cb, lane, [&]() { conv_prep_h(hc, hp, cb, lane); });
      }
      TS(12);
      DRAIN();
      BAR();
      TS(2);
      if (tid == 0) flag_st(ws, F_PQ + (rt * 4 + cg) * 128, tag);
      if (w < 4) {
        const float Dd = bflo(Dv[d]);
        TS(3);
        f32x4 y[8];
        conv_main(y, cb, lane, 0, 8);
        TS(4);
#pragma unroll
        for (int c = 0; c < 8; c++) conv_store(y[c], cb, c, lane, Dd, rw, d);
        DRAIN();
      } else if (w == 7) {
        poll(ws, F_W + lane * 128, true, tag);
      }
      BAR();
      if (tid == 0) flag_st(ws, F_Y + (64 + vb) * 128, tag);
      TS(5); TS(6);
    }
  } else {
    const int pw = xcd * 8 + (r - 24);
    const int c6 = w < 4 ? w : 4 + ((w - 4) >> 1);
    const int d = 80 * (pw >> 3) + 6 * (pw & 7) + c6;
    const unsigned cb = lb + c6 * CHS;
    const bool prep = w < 4 || !(w & 1);
    u32x4 hc[4], hp[4];
    {
      const int rbase = xcd * 256 + ((r - 24) >> 1) * 64, cbase = ((r - 24) & 1) * 320;
      u32x4 kr[2][7], vr[2][4], sp[2][4];
#pragma unroll
      for (int q = 0; q < 2; q++) {
        const int it = w + 8 * q;
        if (q == 0 || w < 2) {
          const int ch = cbase + (it >> 1) * 64 + (lane & 7) * 8;
          const int s0 = rbase + (it & 1) * 32 + (lane >> 3) * 4;
#pragma unroll
          for (int j = 0; j < 7; j++) { int s = s0 - 3 + j; kr[q][j] = *(const u32x4*)(Bk + (size_t)(s < 0 ? 0 : s) * D + ch); }
#pragma unroll
          for (int j = 0; j < 4; j++) vr[q][j] = *(const u32x4*)(Bv + (size_t)(s0 + j) * D + ch);
#pragma unroll
          for (int j = 0; j < 4; j++) sp[q][j] = ((const u32x4*)(shk + ch * 4))[j];
        }
      }
      if (prep) conv_load_h(hc, hp, hk + (size_t)d * S, lane);
      if (w == 7) e0 = flag_ld(ws, F_E);
      SBAR();
#pragma unroll
      for (int q = 0; q < 2; q++) {
        const int it = w + 8 * q;
        if (q == 0 || w < 2) {
          const int s0 = rbase + (it & 1) * 32 + (lane >> 3) * 4;
          if (s0 == 0) { kr[q][0] = (u32x4){0, 0, 0, 0}; kr[q][1] = kr[q][0]; kr[q][2] = kr[q][0]; }
          const int rg = (it & 1) * 8 + (lane >> 3);
          const unsigned la = lb + L_UIMG + ((it >> 1) * 64 + (lane & 7) * 8) * 128 + (((rg >> 1) ^ (lane & 7)) << 4) + (rg & 1) * 8;
#pragma unroll
          for (int c = 0; c < 8; c++) {
            const unsigned s01 = sp[q][c >> 1][(c & 1) * 2], s23 = sp[q][c >> 1][(c & 1) * 2 + 1];
            const float w0 = bflo(s01), w1 = bfhi(s01), w2 = bflo(s23), w3 = bfhi(s23);
            float kk[7], u[4];
#pragma unroll
            for (int j = 0; j < 7; j++) { unsigned dd = kr[q][j][c >> 1]; kk[j] = (c & 1) ? bfhi(dd) : bflo(dd); }
#pragma unroll
            for (int j = 0; j < 4; j++) {
              unsigned dd = vr[q][j][c >> 1]; float v = (c & 1) ? bfhi(dd) : bflo(dd);
              float ks = kk[j] * w3; ks = fmaf(kk[j + 1], w2, ks); ks = fmaf(kk[j + 2], w1, ks); ks = fmaf(kk[j + 3], w0, ks);
              u[j] = ks * v;
            }
            *(LAS u32x2*)(size_t)(la + c * 128) = (u32x2){pk2(u[0], u[1]), pk2(u[2], u[3])};
          }
        }
      }
      PUT_TAG();
      BAR();
#pragma unroll
      for (int i = 0; i < 5; i++) {
        const int chl = (w * 5 + i) * 8 + (lane >> 3), k = lane & 7;
        const u32x4 v = *(const LAS u32x4*)(size_t)(lb + L_UIMG + chl * 128 + ((k ^ ((chl >> 3) & 7)) << 4));
        __builtin_amdgcn_raw_buffer_store_b128(v, rw, OFF_UT + (cbase + chl) * 4096 + rbase * 2 + k * 16, 0, SC1);
      }
      TS(1);
    }
    if (w < 5 && prep) conv_prep_h(hc, hp, cb, lane);
    DRAIN();
    BAR();
    TS(2);
    const unsigned tag = TAG;
    if (tid == 0) flag_st(ws, F_U + pw * 128, tag);
    const float Dd = bflo(Dv[d]);
    if (prep) {
      if (w == 6) conv_prep_h(hc, hp, cb, lane);
      poll(ws, F_U + lane * 128, true, tag);
      conv_load_u(rw, d, cb, lane, [&]() {});
    }
    BAR();
    TS(3);
    f32x4 y[8];
    conv_main(y, cb, lane, w < 4 ? 0 : (w & 1) * 4, w < 4 ? 8 : (w & 1) * 4 + 4);
    TS(4);
    if (w < 4) {
#pragma unroll
      for (int c = 0; c < 8; c++) conv_store(y[c], cb, c, lane, Dd, rw, d);
    }
    BAR();
    if (w >= 4) {
#pragma unroll
      for (int c = 0; c < 4; c++) { const f32x4 lo = y[c], hi = y[4 + c]; *(LAS f32x4*)(size_t)(cb + ((w & 1) * 4 + c) * 1024 + lane * 16) = (w & 1) ? lo : hi; }
    }
    BAR();
    if (w >= 4) {
#pragma unroll
      for (int c = 0; c < 4; c++) {
        const f32x4 o_ = *(const LAS f32x4*)(size_t)(cb + (((w & 1) ^ 1) * 4 + c) * 1024 + lane * 16);
        const f32x4 lo = y[c], hi = y[4 + c];
        const f32x4 mine = (w & 1) ? hi : lo;
        conv_store((w & 1) ? o_ + mine : mine + o_, cb, (w & 1) * 4 + c, lane, Dd, rw, d);
      }
    }
    if (w == 0) poll(ws, F_W + lane * 128, true, tag);
    DRAIN();
    BAR();
    if (tid == 0) flag_st(ws, F_Y + pw * 128, tag);
    TS(5); TS(6);
  }

  {
    const unsigned tag = TAG;
    const int rt = (xcd & 3) * 8 + (r & 7), ct = (xcd >> 2) * 4 + (r >> 3);
    const int sw_ = w & 3, kk = w >> 2;
    const i32x4 sws = make_srd(ws, WS_USED);
    TS(18);
    const int srow = rt * 64 + sw_ * 16 + fr;
    u32x2 xv[3];
#pragma unroll
    for (int t = 0; t < 3; t++) { const int tt = kk ? (t < 2 ? 3 + t : 4) : t; xv[t] = *(const u32x2*)(x + (size_t)srow * D + ct * 80 + 16 * tt + 4 * g4); }
    SBAR();
    const int rot = ((ct & 3) * 5) >> 1;
    const unsigned wa0 = lb + kk * 5120 + lane * 16;
    bf16x8 wf0[5][5];
    auto read_wf0 = [&]() {
#pragma unroll
      for (int s = 0; s < 5; s++) {
        int q = s + rot; q = q >= 10 ? q - 10 : q;
#pragma unroll
        for (int t = 0; t < 5; t++) wf0[s][t] = lds_b128(wa0 + q * 10240 + t * 1024);
      }
    };
    if (r < 16) read_wf0();
    SBAR();
    if (w == 0) {
      poll(ws, F_O + ((rt >> 1) * 8 + (lane & 7)) * 128, lane < 8, tag);
    } else if (w == 7) {
      poll(ws, F_Y + ((lane & 8 ? 64 : 0) + ct * 8 + (lane & 7)) * 128, lane < 16, tag);
    } else {
      if (r >= 16) {
        const unsigned vwv = OFF_WOP + ct * 102400 + lane * 16;
        for (int i = w - 1; i < 100; i += 6) { int pc = 12 * (r & 7) + i; pc = __builtin_amdgcn_readfirstlane(pc >= 100 ? pc - 100 : pc); glds16_sc1(lb + pc * 1024, vwv, sws, pc * 1024); }
      }
      if (w == 6 && b == 255) {
        for (int spins = 0; spins < (1 << 17); spins++) {
          const unsigned a0 = flag_ld(ws, F_U + lane * 128), a1 = flag_ld(ws, F_U + (64 + lane) * 128);
          const unsigned a2 = flag_ld(ws, F_U + (128 + lane) * 128), a3 = flag_ld(ws, F_U + (192 + lane) * 128);
          if (__all(a0 == tag && a1 == tag && a2 == tag && a3 == tag)) break;
          __builtin_amdgcn_s_sleep(2);
        }
        if (lane == 0) flag_st(ws, F_E, tag);
      }
      DRAIN();
    }
    BAR();
    TS(7);
    asm volatile("" :: "v"(xv[0]), "v"(xv[1]), "v"(xv[2]));
    bf16x8 of[10];
    {
      const unsigned vo = OFF_O + (rt * 4 + sw_) * 20480 + kk * 1024 + lane * 16;
#pragma unroll
      for (int s = 0; s < 10; s++) { int q = s + rot; q = __builtin_amdgcn_readfirstlane(q >= 10 ? q - 10 : q); bload16_sc1(of[s], vo, sws, q * 2048); }
    }
    if (w == 7) {
      for (int pc = 0; pc < 10; pc++)
        glds16_sc1(lb + L_Y + pc * 1024, OFF_YT + (ct * 80 + pc * 8 + (lane >> 3)) * 4096 + rt * 128 + (lane & 7) * 16, sws, 0);
    }
    float g;
    asm volatile("buffer_load_dword %0, %1, %2, 0 offen sc1" : "=v"(g) : "v"(OFF_G + srow * 4), "s"(sws) : "memory");
    TS(8);
    if (r >= 16) read_wf0();
    f32x4 acc[5];
#pragma unroll
    for (int t = 0; t < 5; t++) acc[t] = (f32x4){0.f, 0.f, 0.f, 0.f};
    {
      bf16x8 wf1[5][5];
#pragma unroll
      for (int s = 0; s < 5; s++) {
        int q = 5 + s + rot; q = q >= 10 ? q - 10 : q;
#pragma unroll
        for (int t = 0; t < 5; t++) wf1[s][t] = lds_b128(wa0 + q * 10240 + t * 1024);
      }
      SBAR();
      if (w == 7) asm volatile("s_waitcnt vmcnt(16)" ::: "memory"); else asm volatile("s_waitcnt vmcnt(6)" ::: "memory");
      asm volatile("" : "+v"(of[0]), "+v"(of[1]), "+v"(of[2]), "+v"(of[3]), "+v"(of[4]) :: "memory");
      SBAR();
#pragma unroll
      for (int s = 0; s < 5; s++)
#pragma unroll
        for (int t = 0; t < 5; t++) acc[t] = mfma16(wf0[s][t], of[s], acc[t]);
      SBAR();
      if (w == 7) asm volatile("s_waitcnt vmcnt(11)" ::: "memory"); else asm volatile("s_waitcnt vmcnt(1)" ::: "memory");
      asm volatile("" : "+v"(of[5]), "+v"(of[6]), "+v"(of[7]), "+v"(of[8]), "+v"(of[9]) :: "memory");
      SBAR();
#pragma unroll
      for (int s = 0; s < 5; s++)
#pragma unroll
        for (int t = 0; t < 5; t++) acc[t] = mfma16(wf1[s][t], of[5 + s], acc[t]);
      SBAR();
    }
    TS(9);
    const unsigned ra = lb + L_RED + (sw_ * 5) * 1024 + lane * 16;
    if (kk == 0) { *(LAS f32x4*)(size_t)(ra + 3 * 1024) = acc[3]; *(LAS f32x4*)(size_t)(ra + 4 * 1024) = acc[4]; }
    else { *(LAS f32x4*)(size_t)(ra) = acc[0]; *(LAS f32x4*)(size_t)(ra + 1024) = acc[1]; *(LAS f32x4*)(size_t)(ra + 2048) = acc[2]; }
    asm volatile("s_waitcnt vmcnt(0)" : "+v"(g) :: "memory");
    BAR();
    TS(20);
    const float omg = 1.f - g;
    const int nt = kk ? 2 : 3;
#pragma unroll
    for (int t = 0; t < 3; t++) {
      if (t < nt) {
        const int tt = kk ? 3 + t : t;
        const f32x4 a0 = acc[t], a1 = acc[3 + (t < 2 ? t : 1)];
        f32x4 a = *(const LAS f32x4*)(size_t)(ra + tt * 1024) + (kk ? a1 : a0);
        u32x2 yv = lds_tr(lb + L_Y + (16 * tt + 4 * g4 + (fr >> 2)) * 128 + (sw_ * 16 + 4 * (fr & 3)) * 2);
        float xs[4] = {bflo(xv[t].x), bfhi(xv[t].x), bflo(xv[t].y), bfhi(xv[t].y)};
        float ys[4] = {bflo(yv.x), bfhi(yv.x), bflo(yv.y), bfhi(yv.y)};
        float r_[4];
#pragma unroll
        for (int q = 0; q < 4; q++) r_[q] = fmaf(g, a[q], omg * (xs[q] * ys[q]));
        *(u32x2*)(out + (size_t)srow * D + ct * 80 + 16 * tt + 4 * g4) = (u32x2){pk2(r_[0], r_[1]), pk2(r_[2], r_[3])};
      }
    }
    TS(10); TS_OUT(21);
  }
}

extern "C" void kernel_launch(void* const* d_in, const int* in_sizes, int n_in, void* d_out, int out_size, void* d_ws, size_t ws_size,
                              hipStream_t stream) {
  const u16* x = (const u16*)d_in[0]; const u16* wqkv = (const u16*)d_in[1]; const u16* wo = (const u16*)d_in[2];
  const u16* Bk = (const u16*)d_in[4]; const u16* Bv = (const u16*)d_in[5]; const u16* shk = (const u16*)d_in[6];
  const u16* hk = (const u16*)d_in[7]; const u16* Dv = (const u16*)d_in[8]; const u16* gw = (const u16*)d_in[9];
  (void)hipFuncSetAttribute((const void*)fused, hipFuncAttributeMaxDynamicSharedMemorySize, LDS_TOTAL);
  fused<<<256, 512, LDS_TOTAL, stream>>>(x, wqkv, wo, Bk, Bv, shk, hk, Dv, gw, (u16*)d_out, (char*)d_ws);
}
```

```cpp
#include <hip/hip_runtime.h>
#include <hip/hip_bf16.h>
#include <type_traits>
#define NO_PF

typedef unsigned short u16;
typedef __bf16 bf16x2 __attribute__((ext_vector_type(2)));
typedef __bf16 bf16x8 __attribute__((ext_vector_type(8)));
typedef float f32x4 __attribute__((ext_vector_type(4)));
typedef unsigned u32x4 __attribute__((ext_vector_type(4)));
typedef unsigned u32x2 __attribute__((ext_vector_type(2)));
typedef short v4i16_t __attribute__((ext_vector_type(4)));
typedef int i32x4 __attribute__((ext_vector_type(4)));
typedef __attribute__((address_space(1))) unsigned gu32;
#define LAS __attribute__((address_space(3)))
#define SBAR() __builtin_amdgcn_sched_barrier(0)
#define SGB(mask, n) __builtin_amdgcn_sched_group_barrier(mask, n, 0)
#define BAR() asm volatile("s_waitcnt lgkmcnt(0)\n\ts_barrier" ::: "memory")
#define DRAIN() asm volatile("s_waitcnt vmcnt(0)" ::: "memory")
#define SC1 16

#define TS_DECL
#define TSARG
#define TSPAR
#define TS(i)
#define TS_OUT(n)

constexpr int S = 2048, D = 640;
constexpr unsigned SZ = S * D * 2;
constexpr unsigned F_E = 0, F_U = 128, F_PQ = F_U + 64 * 128, F_P = F_PQ + 64 * 128, F_O = F_P + 128 * 128, F_Y = F_O + 128 * 128, F_W = F_Y + 128 * 128;
constexpr unsigned OFF_PQ = 0x20000, OFF_P = OFF_PQ + 128 * 8 * 3 * 1024, OFF_UT = OFF_P + (1u << 19), OFF_G = OFF_UT + SZ, OFF_WOP = OFF_G + 8192,
                   OFF_O = OFF_WOP + (1u << 20), OFF_YT = OFF_O + SZ, WS_USED = OFF_YT + SZ;
constexpr int XS = 16384, WSB = 20480, STG = XS + WSB;
constexpr int L_EXTRA = 4 * STG;
constexpr int L_TAG = L_EXTRA, L_CNT = L_EXTRA + 16, L_T = L_EXTRA + 64;
constexpr int L_SCR = L_T + 6144 + 5120;
constexpr int LDS_TOTAL = L_SCR + 256;
constexpr int CPS = 4160, UOFF = 4 * CPS, CHS = UOFF + 4608;
constexpr int L_UIMG = 5 * CHS;
constexpr int L_Y = 102400, L_RED = L_Y + 10240;

__device__ __forceinline__ unsigned pk2(float a, float b) { bf16x2 r = {(__bf16)a, (__bf16)b}; return __builtin_bit_cast(unsigned, r); }
__device__ __forceinline__ float rbf(float a) { return (float)(__bf16)a; }
__device__ __forceinline__ float bflo(unsigned v) { return __uint_as_float(v << 16); }
__device__ __forceinline__ float bfhi(unsigned v) { return __uint_as_float(v & 0xffff0000u); }
__device__ __forceinline__ float phi(float t) {
  return __builtin_amdgcn_fmed3f(t, 0.f, 3.0e38f) + (__builtin_amdgcn_fmed3f(__builtin_amdgcn_exp2f(t * 1.44269504f), 0.f, 1.f) + 1e-6f);
}
typedef float f32x2 __attribute__((ext_vector_type(2)));
__device__ __forceinline__ unsigned phi_pk(float a, float b) {
  const f32x2 t = {a, b};
  const f32x2 y = t * (f32x2){1.44269504f, 1.44269504f};
  const f32x2 e = {__builtin_amdgcn_fmed3f(__builtin_amdgcn_exp2f(y.x), 0.f, 1.f), __builtin_amdgcn_fmed3f(__builtin_amdgcn_exp2f(y.y), 0.f, 1.f)};
  const f32x2 m = {__builtin_amdgcn_fmed3f(a, 0.f, 3.0e38f), __builtin_amdgcn_fmed3f(b, 0.f, 3.0e38f)};
  const f32x2 r = m + (e + (f32x2){1e-6f, 1e-6f});
  return pk2(r.x, r.y);
}
__device__ __forceinline__ i32x4 make_srd(const void* p, unsigned bytes) {
  unsigned long long a = (unsigned long long)p; i32x4 r;
  r.x = __builtin_amdgcn_readfirstlane((int)(unsigned)a); r.y = __builtin_amdgcn_readfirstlane((int)(unsigned)(a >> 32));
  r.z = __builtin_amdgcn_readfirstlane((int)bytes); r.w = 0x00020000; return r;
}
__device__ __forceinline__ void glds16(unsigned ldsaddr, unsigned voff, i32x4 srd, unsigned soff) {
  asm volatile("s_mov_b32 m0, %0\n\ts_nop 0\n\tbuffer_load_dwordx4 %1, %2, %3 offen lds" :: "s"(ldsaddr), "v"(voff), "s"(srd), "s"(soff) : "memory");
}
__device__ __forceinline__ void glds16_sc1(unsigned ldsaddr, unsigned voff, i32x4 srd, unsigned soff) {
  asm volatile("s_mov_b32 m0, %0\n\ts_nop 0\n\tbuffer_load_dwordx4 %1, %2, %3 offen sc1 lds" :: "s"(ldsaddr), "v"(voff), "s"(srd), "s"(soff) : "memory");
}
__device__ __forceinline__ void bload16_sc1(bf16x8& dst, unsigned voff, i32x4 srd, unsigned soff) {
  asm volatile("buffer_load_dwordx4 %0, %1, %2, %3 offen sc1" : "=v"(dst) : "v"(voff), "s"(srd), "s"(soff) : "memory");
}
template <int N> __device__ __forceinline__ void wait_bar() { asm volatile("s_waitcnt vmcnt(%0) lgkmcnt(0)\n\ts_barrier" :: "n"(N) : "memory"); }
__device__ __forceinline__ bf16x8 lds_b128(unsigned addr) { return *(const LAS bf16x8*)(size_t)addr; }
__device__ __forceinline__ u32x2 lds_b64(unsigned addr) { return *(const LAS u32x2*)(size_t)addr; }
__device__ __forceinline__ u32x2 lds_tr(unsigned addr) { return __builtin_bit_cast(u32x2, __builtin_amdgcn_ds_read_tr16_b64_v4i16((LAS v4i16_t*)(size_t)addr)); }
__device__ __forceinline__ bf16x8 mk8(u32x2 lo, u32x2 hi) { u32x4 t = {lo.x, lo.y, hi.x, hi.y}; return __builtin_bit_cast(bf16x8, t); }
__device__ __forceinline__ f32x4 mfma16(bf16x8 a, bf16x8 b, f32x4 c) { return __builtin_amdgcn_mfma_f32_16x16x32_bf16(a, b, c, 0, 0, 0); }

__device__ __forceinline__ u32x4 pair16(u32x2 a, u32x2 b) {
  const auto r0 = __builtin_amdgcn_permlane16_swap(a.x, b.x, false, false);
  const auto r1 = __builtin_amdgcn_permlane16_swap(a.y, b.y, false, false);
  return (u32x4){r0[0], r1[0], r0[1], r1[1]};
}
__device__ __forceinline__ void store_frags(const u32x2 (&v)[5], __amdgpu_buffer_rsrc_t rw, unsigned base, int k0, int fr, int g4) {
#pragma unroll
  for (int j = 0; j < 4; j += 2) {
    const int k = k0 + 16 * (j + (g4 & 1)) + 8 * (g4 >> 1);
    __builtin_amdgcn_raw_buffer_store_b128(pair16(v[j], v[j + 1]), rw, base + (k >> 5) * 1024 + (((k >> 3) & 3) * 16 + fr) * 16, 0, SC1);
  }
  const int k = k0 + 64 + 4 * g4;
  __builtin_amdgcn_raw_buffer_store_b64(v[4], rw, base + (k >> 5) * 1024 + (((k >> 3) & 3) * 16 + fr) * 16 + ((k >> 2) & 1) * 8, 0, SC1);
}

__device__ __forceinline__ void lds_sync(unsigned cnt_addr, int lane, unsigned n) {
  asm volatile("s_waitcnt lgkmcnt(0)" ::: "memory");
  if (lane == 0) __hip_atomic_fetch_add((LAS unsigned*)(size_t)cnt_addr, 1u, __ATOMIC_RELAXED, __HIP_MEMORY_SCOPE_WORKGROUP);
  while (__builtin_amdgcn_readfirstlane(__hip_atomic_load((LAS unsigned*)(size_t)cnt_addr, __ATOMIC_RELAXED, __HIP_MEMORY_SCOPE_WORKGROUP)) < n) {}
  asm volatile("" ::: "memory");
}
__device__ __forceinline__ bool lds_last(unsigned cnt_addr, int lane, unsigned n) {
  unsigned old = 0;
  if (lane == 0) old = __hip_atomic_fetch_add((LAS unsigned*)(size_t)cnt_addr, 1u, __ATOMIC_RELAXED, __HIP_MEMORY_SCOPE_WORKGROUP);
  return __builtin_amdgcn_readfirstlane(old) == n - 1;
}

__device__ __forceinline__ unsigned flag_ld(const char* ws, unsigned off) { return __hip_atomic_load((const gu32*)(ws + off), __ATOMIC_RELAXED, __HIP_MEMORY_SCOPE_AGENT); }
__device__ __forceinline__ void flag_st(char* ws, unsigned off, unsigned v) { __hip_atomic_store((gu32*)(ws + off), v, __ATOMIC_RELAXED, __HIP_MEMORY_SCOPE_AGENT); }
__device__ __forceinline__ void poll(const char* ws, unsigned off, bool act, unsigned tag) {
  const unsigned o0 = __builtin_amdgcn_readfirstlane(off);
  off = act ? off : o0;
  unsigned a, b, n = 1u << 17;
  asm volatile(
      "global_load_dword %0, %3, %4 sc1\n\t"
      "s_sleep 6\n"
      "1:\n\t"
      "global_load_dword %1, %3, %4 sc1\n\t"
      "s_waitcnt vmcnt(1)\n\t"
      "v_cmp_ne_u32 vcc, %0, %5\n\t"
      "s_cbranch_vccz 2f\n\t"
      "global_load_dword %0, %3, %4 sc1\n\t"
      "s_waitcnt vmcnt(1)\n\t"
      "v_cmp_ne_u32 vcc, %1, %5\n\t"
      "s_cbranch_vccz 2f\n\t"
      "s_sub_u32 %2, %2, 1\n\t"
      "s_cbranch_scc0 1b\n"
      "2:\n\t"
      "s_waitcnt vmcnt(0)"
      : "=&v"(a), "=&v"(b), "+s"(n) : "v"(off), "s"(ws), "v"(tag) : "vcc", "scc", "memory");
}

template <bool KV>
__device__ __forceinline__ void gemm_compute(f32x4 (&acc)[4][5], unsigned xa0, unsigned wa0, unsigned wa1 TSPAR) {
  bf16x8 xa[4], xb[4], wa[5], wb[5];
  auto load = [&](bf16x8 (&xf)[4], bf16x8 (&wf)[5], int buf, int kk) {
    const unsigned bo = buf * STG;
    const unsigned xa = (xa0 + bo) ^ (kk * 64);
    const unsigned a0 = wa0 + bo + kk * 10240, a1 = wa1 + bo + kk * 10240;
#pragma unroll
    for (int i = 0; i < 4; i++) xf[i] = lds_b128(xa + 2048 * i);
#pragma unroll
    for (int t = 0; t < 5; t++) { const unsigned a = ((t & 1) ? a1 : a0) + 32 * t; wf[t] = mk8(lds_tr(a), lds_tr(a + 1280)); }
  };
  auto mma = [&](const bf16x8 (&xf)[4], const bf16x8 (&wf)[5]) {
#pragma unroll
    for (int t = 0; t < 5; t++)
#pragma unroll
      for (int i = 0; i < 4; i++) acc[i][t] = KV ? mfma16(xf[i], wf[t], acc[i][t]) : mfma16(wf[t], xf[i], acc[i][t]);
  };
  auto interleave = [&]() {
#pragma unroll
    for (int t = 0; t < 14; t++) { SGB(0x8, 1); SGB(0x100, 1); }
    SGB(0x8, 6);
  };
  BAR();
  TS(21);
  load(xa, wa, 0, 0);
  SBAR();
  for (int s = 0; s < 9; s++) {
    load(xb, wb, s & 3, 1); mma(xa, wa); interleave();
    SBAR();
    BAR();
    if (s == 0) TS(22);
    if (s == 4) TS(23);
    load(xa, wa, (s + 1) & 3, 0); mma(xb, wb); interleave();
    SBAR();
  }
  load(xb, wb, 1, 1); mma(xa, wa); interleave();
  SBAR();
  mma(xb, wb);
}

__device__ __forceinline__ int gemm_s0(int rt, int cg, bool isKV) { return (((rt & 3) * 5 + 1) / 2 + (isKV ? 2 * (cg & 3) : 8 + (cg & 1))) % 10; }
__device__ __forceinline__ void gemm_vx(unsigned (&vx)[2], int L, int lane, int rt) {
#pragma unroll
  for (int i = 0; i < 2; i++) vx[i] = (rt * 128 + L * 64 + i * 8 + (lane >> 3)) * 1280 + (((lane & 7) ^ ((4 * i + (lane >> 4)) & 7)) << 4);
}
__device__ __forceinline__ void gemm_first_x(int L, int lane, unsigned lb, const u16* x, int rt, int cg, bool isKV) {
  const i32x4 sx = make_srd(x, S * D * 2);
  unsigned vx[2];
  gemm_vx(vx, L, lane, rt);
  const int s0 = gemm_s0(rt, cg, isKV);
#pragma unroll
  for (int p = 0; p < 8; p++) glds16(lb + (L * 8 + p) * 1024, vx[p & 1], sx, s0 * 128 + (p >> 1) * (16 * 1280));
}
__device__ __forceinline__ void gemm_loader(int L, int lane, unsigned lb, const u16* x, const u16* wqkv, int rt, int cg, bool isKV) {
  const int s0 = gemm_s0(rt, cg, isKV);
  const i32x4 sx = make_srd(x, S * D * 2), sw = make_srd(wqkv, D * 3 * D * 2);
  unsigned vx[2], vw[5];
  gemm_vx(vx, L, lane, rt);
#pragma unroll
  for (int i = 0; i < 5; i++) {
    const int ci = i * 64 + lane, k = (ci * 3277) >> 16, pos = ci - k * 20, gc = pos ^ (((k >> 3) & 1) << 1), tl = gc >> 1;
    unsigned col;
    if (isKV) { const int isv = (tl >= 3 && tl < 5) || tl >= 7; const int tt = tl < 3 ? tl : tl < 5 ? tl - 3 : tl < 7 ? tl - 2 : tl - 5;
                col = (isv ? 2560u : 1280u) + 160u * cg + tt * 32; }
    else col = 320u * cg + tl * 32;
    vw[i] = (L * 32 + k) * 3840 + col + (gc & 1) * 16;
  }
  auto stage = [&](int st_, int buf) {
    const unsigned base = lb + buf * STG;
    int st = st_ + s0; st = st >= 10 ? st - 10 : st;
#pragma unroll
    for (int p = 0; p < 10; p++) {
      glds16(base + XS + (L * 10 + p) * 1024, vw[p % 5], sw, st * (64 * 3840) + (p / 5) * (16 * 3840));
      if (p < 8) glds16(base + (L * 8 + p) * 1024, vx[p & 1], sx, st * 128 + (p >> 1) * (16 * 1280));
    }
  };
#pragma unroll
  for (int p = 0; p < 10; p++) glds16(lb + XS + (L * 10 + p) * 1024, vw[p % 5], sw, s0 * (64 * 3840) + (p / 5) * (16 * 3840));
  stage(1, 1); stage(2, 2);
  wait_bar<36>();
  stage(3, 3);
  for (int s = 0; s < 9; s++) {
    if (s < 7) wait_bar<36>(); else if (s == 7) wait_bar<18>(); else wait_bar<0>();
    if (s < 6) stage(s + 4, s & 3);
  }
}

template <int T> struct StatTile {
  static constexpr bool isz = T >= 13;
  static constexpr int i = isz ? T - 13 : (T + 1) / 3;
  static constexpr int j = isz ? 0 : T - (i == 0 ? 0 : 3 * i - 1) + (i == 0 ? 0 : i - 1);
};
template <int T> __device__ __forceinline__ f32x4 stat_mma(unsigned la) {
  const u32x4 o1 = {0x3F803F80u, 0x3F803F80u, 0x3F803F80u, 0x3F803F80u};
  bf16x8 A[4], B[4];
#pragma unroll
  for (int m = 0; m < 4; m++) {
    A[m] = lds_b128(la + StatTile<T>::i * 1024 + m * 10240);
    B[m] = StatTile<T>::isz ? __builtin_bit_cast(bf16x8, o1) : lds_b128(la + (5 + StatTile<T>::j) * 1024 + m * 10240);
  }
  f32x4 d4 = {0.f, 0.f, 0.f, 0.f};
#pragma unroll
  for (int m = 0; m < 4; m++) d4 = mfma16(A[m], B[m], d4);
  return d4;
}
template <int T> __device__ __forceinline__ void stat_store(f32x4 d4, int lane, __amdgpu_buffer_rsrc_t rw, unsigned pbase) {
  const int fr = lane & 15, g4 = lane >> 4;
  const int Dg = 16 * StatTile<T>::i + 4 * g4, h = (Dg * 205) >> 12, dd = Dg - 20 * h;
  const int ee = StatTile<T>::isz ? 20 : 16 * StatTile<T>::j + fr - 20 * h;
  if (StatTile<T>::isz ? fr == 0 : (ee >= 0 && ee < 20))
    __builtin_amdgcn_raw_buffer_store_b64((u32x2){pk2(d4[0], d4[1]), pk2(d4[2], d4[3])}, rw, pbase + (h * 420 + ee * 20 + dd) * 2, 0, SC1);
}
template <int W> __device__ __forceinline__ void stat_tiles(unsigned lb, int lane, __amdgpu_buffer_rsrc_t rw, unsigned pbase) {
  const unsigned la = lb + lane * 16;
  const f32x4 a = stat_mma<W>(la), b = stat_mma<W + 8>(la);
  if constexpr (W < 2) { const f32x4 c = stat_mma<W + 16>(la); stat_store<W + 16>(c, lane, rw, pbase); }
  stat_store<W>(a, lane, rw, pbase);
  stat_store<W + 8>(b, lane, rw, pbase);
}

__device__ __forceinline__ void conv_load_h(u32x4 (&cur)[4], u32x4 (&prv)[4], const u16* __restrict__ hrow, int lane) {
#pragma unroll
  for (int it = 0; it < 4; it++) {
    const int i = it * 64 + lane;
    cur[it] = *(const u32x4*)(hrow + i * 8); prv[it] = *(const u32x4*)(hrow + (i > 0 ? i * 8 - 8 : 0));
  }
}
__device__ __forceinline__ void conv_prep_h(const u32x4 (&cur)[4], const u32x4 (&prv_)[4], unsigned cb, int lane) {
#pragma unroll
  for (int it = 0; it < 4; it++) {
    u32x4 prv = prv_[it];
    if (it == 0 && lane == 0) prv = (u32x4){0, 0, 0, 0};
    const unsigned W_[8] = {prv.x, prv.y, prv.z, prv.w, cur[it].x, cur[it].y, cur[it].z, cur[it].w};
    const int c = 255 - (it * 64 + lane);
#pragma unroll
    for (int e = 0; e < 4; e++) {
      u32x4 ov;
#pragma unroll
      for (int q = 0; q < 4; q++) {
        const int i0 = 15 - e - 2 * q, i1 = i0 - 1;
        const unsigned sel = ((i0 & 1) ? 0x0706u : 0x0504u) | (((i1 & 1) ? 0x0302u : 0x0100u) << 16);
        ov[q] = __builtin_amdgcn_perm(W_[i0 >> 1], W_[i1 >> 1], sel);
      }
      *(LAS u32x4*)(size_t)(cb + e * CPS + c * 16) = ov;
    }
  }
  if (lane < 8) *(LAS u32x4*)(size_t)(cb + (lane >> 1) * CPS + (256 + (lane & 1)) * 16) = (u32x4){0, 0, 0, 0};
  if (lane < 32) *(LAS u32x4*)(size_t)(cb + UOFF + lane * 16) = (u32x4){0, 0, 0, 0};
}
template <class F>
__device__ __forceinline__ void conv_load_u(__amdgpu_buffer_rsrc_t rw, int d, unsigned cb, int lane, F&& between) {
  u32x4 uv[4];
#pragma unroll
  for (int it = 0; it < 4; it++) uv[it] = __builtin_amdgcn_raw_buffer_load_b128(rw, OFF_UT + d * 4096 + (it * 64 + lane) * 16, 0, SC1);
  SBAR();
  between();
  SBAR();
#pragma unroll
  for (int it = 0; it < 4; it++) *(LAS u32x4*)(size_t)(cb + UOFF + 512 + (it * 64 + lane) * 16) = uv[it];
}
struct ConvFrags { u32x2 al[8], ah[8]; u32x4 B[8]; };
__device__ __forceinline__ void conv_read(ConvFrags& f, unsigned aa, unsigned ba) {
  asm volatile(
      "ds_read_b128 %16, %25\n\tds_read_b64 %7, %24 offset:3584\n\tds_read_b64 %15, %24 offset:3592\n\t"
      "ds_read_b128 %17, %25 offset:512\n\tds_read_b128 %18, %25 offset:1024\n\tds_read_b128 %19, %25 offset:1536\n\t"
      "ds_read_b128 %20, %25 offset:2048\n\tds_read_b128 %21, %25 offset:2560\n\tds_read_b128 %22, %25 offset:3072\n\t"
      "ds_read_b128 %23, %25 offset:3584\n\t"
      "ds_read_b64 %6, %24 offset:3072\n\tds_read_b64 %14, %24 offset:3080\n\t"
      "ds_read_b64 %5, %24 offset:2560\n\tds_read_b64 %13, %24 offset:2568\n\t"
      "ds_read_b64 %4, %24 offset:2048\n\tds_read_b64 %12, %24 offset:2056\n\t"
      "ds_read_b64 %3, %24 offset:1536\n\tds_read_b64 %11, %24 offset:1544\n\t"
      "ds_read_b64 %2, %24 offset:1024\n\tds_read_b64 %10, %24 offset:1032\n\t"
      "ds_read_b64 %1, %24 offset:512\n\tds_read_b64 %9, %24 offset:520\n\t"
      "ds_read_b64 %0, %24\n\tds_read_b64 %8, %24 offset:8"
      : "=&v"(f.al[0]), "=&v"(f.al[1]), "=&v"(f.al[2]), "=&v"(f.al[3]), "=&v"(f.al[4]), "=&v"(f.al[5]), "=&v"(f.al[6]), "=&v"(f.al[7]),
        "=&v"(f.ah[0]), "=&v"(f.ah[1]), "=&v"(f.ah[2]), "=&v"(f.ah[3]), "=&v"(f.ah[4]), "=&v"(f.ah[5]), "=&v"(f.ah[6]), "=&v"(f.ah[7]),
        "=&v"(f.B[0]), "=&v"(f.B[1]), "=&v"(f.B[2]), "=&v"(f.B[3]), "=&v"(f.B[4]), "=&v"(f.B[5]), "=&v"(f.B[6]), "=&v"(f.B[7])
      : "v"(aa), "v"(ba) : "memory");
}
__device__ __forceinline__ void conv_wait(ConvFrags& f) {
  asm volatile("s_waitcnt lgkmcnt(0)"
      : "+v"(f.al[0]), "+v"(f.al[1]), "+v"(f.al[2]), "+v"(f.al[3]), "+v"(f.al[4]), "+v"(f.al[5]), "+v"(f.al[6]), "+v"(f.al[7]),
        "+v"(f.ah[0]), "+v"(f.ah[1]), "+v"(f.ah[2]), "+v"(f.ah[3]), "+v"(f.ah[4]), "+v"(f.ah[5]), "+v"(f.ah[6]), "+v"(f.ah[7]),
        "+v"(f.B[0]), "+v"(f.B[1]), "+v"(f.B[2]), "+v"(f.B[3]), "+v"(f.B[4]), "+v"(f.B[5]), "+v"(f.B[6]), "+v"(f.B[7]) :: "memory");
}
template <int K> __device__ __forceinline__ void conv_read1(ConvFrags& f, unsigned aa, unsigned ba) {
  if constexpr (K < 8) asm volatile("ds_read_b128 %0, %1 offset:%2" : "=&v"(f.B[K]) : "v"(ba), "n"(512 * K) : "memory");
  else if constexpr (K < 16) asm volatile("ds_read_b64 %0, %1 offset:%2" : "=&v"(f.al[K - 8]) : "v"(aa), "n"(512 * (K - 8)) : "memory");
  else if constexpr (K < 24) asm volatile("ds_read_b64 %0, %1 offset:%2" : "=&v"(f.ah[K - 16]) : "v"(aa), "n"(512 * (K - 16) + 8) : "memory");
}
template <int K = 0> __device__ __forceinline__ void conv_step(f32x4 (&acc)[8], const ConvFrags& f, ConvFrags& nx, unsigned aa, unsigned ba) {
  if constexpr (K < 36) {
    constexpr int a = K < 8 ? 0 : K < 15 ? 1 : K < 21 ? 2 : K < 26 ? 3 : K < 30 ? 4 : K < 33 ? 5 : K < 35 ? 6 : 7;
    constexpr int i = K - (a == 0 ? 0 : a == 1 ? 8 : a == 2 ? 15 : a == 3 ? 21 : a == 4 ? 26 : a == 5 ? 30 : a == 6 ? 33 : 35);
    acc[a + i] = mfma16(mk8(f.al[7 - a], f.ah[7 - a]), __builtin_bit_cast(bf16x8, f.B[i]), acc[a + i]);
    conv_read1<K>(nx, aa, ba);
    SBAR();
    conv_step<K + 1>(acc, f, nx, aa, ba);
  }
}
__device__ __forceinline__ void conv_main(f32x4 (&acc)[8], unsigned cb, int lane, int c0, int c1) {
  const int fr = lane & 15, g4 = lane >> 4;
  const unsigned abase = cb + (3 - (fr & 3)) * CPS + 2 * (2044 - 16 * 112 - 16 * (g4 >> 1) + 8 * (g4 & 1) - (fr & ~3));
  const unsigned bbase = cb + UOFF + 512 + 2 * (16 * fr - 16 * (g4 >> 1) + 8 * (g4 & 1));
#pragma unroll
  for (int c = 0; c < 8; c++) acc[c] = (f32x4){0.f, 0.f, 0.f, 0.f};
  ConvFrags f0, f1;
  asm volatile("s_waitcnt lgkmcnt(0)" ::: "memory");
  conv_read(f0, abase - 64 * c0, bbase - 64 * c0);
  for (int cc = c0; cc < c1; cc += 2) {
    conv_wait(f0);
    SBAR();
    conv_step(acc, f0, f1, abase - 64 * (cc + 1), bbase - 64 * (cc + 1));
    conv_wait(f1);
    SBAR();
    const int cn = cc + 2 < c1 ? cc + 2 : cc + 1;
    conv_step(acc, f1, f0, abase - 64 * cn, bbase - 64 * cn);
  }
  conv_wait(f0);
}
__device__ __forceinline__ void conv_store(f32x4 y, unsigned cb, int c, int lane, float Dd, __amdgpu_buffer_rsrc_t rw, int d) {
  const int fr = lane & 15, g4 = lane >> 4;
  const u32x2 uu = lds_b64(cb + UOFF + 512 + 2 * (256 * c + 16 * fr + 4 * g4));
  const float r0 = fmaf(bflo(uu.x), Dd, y[0]), r1 = fmaf(bfhi(uu.x), Dd, y[1]);
  const float r2 = fmaf(bflo(uu.y), Dd, y[2]), r3 = fmaf(bfhi(uu.y), Dd, y[3]);
  __builtin_amdgcn_raw_buffer_store_b64((u32x2){pk2(r0, r1), pk2(r2, r3)}, rw, OFF_YT + d * 4096 + (256 * c + 16 * fr + 4 * g4) * 2, 0, SC1);
}

__global__ __launch_bounds__(512, 2) void fused(const u16* __restrict__ x, const u16* __restrict__ wqkv, const u16* __restrict__ wo,
                                                const u16* __restrict__ Bk, const u16* __restrict__ Bv, const u16* __restrict__ shk,
                                                const u16* __restrict__ hk, const u16* __restrict__ Dv, const u16* __restrict__ gw,
                                                u16* __restrict__ out, char* __restrict__ ws) {
  extern __shared__ __attribute__((aligned(16))) char sm[];
  const int tid = threadIdx.x, lane = tid & 63;
  const int w = __builtin_amdgcn_readfirstlane(tid >> 6);
  const int b = blockIdx.x, xcd = b & 7, r = b >> 3;
  const int fr = lane & 15, g4 = lane >> 4;
  const unsigned lb = (unsigned)(size_t)(LAS char*)sm;
  const __amdgpu_buffer_rsrc_t rw = __builtin_amdgcn_make_buffer_rsrc(ws, 0, WS_USED, 0x00020000);
  if (r < 24 && (w >> 1) == 2) {
    const bool kv_ = r < 16;
    gemm_first_x(w - 4, lane, lb, x, (xcd & 3) * 4 + (r & 3), kv_ ? (xcd >> 2) * 4 + (r >> 2) : (xcd >> 2) * 2 + ((r - 16) >> 2), kv_);
  }
  TS_DECL TS(0);
  unsigned e0 = 0;
#define PUT_TAG() do { if (w == 7 && lane == 0) { *(LAS unsigned*)(size_t)(lb + L_TAG) = e0 + 1u; *(LAS u32x4*)(size_t)(lb + L_CNT) = (u32x4){0u, 0u, 0u, 0u}; } } while (0)
#define TAG (*(const LAS unsigned*)(size_t)(lb + L_TAG))
  if (r < 24) {
    const bool isKV = r < 16;
    const int rt = (xcd & 3) * 4 + (r & 3);
    const int cg = isKV ? (xcd >> 2) * 4 + (r >> 2) : (xcd >> 2) * 2 + ((r - 16) >> 2);
    f32x4 acc[4][5];
    const int rp = (w >> 1) & 1, hf = w & 1;
    if (w < 4) {
      const unsigned xa0 = lb + (rp * 64 + fr) * 128 + ((g4 ^ ((fr >> 1) & 7)) << 4);
      const unsigned wl = lb + XS + (8 * g4 + (fr >> 2)) * 320 + ((lane & 3) >> 1) * 16 + (lane & 1) * 8 + hf * 160;
      const unsigned sgn = (g4 & 1) * 32;
      const unsigned wa0 = hf ? wl - sgn : wl + sgn, wa1 = hf ? wl + sgn : wl - sgn;
#pragma unroll
      for (int i = 0; i < 4; i++)
#pragma unroll
        for (int t = 0; t < 5; t++) acc[i][t] = (f32x4){0.f, 0.f, 0.f, 0.f};
      if (isKV) gemm_compute<true>(acc, xa0, wa0, wa1 TSARG); else gemm_compute<false>(acc, xa0, wa0, wa1 TSARG);
    } else if (w < 6) {
      gemm_loader(w - 4, lane, lb, x, wqkv, rt, cg, isKV);
    } else {
      const int w6 = w - 6;
      const char* pfa;
      {
        const int q = rt & 3, u = isKV ? (cg & 3) : 4 + (cg & 1);
        if (lane < 32) {
          const int row = 16 * q + 8 * w6 + (lane >> 2);
          const unsigned col = isKV ? ((lane & 2) ? 2560u : 1280u) + 160u * cg : 320u * cg;
          const unsigned ln = isKV ? (lane & 1) : ((lane & 3) == 3 ? 2 : (lane & 3));
          pfa = (const char*)wqkv + row * 3840 + ((col >> 7) + ln) * 128;
        } else {
          int row = u * 22 + w6 * 11 + (lane - 32); row = (lane < 43 && row < 128) ? row : u * 22;
          pfa = (const char*)x + (rt * 128 + row) * 1280;
        }
      }
      const int pfs0 = gemm_s0(rt, cg, isKV);
      auto prefetch = [&](int j) {
      };
      SBAR();
      if (w == 7) e0 = flag_ld(ws, F_E);
      if (isKV) {
        const int l16 = lane & 15, row0 = rt * 128 + cg * 16 + w6 * 8;
        const u32x4 g0 = *(const u32x4*)(gw + lane * 8), g1 = *(const u32x4*)(gw + 512 + l16 * 8);
        u32x4 gx0[8], gx1[8];
#pragma unroll
        for (int i = 0; i < 8; i++) {
          const u16* xr = x + (size_t)(row0 + i) * D;
          gx0[i] = *(const u32x4*)(xr + lane * 8); gx1[i] = *(const u32x4*)(xr + 512 + l16 * 8);
        }
        SBAR();
        BAR(); prefetch(4); prefetch(5); BAR(); prefetch(6); prefetch(7); BAR(); prefetch(8); prefetch(9);
        float a[8];
        const float m1 = lane < 16 ? 1.f : 0.f;
#pragma unroll
        for (int i = 0; i < 8; i++) {
          float a0 = 0.f, a1 = 0.f;
#pragma unroll
          for (int j = 0; j < 4; j++) {
            a0 = fmaf(bflo(gx0[i][j]), bflo(g0[j]), a0); a0 = fmaf(bfhi(gx0[i][j]), bfhi(g0[j]), a0);
            a1 = fmaf(bflo(gx1[i][j]), bflo(g1[j]), a1); a1 = fmaf(bfhi(gx1[i][j]), bfhi(g1[j]), a1);
          }
          a[i] = fmaf(a1, m1, a0);
          if (i == 3) { SBAR(); BAR(); SBAR(); }
        }
        SBAR(); BAR(); SBAR();
#pragma unroll
        for (int i = 0; i < 4; i++) { const float lo = a[i], hi = a[i + 4]; const bool up = lane & 4; a[i] = (up ? hi : lo) + __shfl_xor(up ? lo : hi, 4); }
#pragma unroll
        for (int i = 0; i < 2; i++) { const float lo = a[i], hi = a[i + 2]; const bool up = lane & 2; a[i] = (up ? hi : lo) + __shfl_xor(up ? lo : hi, 2); }
        { const float lo = a[0], hi = a[1]; const bool up = lane & 1; a[0] = (up ? hi : lo) + __shfl_xor(up ? lo : hi, 1); }
        a[0] += __shfl_xor(a[0], 8); a[0] += __shfl_xor(a[0], 16); a[0] += __shfl_xor(a[0], 32);
        if (lane < 8) __builtin_amdgcn_raw_buffer_store_b32(__float_as_uint(rbf(1.f / (1.f + __expf(-rbf(a[0]))))), rw, OFF_G + (row0 + lane) * 4, 0, SC1);
        PUT_TAG();
        SBAR();
        BAR(); BAR(); BAR(); BAR(); BAR();
      } else {
        const int hw = (xcd * 8 + (r - 16)) * 2 + w6;
        const unsigned scr = lb + L_T + w6 * 6144;
#pragma unroll
        for (int bt = 0; bt < 2; bt++) {
          const int blk = bt == 0 ? hw : (hw < 32 ? 128 + hw : hw), ct = (blk * 3277) >> 16, ks = blk - 20 * ct;
          u32x4 v[5];
#pragma unroll
          for (int i = 0; i < 5; i++) {
            const int c = i * 64 + lane, row = (c * 6554) >> 16, ch = c - 10 * row;
            v[i] = *(const u32x4*)(wo + (size_t)(32 * ks + row) * D + 80 * ct + 8 * ch);
          }
          SBAR();
          BAR();
          if (bt == 0) { PUT_TAG(); prefetch(4); prefetch(5); }
          BAR();
          if (bt == 0) { prefetch(6); prefetch(7); }
          if (bt == 0 || hw < 32) {
#pragma unroll
            for (int i = 0; i < 5; i++) *(LAS u32x4*)(size_t)(scr + (i * 64 + lane) * 16) = v[i];
            const unsigned ta = scr + (8 * g4 + (fr >> 2)) * 160 + (lane & 3) * 8;
#pragma unroll
            for (int t = 0; t < 5; t++) {
              const u32x2 lo = lds_tr(ta + 32 * t), hi = lds_tr(ta + 32 * t + 640);
              __builtin_amdgcn_raw_buffer_store_b128((u32x4){lo.x, lo.y, hi.x, hi.y}, rw, OFF_WOP + (blk * 5 + t) * 1024 + lane * 16, 0, SC1);
            }
          }
          SBAR();
          BAR();
          if (bt == 0) { prefetch(8); prefetch(9); }
          BAR();
        }
        DRAIN();
        if (lds_last(lb + L_CNT + 8, lane, 2) && lane == 0) flag_st(ws, F_W + (xcd * 8 + (r - 16)) * 128, TAG);
        BAR(); BAR();
      }
    }
    TS(1);
    BAR();
    TS(11);
    const unsigned tag = TAG;
    if (isKV) {
      if (w < 4) {
#pragma unroll
        for (int t = 0; t < 5; t++) {
          const bool isk = hf ? t < 2 : t < 3;
          const int slot = hf ? (t < 2 ? 3 + t : 5 + t) : (t < 3 ? t : 2 + t);
#pragma unroll
          for (int m = 0; m < 2; m++) {
            u32x4 f;
            if (isk) f = (u32x4){phi_pk(acc[2 * m][t][0], acc[2 * m][t][1]), phi_pk(acc[2 * m][t][2], acc[2 * m][t][3]),
                                 phi_pk(acc[2 * m + 1][t][0], acc[2 * m + 1][t][1]), phi_pk(acc[2 * m + 1][t][2], acc[2 * m + 1][t][3])};
            else f = (u32x4){pk2(acc[2 * m][t][0], acc[2 * m][t][1]), pk2(acc[2 * m][t][2], acc[2 * m][t][3]),
                             pk2(acc[2 * m + 1][t][0], acc[2 * m + 1][t][1]), pk2(acc[2 * m + 1][t][2], acc[2 * m + 1][t][3])};
            *(LAS u32x4*)(size_t)(lb + ((rp * 2 + m) * 10 + slot) * 1024 + lane * 16) = f;
          }
        }
      } else {
        for (int i = tid - 256; i < 6048 / 16; i += 256) *(LAS u32x4*)(size_t)(lb + L_T + i * 16) = (u32x4){0, 0, 0, 0};
        if (w == 7) poll(ws, F_W + lane * 128, true, tag);
      }
      TS(12);
      BAR();
      TS(2);
      switch (w) {
        case 0: stat_tiles<0>(lb, lane, rw, OFF_P + (rt * 8 + cg) * 3360); break;
        case 1: stat_tiles<1>(lb, lane, rw, OFF_P + (rt * 8 + cg) * 3360); break;
        case 2: stat_tiles<2>(lb, lane, rw, OFF_P + (rt * 8 + cg) * 3360); break;
        case 3: stat_tiles<3>(lb, lane, rw, OFF_P + (rt * 8 + cg) * 3360); break;
        case 4: stat_tiles<4>(lb, lane, rw, OFF_P + (rt * 8 + cg) * 3360); break;
        case 5: stat_tiles<5>(lb, lane, rw, OFF_P + (rt * 8 + cg) * 3360); break;
        case 6: stat_tiles<6>(lb, lane, rw, OFF_P + (rt * 8 + cg) * 3360); break;
        default: stat_tiles<7>(lb, lane, rw, OFF_P + (rt * 8 + cg) * 3360); break;
      }
      TS(14);
      DRAIN();
      TS(15);
      BAR();
      TS(3);
      if (w == 0) {
        if (lane == 0) flag_st(ws, F_P + (rt * 8 + cg) * 128, tag);
        poll(ws, lane < 16 ? F_P + (lane * 8 + cg) * 128 : F_PQ + (rt * 4 + (cg >> 1)) * 128, lane <= 16, tag);
      } else if (w >= 4) {
        const i32x4 sws = make_srd(ws, WS_USED);
        const unsigned vwv = OFF_WOP + ((xcd >> 2) * 4 + (r >> 3)) * 102400 + lane * 16;
        for (int i = w - 4; i < 100; i += 4) {
          int pc = 12 * (r & 7) + i; pc = __builtin_amdgcn_readfirstlane(pc >= 100 ? pc - 100 : pc);
          glds16_sc1(lb + pc * 1024, vwv, sws, pc * 1024);
        }
      }
      constexpr int PJ[9] = {0, 1, 1, 2, 2, 3, 3, 4, 4}, PK[9] = {0, 0, 1, 0, 1, 1, 2, 1, 2};
      unsigned an[9], ad[9];
#pragma unroll
      for (int p = 0; p < 9; p++) {
        const int E = 16 * PJ[p] + fr, h = (E * 205) >> 12, e = E - 20 * h;
        int o_ = 32 * PK[p] + 8 * g4 - 20 * h; o_ = o_ < -8 ? -8 : o_ > 20 ? 20 : o_;
        an[p] = lb + L_T + (h * 21 + e) * 72 + (8 + o_) * 2; ad[p] = lb + L_T + (h * 21 + 20) * 72 + (8 + o_) * 2;
      }
      BAR();
      TS(4);
      bf16x8 Bq[3];
      {
        const unsigned prow = OFF_PQ + ((rt * 8 + w) * 8 + cg) * 3072 + lane * 16;
#pragma unroll
        for (int ks = 0; ks < 3; ks++) Bq[ks] = __builtin_bit_cast(bf16x8, __builtin_amdgcn_raw_buffer_load_b128(rw, prow + 1024 * ks, 0, SC1));
      }
      if (tid < 210) {
        const unsigned pb = OFF_P + cg * 3360 + tid * 16;
        u32x4 v[16];
#pragma unroll
        for (int k = 0; k < 16; k++) v[k] = __builtin_amdgcn_raw_buffer_load_b128(rw, pb, (k ^ rt) * (8 * 3360), SC1);
        unsigned o4[4];
#pragma unroll
        for (int c = 0; c < 4; c++) {
          float lo[16], hi[16];
#pragma unroll
          for (int k = 0; k < 16; k++) { lo[k] = bflo(v[k][c]); hi[k] = bfhi(v[k][c]); }
#pragma unroll
          for (int st = 1; st < 16; st <<= 1)
#pragma unroll
            for (int k = 0; k < 16; k += 2 * st) { lo[k] += lo[k + st]; hi[k] += hi[k + st]; }
          o4[c] = pk2(lo[0], hi[0]);
        }
        TS(16);
#pragma unroll
        for (int q = 0; q < 2; q++) {
          const int i4 = 2 * tid + q, row = (i4 * 13108) >> 16, d = 4 * (i4 - row * 5);
          *(LAS u32x2*)(size_t)(lb + L_T + row * 72 + (8 + d) * 2) = (u32x2){o4[2 * q], o4[2 * q + 1]};
        }
      }
      BAR();
      TS(5);
      {
        if (g4 >= 2) Bq[2] = __builtin_bit_cast(bf16x8, (u32x4){0, 0, 0, 0});
        f32x4 nu[5], de[5];
#pragma unroll
        for (int j = 0; j < 5; j++) { nu[j] = (f32x4){0.f, 0.f, 0.f, 0.f}; de[j] = nu[j]; }
        bf16x8 An[9], Ad[9];
#pragma unroll
        for (int p = 0; p < 9; p++) {
          unsigned an8 = an[p] + 8, ad8 = ad[p] + 8;
          asm volatile("" : "+v"(an8), "+v"(ad8));
          An[p] = mk8(lds_b64(an[p]), lds_b64(an8)); Ad[p] = mk8(lds_b64(ad[p]), lds_b64(ad8));
        }
#pragma unroll
        for (int p = 0; p < 9; p++) { de[PJ[p]] = mfma16(Ad[p], Bq[PK[p]], de[PJ[p]]); nu[PJ[p]] = mfma16(An[p], Bq[PK[p]], nu[PJ[p]]); }
        u32x2 ov[5];
#pragma unroll
        for (int j = 0; j < 5; j++) {
          const float rd = __builtin_amdgcn_rcpf(de[j][0] + 1e-6f);
          ov[j] = (u32x2){pk2(nu[j][0] * rd, nu[j][1] * rd), pk2(nu[j][2] * rd, nu[j][3] * rd)};
        }
        store_frags(ov, rw, OFF_O + (rt * 8 + w) * 20480, 80 * cg, fr, g4);
      }
      TS(17);
      DRAIN();
      BAR();
      if (tid == 0) flag_st(ws, F_O + (rt * 8 + cg) * 128, tag);
      TS(6);
    } else {
      const int vb = xcd * 8 + (r - 16);
      const int d = 80 * (vb >> 3) + 48 + 4 * (vb & 7) + (w & 3);
      const unsigned cb = lb + (w & 3) * CHS;
      if (w < 4) {
#pragma unroll
        for (int i = 0; i < 4; i++) {
          u32x2 pv[5];
#pragma unroll
          for (int t = 0; t < 5; t++) pv[t] = (u32x2){phi_pk(acc[i][t][0], acc[i][t][1]), phi_pk(acc[i][t][2], acc[i][t][3])};
          store_frags(pv, rw, OFF_PQ + ((rt * 8 + rp * 4 + i) * 8 + cg * 2 + hf) * 3072, 0, fr, g4);
        }
      } else {
        u32x4 hc[4], hp[4];
        conv_load_h(hc, hp, hk + (size_t)d * S, lane);
        poll(ws, F_U + lane * 128, true, tag);
        conv_load_u(rw, d, cb, lane, [&]() { conv_prep_h(hc, hp, cb, lane); });
      }
      TS(12);
      DRAIN();
      BAR();
      TS(2);
      if (tid == 0) flag_st(ws, F_PQ + (rt * 4 + cg) * 128, tag);
      if (w < 4) {
        const float Dd = bflo(Dv[d]);
        TS(3);
        f32x4 y[8];
        conv_main(y, cb, lane, 0, 8);
        TS(4);
#pragma unroll
        for (int c = 0; c < 8; c++) conv_store(y[c], cb, c, lane, Dd, rw, d);
        DRAIN();
      } else if (w == 7) {
        poll(ws, F_W + lane * 128, true, tag);
      }
      BAR();
      if (tid == 0) flag_st(ws, F_Y + (64 + vb) * 128, tag);
      TS(5); TS(6);
    }
  } else {
    const int pw = xcd * 8 + (r - 24);
    const int c6 = w < 4 ? w : 4 + ((w - 4) >> 1);
    const int d = 80 * (pw >> 3) + 6 * (pw & 7) + c6;
    const unsigned cb = lb + c6 * CHS;
    const bool prep = w < 4 || !(w & 1);
    u32x4 hc[4], hp[4];
    {
      const int rbase = xcd * 256 + ((r - 24) >> 1) * 64, cbase = ((r - 24) & 1) * 320;
      u32x4 kr[2][7], vr[2][4], sp[2][4];
#pragma unroll
      for (int q = 0; q < 2; q++) {
        const int it = w + 8 * q;
        if (q == 0 || w < 2) {
          const int ch = cbase + (it >> 1) * 64 + (lane & 7) * 8;
          const int s0 = rbase + (it & 1) * 32 + (lane >> 3) * 4;
#pragma unroll
          for (int j = 0; j < 7; j++) { int s = s0 - 3 + j; kr[q][j] = *(const u32x4*)(Bk + (size_t)(s < 0 ? 0 : s) * D + ch); }
#pragma unroll
          for (int j = 0; j < 4; j++) vr[q][j] = *(const u32x4*)(Bv + (size_t)(s0 + j) * D + ch);
#pragma unroll
          for (int j = 0; j < 4; j++) sp[q][j] = ((const u32x4*)(shk + ch * 4))[j];
        }
      }
      if (prep) conv_load_h(hc, hp, hk + (size_t)d * S, lane);
      if (w == 7) e0 = flag_ld(ws, F_E);
      SBAR();
#pragma unroll
      for (int q = 0; q < 2; q++) {
        const int it = w + 8 * q;
        if (q == 0 || w < 2) {
          const int s0 = rbase + (it & 1) * 32 + (lane >> 3) * 4;
          if (s0 == 0) { kr[q][0] = (u32x4){0, 0, 0, 0}; kr[q][1] = kr[q][0]; kr[q][2] = kr[q][0]; }
          const int rg = (it & 1) * 8 + (lane >> 3);
          const unsigned la = lb + L_UIMG + ((it >> 1) * 64 + (lane & 7) * 8) * 128 + (((rg >> 1) ^ (lane & 7)) << 4) + (rg & 1) * 8;
#pragma unroll
          for (int c = 0; c < 8; c++) {
            const unsigned s01 = sp[q][c >> 1][(c & 1) * 2], s23 = sp[q][c >> 1][(c & 1) * 2 + 1];
            const float w0 = bflo(s01), w1 = bfhi(s01), w2 = bflo(s23), w3 = bfhi(s23);
            float kk[7], u[4];
#pragma unroll
            for (int j = 0; j < 7; j++) { unsigned dd = kr[q][j][c >> 1]; kk[j] = (c & 1) ? bfhi(dd) : bflo(dd); }
#pragma unroll
            for (int j = 0; j < 4; j++) {
              unsigned dd = vr[q][j][c >> 1]; float v = (c & 1) ? bfhi(dd) : bflo(dd);
              float ks = kk[j] * w3; ks = fmaf(kk[j + 1], w2, ks); ks = fmaf(kk[j + 2], w1, ks); ks = fmaf(kk[j + 3], w0, ks);
              u[j] = ks * v;
            }
            *(LAS u32x2*)(size_t)(la + c * 128) = (u32x2){pk2(u[0], u[1]), pk2(u[2], u[3])};
          }
        }
      }
      PUT_TAG();
      BAR();
#pragma unroll
      for (int i = 0; i < 5; i++) {
        const int chl = (w * 5 + i) * 8 + (lane >> 3), k = lane & 7;
        const u32x4 v = *(const LAS u32x4*)(size_t)(lb + L_UIMG + chl * 128 + ((k ^ ((chl >> 3) & 7)) << 4));
        __builtin_amdgcn_raw_buffer_store_b128(v, rw, OFF_UT + (cbase + chl) * 4096 + rbase * 2 + k * 16, 0, SC1);
      }
      TS(1);
    }
    if (w < 5 && prep) conv_prep_h(hc, hp, cb, lane);
    DRAIN();
    BAR();
    TS(2);
    const unsigned tag = TAG;
    if (tid == 0) flag_st(ws, F_U + pw * 128, tag);
    const float Dd = bflo(Dv[d]);
    if (prep) {
      if (w == 6) conv_prep_h(hc, hp, cb, lane);
      poll(ws, F_U + lane * 128, true, tag);
      conv_load_u(rw, d, cb, lane, [&]() {});
    }
    BAR();
    TS(3);
    f32x4 y[8];
    conv_main(y, cb, lane, w < 4 ? 0 : (w & 1) * 4, w < 4 ? 8 : (w & 1) * 4 + 4);
    TS(4);
    if (w < 4) {
#pragma unroll
      for (int c = 0; c < 8; c++) conv_store(y[c], cb, c, lane, Dd, rw, d);
    }
    BAR();
    if (w >= 4) {
#pragma unroll
      for (int c = 0; c < 4; c++) { const f32x4 lo = y[c], hi = y[4 + c]; *(LAS f32x4*)(size_t)(cb + ((w & 1) * 4 + c) * 1024 + lane * 16) = (w & 1) ? lo : hi; }
    }
    BAR();
    if (w >= 4) {
#pragma unroll
      for (int c = 0; c < 4; c++) {
        const f32x4 o_ = *(const LAS f32x4*)(size_t)(cb + (((w & 1) ^ 1) * 4 + c) * 1024 + lane * 16);
        const f32x4 lo = y[c], hi = y[4 + c];
        const f32x4 mine = (w & 1) ? hi : lo;
        conv_store((w & 1) ? o_ + mine : mine + o_, cb, (w & 1) * 4 + c, lane, Dd, rw, d);
      }
    }
    if (w == 0) poll(ws, F_W + lane * 128, true, tag);
    DRAIN();
    BAR();
    if (tid == 0) flag_st(ws, F_Y + pw * 128, tag);
    TS(5); TS(6);
  }

  {
    const unsigned tag = TAG;
    const int rt = (xcd & 3) * 8 + (r & 7), ct = (xcd >> 2) * 4 + (r >> 3);
    const int sw_ = w & 3, kk = w >> 2;
    const i32x4 sws = make_srd(ws, WS_USED);
    TS(18);
    const int srow = rt * 64 + sw_ * 16 + fr;
    u32x2 xv[3];
#pragma unroll
    for (int t = 0; t < 3; t++) { const int tt = kk ? (t < 2 ? 3 + t : 4) : t; xv[t] = *(const u32x2*)(x + (size_t)srow * D + ct * 80 + 16 * tt + 4 * g4); }
    SBAR();
    const int rot = ((ct & 3) * 5) >> 1;
    const unsigned wa0 = lb + kk * 5120 + lane * 16;
    bf16x8 wf0[5][5];
    auto read_wf0 = [&]() {
#pragma unroll
      for (int s = 0; s < 5; s++) {
        int q = s + rot; q = q >= 10 ? q - 10 : q;
#pragma unroll
        for (int t = 0; t < 5; t++) wf0[s][t] = lds_b128(wa0 + q * 10240 + t * 1024);
      }
    };
    if (r < 16) read_wf0();
    SBAR();
    if (w == 0) {
      poll(ws, F_O + ((rt >> 1) * 8 + (lane & 7)) * 128, lane < 8, tag);
    } else if (w == 7) {
      poll(ws, F_Y + ((lane & 8 ? 64 : 0) + ct * 8 + (lane & 7)) * 128, lane < 16, tag);
    } else {
      if (r >= 16) {
        const unsigned vwv = OFF_WOP + ct * 102400 + lane * 16;
        for (int i = w - 1; i < 100; i += 6) { int pc = 12 * (r & 7) + i; pc = __builtin_amdgcn_readfirstlane(pc >= 100 ? pc - 100 : pc); glds16_sc1(lb + pc * 1024, vwv, sws, pc * 1024); }
      }
      if (w == 6 && b == 255) {
        for (int spins = 0; spins < (1 << 17); spins++) {
          const unsigned a0 = flag_ld(ws, F_U + lane * 128), a1 = flag_ld(ws, F_U + (64 + lane) * 128);
          const unsigned a2 = flag_ld(ws, F_U + (128 + lane) * 128), a3 = flag_ld(ws, F_U + (192 + lane) * 128);
          if (__all(a0 == tag && a1 == tag && a2 == tag && a3 == tag)) break;
          __builtin_amdgcn_s_sleep(2);
        }
        if (lane == 0) flag_st(ws, F_E, tag);
      }
      DRAIN();
    }
    BAR();
    TS(7);
    asm volatile("" :: "v"(xv[0]), "v"(xv[1]), "v"(xv[2]));
    bf16x8 of[10];
    {
      const unsigned vo = OFF_O + (rt * 4 + sw_) * 20480 + kk * 1024 + lane * 16;
#pragma unroll
      for (int s = 0; s < 10; s++) { int q = s + rot; q = __builtin_amdgcn_readfirstlane(q >= 10 ? q - 10 : q); bload16_sc1(of[s], vo, sws, q * 2048); }
    }
    if (w == 7) {
      for (int pc = 0; pc < 10; pc++)
        glds16_sc1(lb + L_Y + pc * 1024, OFF_YT + (ct * 80 + pc * 8 + (lane >> 3)) * 4096 + rt * 128 + (lane & 7) * 16, sws, 0);
    }
    float g;
    asm volatile("buffer_load_dword %0, %1, %2, 0 offen sc1" : "=v"(g) : "v"(OFF_G + srow * 4), "s"(sws) : "memory");
    TS(8);
    if (r >= 16) read_wf0();
    f32x4 acc[5];
#pragma unroll
    for (int t = 0; t < 5; t++) acc[t] = (f32x4){0.f, 0.f, 0.f, 0.f};
    {
      bf16x8 wf1[5][5];
#pragma unroll
      for (int s = 0; s < 5; s++) {
        int q = 5 + s + rot; q = q >= 10 ? q - 10 : q;
#pragma unroll
        for (int t = 0; t < 5; t++) wf1[s][t] = lds_b128(wa0 + q * 10240 + t * 1024);
      }
      SBAR();
      if (w == 7) asm volatile("s_waitcnt vmcnt(16)" ::: "memory"); else asm volatile("s_waitcnt vmcnt(6)" ::: "memory");
      asm volatile("" : "+v"(of[0]), "+v"(of[1]), "+v"(of[2]), "+v"(of[3]), "+v"(of[4]) :: "memory");
      SBAR();
#pragma unroll
      for (int s = 0; s < 5; s++)
#pragma unroll
        for (int t = 0; t < 5; t++) acc[t] = mfma16(wf0[s][t], of[s], acc[t]);
      SBAR();
      if (w == 7) asm volatile("s_waitcnt vmcnt(11)" ::: "memory"); else asm volatile("s_waitcnt vmcnt(1)" ::: "memory");
      asm volatile("" : "+v"(of[5]), "+v"(of[6]), "+v"(of[7]), "+v"(of[8]), "+v"(of[9]) :: "memory");
      SBAR();
#pragma unroll
      for (int s = 0; s < 5; s++)
#pragma unroll
        for (int t = 0; t < 5; t++) acc[t] = mfma16(wf1[s][t], of[5 + s], acc[t]);
      SBAR();
    }
    TS(9);
    const unsigned ra = lb + L_RED + (sw_ * 5) * 1024 + lane * 16;
    if (kk == 0) { *(LAS f32x4*)(size_t)(ra + 3 * 1024) = acc[3]; *(LAS f32x4*)(size_t)(ra + 4 * 1024) = acc[4]; }
    else { *(LAS f32x4*)(size_t)(ra) = acc[0]; *(LAS f32x4*)(size_t)(ra + 1024) = acc[1]; *(LAS f32x4*)(size_t)(ra + 2048) = acc[2]; }
    asm volatile("s_waitcnt vmcnt(0)" : "+v"(g) :: "memory");
    BAR();
    TS(20);
    const float omg = 1.f - g;
    const __amdgpu_buffer_rsrc_t ro = __builtin_amdgcn_make_buffer_rsrc(out, 0, SZ, 0x00020000);
    u32x2 rv[3];
#pragma unroll
    for (int t = 0; t < 3; t++) {
      const int tt = kk ? 3 + (t < 2 ? t : 1) : t;
      const f32x4 a0 = acc[t], a1 = acc[3 + (t < 2 ? t : 1)];
      const f32x4 a = *(const LAS f32x4*)(size_t)(ra + tt * 1024) + (kk ? a1 : a0);
      const u32x2 yv = lds_tr(lb + L_Y + (16 * tt + 4 * g4 + (fr >> 2)) * 128 + (sw_ * 16 + 4 * (fr & 3)) * 2);
      const float xs[4] = {bflo(xv[t].x), bfhi(xv[t].x), bflo(xv[t].y), bfhi(xv[t].y)};
      const float ys[4] = {bflo(yv.x), bfhi(yv.x), bflo(yv.y), bfhi(yv.y)};
      float r_[4];
#pragma unroll
      for (int q = 0; q < 4; q++) r_[q] = fmaf(g, a[q], omg * (xs[q] * ys[q]));
      rv[t] = (u32x2){pk2(r_[0], r_[1]), pk2(r_[2], r_[3])};
    }
    __builtin_amdgcn_raw_buffer_store_b128(pair16(rv[0], rv[1]), ro, (srow * D + ct * 80 + 16 * ((kk ? 3 : 0) + (g4 & 1)) + 8 * (g4 >> 1)) * 2, 0, SC1);
    if (kk == 0) __builtin_amdgcn_raw_buffer_store_b64(rv[2], ro, (srow * D + ct * 80 + 16 * (2) + 4 * g4) * 2, 0, SC1);
    TS(10); TS_OUT(21);
  }
}

extern "C" void kernel_launch(void* const* d_in, const int* in_sizes, int n_in, void* d_out, int out_size, void* d_ws, size_t ws_size,
                              hipStream_t stream) {
  const u16* x = (const u16*)d_in[0]; const u16* wqkv = (const u16*)d_in[1]; const u16* wo = (const u16*)d_in[2];
  const u16* Bk = (const u16*)d_in[4]; const u16* Bv = (const u16*)d_in[5]; const u16* shk = (const u16*)d_in[6];
  const u16* hk = (const u16*)d_in[7]; const u16* Dv = (const u16*)d_in[8]; const u16* gw = (const u16*)d_in[9];
  (void)hipFuncSetAttribute((const void*)fused, hipFuncAttributeMaxDynamicSharedMemorySize, LDS_TOTAL);
  fused<<<256, 512, LDS_TOTAL, stream>>>(x, wqkv, wo, Bk, Bv, shk, hk, Dv, gw, (u16*)d_out, (char*)d_ws);
}
```

```cpp
#include <hip/hip_runtime.h>
#include <hip/hip_bf16.h>
#include <type_traits>
#define NO_PF

typedef unsigned short u16;
typedef __bf16 bf16x2 __attribute__((ext_vector_type(2)));
typedef __bf16 bf16x8 __attribute__((ext_vector_type(8)));
typedef float f32x4 __attribute__((ext_vector_type(4)));
typedef unsigned u32x4 __attribute__((ext_vector_type(4)));
typedef unsigned u32x2 __attribute__((ext_vector_type(2)));
typedef short v4i16_t __attribute__((ext_vector_type(4)));
typedef int i32x4 __attribute__((ext_vector_type(4)));
typedef __attribute__((address_space(1))) unsigned gu32;
#define LAS __attribute__((address_space(3)))
#define SBAR() __builtin_amdgcn_sched_barrier(0)
#define SGB(mask, n) __builtin_amdgcn_sched_group_barrier(mask, n, 0)
#define BAR() asm volatile("s_waitcnt lgkmcnt(0)\n\ts_barrier" ::: "memory")
#define DRAIN() asm volatile("s_waitcnt vmcnt(0)" ::: "memory")
#define SC1 16

#define TS_DECL
#define TSARG
#define TSPAR
#define TS(i)
#define TS_OUT(n)

constexpr int S = 2048, D = 640;
constexpr unsigned SZ = S * D * 2;
constexpr unsigned F_E = 0, F_U = 128, F_PQ = F_U + 64 * 128, F_P = F_PQ + 64 * 128, F_O = F_P + 128 * 128, F_Y = F_O + 128 * 128, F_W = F_Y + 128 * 128;
constexpr unsigned OFF_PQ = 0x20000, OFF_P = OFF_PQ + 128 * 8 * 3 * 1024, OFF_UT = OFF_P + (1u << 19), OFF_G = OFF_UT + SZ, OFF_WOP = OFF_G + 8192,
                   OFF_O = OFF_WOP + (1u << 20), OFF_YT = OFF_O + SZ, WS_USED = OFF_YT + SZ;
constexpr int XS = 16384, WSB = 20480, STG = XS + WSB;
constexpr int L_EXTRA = 4 * STG;
constexpr int L_TAG = L_EXTRA, L_CNT = L_EXTRA + 16, L_T = L_EXTRA + 64;
constexpr int L_SCR = L_T + 6144 + 5120;
constexpr int LDS_TOTAL = L_SCR + 256;
constexpr int CPS = 4160, UOFF = 4 * CPS, CHS = UOFF + 4608;
constexpr int L_UIMG = 5 * CHS;
constexpr int L_Y = 102400, L_RED = L_Y + 10240;

__device__ __forceinline__ unsigned pk2(float a, float b) { bf16x2 r = {(__bf16)a, (__bf16)b}; return __builtin_bit_cast(unsigned, r); }
__device__ __forceinline__ float rbf(float a) { return (float)(__bf16)a; }
__device__ __forceinline__ float bflo(unsigned v) { return __uint_as_float(v << 16); }
__device__ __forceinline__ float bfhi(unsigned v) { return __uint_as_float(v & 0xffff0000u); }
__device__ __forceinline__ float phi(float t) {
  return __builtin_amdgcn_fmed3f(t, 0.f, 3.0e38f) + (__builtin_amdgcn_fmed3f(__builtin_amdgcn_exp2f(t * 1.44269504f), 0.f, 1.f) + 1e-6f);
}
typedef float f32x2 __attribute__((ext_vector_type(2)));
__device__ __forceinline__ unsigned phi_pk(float a, float b) {
  const f32x2 t = {a, b};
  const f32x2 y = t * (f32x2){1.44269504f, 1.44269504f};
  const f32x2 e = {__builtin_amdgcn_fmed3f(__builtin_amdgcn_exp2f(y.x), 0.f, 1.f), __builtin_amdgcn_fmed3f(__builtin_amdgcn_exp2f(y.y), 0.f, 1.f)};
  const f32x2 m = {__builtin_amdgcn_fmed3f(a, 0.f, 3.0e38f), __builtin_amdgcn_fmed3f(b, 0.f, 3.0e38f)};
  const f32x2 r = m + (e + (f32x2){1e-6f, 1e-6f});
  return pk2(r.x, r.y);
}
__device__ __forceinline__ i32x4 make_srd(const void* p, unsigned bytes) {
  unsigned long long a = (unsigned long long)p; i32x4 r;
  r.x = __builtin_amdgcn_readfirstlane((int)(unsigned)a); r.y = __builtin_amdgcn_readfirstlane((int)(unsigned)(a >> 32));
  r.z = __builtin_amdgcn_readfirstlane((int)bytes); r.w = 0x00020000; return r;
}
__device__ __forceinline__ void glds16(unsigned ldsaddr, unsigned voff, i32x4 srd, unsigned soff) {
  asm volatile("s_mov_b32 m0, %0\n\ts_nop 0\n\tbuffer_load_dwordx4 %1, %2, %3 offen lds" :: "s"(ldsaddr), "v"(voff), "s"(srd), "s"(soff) : "memory");
}
__device__ __forceinline__ void glds16_sc1(unsigned ldsaddr, unsigned voff, i32x4 srd, unsigned soff) {
  asm volatile("s_mov_b32 m0, %0\n\ts_nop 0\n\tbuffer_load_dwordx4 %1, %2, %3 offen sc1 lds" :: "s"(ldsaddr), "v"(voff), "s"(srd), "s"(soff) : "memory");
}
__device__ __forceinline__ void bload16_sc1(bf16x8& dst, unsigned voff, i32x4 srd, unsigned soff) {
  asm volatile("buffer_load_dwordx4 %0, %1, %2, %3 offen sc1" : "=v"(dst) : "v"(voff), "s"(srd), "s"(soff) : "memory");
}
template <int N> __device__ __forceinline__ void wait_bar() { asm volatile("s_waitcnt vmcnt(%0) lgkmcnt(0)\n\ts_barrier" :: "n"(N) : "memory"); }
__device__ __forceinline__ bf16x8 lds_b128(unsigned addr) { return *(const LAS bf16x8*)(size_t)addr; }
__device__ __forceinline__ u32x2 lds_b64(unsigned addr) { return *(const LAS u32x2*)(size_t)addr; }
__device__ __forceinline__ u32x2 lds_tr(unsigned addr) { return __builtin_bit_cast(u32x2, __builtin_amdgcn_ds_read_tr16_b64_v4i16((LAS v4i16_t*)(size_t)addr)); }
__device__ __forceinline__ bf16x8 mk8(u32x2 lo, u32x2 hi) { u32x4 t = {lo.x, lo.y, hi.x, hi.y}; return __builtin_bit_cast(bf16x8, t); }
__device__ __forceinline__ f32x4 mfma16(bf16x8 a, bf16x8 b, f32x4 c) { return __builtin_amdgcn_mfma_f32_16x16x32_bf16(a, b, c, 0, 0, 0); }

__device__ __forceinline__ u32x4 pair16(u32x2 a, u32x2 b) {
  const auto r0 = __builtin_amdgcn_permlane16_swap(a.x, b.x, false, false);
  const auto r1 = __builtin_amdgcn_permlane16_swap(a.y, b.y, false, false);
  return (u32x4){r0[0], r1[0], r0[1], r1[1]};
}
__device__ __forceinline__ void store_frags(const u32x2 (&v)[5], __amdgpu_buffer_rsrc_t rw, unsigned base, int k0, int fr, int g4) {
#pragma unroll
  for (int j = 0; j < 4; j += 2) {
    const int k = k0 + 16 * (j + (g4 & 1)) + 8 * (g4 >> 1);
    __builtin_amdgcn_raw_buffer_store_b128(pair16(v[j], v[j + 1]), rw, base + (k >> 5) * 1024 + (((k >> 3) & 3) * 16 + fr) * 16, 0, SC1);
  }
  const int k = k0 + 64 + 4 * g4;
  __builtin_amdgcn_raw_buffer_store_b64(v[4], rw, base + (k >> 5) * 1024 + (((k >> 3) & 3) * 16 + fr) * 16 + ((k >> 2) & 1) * 8, 0, SC1);
}

__device__ __forceinline__ void lds_sync(unsigned cnt_addr, int lane, unsigned n) {
  asm volatile("s_waitcnt lgkmcnt(0)" ::: "memory");
  if (lane == 0) __hip_atomic_fetch_add((LAS unsigned*)(size_t)cnt_addr, 1u, __ATOMIC_RELAXED, __HIP_MEMORY_SCOPE_WORKGROUP);
  while (__builtin_amdgcn_readfirstlane(__hip_atomic_load((LAS unsigned*)(size_t)cnt_addr, __ATOMIC_RELAXED, __HIP_MEMORY_SCOPE_WORKGROUP)) < n) {}
  asm volatile("" ::: "memory");
}
__device__ __forceinline__ bool lds_last(unsigned cnt_addr, int lane, unsigned n) {
  unsigned old = 0;
  if (lane == 0) old = __hip_atomic_fetch_add((LAS unsigned*)(size_t)cnt_addr, 1u, __ATOMIC_RELAXED, __HIP_MEMORY_SCOPE_WORKGROUP);
  return __builtin_amdgcn_readfirstlane(old) == n - 1;
}

__device__ __forceinline__ unsigned flag_ld(const char* ws, unsigned off) { return __hip_atomic_load((const gu32*)(ws + off), __ATOMIC_RELAXED, __HIP_MEMORY_SCOPE_AGENT); }
__device__ __forceinline__ void flag_st(char* ws, unsigned off, unsigned v) { __hip_atomic_store((gu32*)(ws + off), v, __ATOMIC_RELAXED, __HIP_MEMORY_SCOPE_AGENT); }
__device__ __forceinline__ void poll(const char* ws, unsigned off, bool act, unsigned tag) {
  const unsigned o0 = __builtin_amdgcn_readfirstlane(off);
  off = act ? off : o0;
  unsigned a, b, n = 1u << 17;
  asm volatile(
      "global_load_dword %0, %3, %4 sc1\n\t"
      "s_sleep 6\n"
      "1:\n\t"
      "global_load_dword %1, %3, %4 sc1\n\t"
      "s_waitcnt vmcnt(1)\n\t"
      "v_cmp_ne_u32 vcc, %0, %5\n\t"
      "s_cbranch_vccz 2f\n\t"
      "global_load_dword %0, %3, %4 sc1\n\t"
      "s_waitcnt vmcnt(1)\n\t"
      "v_cmp_ne_u32 vcc, %1, %5\n\t"
      "s_cbranch_vccz 2f\n\t"
      "s_sub_u32 %2, %2, 1\n\t"
      "s_cbranch_scc0 1b\n"
      "2:\n\t"
      "s_waitcnt vmcnt(0)"
      : "=&v"(a), "=&v"(b), "+s"(n) : "v"(off), "s"(ws), "v"(tag) : "vcc", "scc", "memory");
}

template <bool KV>
__device__ __forceinline__ void gemm_compute(f32x4 (&acc)[4][5], unsigned xa0, unsigned wa0, unsigned wa1 TSPAR) {
  bf16x8 xa[4], xb[4], wa[5], wb[5];
  auto load = [&](bf16x8 (&xf)[4], bf16x8 (&wf)[5], int buf, int kk) {
    const unsigned bo = buf * STG;
    const unsigned xa = (xa0 + bo) ^ (kk * 64);
    const unsigned a0 = wa0 + bo + kk * 10240, a1 = wa1 + bo + kk * 10240;
#pragma unroll
    for (int i = 0; i < 4; i++) xf[i] = lds_b128(xa + 2048 * i);
#pragma unroll
    for (int t = 0; t < 5; t++) { const unsigned a = ((t & 1) ? a1 : a0) + 32 * t; wf[t] = mk8(lds_tr(a), lds_tr(a + 1280)); }
  };
  auto mma = [&](const bf16x8 (&xf)[4], const bf16x8 (&wf)[5]) {
#pragma unroll
    for (int t = 0; t < 5; t++)
#pragma unroll
      for (int i = 0; i < 4; i++) acc[i][t] = KV ? mfma16(xf[i], wf[t], acc[i][t]) : mfma16(wf[t], xf[i], acc[i][t]);
  };
  auto interleave = [&]() {
#pragma unroll
    for (int t = 0; t < 14; t++) { SGB(0x8, 1); SGB(0x100, 1); }
    SGB(0x8, 6);
  };
  BAR();
  TS(21);
  load(xa, wa, 0, 0);
  SBAR();
  for (int s = 0; s < 9; s++) {
    load(xb, wb, s & 3, 1); mma(xa, wa); interleave();
    SBAR();
    BAR();
    if (s == 0) TS(22);
    if (s == 4) TS(23);
    load(xa, wa, (s + 1) & 3, 0); mma(xb, wb); interleave();
    SBAR();
  }
  load(xb, wb, 1, 1); mma(xa, wa); interleave();
  SBAR();
  mma(xb, wb);
}

__device__ __forceinline__ int gemm_s0(int rt, int cg, bool isKV) { return (((rt & 3) * 5 + 1) / 2 + (isKV ? 2 * (cg & 3) : 8 + (cg & 1))) % 10; }
__device__ __forceinline__ void gemm_vx(unsigned (&vx)[2], int L, int lane, int rt) {
#pragma unroll
  for (int i = 0; i < 2; i++) vx[i] = (rt * 128 + L * 64 + i * 8 + (lane >> 3)) * 1280 + (((lane & 7) ^ ((4 * i + (lane >> 4)) & 7)) << 4);
}
__device__ __forceinline__ void gemm_first_x(int L, int lane, unsigned lb, const u16* x, int rt, int cg, bool isKV) {
  const i32x4 sx = make_srd(x, S * D * 2);
  unsigned vx[2];
  gemm_vx(vx, L, lane, rt);
  const int s0 = gemm_s0(rt, cg, isKV);
#pragma unroll
  for (int p = 0; p < 8; p++) glds16(lb + (L * 8 + p) * 1024, vx[p & 1], sx, s0 * 128 + (p >> 1) * (16 * 1280));
}
__device__ __forceinline__ void gemm_loader(int L, int lane, unsigned lb, const u16* x, const u16* wqkv, int rt, int cg, bool isKV) {
  const int s0 = gemm_s0(rt, cg, isKV);
  const i32x4 sx = make_srd(x, S * D * 2), sw = make_srd(wqkv, D * 3 * D * 2);
  unsigned vx[2], vw[5];
  gemm_vx(vx, L, lane, rt);
#pragma unroll
  for (int i = 0; i < 5; i++) {
    const int ci = i * 64 + lane, k = (ci * 3277) >> 16, pos = ci - k * 20, gc = pos ^ (((k >> 3) & 1) << 1), tl = gc >> 1;
    unsigned col;
    if (isKV) { const int isv = (tl >= 3 && tl < 5) || tl >= 7; const int tt = tl < 3 ? tl : tl < 5 ? tl - 3 : tl < 7 ? tl - 2 : tl - 5;
                col = (isv ? 2560u : 1280u) + 160u * cg + tt * 32; }
    else col = 320u * cg + tl * 32;
    vw[i] = (L * 32 + k) * 3840 + col + (gc & 1) * 16;
  }
  auto stage = [&](int st_, int buf) {
    const unsigned base = lb + buf * STG;
    int st = st_ + s0; st = st >= 10 ? st - 10 : st;
#pragma unroll
    for (int p = 0; p < 10; p++) {
      glds16(base + XS + (L * 10 + p) * 1024, vw[p % 5], sw, st * (64 * 3840) + (p / 5) * (16 * 3840));
      if (p < 8) glds16(base + (L * 8 + p) * 1024, vx[p & 1], sx, st * 128 + (p >> 1) * (16 * 1280));
    }
  };
#pragma unroll
  for (int p = 0; p < 10; p++) glds16(lb + XS + (L * 10 + p) * 1024, vw[p % 5], sw, s0 * (64 * 3840) + (p / 5) * (16 * 3840));
  stage(1, 1); stage(2, 2);
  wait_bar<36>();
  stage(3, 3);
  for (int s = 0; s < 9; s++) {
    if (s < 7) wait_bar<36>(); else if (s == 7) wait_bar<18>(); else wait_bar<0>();
    if (s < 6) stage(s + 4, s & 3);
  }
}

template <int T> struct StatTile {
  static constexpr bool isz = T >= 13;
  static constexpr int i = isz ? T - 13 : (T + 1) / 3;
  static constexpr int j = isz ? 0 : T - (i == 0 ? 0 : 3 * i - 1) + (i == 0 ? 0 : i - 1);
};
template <int T> __device__ __forceinline__ f32x4 stat_mma(unsigned la) {
  const u32x4 o1 = {0x3F803F80u, 0x3F803F80u, 0x3F803F80u, 0x3F803F80u};
  bf16x8 A[4], B[4];
#pragma unroll
  for (int m = 0; m < 4; m++) {
    A[m] = lds_b128(la + StatTile<T>::i * 1024 + m * 10240);
    B[m] = StatTile<T>::isz ? __builtin_bit_cast(bf16x8, o1) : lds_b128(la + (5 + StatTile<T>::j) * 1024 + m * 10240);
  }
  f32x4 d4 = {0.f, 0.f, 0.f, 0.f};
#pragma unroll
  for (int m = 0; m < 4; m++) d4 = mfma16(A[m], B[m], d4);
  return d4;
}
template <int T> __device__ __forceinline__ void stat_store(f32x4 d4, int lane, __amdgpu_buffer_rsrc_t rw, unsigned pbase) {
  const int fr = lane & 15, g4 = lane >> 4;
  const int Dg = 16 * StatTile<T>::i + 4 * g4, h = (Dg * 205) >> 12, dd = Dg - 20 * h;
  const int ee = StatTile<T>::isz ? 20 : 16 * StatTile<T>::j + fr - 20 * h;
  if (StatTile<T>::isz ? fr == 0 : (ee >= 0 && ee < 20))
    __builtin_amdgcn_raw_buffer_store_b64((u32x2){pk2(d4[0], d4[1]), pk2(d4[2], d4[3])}, rw, pbase + (h * 420 + ee * 20 + dd) * 2, 0, SC1);
}
template <int W> __device__ __forceinline__ void stat_tiles(unsigned lb, int lane, __amdgpu_buffer_rsrc_t rw, unsigned pbase) {
  const unsigned la = lb + lane * 16;
  const f32x4 a = stat_mma<W>(la), b = stat_mma<W + 8>(la);
  if constexpr (W < 2) { const f32x4 c = stat_mma<W + 16>(la); stat_store<W + 16>(c, lane, rw, pbase); }
  stat_store<W>(a, lane, rw, pbase);
  stat_store<W + 8>(b, lane, rw, pbase);
}

__device__ __forceinline__ void conv_load_h(u32x4 (&cur)[4], u32x4 (&prv)[4], const u16* __restrict__ hrow, int lane) {
#pragma unroll
  for (int it = 0; it < 4; it++) {
    const int i = it * 64 + lane;
    cur[it] = *(const u32x4*)(hrow + i * 8); prv[it] = *(const u32x4*)(hrow + (i > 0 ? i * 8 - 8 : 0));
  }
}
__device__ __forceinline__ void conv_prep_h(const u32x4 (&cur)[4], const u32x4 (&prv_)[4], unsigned cb, int lane) {
#pragma unroll
  for (int it = 0; it < 4; it++) {
    u32x4 prv = prv_[it];
    if (it == 0 && lane == 0) prv = (u32x4){0, 0, 0, 0};
    const unsigned W_[8] = {prv.x, prv.y, prv.z, prv.w, cur[it].x, cur[it].y, cur[it].z, cur[it].w};
    const int c = 255 - (it * 64 + lane);
#pragma unroll
    for (int e = 0; e < 4; e++) {
      u32x4 ov;
#pragma unroll
      for (int q = 0; q < 4; q++) {
        const int i0 = 15 - e - 2 * q, i1 = i0 - 1;
        const unsigned sel = ((i0 & 1) ? 0x0706u : 0x0504u) | (((i1 & 1) ? 0x0302u : 0x0100u) << 16);
        ov[q] = __builtin_amdgcn_perm(W_[i0 >> 1], W_[i1 >> 1], sel);
      }
      *(LAS u32x4*)(size_t)(cb + e * CPS + c * 16) = ov;
    }
  }
  if (lane < 8) *(LAS u32x4*)(size_t)(cb + (lane >> 1) * CPS + (256 + (lane & 1)) * 16) = (u32x4){0, 0, 0, 0};
  if (lane < 32) *(LAS u32x4*)(size_t)(cb + UOFF + lane * 16) = (u32x4){0, 0, 0, 0};
}
template <class F>
__device__ __forceinline__ void conv_load_u(__amdgpu_buffer_rsrc_t rw, int d, unsigned cb, int lane, F&& between) {
  u32x4 uv[4];
#pragma unroll
  for (int it = 0; it < 4; it++) uv[it] = __builtin_amdgcn_raw_buffer_load_b128(rw, OFF_UT + d * 4096 + (it * 64 + lane) * 16, 0, SC1);
  SBAR();
  between();
  SBAR();
#pragma unroll
  for (int it = 0; it < 4; it++) *(LAS u32x4*)(size_t)(cb + UOFF + 512 + (it * 64 + lane) * 16) = uv[it];
}
struct ConvFrags { u32x2 al[8], ah[8]; u32x4 B[8]; };
__device__ __forceinline__ void conv_read(ConvFrags& f, unsigned aa, unsigned ba) {
  asm volatile(
      "ds_read_b128 %16, %25\n\tds_read_b64 %7, %24 offset:3584\n\tds_read_b64 %15, %24 offset:3592\n\t"
      "ds_read_b128 %17, %25 offset:512\n\tds_read_b128 %18, %25 offset:1024\n\tds_read_b128 %19, %25 offset:1536\n\t"
      "ds_read_b128 %20, %25 offset:2048\n\tds_read_b128 %21, %25 offset:2560\n\tds_read_b128 %22, %25 offset:3072\n\t"
      "ds_read_b128 %23, %25 offset:3584\n\t"
      "ds_read_b64 %6, %24 offset:3072\n\tds_read_b64 %14, %24 offset:3080\n\t"
      "ds_read_b64 %5, %24 offset:2560\n\tds_read_b64 %13, %24 offset:2568\n\t"
      "ds_read_b64 %4, %24 offset:2048\n\tds_read_b64 %12, %24 offset:2056\n\t"
      "ds_read_b64 %3, %24 offset:1536\n\tds_read_b64 %11, %24 offset:1544\n\t"
      "ds_read_b64 %2, %24 offset:1024\n\tds_read_b64 %10, %24 offset:1032\n\t"
      "ds_read_b64 %1, %24 offset:512\n\tds_read_b64 %9, %24 offset:520\n\t"
      "ds_read_b64 %0, %24\n\tds_read_b64 %8, %24 offset:8"
      : "=&v"(f.al[0]), "=&v"(f.al[1]), "=&v"(f.al[2]), "=&v"(f.al[3]), "=&v"(f.al[4]), "=&v"(f.al[5]), "=&v"(f.al[6]), "=&v"(f.al[7]),
        "=&v"(f.ah[0]), "=&v"(f.ah[1]), "=&v"(f.ah[2]), "=&v"(f.ah[3]), "=&v"(f.ah[4]), "=&v"(f.ah[5]), "=&v"(f.ah[6]), "=&v"(f.ah[7]),
        "=&v"(f.B[0]), "=&v"(f.B[1]), "=&v"(f.B[2]), "=&v"(f.B[3]), "=&v"(f.B[4]), "=&v"(f.B[5]), "=&v"(f.B[6]), "=&v"(f.B[7])
      : "v"(aa), "v"(ba) : "memory");
}
__device__ __forceinline__ void conv_wait(ConvFrags& f) {
  asm volatile("s_waitcnt lgkmcnt(0)"
      : "+v"(f.al[0]), "+v"(f.al[1]), "+v"(f.al[2]), "+v"(f.al[3]), "+v"(f.al[4]), "+v"(f.al[5]), "+v"(f.al[6]), "+v"(f.al[7]),
        "+v"(f.ah[0]), "+v"(f.ah[1]), "+v"(f.ah[2]), "+v"(f.ah[3]), "+v"(f.ah[4]), "+v"(f.ah[5]), "+v"(f.ah[6]), "+v"(f.ah[7]),
        "+v"(f.B[0]), "+v"(f.B[1]), "+v"(f.B[2]), "+v"(f.B[3]), "+v"(f.B[4]), "+v"(f.B[5]), "+v"(f.B[6]), "+v"(f.B[7]) :: "memory");
}
template <int K> __device__ __forceinline__ void conv_read1(ConvFrags& f, unsigned aa, unsigned ba) {
  if constexpr (K < 8) asm volatile("ds_read_b128 %0, %1 offset:%2" : "=&v"(f.B[K]) : "v"(ba), "n"(512 * K) : "memory");
  else if constexpr (K < 16) asm volatile("ds_read_b64 %0, %1 offset:%2" : "=&v"(f.al[K - 8]) : "v"(aa), "n"(512 * (K - 8)) : "memory");
  else if constexpr (K < 24) asm volatile("ds_read_b64 %0, %1 offset:%2" : "=&v"(f.ah[K - 16]) : "v"(aa), "n"(512 * (K - 16) + 8) : "memory");
}
template <int K = 0> __device__ __forceinline__ void conv_step(f32x4 (&acc)[8], const ConvFrags& f, ConvFrags& nx, unsigned aa, unsigned ba) {
  if constexpr (K < 36) {
    constexpr int a = K < 8 ? 0 : K < 15 ? 1 : K < 21 ? 2 : K < 26 ? 3 : K < 30 ? 4 : K < 33 ? 5 : K < 35 ? 6 : 7;
    constexpr int i = K - (a == 0 ? 0 : a == 1 ? 8 : a == 2 ? 15 : a == 3 ? 21 : a == 4 ? 26 : a == 5 ? 30 : a == 6 ? 33 : 35);
    acc[a + i] = mfma16(mk8(f.al[7 - a], f.ah[7 - a]), __builtin_bit_cast(bf16x8, f.B[i]), acc[a + i]);
    conv_read1<K>(nx, aa, ba);
    SBAR();
    conv_step<K + 1>(acc, f, nx, aa, ba);
  }
}
__device__ __forceinline__ void conv_main(f32x4 (&acc)[8], unsigned cb, int lane, int c0, int c1) {
  const int fr = lane & 15, g4 = lane >> 4;
  const unsigned abase = cb + (3 - (fr & 3)) * CPS + 2 * (2044 - 16 * 112 - 16 * (g4 >> 1) + 8 * (g4 & 1) - (fr & ~3));
  const unsigned bbase = cb + UOFF + 512 + 2 * (16 * fr - 16 * (g4 >> 1) + 8 * (g4 & 1));
#pragma unroll
  for (int c = 0; c < 8; c++) acc[c] = (f32x4){0.f, 0.f, 0.f, 0.f};
  ConvFrags f0, f1;
  asm volatile("s_waitcnt lgkmcnt(0)" ::: "memory");
  conv_read(f0, abase - 64 * c0, bbase - 64 * c0);
  for (int cc = c0; cc < c1; cc += 2) {
    conv_wait(f0);
    SBAR();
    conv_step(acc, f0, f1, abase - 64 * (cc + 1), bbase - 64 * (cc + 1));
    conv_wait(f1);
    SBAR();
    const int cn = cc + 2 < c1 ? cc + 2 : cc + 1;
    conv_step(acc, f1, f0, abase - 64 * cn, bbase - 64 * cn);
  }
  conv_wait(f0);
}
__device__ __forceinline__ void conv_store(f32x4 y, unsigned cb, int c, int lane, float Dd, __amdgpu_buffer_rsrc_t rw, int d) {
  const int fr = lane & 15, g4 = lane >> 4;
  const u32x2 uu = lds_b64(cb + UOFF + 512 + 2 * (256 * c + 16 * fr + 4 * g4));
  const float r0 = fmaf(bflo(uu.x), Dd, y[0]), r1 = fmaf(bfhi(uu.x), Dd, y[1]);
  const float r2 = fmaf(bflo(uu.y), Dd, y[2]), r3 = fmaf(bfhi(uu.y), Dd, y[3]);
  __builtin_amdgcn_raw_buffer_store_b64((u32x2){pk2(r0, r1), pk2(r2, r3)}, rw, OFF_YT + d * 4096 + (256 * c + 16 * fr + 4 * g4) * 2, 0, SC1);
}

__global__ __launch_bounds__(512, 2) void fused(const u16* __restrict__ x, const u16* __restrict__ wqkv, const u16* __restrict__ wo,
                                                const u16* __restrict__ Bk, const u16* __restrict__ Bv, const u16* __restrict__ shk,
                                                const u16* __restrict__ hk, const u16* __restrict__ Dv, const u16* __restrict__ gw,
                                                u16* __restrict__ out, char* __restrict__ ws) {
  extern __shared__ __attribute__((aligned(16))) char sm[];
  const int tid = threadIdx.x, lane = tid & 63;
  const int w = __builtin_amdgcn_readfirstlane(tid >> 6);
  const int b = blockIdx.x, xcd = b & 7, r = b >> 3;
  const int fr = lane & 15, g4 = lane >> 4;
  const unsigned lb = (unsigned)(size_t)(LAS char*)sm;
  const __amdgpu_buffer_rsrc_t rw = __builtin_amdgcn_make_buffer_rsrc(ws, 0, WS_USED, 0x00020000);
  if (__builtin_expect(r < 24 && (w >> 1) == 2, 1)) {
    const bool kv_ = r < 16;
    const int rt_ = (xcd & 3) * 4 + (r & 3), cg_ = kv_ ? (xcd >> 2) * 4 + (r >> 2) : (xcd >> 2) * 2 + ((r - 16) >> 2);
    gemm_first_x(w - 4, lane, lb, x, rt_, cg_, kv_);
    gemm_loader(w - 4, lane, lb, x, wqkv, rt_, cg_, kv_);
  }
  TS_DECL TS(0);
  unsigned e0 = 0;
#define PUT_TAG() do { if (w == 7 && lane == 0) { *(LAS unsigned*)(size_t)(lb + L_TAG) = e0 + 1u; *(LAS u32x4*)(size_t)(lb + L_CNT) = (u32x4){0u, 0u, 0u, 0u}; } } while (0)
#define TAG (*(const LAS unsigned*)(size_t)(lb + L_TAG))
  if (r < 24) {
    const bool isKV = r < 16;
    const int rt = (xcd & 3) * 4 + (r & 3);
    const int cg = isKV ? (xcd >> 2) * 4 + (r >> 2) : (xcd >> 2) * 2 + ((r - 16) >> 2);
    f32x4 acc[4][5];
    const int rp = (w >> 1) & 1, hf = w & 1;
    if (w < 4) {
      const unsigned xa0 = lb + (rp * 64 + fr) * 128 + ((g4 ^ ((fr >> 1) & 7)) << 4);
      const unsigned wl = lb + XS + (8 * g4 + (fr >> 2)) * 320 + ((lane & 3) >> 1) * 16 + (lane & 1) * 8 + hf * 160;
      const unsigned sgn = (g4 & 1) * 32;
      const unsigned wa0 = hf ? wl - sgn : wl + sgn, wa1 = hf ? wl + sgn : wl - sgn;
#pragma unroll
      for (int i = 0; i < 4; i++)
#pragma unroll
        for (int t = 0; t < 5; t++) acc[i][t] = (f32x4){0.f, 0.f, 0.f, 0.f};
      if (isKV) gemm_compute<true>(acc, xa0, wa0, wa1 TSARG); else gemm_compute<false>(acc, xa0, wa0, wa1 TSARG);
    } else if (w >= 6) {
      const int w6 = w - 6;
      const char* pfa;
      {
        const int q = rt & 3, u = isKV ? (cg & 3) : 4 + (cg & 1);
        if (lane < 32) {
          const int row = 16 * q + 8 * w6 + (lane >> 2);
          const unsigned col = isKV ? ((lane & 2) ? 2560u : 1280u) + 160u * cg : 320u * cg;
          const unsigned ln = isKV ? (lane & 1) : ((lane & 3) == 3 ? 2 : (lane & 3));
          pfa = (const char*)wqkv + row * 3840 + ((col >> 7) + ln) * 128;
        } else {
          int row = u * 22 + w6 * 11 + (lane - 32); row = (lane < 43 && row < 128) ? row : u * 22;
          pfa = (const char*)x + (rt * 128 + row) * 1280;
        }
      }
      const int pfs0 = gemm_s0(rt, cg, isKV);
      auto prefetch = [&](int j) {
      };
      SBAR();
      if (w == 7) e0 = flag_ld(ws, F_E);
      if (isKV) {
        const int l16 = lane & 15, row0 = rt * 128 + cg * 16 + w6 * 8;
        const u32x4 g0 = *(const u32x4*)(gw + lane * 8), g1 = *(const u32x4*)(gw + 512 + l16 * 8);
        u32x4 gx0[8], gx1[8];
#pragma unroll
        for (int i = 0; i < 8; i++) {
          const u16* xr = x + (size_t)(row0 + i) * D;
          gx0[i] = *(const u32x4*)(xr + lane * 8); gx1[i] = *(const u32x4*)(xr + 512 + l16 * 8);
        }
        SBAR();
        BAR(); prefetch(4); prefetch(5); BAR(); prefetch(6); prefetch(7); BAR(); prefetch(8); prefetch(9);
        float a[8];
        const float m1 = lane < 16 ? 1.f : 0.f;
#pragma unroll
        for (int i = 0; i < 8; i++) {
          float a0 = 0.f, a1 = 0.f;
#pragma unroll
          for (int j = 0; j < 4; j++) {
            a0 = fmaf(bflo(gx0[i][j]), bflo(g0[j]), a0); a0 = fmaf(bfhi(gx0[i][j]), bfhi(g0[j]), a0);
            a1 = fmaf(bflo(gx1[i][j]), bflo(g1[j]), a1); a1 = fmaf(bfhi(gx1[i][j]), bfhi(g1[j]), a1);
          }
          a[i] = fmaf(a1, m1, a0);
          if (i == 3) { SBAR(); BAR(); SBAR(); }
        }
        SBAR(); BAR(); SBAR();
#pragma unroll
        for (int i = 0; i < 4; i++) { const float lo = a[i], hi = a[i + 4]; const bool up = lane & 4; a[i] = (up ? hi : lo) + __shfl_xor(up ? lo : hi, 4); }
#pragma unroll
        for (int i = 0; i < 2; i++) { const float lo = a[i], hi = a[i + 2]; const bool up = lane & 2; a[i] = (up ? hi : lo) + __shfl_xor(up ? lo : hi, 2); }
        { const float lo = a[0], hi = a[1]; const bool up = lane & 1; a[0] = (up ? hi : lo) + __shfl_xor(up ? lo : hi, 1); }
        a[0] += __shfl_xor(a[0], 8); a[0] += __shfl_xor(a[0], 16); a[0] += __shfl_xor(a[0], 32);
        if (lane < 8) __builtin_amdgcn_raw_buffer_store_b32(__float_as_uint(rbf(1.f / (1.f + __expf(-rbf(a[0]))))), rw, OFF_G + (row0 + lane) * 4, 0, SC1);
        PUT_TAG();
        SBAR();
        BAR(); BAR(); BAR(); BAR(); BAR();
      } else {
        const int hw = (xcd * 8 + (r - 16)) * 2 + w6;
        const unsigned scr = lb + L_T + w6 * 6144;
#pragma unroll
        for (int bt = 0; bt < 2; bt++) {
          const int blk = bt == 0 ? hw : (hw < 32 ? 128 + hw : hw), ct = (blk * 3277) >> 16, ks = blk - 20 * ct;
          u32x4 v[5];
#pragma unroll
          for (int i = 0; i < 5; i++) {
            const int c = i * 64 + lane, row = (c * 6554) >> 16, ch = c - 10 * row;
            v[i] = *(const u32x4*)(wo + (size_t)(32 * ks + row) * D + 80 * ct + 8 * ch);
          }
          SBAR();
          BAR();
          if (bt == 0) { PUT_TAG(); prefetch(4); prefetch(5); }
          BAR();
          if (bt == 0) { prefetch(6); prefetch(7); }
          if (bt == 0 || hw < 32) {
#pragma unroll
            for (int i = 0; i < 5; i++) *(LAS u32x4*)(size_t)(scr + (i * 64 + lane) * 16) = v[i];
            const unsigned ta = scr + (8 * g4 + (fr >> 2)) * 160 + (lane & 3) * 8;
#pragma unroll
            for (int t = 0; t < 5; t++) {
              const u32x2 lo = lds_tr(ta + 32 * t), hi = lds_tr(ta + 32 * t + 640);
              __builtin_amdgcn_raw_buffer_store_b128((u32x4){lo.x, lo.y, hi.x, hi.y}, rw, OFF_WOP + (blk * 5 + t) * 1024 + lane * 16, 0, SC1);
            }
          }
          SBAR();
          BAR();
          if (bt == 0) { prefetch(8); prefetch(9); }
          BAR();
        }
        DRAIN();
        if (lds_last(lb + L_CNT + 8, lane, 2) && lane == 0) flag_st(ws, F_W + (xcd * 8 + (r - 16)) * 128, TAG);
        BAR(); BAR();
      }
    }
    TS(1);
    BAR();
    TS(11);
    const unsigned tag = TAG;
    if (isKV) {
      if (w < 4) {
#pragma unroll
        for (int t = 0; t < 5; t++) {
          const bool isk = hf ? t < 2 : t < 3;
          const int slot = hf ? (t < 2 ? 3 + t : 5 + t) : (t < 3 ? t : 2 + t);
#pragma unroll
          for (int m = 0; m < 2; m++) {
            u32x4 f;
            if (isk) f = (u32x4){phi_pk(acc[2 * m][t][0], acc[2 * m][t][1]), phi_pk(acc[2 * m][t][2], acc[2 * m][t][3]),
                                 phi_pk(acc[2 * m + 1][t][0], acc[2 * m + 1][t][1]), phi_pk(acc[2 * m + 1][t][2], acc[2 * m + 1][t][3])};
            else f = (u32x4){pk2(acc[2 * m][t][0], acc[2 * m][t][1]), pk2(acc[2 * m][t][2], acc[2 * m][t][3]),
                             pk2(acc[2 * m + 1][t][0], acc[2 * m + 1][t][1]), pk2(acc[2 * m + 1][t][2], acc[2 * m + 1][t][3])};
            *(LAS u32x4*)(size_t)(lb + ((rp * 2 + m) * 10 + slot) * 1024 + lane * 16) = f;
          }
        }
      } else {
        for (int i = tid - 256; i < 6048 / 16; i += 256) *(LAS u32x4*)(size_t)(lb + L_T + i * 16) = (u32x4){0, 0, 0, 0};
        if (w == 7) poll(ws, F_W + lane * 128, true, tag);
      }
      TS(12);
      BAR();
      TS(2);
      switch (w) {
        case 0: stat_tiles<0>(lb, lane, rw, OFF_P + (rt * 8 + cg) * 3360); break;
        case 1: stat_tiles<1>(lb, lane, rw, OFF_P + (rt * 8 + cg) * 3360); break;
        case 2: stat_tiles<2>(lb, lane, rw, OFF_P + (rt * 8 + cg) * 3360); break;
        case 3: stat_tiles<3>(lb, lane, rw, OFF_P + (rt * 8 + cg) * 3360); break;
        case 4: stat_tiles<4>(lb, lane, rw, OFF_P + (rt * 8 + cg) * 3360); break;
        case 5: stat_tiles<5>(lb, lane, rw, OFF_P + (rt * 8 + cg) * 3360); break;
        case 6: stat_tiles<6>(lb, lane, rw, OFF_P + (rt * 8 + cg) * 3360); break;
        default: stat_tiles<7>(lb, lane, rw, OFF_P + (rt * 8 + cg) * 3360); break;
      }
      TS(14);
      DRAIN();
      TS(15);
      BAR();
      TS(3);
      if (w == 0) {
        if (lane == 0) flag_st(ws, F_P + (rt * 8 + cg) * 128, tag);
        poll(ws, lane < 16 ? F_P + (lane * 8 + cg) * 128 : F_PQ + (rt * 4 + (cg >> 1)) * 128, lane <= 16, tag);
      } else if (w >= 4) {
        const i32x4 sws = make_srd(ws, WS_USED);
        const unsigned vwv = OFF_WOP + ((xcd >> 2) * 4 + (r >> 3)) * 102400 + lane * 16;
        for (int i = w - 4; i < 100; i += 4) {
          int pc = 12 * (r & 7) + i; pc = __builtin_amdgcn_readfirstlane(pc >= 100 ? pc - 100 : pc);
          glds16_sc1(lb + pc * 1024, vwv, sws, pc * 1024);
        }
      }
      constexpr int PJ[9] = {0, 1, 1, 2, 2, 3, 3, 4, 4}, PK[9] = {0, 0, 1, 0, 1, 1, 2, 1, 2};
      unsigned an[9], ad[9];
#pragma unroll
      for (int p = 0; p < 9; p++) {
        const int E = 16 * PJ[p] + fr, h = (E * 205) >> 12, e = E - 20 * h;
        int o_ = 32 * PK[p] + 8 * g4 - 20 * h; o_ = o_ < -8 ? -8 : o_ > 20 ? 20 : o_;
        an[p] = lb + L_T + (h * 21 + e) * 72 + (8 + o_) * 2; ad[p] = lb + L_T + (h * 21 + 20) * 72 + (8 + o_) * 2;
      }
      BAR();
      TS(4);
      bf16x8 Bq[3];
      {
        const unsigned prow = OFF_PQ + ((rt * 8 + w) * 8 + cg) * 3072 + lane * 16;
#pragma unroll
        for (int ks = 0; ks < 3; ks++) Bq[ks] = __builtin_bit_cast(bf16x8, __builtin_amdgcn_raw_buffer_load_b128(rw, prow + 1024 * ks, 0, SC1));
      }
      if (tid < 210) {
        const unsigned pb = OFF_P + cg * 3360 + tid * 16;
        u32x4 v[16];
#pragma unroll
        for (int k = 0; k < 16; k++) v[k] = __builtin_amdgcn_raw_buffer_load_b128(rw, pb, (k ^ rt) * (8 * 3360), SC1);
        unsigned o4[4];
#pragma unroll
        for (int c = 0; c < 4; c++) {
          float lo[16], hi[16];
#pragma unroll
          for (int k = 0; k < 16; k++) { lo[k] = bflo(v[k][c]); hi[k] = bfhi(v[k][c]); }
#pragma unroll
          for (int st = 1; st < 16; st <<= 1)
#pragma unroll
            for (int k = 0; k < 16; k += 2 * st) { lo[k] += lo[k + st]; hi[k] += hi[k + st]; }
          o4[c] = pk2(lo[0], hi[0]);
        }
        TS(16);
#pragma unroll
        for (int q = 0; q < 2; q++) {
          const int i4 = 2 * tid + q, row = (i4 * 13108) >> 16, d = 4 * (i4 - row * 5);
          *(LAS u32x2*)(size_t)(lb + L_T + row * 72 + (8 + d) * 2) = (u32x2){o4[2 * q], o4[2 * q + 1]};
        }
      }
      BAR();
      TS(5);
      {
        if (g4 >= 2) Bq[2] = __builtin_bit_cast(bf16x8, (u32x4){0, 0, 0, 0});
        f32x4 nu[5], de[5];
#pragma unroll
        for (int j = 0; j < 5; j++) { nu[j] = (f32x4){0.f, 0.f, 0.f, 0.f}; de[j] = nu[j]; }
        bf16x8 An[9], Ad[9];
#pragma unroll
        for (int p = 0; p < 9; p++) {
          unsigned an8 = an[p] + 8, ad8 = ad[p] + 8;
          asm volatile("" : "+v"(an8), "+v"(ad8));
          An[p] = mk8(lds_b64(an[p]), lds_b64(an8)); Ad[p] = mk8(lds_b64(ad[p]), lds_b64(ad8));
        }
#pragma unroll
        for (int p = 0; p < 9; p++) { de[PJ[p]] = mfma16(Ad[p], Bq[PK[p]], de[PJ[p]]); nu[PJ[p]] = mfma16(An[p], Bq[PK[p]], nu[PJ[p]]); }
        u32x2 ov[5];
#pragma unroll
        for (int j = 0; j < 5; j++) {
          const float rd = __builtin_amdgcn_rcpf(de[j][0] + 1e-6f);
          ov[j] = (u32x2){pk2(nu[j][0] * rd, nu[j][1] * rd), pk2(nu[j][2] * rd, nu[j][3] * rd)};
        }
        store_frags(ov, rw, OFF_O + (rt * 8 + w) * 20480, 80 * cg, fr, g4);
      }
      TS(17);
      DRAIN();
      BAR();
      if (tid == 0) flag_st(ws, F_O + (rt * 8 + cg) * 128, tag);
      TS(6);
    } else {
      const int vb = xcd * 8 + (r - 16);
      const int d = 80 * (vb >> 3) + 48 + 4 * (vb & 7) + (w & 3);
      const unsigned cb = lb + (w & 3) * CHS;
      if (w < 4) {
#pragma unroll
        for (int i = 0; i < 4; i++) {
          u32x2 pv[5];
#pragma unroll
          for (int t = 0; t < 5; t++) pv[t] = (u32x2){phi_pk(acc[i][t][0], acc[i][t][1]), phi_pk(acc[i][t][2], acc[i][t][3])};
          store_frags(pv, rw, OFF_PQ + ((rt * 8 + rp * 4 + i) * 8 + cg * 2 + hf) * 3072, 0, fr, g4);
        }
      } else {
        u32x4 hc[4], hp[4];
        conv_load_h(hc, hp, hk + (size_t)d * S, lane);
        poll(ws, F_U + lane * 128, true, tag);
        conv_load_u(rw, d, cb, lane, [&]() { conv_prep_h(hc, hp, cb, lane); });
      }
      TS(12);
      DRAIN();
      BAR();
      TS(2);
      if (tid == 0) flag_st(ws, F_PQ + (rt * 4 + cg) * 128, tag);
      if (w < 4) {
        const float Dd = bflo(Dv[d]);
        TS(3);
        f32x4 y[8];
        conv_main(y, cb, lane, 0, 8);
        TS(4);
#pragma unroll
        for (int c = 0; c < 8; c++) conv_store(y[c], cb, c, lane, Dd, rw, d);
        DRAIN();
      } else if (w == 7) {
        poll(ws, F_W + lane * 128, true, tag);
      }
      BAR();
      if (tid == 0) flag_st(ws, F_Y + (64 + vb) * 128, tag);
      TS(5); TS(6);
    }
  } else {
    const int pw = xcd * 8 + (r - 24);
    const int c6 = w < 4 ? w : 4 + ((w - 4) >> 1);
    const int d = 80 * (pw >> 3) + 6 * (pw & 7) + c6;
    const unsigned cb = lb + c6 * CHS;
    const bool prep = w < 4 || !(w & 1);
    u32x4 hc[4], hp[4];
    {
      const int rbase = xcd * 256 + ((r - 24) >> 1) * 64, cbase = ((r - 24) & 1) * 320;
      u32x4 kr[2][7], vr[2][4], sp[2][4];
#pragma unroll
      for (int q = 0; q < 2; q++) {
        const int it = w + 8 * q;
        if (q == 0 || w < 2) {
          const int ch = cbase + (it >> 1) * 64 + (lane & 7) * 8;
          const int s0 = rbase + (it & 1) * 32 + (lane >> 3) * 4;
#pragma unroll
          for (int j = 0; j < 7; j++) { int s = s0 - 3 + j; kr[q][j] = *(const u32x4*)(Bk + (size_t)(s < 0 ? 0 : s) * D + ch); }
#pragma unroll
          for (int j = 0; j < 4; j++) vr[q][j] = *(const u32x4*)(Bv + (size_t)(s0 + j) * D + ch);
#pragma unroll
          for (int j = 0; j < 4; j++) sp[q][j] = ((const u32x4*)(shk + ch * 4))[j];
        }
      }
      if (prep) conv_load_h(hc, hp, hk + (size_t)d * S, lane);
      if (w == 7) e0 = flag_ld(ws, F_E);
      SBAR();
#pragma unroll
      for (int q = 0; q < 2; q++) {
        const int it = w + 8 * q;
        if (q == 0 || w < 2) {
          const int s0 = rbase + (it & 1) * 32 + (lane >> 3) * 4;
          if (s0 == 0) { kr[q][0] = (u32x4){0, 0, 0, 0}; kr[q][1] = kr[q][0]; kr[q][2] = kr[q][0]; }
          const int rg = (it & 1) * 8 + (lane >> 3);
          const unsigned la = lb + L_UIMG + ((it >> 1) * 64 + (lane & 7) * 8) * 128 + (((rg >> 1) ^ (lane & 7)) << 4) + (rg & 1) * 8;
#pragma unroll
          for (int c = 0; c < 8; c++) {
            const unsigned s01 = sp[q][c >> 1][(c & 1) * 2], s23 = sp[q][c >> 1][(c & 1) * 2 + 1];
            const float w0 = bflo(s01), w1 = bfhi(s01), w2 = bflo(s23), w3 = bfhi(s23);
            float kk[7], u[4];
#pragma unroll
            for (int j = 0; j < 7; j++) { unsigned dd = kr[q][j][c >> 1]; kk[j] = (c & 1) ? bfhi(dd) : bflo(dd); }
#pragma unroll
            for (int j = 0; j < 4; j++) {
              unsigned dd = vr[q][j][c >> 1]; float v = (c & 1) ? bfhi(dd) : bflo(dd);
              float ks = kk[j] * w3; ks = fmaf(kk[j + 1], w2, ks); ks = fmaf(kk[j + 2], w1, ks); ks = fmaf(kk[j + 3], w0, ks);
              u[j] = ks * v;
            }
            *(LAS u32x2*)(size_t)(la + c * 128) = (u32x2){pk2(u[0], u[1]), pk2(u[2], u[3])};
          }
        }
      }
      PUT_TAG();
      BAR();
#pragma unroll
      for (int i = 0; i < 5; i++) {
        const int chl = (w * 5 + i) * 8 + (lane >> 3), k = lane & 7;
        const u32x4 v = *(const LAS u32x4*)(size_t)(lb + L_UIMG + chl * 128 + ((k ^ ((chl >> 3) & 7)) << 4));
        __builtin_amdgcn_raw_buffer_store_b128(v, rw, OFF_UT + (cbase + chl) * 4096 + rbase * 2 + k * 16, 0, SC1);
      }
      TS(1);
    }
    if (w < 5 && prep) conv_prep_h(hc, hp, cb, lane);
    DRAIN();
    BAR();
    TS(2);
    const unsigned tag = TAG;
    if (tid == 0) flag_st(ws, F_U + pw * 128, tag);
    const float Dd = bflo(Dv[d]);
    if (prep) {
      if (w == 6) conv_prep_h(hc, hp, cb, lane);
      poll(ws, F_U + lane * 128, true, tag);
      conv_load_u(rw, d, cb, lane, [&]() {});
    }
    BAR();
    TS(3);
    f32x4 y[8];
    conv_main(y, cb, lane, w < 4 ? 0 : (w & 1) * 4, w < 4 ? 8 : (w & 1) * 4 + 4);
    TS(4);
    if (w < 4) {
#pragma unroll
      for (int c = 0; c < 8; c++) conv_store(y[c], cb, c, lane, Dd, rw, d);
    }
    BAR();
    if (w >= 4) {
#pragma unroll
      for (int c = 0; c < 4; c++) { const f32x4 lo = y[c], hi = y[4 + c]; *(LAS f32x4*)(size_t)(cb + ((w & 1) * 4 + c) * 1024 + lane * 16) = (w & 1) ? lo : hi; }
    }
    BAR();
    if (w >= 4) {
#pragma unroll
      for (int c = 0; c < 4; c++) {
        const f32x4 o_ = *(const LAS f32x4*)(size_t)(cb + (((w & 1) ^ 1) * 4 + c) * 1024 + lane * 16);
        const f32x4 lo = y[c], hi = y[4 + c];
        const f32x4 mine = (w & 1) ? hi : lo;
        conv_store((w & 1) ? o_ + mine : mine + o_, cb, (w & 1) * 4 + c, lane, Dd, rw, d);
      }
    }
    if (w == 0) poll(ws, F_W + lane * 128, true, tag);
    DRAIN();
    BAR();
    if (tid == 0) flag_st(ws, F_Y + pw * 128, tag);
    TS(5); TS(6);
  }

  {
    const unsigned tag = TAG;
    const int rt = (xcd & 3) * 8 + (r & 7), ct = (xcd >> 2) * 4 + (r >> 3);
    const int sw_ = w & 3, kk = w >> 2;
    const i32x4 sws = make_srd(ws, WS_USED);
    TS(18);
    const int srow = rt * 64 + sw_ * 16 + fr;
    u32x2 xv[3];
#pragma unroll
    for (int t = 0; t < 3; t++) { const int tt = kk ? (t < 2 ? 3 + t : 4) : t; xv[t] = *(const u32x2*)(x + (size_t)srow * D + ct * 80 + 16 * tt + 4 * g4); }
    SBAR();
    const int rot = ((ct & 3) * 5) >> 1;
    const unsigned wa0 = lb + kk * 5120 + lane * 16;
    bf16x8 wf0[5][5];
    auto read_wf0 = [&]() {
#pragma unroll
      for (int s = 0; s < 5; s++) {
        int q = s + rot; q = q >= 10 ? q - 10 : q;
#pragma unroll
        for (int t = 0; t < 5; t++) wf0[s][t] = lds_b128(wa0 + q * 10240 + t * 1024);
      }
    };
    if (r < 16) read_wf0();
    SBAR();
    if (w == 0) {
      poll(ws, F_O + ((rt >> 1) * 8 + (lane & 7)) * 128, lane < 8, tag);
    } else if (w == 7) {
      poll(ws, F_Y + ((lane & 8 ? 64 : 0) + ct * 8 + (lane & 7)) * 128, lane < 16, tag);
    } else {
      if (r >= 16) {
        const unsigned vwv = OFF_WOP + ct * 102400 + lane * 16;
        for (int i = w - 1; i < 100; i += 6) { int pc = 12 * (r & 7) + i; pc = __builtin_amdgcn_readfirstlane(pc >= 100 ? pc - 100 : pc); glds16_sc1(lb + pc * 1024, vwv, sws, pc * 1024); }
      }
      if (w == 6 && b == 255) {
        for (int spins = 0; spins < (1 << 17); spins++) {
          const unsigned a0 = flag_ld(ws, F_U + lane * 128), a1 = flag_ld(ws, F_U + (64 + lane) * 128);
          const unsigned a2 = flag_ld(ws, F_U + (128 + lane) * 128), a3 = flag_ld(ws, F_U + (192 + lane) * 128);
          if (__all(a0 == tag && a1 == tag && a2 == tag && a3 == tag)) break;
          __builtin_amdgcn_s_sleep(2);
        }
        if (lane == 0) flag_st(ws, F_E, tag);
      }
      DRAIN();
    }
    BAR();
    TS(7);
    asm volatile("" :: "v"(xv[0]), "v"(xv[1]), "v"(xv[2]));
    bf16x8 of[10];
    {
      const unsigned vo = OFF_O + (rt * 4 + sw_) * 20480 + kk * 1024 + lane * 16;
#pragma unroll
      for (int s = 0; s < 10; s++) { int q = s + rot; q = __builtin_amdgcn_readfirstlane(q >= 10 ? q - 10 : q); bload16_sc1(of[s], vo, sws, q * 2048); }
    }
    if (w == 7) {
      for (int pc = 0; pc < 10; pc++)
        glds16_sc1(lb + L_Y + pc * 1024, OFF_YT + (ct * 80 + pc * 8 + (lane >> 3)) * 4096 + rt * 128 + (lane & 7) * 16, sws, 0);
    }
    float g;
    asm volatile("buffer_load_dword %0, %1, %2, 0 offen sc1" : "=v"(g) : "v"(OFF_G + srow * 4), "s"(sws) : "memory");
    TS(8);
    if (r >= 16) read_wf0();
    f32x4 acc[5];
#pragma unroll
    for (int t = 0; t < 5; t++) acc[t] = (f32x4){0.f, 0.f, 0.f, 0.f};
    {
      bf16x8 wf1[5][5];
#pragma unroll
      for (int s = 0; s < 5; s++) {
        int q = 5 + s + rot; q = q >= 10 ? q - 10 : q;
#pragma unroll
        for (int t = 0; t < 5; t++) wf1[s][t] = lds_b128(wa0 + q * 10240 + t * 1024);
      }
      SBAR();
      if (w == 7) asm volatile("s_waitcnt vmcnt(16)" ::: "memory"); else asm volatile("s_waitcnt vmcnt(6)" ::: "memory");
      asm volatile("" : "+v"(of[0]), "+v"(of[1]), "+v"(of[2]), "+v"(of[3]), "+v"(of[4]) :: "memory");
      SBAR();
#pragma unroll
      for (int s = 0; s < 5; s++)
#pragma unroll
        for (int t = 0; t < 5; t++) acc[t] = mfma16(wf0[s][t], of[s], acc[t]);
      SBAR();
      if (w == 7) asm volatile("s_waitcnt vmcnt(11)" ::: "memory"); else asm volatile("s_waitcnt vmcnt(1)" ::: "memory");
      asm volatile("" : "+v"(of[5]), "+v"(of[6]), "+v"(of[7]), "+v"(of[8]), "+v"(of[9]) :: "memory");
      SBAR();
#pragma unroll
      for (int s = 0; s < 5; s++)
#pragma unroll
        for (int t = 0; t < 5; t++) acc[t] = mfma16(wf1[s][t], of[5 + s], acc[t]);
      SBAR();
    }
    TS(9);
    const unsigned ra = lb + L_RED + (sw_ * 5) * 1024 + lane * 16;
    if (kk == 0) { *(LAS f32x4*)(size_t)(ra + 3 * 1024) = acc[3]; *(LAS f32x4*)(size_t)(ra + 4 * 1024) = acc[4]; }
    else { *(LAS f32x4*)(size_t)(ra) = acc[0]; *(LAS f32x4*)(size_t)(ra + 1024) = acc[1]; *(LAS f32x4*)(size_t)(ra + 2048) = acc[2]; }
    asm volatile("s_waitcnt vmcnt(0)" : "+v"(g) :: "memory");
    BAR();
    TS(20);
    const float omg = 1.f - g;
    const __amdgpu_buffer_rsrc_t ro = __builtin_amdgcn_make_buffer_rsrc(out, 0, SZ, 0x00020000);
    u32x2 rv[3];
#pragma unroll
    for (int t = 0; t < 3; t++) {
      const int tt = kk ? 3 + (t < 2 ? t : 1) : t;
      const f32x4 a0 = acc[t], a1 = acc[3 + (t < 2 ? t : 1)];
      const f32x4 a = *(const LAS f32x4*)(size_t)(ra + tt * 1024) + (kk ? a1 : a0);
      const u32x2 yv = lds_tr(lb + L_Y + (16 * tt + 4 * g4 + (fr >> 2)) * 128 + (sw_ * 16 + 4 * (fr & 3)) * 2);
      const float xs[4] = {bflo(xv[t].x), bfhi(xv[t].x), bflo(xv[t].y), bfhi(xv[t].y)};
      const float ys[4] = {bflo(yv.x), bfhi(yv.x), bflo(yv.y), bfhi(yv.y)};
      float r_[4];
#pragma unroll
      for (int q = 0; q < 4; q++) r_[q] = fmaf(g, a[q], omg * (xs[q] * ys[q]));
      rv[t] = (u32x2){pk2(r_[0], r_[1]), pk2(r_[2], r_[3])};
    }
    __builtin_amdgcn_raw_buffer_store_b128(pair16(rv[0], rv[1]), ro, (srow * D + ct * 80 + 16 * ((kk ? 3 : 0) + (g4 & 1)) + 8 * (g4 >> 1)) * 2, 0, SC1);
    if (kk == 0) __builtin_amdgcn_raw_buffer_store_b64(rv[2], ro, (srow * D + ct * 80 + 16 * (2) + 4 * g4) * 2, 0, SC1);
    TS(10); TS_OUT(21);
  }
}

extern "C" void kernel_launch(void* const* d_in, const int* in_sizes, int n_in, void* d_out, int out_size, void* d_ws, size_t ws_size,
                              hipStream_t stream) {
  const u16* x = (const u16*)d_in[0]; const u16* wqkv = (const u16*)d_in[1]; const u16* wo = (const u16*)d_in[2];
  const u16* Bk = (const u16*)d_in[4]; const u16* Bv = (const u16*)d_in[5]; const u16* shk = (const u16*)d_in[6];
  const u16* hk = (const u16*)d_in[7]; const u16* Dv = (const u16*)d_in[8]; const u16* gw = (const u16*)d_in[9];
  (void)hipFuncSetAttribute((const void*)fused, hipFuncAttributeMaxDynamicSharedMemorySize, LDS_TOTAL);
  fused<<<256, 512, LDS_TOTAL, stream>>>(x, wqkv, wo, Bk, Bv, shk, hk, Dv, gw, (u16*)d_out, (char*)d_ws);
}
```

```cpp
#include <hip/hip_runtime.h>
#include <hip/hip_bf16.h>
#include <type_traits>

typedef unsigned short u16;
typedef __bf16 bf16x2 __attribute__((ext_vector_type(2)));
typedef __bf16 bf16x8 __attribute__((ext_vector_type(8)));
typedef float f32x4 __attribute__((ext_vector_type(4)));
typedef unsigned u32x4 __attribute__((ext_vector_type(4)));
typedef unsigned u32x2 __attribute__((ext_vector_type(2)));
typedef short v4i16_t __attribute__((ext_vector_type(4)));
typedef int i32x4 __attribute__((ext_vector_type(4)));
typedef __attribute__((address_space(1))) unsigned gu32;
#define LAS __attribute__((address_space(3)))
#define SBAR() __builtin_amdgcn_sched_barrier(0)
#define SGB(mask, n) __builtin_amdgcn_sched_group_barrier(mask, n, 0)
#define BAR() asm volatile("s_waitcnt lgkmcnt(0)\n\ts_barrier" ::: "memory")
#define DRAIN() asm volatile("s_waitcnt vmcnt(0)" ::: "memory")
#define SC1 16

constexpr int S = 2048, D = 640;
constexpr unsigned SZ = S * D * 2;
constexpr unsigned F_E = 0, F_U = 128, F_PQ = F_U + 64 * 128, F_P = F_PQ + 64 * 128, F_O = F_P + 128 * 128, F_Y = F_O + 128 * 128, F_W = F_Y + 128 * 128;
constexpr unsigned OFF_PQ = 0x20000, OFF_P = OFF_PQ + 128 * 8 * 3 * 1024, OFF_UT = OFF_P + (1u << 19), OFF_G = OFF_UT + SZ, OFF_WOP = OFF_G + 8192,
                   OFF_O = OFF_WOP + (1u << 20), OFF_YT = OFF_O + SZ, WS_USED = OFF_YT + SZ;
constexpr int XS = 16384, WSB = 20480, STG = XS + WSB;
constexpr int L_EXTRA = 4 * STG;
constexpr int L_TAG = L_EXTRA, L_CNT = L_EXTRA + 16, L_T = L_EXTRA + 64;
constexpr int LDS_TOTAL = L_T + 6144 + 5120;
constexpr int CPS = 4160, UOFF = 4 * CPS, CHS = UOFF + 4608;
constexpr int L_UIMG = 5 * CHS;
constexpr int L_Y = 102400, L_RED = L_Y + 10240;

__device__ __forceinline__ unsigned pk2(float a, float b) { bf16x2 r = {(__bf16)a, (__bf16)b}; return __builtin_bit_cast(unsigned, r); }
__device__ __forceinline__ float rbf(float a) { return (float)(__bf16)a; }
__device__ __forceinline__ float bflo(unsigned v) { return __uint_as_float(v << 16); }
__device__ __forceinline__ float bfhi(unsigned v) { return __uint_as_float(v & 0xffff0000u); }
typedef float f32x2 __attribute__((ext_vector_type(2)));
__device__ __forceinline__ unsigned phi_pk(float a, float b) {
  const f32x2 t = {a, b};
  const f32x2 y = t * (f32x2){1.44269504f, 1.44269504f};
  const f32x2 e = {__builtin_amdgcn_fmed3f(__builtin_amdgcn_exp2f(y.x), 0.f, 1.f), __builtin_amdgcn_fmed3f(__builtin_amdgcn_exp2f(y.y), 0.f, 1.f)};
  const f32x2 m = {__builtin_amdgcn_fmed3f(a, 0.f, 3.0e38f), __builtin_amdgcn_fmed3f(b, 0.f, 3.0e38f)};
  const f32x2 r = m + (e + (f32x2){1e-6f, 1e-6f});
  return pk2(r.x, r.y);
}
__device__ __forceinline__ i32x4 make_srd(const void* p, unsigned bytes) {
  unsigned long long a = (unsigned long long)p; i32x4 r;
  r.x = __builtin_amdgcn_readfirstlane((int)(unsigned)a); r.y = __builtin_amdgcn_readfirstlane((int)(unsigned)(a >> 32));
  r.z = __builtin_amdgcn_readfirstlane((int)bytes); r.w = 0x00020000; return r;
}
__device__ __forceinline__ void glds16(unsigned ldsaddr, unsigned voff, i32x4 srd, unsigned soff) {
  asm volatile("s_mov_b32 m0, %0\n\ts_nop 0\n\tbuffer_load_dwordx4 %1, %2, %3 offen lds" :: "s"(ldsaddr), "v"(voff), "s"(srd), "s"(soff) : "memory");
}
__device__ __forceinline__ void glds16_sc1(unsigned ldsaddr, unsigned voff, i32x4 srd, unsigned soff) {
  asm volatile("s_mov_b32 m0, %0\n\ts_nop 0\n\tbuffer_load_dwordx4 %1, %2, %3 offen sc1 lds" :: "s"(ldsaddr), "v"(voff), "s"(srd), "s"(soff) : "memory");
}
__device__ __forceinline__ void bload16_sc1(bf16x8& dst, unsigned voff, i32x4 srd, unsigned soff) {
  asm volatile("buffer_load_dwordx4 %0, %1, %2, %3 offen sc1" : "=v"(dst) : "v"(voff), "s"(srd), "s"(soff) : "memory");
}
template <int N> __device__ __forceinline__ void wait_bar() { asm volatile("s_waitcnt vmcnt(%0) lgkmcnt(0)\n\ts_barrier" :: "n"(N) : "memory"); }
__device__ __forceinline__ bf16x8 lds_b128(unsigned addr) { return *(const LAS bf16x8*)(size_t)addr; }
__device__ __forceinline__ u32x2 lds_b64(unsigned addr) { return *(const LAS u32x2*)(size_t)addr; }
__device__ __forceinline__ u32x2 lds_tr(unsigned addr) { return __builtin_bit_cast(u32x2, __builtin_amdgcn_ds_read_tr16_b64_v4i16((LAS v4i16_t*)(size_t)addr)); }
__device__ __forceinline__ bf16x8 mk8(u32x2 lo, u32x2 hi) { u32x4 t = {lo.x, lo.y, hi.x, hi.y}; return __builtin_bit_cast(bf16x8, t); }
__device__ __forceinline__ f32x4 mfma16(bf16x8 a, bf16x8 b, f32x4 c) { return __builtin_amdgcn_mfma_f32_16x16x32_bf16(a, b, c, 0, 0, 0); }

__device__ __forceinline__ u32x4 pair16(u32x2 a, u32x2 b) {
  const auto r0 = __builtin_amdgcn_permlane16_swap(a.x, b.x, false, false);
  const auto r1 = __builtin_amdgcn_permlane16_swap(a.y, b.y, false, false);
  return (u32x4){r0[0], r1[0], r0[1], r1[1]};
}
__device__ __forceinline__ void store_frags(const u32x2 (&v)[5], __amdgpu_buffer_rsrc_t rw, unsigned base, int k0, int fr, int g4) {
#pragma unroll
  for (int j = 0; j < 4; j += 2) {
    const int k = k0 + 16 * (j + (g4 & 1)) + 8 * (g4 >> 1);
    __builtin_amdgcn_raw_buffer_store_b128(pair16(v[j], v[j + 1]), rw, base + (k >> 5) * 1024 + (((k >> 3) & 3) * 16 + fr) * 16, 0, SC1);
  }
  const int k = k0 + 64 + 4 * g4;
  __builtin_amdgcn_raw_buffer_store_b64(v[4], rw, base + (k >> 5) * 1024 + (((k >> 3) & 3) * 16 + fr) * 16 + ((k >> 2) & 1) * 8, 0, SC1);
}

__device__ __forceinline__ bool lds_last(unsigned cnt_addr, int lane, unsigned n) {
  unsigned old = 0;
  if (lane == 0) old = __hip_atomic_fetch_add((LAS unsigned*)(size_t)cnt_addr, 1u, __ATOMIC_RELAXED, __HIP_MEMORY_SCOPE_WORKGROUP);
  return __builtin_amdgcn_readfirstlane(old) == n - 1;
}

__device__ __forceinline__ unsigned flag_ld(const char* ws, unsigned off) { return __hip_atomic_load((const gu32*)(ws + off), __ATOMIC_RELAXED, __HIP_MEMORY_SCOPE_AGENT); }
__device__ __forceinline__ void flag_st(char* ws, unsigned off, unsigned v) { __hip_atomic_store((gu32*)(ws + off), v, __ATOMIC_RELAXED, __HIP_MEMORY_SCOPE_AGENT); }
__device__ __forceinline__ void poll(const char* ws, unsigned off, bool act, unsigned tag) {
  const unsigned o0 = __builtin_amdgcn_readfirstlane(off);
  off = act ? off : o0;
  unsigned a, b, n = 1u << 17;
  asm volatile(
      "global_load_dword %0, %3, %4 sc1\n\t"
      "s_sleep 6\n"
      "1:\n\t"
      "global_load_dword %1, %3, %4 sc1\n\t"
      "s_waitcnt vmcnt(1)\n\t"
      "v_cmp_ne_u32 vcc, %0, %5\n\t"
      "s_cbranch_vccz 2f\n\t"
      "global_load_dword %0, %3, %4 sc1\n\t"
      "s_waitcnt vmcnt(1)\n\t"
      "v_cmp_ne_u32 vcc, %1, %5\n\t"
      "s_cbranch_vccz 2f\n\t"
      "s_sub_u32 %2, %2, 1\n\t"
      "s_cbranch_scc0 1b\n"
      "2:\n\t"
      "s_waitcnt vmcnt(0)"
      : "=&v"(a), "=&v"(b), "+s"(n) : "v"(off), "s"(ws), "v"(tag) : "vcc", "scc", "memory");
}

template <bool KV>
__device__ __forceinline__ void gemm_compute(f32x4 (&acc)[4][5], unsigned xa0, unsigned wa0, unsigned wa1) {
  bf16x8 xa[4], xb[4], wa[5], wb[5];
  auto load = [&](bf16x8 (&xf)[4], bf16x8 (&wf)[5], int buf, int kk) {
    const unsigned bo = buf * STG;
    const unsigned xa = (xa0 + bo) ^ (kk * 64);
    const unsigned a0 = wa0 + bo + kk * 10240, a1 = wa1 + bo + kk * 10240;
#pragma unroll
    for (int i = 0; i < 4; i++) xf[i] = lds_b128(xa + 2048 * i);
#pragma unroll
    for (int t = 0; t < 5; t++) { const unsigned a = ((t & 1) ? a1 : a0) + 32 * t; wf[t] = mk8(lds_tr(a), lds_tr(a + 1280)); }
  };
  auto mma = [&](const bf16x8 (&xf)[4], const bf16x8 (&wf)[5]) {
#pragma unroll
    for (int t = 0; t < 5; t++)
#pragma unroll
      for (int i = 0; i < 4; i++) acc[i][t] = KV ? mfma16(xf[i], wf[t], acc[i][t]) : mfma16(wf[t], xf[i], acc[i][t]);
  };
  auto interleave = [&]() {
#pragma unroll
    for (int t = 0; t < 14; t++) { SGB(0x8, 1); SGB(0x100, 1); }
    SGB(0x8, 6);
  };
  BAR();
  load(xa, wa, 0, 0);
  SBAR();
  for (int s = 0; s < 9; s++) {
    load(xb, wb, s & 3, 1); mma(xa, wa); interleave();
    SBAR();
    BAR();
    load(xa, wa, (s + 1) & 3, 0); mma(xb, wb); interleave();
    SBAR();
  }
  load(xb, wb, 1, 1); mma(xa, wa); interleave();
  SBAR();
  mma(xb, wb);
}

__device__ __forceinline__ int gemm_s0(int rt, int cg, bool isKV) { return (((rt & 3) * 5 + 1) / 2 + (isKV ? 2 * (cg & 3) : 8 + (cg & 1))) % 10; }
__device__ __forceinline__ void gemm_vx(unsigned (&vx)[2], int L, int lane, int rt) {
#pragma unroll
  for (int i = 0; i < 2; i++) vx[i] = (rt * 128 + L * 64 + i * 8 + (lane >> 3)) * 1280 + (((lane & 7) ^ ((4 * i + (lane >> 4)) & 7)) << 4);
}
__device__ __forceinline__ void gemm_first_x(int L, int lane, unsigned lb, const u16* x, int rt, int cg, bool isKV) {
  const i32x4 sx = make_srd(x, S * D * 2);
  unsigned vx[2];
  gemm_vx(vx, L, lane, rt);
  const int s0 = gemm_s0(rt, cg, isKV);
#pragma unroll
  for (int p = 0; p < 8; p++) glds16(lb + (L * 8 + p) * 1024, vx[p & 1], sx, s0 * 128 + (p >> 1) * (16 * 1280));
}
__device__ __forceinline__ void gemm_loader(int L, int lane, unsigned lb, const u16* x, const u16* wqkv, int rt, int cg, bool isKV) {
  const int s0 = gemm_s0(rt, cg, isKV);
  const i32x4 sx = make_srd(x, S * D * 2), sw = make_srd(wqkv, D * 3 * D * 2);
  unsigned vx[2], vw[5];
  gemm_vx(vx, L, lane, rt);
#pragma unroll
  for (int i = 0; i < 5; i++) {
    const int ci = i * 64 + lane, k = (ci * 3277) >> 16, pos = ci - k * 20, gc = pos ^ (((k >> 3) & 1) << 1), tl = gc >> 1;
    unsigned col;
    if (isKV) { const int isv = (tl >= 3 && tl < 5) || tl >= 7; const int tt = tl < 3 ? tl : tl < 5 ? tl - 3 : tl < 7 ? tl - 2 : tl - 5;
                col = (isv ? 2560u : 1280u) + 160u * cg + tt * 32; }
    else col = 320u * cg + tl * 32;
    vw[i] = (L * 32 + k) * 3840 + col + (gc & 1) * 16;
  }
  auto stage = [&](int st_, int buf) {
    const unsigned base = lb + buf * STG;
    int st = st_ + s0; st = st >= 10 ? st - 10 : st;
#pragma unroll
    for (int p = 0; p < 10; p++) {
      glds16(base + XS + (L * 10 + p) * 1024, vw[p % 5], sw, st * (64 * 3840) + (p / 5) * (16 * 3840));
      if (p < 8) glds16(base + (L * 8 + p) * 1024, vx[p & 1], sx, st * 128 + (p >> 1) * (16 * 1280));
    }
  };
#pragma unroll
  for (int p = 0; p < 10; p++) glds16(lb + XS + (L * 10 + p) * 1024, vw[p % 5], sw, s0 * (64 * 3840) + (p / 5) * (16 * 3840));
  stage(1, 1); stage(2, 2);
  wait_bar<36>();
  stage(3, 3);
  for (int s = 0; s < 9; s++) {
    if (s < 7) wait_bar<36>(); else if (s == 7) wait_bar<18>(); else wait_bar<0>();
    if (s < 6) stage(s + 4, s & 3);
  }
}

template <int T> struct StatTile {
  static constexpr bool isz = T >= 13;
  static constexpr int i = isz ? T - 13 : (T + 1) / 3;
  static constexpr int j = isz ? 0 : T - (i == 0 ? 0 : 3 * i - 1) + (i == 0 ? 0 : i - 1);
};
template <int T> __device__ __forceinline__ f32x4 stat_mma(unsigned la) {
  const u32x4 o1 = {0x3F803F80u, 0x3F803F80u, 0x3F803F80u, 0x3F803F80u};
  bf16x8 A[4], B[4];
#pragma unroll
  for (int m = 0; m < 4; m++) {
    A[m] = lds_b128(la + StatTile<T>::i * 1024 + m * 10240);
    B[m] = StatTile<T>::isz ? __builtin_bit_cast(bf16x8, o1) : lds_b128(la + (5 + StatTile<T>::j) * 1024 + m * 10240);
  }
  f32x4 d4 = {0.f, 0.f, 0.f, 0.f};
#pragma unroll
  for (int m = 0; m < 4; m++) d4 = mfma16(A[m], B[m], d4);
  return d4;
}
template <int T> __device__ __forceinline__ void stat_store(f32x4 d4, int lane, __amdgpu_buffer_rsrc_t rw, unsigned pbase) {
  const int fr = lane & 15, g4 = lane >> 4;
  const int Dg = 16 * StatTile<T>::i + 4 * g4, h = (Dg * 205) >> 12, dd = Dg - 20 * h;
  const int ee = StatTile<T>::isz ? 20 : 16 * StatTile<T>::j + fr - 20 * h;
  if (StatTile<T>::isz ? fr == 0 : (ee >= 0 && ee < 20))
    __builtin_amdgcn_raw_buffer_store_b64((u32x2){pk2(d4[0], d4[1]), pk2(d4[2], d4[3])}, rw, pbase + (h * 420 + ee * 20 + dd) * 2, 0, SC1);
}
template <int W> __device__ __forceinline__ void stat_tiles(unsigned lb, int lane, __amdgpu_buffer_rsrc_t rw, unsigned pbase) {
  const unsigned la = lb + lane * 16;
  const f32x4 a = stat_mma<W>(la), b = stat_mma<W + 8>(la);
  if constexpr (W < 2) { const f32x4 c = stat_mma<W + 16>(la); stat_store<W + 16>(c, lane, rw, pbase); }
  stat_store<W>(a, lane, rw, pbase);
  stat_store<W + 8>(b, lane, rw, pbase);
}

__device__ __forceinline__ void conv_load_h(u32x4 (&cur)[4], u32x4 (&prv)[4], const u16* __restrict__ hrow, int lane) {
#pragma unroll
  for (int it = 0; it < 4; it++) {
    const int i = it * 64 + lane;
    cur[it] = *(const u32x4*)(hrow + i * 8); prv[it] = *(const u32x4*)(hrow + (i > 0 ? i * 8 - 8 : 0));
  }
}
__device__ __forceinline__ void conv_prep_h(const u32x4 (&cur)[4], const u32x4 (&prv_)[4], unsigned cb, int lane) {
#pragma unroll
  for (int it = 0; it < 4; it++) {
    u32x4 prv = prv_[it];
    if (it == 0 && lane == 0) prv = (u32x4){0, 0, 0, 0};
    const unsigned W_[8] = {prv.x, prv.y, prv.z, prv.w, cur[it].x, cur[it].y, cur[it].z, cur[it].w};
    const int c = 255 - (it * 64 + lane);
#pragma unroll
    for (int e = 0; e < 4; e++) {
      u32x4 ov;
#pragma unroll
      for (int q = 0; q < 4; q++) {
        const int i0 = 15 - e - 2 * q, i1 = i0 - 1;
        const unsigned sel = ((i0 & 1) ? 0x0706u : 0x0504u) | (((i1 & 1) ? 0x0302u : 0x0100u) << 16);
        ov[q] = __builtin_amdgcn_perm(W_[i0 >> 1], W_[i1 >> 1], sel);
      }
      *(LAS u32x4*)(size_t)(cb + e * CPS + c * 16) = ov;
    }
  }
  if (lane < 8) *(LAS u32x4*)(size_t)(cb + (lane >> 1) * CPS + (256 + (lane & 1)) * 16) = (u32x4){0, 0, 0, 0};
  if (lane < 32) *(LAS u32x4*)(size_t)(cb + UOFF + lane * 16) = (u32x4){0, 0, 0, 0};
}
template <class F>
__device__ __forceinline__ void conv_load_u(__amdgpu_buffer_rsrc_t rw, int d, unsigned cb, int lane, F&& between) {
  u32x4 uv[4];
#pragma unroll
  for (int it = 0; it < 4; it++) uv[it] = __builtin_amdgcn_raw_buffer_load_b128(rw, OFF_UT + d * 4096 + (it * 64 + lane) * 16, 0, SC1);
  SBAR();
  between();
  SBAR();
#pragma unroll
  for (int it = 0; it < 4; it++) *(LAS u32x4*)(size_t)(cb + UOFF + 512 + (it * 64 + lane) * 16) = uv[it];
}
struct ConvFrags { u32x2 al[8], ah[8]; u32x4 B[8]; };
__device__ __forceinline__ void conv_read(ConvFrags& f, unsigned aa, unsigned ba) {
  asm volatile(
      "ds_read_b128 %16, %25\n\tds_read_b64 %7, %24 offset:3584\n\tds_read_b64 %15, %24 offset:3592\n\t"
      "ds_read_b128 %17, %25 offset:512\n\tds_read_b128 %18, %25 offset:1024\n\tds_read_b128 %19, %25 offset:1536\n\t"
      "ds_read_b128 %20, %25 offset:2048\n\tds_read_b128 %21, %25 offset:2560\n\tds_read_b128 %22, %25 offset:3072\n\t"
      "ds_read_b128 %23, %25 offset:3584\n\t"
      "ds_read_b64 %6, %24 offset:3072\n\tds_read_b64 %14, %24 offset:3080\n\t"
      "ds_read_b64 %5, %24 offset:2560\n\tds_read_b64 %13, %24 offset:2568\n\t"
      "ds_read_b64 %4, %24 offset:2048\n\tds_read_b64 %12, %24 offset:2056\n\t"
      "ds_read_b64 %3, %24 offset:1536\n\tds_read_b64 %11, %24 offset:1544\n\t"
      "ds_read_b64 %2, %24 offset:1024\n\tds_read_b64 %10, %24 offset:1032\n\t"
      "ds_read_b64 %1, %24 offset:512\n\tds_read_b64 %9, %24 offset:520\n\t"
      "ds_read_b64 %0, %24\n\tds_read_b64 %8, %24 offset:8"
      : "=&v"(f.al[0]), "=&v"(f.al[1]), "=&v"(f.al[2]), "=&v"(f.al[3]), "=&v"(f.al[4]), "=&v"(f.al[5]), "=&v"(f.al[6]), "=&v"(f.al[7]),
        "=&v"(f.ah[0]), "=&v"(f.ah[1]), "=&v"(f.ah[2]), "=&v"(f.ah[3]), "=&v"(f.ah[4]), "=&v"(f.ah[5]), "=&v"(f.ah[6]), "=&v"(f.ah[7]),
        "=&v"(f.B[0]), "=&v"(f.B[1]), "=&v"(f.B[2]), "=&v"(f.B[3]), "=&v"(f.B[4]), "=&v"(f.B[5]), "=&v"(f.B[6]), "=&v"(f.B[7])
      : "v"(aa), "v"(ba) : "memory");
}
__device__ __forceinline__ void conv_wait(ConvFrags& f) {
  asm volatile("s_waitcnt lgkmcnt(0)"
      : "+v"(f.al[0]), "+v"(f.al[1]), "+v"(f.al[2]), "+v"(f.al[3]), "+v"(f.al[4]), "+v"(f.al[5]), "+v"(f.al[6]), "+v"(f.al[7]),
        "+v"(f.ah[0]), "+v"(f.ah[1]), "+v"(f.ah[2]), "+v"(f.ah[3]), "+v"(f.ah[4]), "+v"(f.ah[5]), "+v"(f.ah[6]), "+v"(f.ah[7]),
        "+v"(f.B[0]), "+v"(f.B[1]), "+v"(f.B[2]), "+v"(f.B[3]), "+v"(f.B[4]), "+v"(f.B[5]), "+v"(f.B[6]), "+v"(f.B[7]) :: "memory");
}
template <int K> __device__ __forceinline__ void conv_read1(ConvFrags& f, unsigned aa, unsigned ba) {
  if constexpr (K < 8) asm volatile("ds_read_b128 %0, %1 offset:%2" : "=&v"(f.B[K]) : "v"(ba), "n"(512 * K) : "memory");
  else if constexpr (K < 16) asm volatile("ds_read_b64 %0, %1 offset:%2" : "=&v"(f.al[K - 8]) : "v"(aa), "n"(512 * (K - 8)) : "memory");
  else if constexpr (K < 24) asm volatile("ds_read_b64 %0, %1 offset:%2" : "=&v"(f.ah[K - 16]) : "v"(aa), "n"(512 * (K - 16) + 8) : "memory");
}
template <int K = 0> __device__ __forceinline__ void conv_step(f32x4 (&acc)[8], const ConvFrags& f, ConvFrags& nx, unsigned aa, unsigned ba) {
  if constexpr (K < 36) {
    constexpr int a = K < 8 ? 0 : K < 15 ? 1 : K < 21 ? 2 : K < 26 ? 3 : K < 30 ? 4 : K < 33 ? 5 : K < 35 ? 6 : 7;
    constexpr int i = K - (a == 0 ? 0 : a == 1 ? 8 : a == 2 ? 15 : a == 3 ? 21 : a == 4 ? 26 : a == 5 ? 30 : a == 6 ? 33 : 35);
    acc[a + i] = mfma16(mk8(f.al[7 - a], f.ah[7 - a]), __builtin_bit_cast(bf16x8, f.B[i]), acc[a + i]);
    conv_read1<K>(nx, aa, ba);
    SBAR();
    conv_step<K + 1>(acc, f, nx, aa, ba);
  }
}
__device__ __forceinline__ void conv_main(f32x4 (&acc)[8], unsigned cb, int lane, int c0, int c1) {
  const int fr = lane & 15, g4 = lane >> 4;
  const unsigned abase = cb + (3 - (fr & 3)) * CPS + 2 * (2044 - 16 * 112 - 16 * (g4 >> 1) + 8 * (g4 & 1) - (fr & ~3));
  const unsigned bbase = cb + UOFF + 512 + 2 * (16 * fr - 16 * (g4 >> 1) + 8 * (g4 & 1));
#pragma unroll
  for (int c = 0; c < 8; c++) acc[c] = (f32x4){0.f, 0.f, 0.f, 0.f};
  ConvFrags f0, f1;
  asm volatile("s_waitcnt lgkmcnt(0)" ::: "memory");
  conv_read(f0, abase - 64 * c0, bbase - 64 * c0);
  for (int cc = c0; cc < c1; cc += 2) {
    conv_wait(f0);
    SBAR();
    conv_step(acc, f0, f1, abase - 64 * (cc + 1), bbase - 64 * (cc + 1));
    conv_wait(f1);
    SBAR();
    const int cn = cc + 2 < c1 ? cc + 2 : cc + 1;
    conv_step(acc, f1, f0, abase - 64 * cn, bbase - 64 * cn);
  }
  conv_wait(f0);
}
__device__ __forceinline__ void conv_store(f32x4 y, unsigned cb, int c, int lane, float Dd, __amdgpu_buffer_rsrc_t rw, int d) {
  const int fr = lane & 15, g4 = lane >> 4;
  const u32x2 uu = lds_b64(cb + UOFF + 512 + 2 * (256 * c + 16 * fr + 4 * g4));
  const float r0 = fmaf(bflo(uu.x), Dd, y[0]), r1 = fmaf(bfhi(uu.x), Dd, y[1]);
  const float r2 = fmaf(bflo(uu.y), Dd, y[2]), r3 = fmaf(bfhi(uu.y), Dd, y[3]);
  __builtin_amdgcn_raw_buffer_store_b64((u32x2){pk2(r0, r1), pk2(r2, r3)}, rw, OFF_YT + d * 4096 + (256 * c + 16 * fr + 4 * g4) * 2, 0, SC1);
}

__global__ __launch_bounds__(512, 2) void fused(const u16* __restrict__ x, const u16* __restrict__ wqkv, const u16* __restrict__ wo,
                                                const u16* __restrict__ Bk, const u16* __restrict__ Bv, const u16* __restrict__ shk,
                                                const u16* __restrict__ hk, const u16* __restrict__ Dv, const u16* __restrict__ gw,
                                                u16* __restrict__ out, char* __restrict__ ws) {
  extern __shared__ __attribute__((aligned(16))) char sm[];
  const int tid = threadIdx.x, lane = tid & 63;
  const int w = __builtin_amdgcn_readfirstlane(tid >> 6);
  const int b = blockIdx.x, xcd = b & 7, r = b >> 3;
  const int fr = lane & 15, g4 = lane >> 4;
  const unsigned lb = (unsigned)(size_t)(LAS char*)sm;
  const __amdgpu_buffer_rsrc_t rw = __builtin_amdgcn_make_buffer_rsrc(ws, 0, WS_USED, 0x00020000);
  if (__builtin_expect(r < 24 && (w >> 1) == 2, 1)) {
    const bool kv_ = r < 16;
    const int rt_ = (xcd & 3) * 4 + (r & 3), cg_ = kv_ ? (xcd >> 2) * 4 + (r >> 2) : (xcd >> 2) * 2 + ((r - 16) >> 2);
    gemm_first_x(w - 4, lane, lb, x, rt_, cg_, kv_);
    gemm_loader(w - 4, lane, lb, x, wqkv, rt_, cg_, kv_);
  }
  unsigned e0 = 0;
#define PUT_TAG() do { if (w == 7 && lane == 0) { *(LAS unsigned*)(size_t)(lb + L_TAG) = e0 + 1u; *(LAS u32x4*)(size_t)(lb + L_CNT) = (u32x4){0u, 0u, 0u, 0u}; } } while (0)
#define TAG (*(const LAS unsigned*)(size_t)(lb + L_TAG))
  if (r < 24) {
    const bool isKV = r < 16;
    const int rt = (xcd & 3) * 4 + (r & 3);
    const int cg = isKV ? (xcd >> 2) * 4 + (r >> 2) : (xcd >> 2) * 2 + ((r - 16) >> 2);
    f32x4 acc[4][5];
    const int rp = (w >> 1) & 1, hf = w & 1;
    if (w < 4) {
      const unsigned xa0 = lb + (rp * 64 + fr) * 128 + ((g4 ^ ((fr >> 1) & 7)) << 4);
      const unsigned wl = lb + XS + (8 * g4 + (fr >> 2)) * 320 + ((lane & 3) >> 1) * 16 + (lane & 1) * 8 + hf * 160;
      const unsigned sgn = (g4 & 1) * 32;
      const unsigned wa0 = hf ? wl - sgn : wl + sgn, wa1 = hf ? wl + sgn : wl - sgn;
#pragma unroll
      for (int i = 0; i < 4; i++)
#pragma unroll
        for (int t = 0; t < 5; t++) acc[i][t] = (f32x4){0.f, 0.f, 0.f, 0.f};
      if (isKV) gemm_compute<true>(acc, xa0, wa0, wa1); else gemm_compute<false>(acc, xa0, wa0, wa1);
    } else if (w >= 6) {
      const int w6 = w - 6;
      if (w == 7) e0 = flag_ld(ws, F_E);
      if (isKV) {
        const int l16 = lane & 15, row0 = rt * 128 + cg * 16 + w6 * 8;
        const u32x4 g0 = *(const u32x4*)(gw + lane * 8), g1 = *(const u32x4*)(gw + 512 + l16 * 8);
        u32x4 gx0[8], gx1[8];
#pragma unroll
        for (int i = 0; i < 8; i++) {
          const u16* xr = x + (size_t)(row0 + i) * D;
          gx0[i] = *(const u32x4*)(xr + lane * 8); gx1[i] = *(const u32x4*)(xr + 512 + l16 * 8);
        }
        SBAR();
        BAR(); BAR(); BAR();
        float a[8];
        const float m1 = lane < 16 ? 1.f : 0.f;
#pragma unroll
        for (int i = 0; i < 8; i++) {
          float a0 = 0.f, a1 = 0.f;
#pragma unroll
          for (int j = 0; j < 4; j++) {
            a0 = fmaf(bflo(gx0[i][j]), bflo(g0[j]), a0); a0 = fmaf(bfhi(gx0[i][j]), bfhi(g0[j]), a0);
            a1 = fmaf(bflo(gx1[i][j]), bflo(g1[j]), a1); a1 = fmaf(bfhi(gx1[i][j]), bfhi(g1[j]), a1);
          }
          a[i] = fmaf(a1, m1, a0);
          if (i == 3) { SBAR(); BAR(); SBAR(); }
        }
        SBAR(); BAR(); SBAR();
#pragma unroll
        for (int i = 0; i < 4; i++) { const float lo = a[i], hi = a[i + 4]; const bool up = lane & 4; a[i] = (up ? hi : lo) + __shfl_xor(up ? lo : hi, 4); }
#pragma unroll
        for (int i = 0; i < 2; i++) { const float lo = a[i], hi = a[i + 2]; const bool up = lane & 2; a[i] = (up ? hi : lo) + __shfl_xor(up ? lo : hi, 2); }
        { const float lo = a[0], hi = a[1]; const bool up = lane & 1; a[0] = (up ? hi : lo) + __shfl_xor(up ? lo : hi, 1); }
        a[0] += __shfl_xor(a[0], 8); a[0] += __shfl_xor(a[0], 16); a[0] += __shfl_xor(a[0], 32);
        if (lane < 8) __builtin_amdgcn_raw_buffer_store_b32(__float_as_uint(rbf(1.f / (1.f + __expf(-rbf(a[0]))))), rw, OFF_G + (row0 + lane) * 4, 0, SC1);
        PUT_TAG();
        SBAR();
        BAR(); BAR(); BAR(); BAR(); BAR();
      } else {
        const int hw = (xcd * 8 + (r - 16)) * 2 + w6;
        const unsigned scr = lb + L_T + w6 * 6144;
#pragma unroll
        for (int bt = 0; bt < 2; bt++) {
          const int blk = bt == 0 ? hw : (hw < 32 ? 128 + hw : hw), ct = (blk * 3277) >> 16, ks = blk - 20 * ct;
          u32x4 v[5];
#pragma unroll
          for (int i = 0; i < 5; i++) {
            const int c = i * 64 + lane, row = (c * 6554) >> 16, ch = c - 10 * row;
            v[i] = *(const u32x4*)(wo + (size_t)(32 * ks + row) * D + 80 * ct + 8 * ch);
          }
          SBAR();
          BAR();
          if (bt == 0) PUT_TAG();
          BAR();
          if (bt == 0 || hw < 32) {
#pragma unroll
            for (int i = 0; i < 5; i++) *(LAS u32x4*)(size_t)(scr + (i * 64 + lane) * 16) = v[i];
            const unsigned ta = scr + (8 * g4 + (fr >> 2)) * 160 + (lane & 3) * 8;
#pragma unroll
            for (int t = 0; t < 5; t++) {
              const u32x2 lo = lds_tr(ta + 32 * t), hi = lds_tr(ta + 32 * t + 640);
              __builtin_amdgcn_raw_buffer_store_b128((u32x4){lo.x, lo.y, hi.x, hi.y}, rw, OFF_WOP + (blk * 5 + t) * 1024 + lane * 16, 0, SC1);
            }
          }
          SBAR();
          BAR(); BAR();
        }
        DRAIN();
        if (lds_last(lb + L_CNT + 8, lane, 2) && lane == 0) flag_st(ws, F_W + (xcd * 8 + (r - 16)) * 128, TAG);
        BAR(); BAR();
      }
    }
    BAR();
    const unsigned tag = TAG;
    if (isKV) {
      if (w < 4) {
#pragma unroll
        for (int t = 0; t < 5; t++) {
          const bool isk = hf ? t < 2 : t < 3;
          const int slot = hf ? (t < 2 ? 3 + t : 5 + t) : (t < 3 ? t : 2 + t);
#pragma unroll
          for (int m = 0; m < 2; m++) {
            u32x4 f;
            if (isk) f = (u32x4){phi_pk(acc[2 * m][t][0], acc[2 * m][t][1]), phi_pk(acc[2 * m][t][2], acc[2 * m][t][3]),
                                 phi_pk(acc[2 * m + 1][t][0], acc[2 * m + 1][t][1]), phi_pk(acc[2 * m + 1][t][2], acc[2 * m + 1][t][3])};
            else f = (u32x4){pk2(acc[2 * m][t][0], acc[2 * m][t][1]), pk2(acc[2 * m][t][2], acc[2 * m][t][3]),
                             pk2(acc[2 * m + 1][t][0], acc[2 * m + 1][t][1]), pk2(acc[2 * m + 1][t][2], acc[2 * m + 1][t][3])};
            *(LAS u32x4*)(size_t)(lb + ((rp * 2 + m) * 10 + slot) * 1024 + lane * 16) = f;
          }
        }
      } else {
        for (int i = tid - 256; i < 6048 / 16; i += 256) *(LAS u32x4*)(size_t)(lb + L_T + i * 16) = (u32x4){0, 0, 0, 0};
        if (w == 7) poll(ws, F_W + lane * 128, true, tag);
      }
      BAR();
      switch (w) {
        case 0: stat_tiles<0>(lb, lane, rw, OFF_P + (rt * 8 + cg) * 3360); break;
        case 1: stat_tiles<1>(lb, lane, rw, OFF_P + (rt * 8 + cg) * 3360); break;
        case 2: stat_tiles<2>(lb, lane, rw, OFF_P + (rt * 8 + cg) * 3360); break;
        case 3: stat_tiles<3>(lb, lane, rw, OFF_P + (rt * 8 + cg) * 3360); break;
        case 4: stat_tiles<4>(lb, lane, rw, OFF_P + (rt * 8 + cg) * 3360); break;
        case 5: stat_tiles<5>(lb, lane, rw, OFF_P + (rt * 8 + cg) * 3360); break;
        case 6: stat_tiles<6>(lb, lane, rw, OFF_P + (rt * 8 + cg) * 3360); break;
        default: stat_tiles<7>(lb, lane, rw, OFF_P + (rt * 8 + cg) * 3360); break;
      }
      DRAIN();
      BAR();
      if (w == 0) {
        if (lane == 0) flag_st(ws, F_P + (rt * 8 + cg) * 128, tag);
        poll(ws, lane < 16 ? F_P + (lane * 8 + cg) * 128 : F_PQ + (rt * 4 + (cg >> 1)) * 128, lane <= 16, tag);
      } else if (w >= 4) {
        const i32x4 sws = make_srd(ws, WS_USED);
        const unsigned vwv = OFF_WOP + ((xcd >> 2) * 4 + (r >> 3)) * 102400 + lane * 16;
        for (int i = w - 4; i < 100; i += 4) {
          int pc = 12 * (r & 7) + i; pc = __builtin_amdgcn_readfirstlane(pc >= 100 ? pc - 100 : pc);
          glds16_sc1(lb + pc * 1024, vwv, sws, pc * 1024);
        }
      }
      constexpr int PJ[9] = {0, 1, 1, 2, 2, 3, 3, 4, 4}, PK[9] = {0, 0, 1, 0, 1, 1, 2, 1, 2};
      unsigned an[9], ad[9];
#pragma unroll
      for (int p = 0; p < 9; p++) {
        const int E = 16 * PJ[p] + fr, h = (E * 205) >> 12, e = E - 20 * h;
        int o_ = 32 * PK[p] + 8 * g4 - 20 * h; o_ = o_ < -8 ? -8 : o_ > 20 ? 20 : o_;
        an[p] = lb + L_T + (h * 21 + e) * 72 + (8 + o_) * 2; ad[p] = lb + L_T + (h * 21 + 20) * 72 + (8 + o_) * 2;
      }
      BAR();
      bf16x8 Bq[3];
      {
        const unsigned prow = OFF_PQ + ((rt * 8 + w) * 8 + cg) * 3072 + lane * 16;
#pragma unroll
        for (int ks = 0; ks < 3; ks++) Bq[ks] = __builtin_bit_cast(bf16x8, __builtin_amdgcn_raw_buffer_load_b128(rw, prow + 1024 * ks, 0, SC1));
      }
      if (tid < 210) {
        const unsigned pb = OFF_P + cg * 3360 + tid * 16;
        u32x4 v[16];
#pragma unroll
        for (int k = 0; k < 16; k++) v[k] = __builtin_amdgcn_raw_buffer_load_b128(rw, pb, (k ^ rt) * (8 * 3360), SC1);
        unsigned o4[4];
#pragma unroll
        for (int c = 0; c < 4; c++) {
          float lo[16], hi[16];
#pragma unroll
          for (int k = 0; k < 16; k++) { lo[k] = bflo(v[k][c]); hi[k] = bfhi(v[k][c]); }
#pragma unroll
          for (int st = 1; st < 16; st <<= 1)
#pragma unroll
            for (int k = 0; k < 16; k += 2 * st) { lo[k] += lo[k + st]; hi[k] += hi[k + st]; }
          o4[c] = pk2(lo[0], hi[0]);
        }
#pragma unroll
        for (int q = 0; q < 2; q++) {
          const int i4 = 2 * tid + q, row = (i4 * 13108) >> 16, d = 4 * (i4 - row * 5);
          *(LAS u32x2*)(size_t)(lb + L_T + row * 72 + (8 + d) * 2) = (u32x2){o4[2 * q], o4[2 * q + 1]};
        }
      }
      BAR();
      {
        if (g4 >= 2) Bq[2] = __builtin_bit_cast(bf16x8, (u32x4){0, 0, 0, 0});
        f32x4 nu[5], de[5];
#pragma unroll
        for (int j = 0; j < 5; j++) { nu[j] = (f32x4){0.f, 0.f, 0.f, 0.f}; de[j] = nu[j]; }
        bf16x8 An[9], Ad[9];
#pragma unroll
        for (int p = 0; p < 9; p++) {
          unsigned an8 = an[p] + 8, ad8 = ad[p] + 8;
          asm volatile("" : "+v"(an8), "+v"(ad8));
          An[p] = mk8(lds_b64(an[p]), lds_b64(an8)); Ad[p] = mk8(lds_b64(ad[p]), lds_b64(ad8));
        }
#pragma unroll
        for (int p = 0; p < 9; p++) { de[PJ[p]] = mfma16(Ad[p], Bq[PK[p]], de[PJ[p]]); nu[PJ[p]] = mfma16(An[p], Bq[PK[p]], nu[PJ[p]]); }
        u32x2 ov[5];
#pragma unroll
        for (int j = 0; j < 5; j++) {
          const float rd = __builtin_amdgcn_rcpf(de[j][0] + 1e-6f);
          ov[j] = (u32x2){pk2(nu[j][0] * rd, nu[j][1] * rd), pk2(nu[j][2] * rd, nu[j][3] * rd)};
        }
        store_frags(ov, rw, OFF_O + (rt * 8 + w) * 20480, 80 * cg, fr, g4);
      }
      DRAIN();
      BAR();
      if (tid == 0) flag_st(ws, F_O + (rt * 8 + cg) * 128, tag);
    } else {
      const int vb = xcd * 8 + (r - 16);
      const int d = 80 * (vb >> 3) + 48 + 4 * (vb & 7) + (w & 3);
      const unsigned cb = lb + (w & 3) * CHS;
      if (w < 4) {
#pragma unroll
        for (int i = 0; i < 4; i++) {
          u32x2 pv[5];
#pragma unroll
          for (int t = 0; t < 5; t++) pv[t] = (u32x2){phi_pk(acc[i][t][0], acc[i][t][1]), phi_pk(acc[i][t][2], acc[i][t][3])};
          store_frags(pv, rw, OFF_PQ + ((rt * 8 + rp * 4 + i) * 8 + cg * 2 + hf) * 3072, 0, fr, g4);
        }
      } else {
        u32x4 hc[4], hp[4];
        conv_load_h(hc, hp, hk + (size_t)d * S, lane);
        poll(ws, F_U + lane * 128, true, tag);
        conv_load_u(rw, d, cb, lane, [&]() { conv_prep_h(hc, hp, cb, lane); });
      }
      DRAIN();
      BAR();
      if (tid == 0) flag_st(ws, F_PQ + (rt * 4 + cg) * 128, tag);
      if (w < 4) {
        const float Dd = bflo(Dv[d]);
        f32x4 y[8];
        conv_main(y, cb, lane, 0, 8);
#pragma unroll
        for (int c = 0; c < 8; c++) conv_store(y[c], cb, c, lane, Dd, rw, d);
        DRAIN();
      } else if (w == 7) {
        poll(ws, F_W + lane * 128, true, tag);
      }
      BAR();
      if (tid == 0) flag_st(ws, F_Y + (64 + vb) * 128, tag);
    }
  } else {
    const int pw = xcd * 8 + (r - 24);
    const int c6 = w < 4 ? w : 4 + ((w - 4) >> 1);
    const int d = 80 * (pw >> 3) + 6 * (pw & 7) + c6;
    const unsigned cb = lb + c6 * CHS;
    const bool prep = w < 4 || !(w & 1);
    u32x4 hc[4], hp[4];
    {
      const int rbase = xcd * 256 + ((r - 24) >> 1) * 64, cbase = ((r - 24) & 1) * 320;
      u32x4 kr[2][7], vr[2][4], sp[2][4];
#pragma unroll
      for (int q = 0; q < 2; q++) {
        const int it = w + 8 * q;
        if (q == 0 || w < 2) {
          const int ch = cbase + (it >> 1) * 64 + (lane & 7) * 8;
          const int s0 = rbase + (it & 1) * 32 + (lane >> 3) * 4;
#pragma unroll
          for (int j = 0; j < 7; j++) { int s = s0 - 3 + j; kr[q][j] = *(const u32x4*)(Bk + (size_t)(s < 0 ? 0 : s) * D + ch); }
#pragma unroll
          for (int j = 0; j < 4; j++) vr[q][j] = *(const u32x4*)(Bv + (size_t)(s0 + j) * D + ch);
#pragma unroll
          for (int j = 0; j < 4; j++) sp[q][j] = ((const u32x4*)(shk + ch * 4))[j];
        }
      }
      if (prep) conv_load_h(hc, hp, hk + (size_t)d * S, lane);
      if (w == 7) e0 = flag_ld(ws, F_E);
      SBAR();
#pragma unroll
      for (int q = 0; q < 2; q++) {
        const int it = w + 8 * q;
        if (q == 0 || w < 2) {
          const int s0 = rbase + (it & 1) * 32 + (lane >> 3) * 4;
          if (s0 == 0) { kr[q][0] = (u32x4){0, 0, 0, 0}; kr[q][1] = kr[q][0]; kr[q][2] = kr[q][0]; }
          const int rg = (it & 1) * 8 + (lane >> 3);
          const unsigned la = lb + L_UIMG + ((it >> 1) * 64 + (lane & 7) * 8) * 128 + (((rg >> 1) ^ (lane & 7)) << 4) + (rg & 1) * 8;
#pragma unroll
          for (int c = 0; c < 8; c++) {
            const unsigned s01 = sp[q][c >> 1][(c & 1) * 2], s23 = sp[q][c >> 1][(c & 1) * 2 + 1];
            const float w0 = bflo(s01), w1 = bfhi(s01), w2 = bflo(s23), w3 = bfhi(s23);
            float kk[7], u[4];
#pragma unroll
            for (int j = 0; j < 7; j++) { unsigned dd = kr[q][j][c >> 1]; kk[j] = (c & 1) ? bfhi(dd) : bflo(dd); }
#pragma unroll
            for (int j = 0; j < 4; j++) {
              unsigned dd = vr[q][j][c >> 1]; float v = (c & 1) ? bfhi(dd) : bflo(dd);
              float ks = kk[j] * w3; ks = fmaf(kk[j + 1], w2, ks); ks = fmaf(kk[j + 2], w1, ks); ks = fmaf(kk[j + 3], w0, ks);
              u[j] = ks * v;
            }
            *(LAS u32x2*)(size_t)(la + c * 128) = (u32x2){pk2(u[0], u[1]), pk2(u[2], u[3])};
          }
        }
      }
      PUT_TAG();
      BAR();
#pragma unroll
      for (int i = 0; i < 5; i++) {
        const int chl = (w * 5 + i) * 8 + (lane >> 3), k = lane & 7;
        const u32x4 v = *(const LAS u32x4*)(size_t)(lb + L_UIMG + chl * 128 + ((k ^ ((chl >> 3) & 7)) << 4));
        __builtin_amdgcn_raw_buffer_store_b128(v, rw, OFF_UT + (cbase + chl) * 4096 + rbase * 2 + k * 16, 0, SC1);
      }
    }
    if (w < 5 && prep) conv_prep_h(hc, hp, cb, lane);
    DRAIN();
    BAR();
    const unsigned tag = TAG;
    if (tid == 0) flag_st(ws, F_U + pw * 128, tag);
    const float Dd = bflo(Dv[d]);
    if (prep) {
      if (w == 6) conv_prep_h(hc, hp, cb, lane);
      poll(ws, F_U + lane * 128, true, tag);
      conv_load_u(rw, d, cb, lane, [&]() {});
    }
    BAR();
    f32x4 y[8];
    conv_main(y, cb, lane, w < 4 ? 0 : (w & 1) * 4, w < 4 ? 8 : (w & 1) * 4 + 4);
    if (w < 4) {
#pragma unroll
      for (int c = 0; c < 8; c++) conv_store(y[c], cb, c, lane, Dd, rw, d);
    }
    BAR();
    if (w >= 4) {
#pragma unroll
      for (int c = 0; c < 4; c++) { const f32x4 lo = y[c], hi = y[4 + c]; *(LAS f32x4*)(size_t)(cb + ((w & 1) * 4 + c) * 1024 + lane * 16) = (w & 1) ? lo : hi; }
    }
    BAR();
    if (w >= 4) {
#pragma unroll
      for (int c = 0; c < 4; c++) {
        const f32x4 o_ = *(const LAS f32x4*)(size_t)(cb + (((w & 1) ^ 1) * 4 + c) * 1024 + lane * 16);
        const f32x4 lo = y[c], hi = y[4 + c];
        const f32x4 mine = (w & 1) ? hi : lo;
        conv_store((w & 1) ? o_ + mine : mine + o_, cb, (w & 1) * 4 + c, lane, Dd, rw, d);
      }
    }
    if (w == 0) poll(ws, F_W + lane * 128, true, tag);
    DRAIN();
    BAR();
    if (tid == 0) flag_st(ws, F_Y + pw * 128, tag);
  }

  {
    const unsigned tag = TAG;
    const int rt = (xcd & 3) * 8 + (r & 7), ct = (xcd >> 2) * 4 + (r >> 3);
    const int sw_ = w & 3, kk = w >> 2;
    const i32x4 sws = make_srd(ws, WS_USED);
    const int srow = rt * 64 + sw_ * 16 + fr;
    u32x2 xv[3];
#pragma unroll
    for (int t = 0; t < 3; t++) { const int tt = kk ? (t < 2 ? 3 + t : 4) : t; xv[t] = *(const u32x2*)(x + (size_t)srow * D + ct * 80 + 16 * tt + 4 * g4); }
    SBAR();
    const int rot = ((ct & 3) * 5) >> 1;
    const unsigned wa0 = lb + kk * 5120 + lane * 16;
    bf16x8 wf0[5][5];
    auto read_wf0 = [&]() {
#pragma unroll
      for (int s = 0; s < 5; s++) {
        int q = s + rot; q = q >= 10 ? q - 10 : q;
#pragma unroll
        for (int t = 0; t < 5; t++) wf0[s][t] = lds_b128(wa0 + q * 10240 + t * 1024);
      }
    };
    if (r < 16) read_wf0();
    SBAR();
    if (w == 0) {
      poll(ws, F_O + ((rt >> 1) * 8 + (lane & 7)) * 128, lane < 8, tag);
    } else if (w == 7) {
      poll(ws, F_Y + ((lane & 8 ? 64 : 0) + ct * 8 + (lane & 7)) * 128, lane < 16, tag);
    } else {
      if (r >= 16) {
        const unsigned vwv = OFF_WOP + ct * 102400 + lane * 16;
        for (int i = w - 1; i < 100; i += 6) { int pc = 12 * (r & 7) + i; pc = __builtin_amdgcn_readfirstlane(pc >= 100 ? pc - 100 : pc); glds16_sc1(lb + pc * 1024, vwv, sws, pc * 1024); }
      }
      if (w == 6 && b == 255) {
        for (int spins = 0; spins < (1 << 17); spins++) {
          const unsigned a0 = flag_ld(ws, F_U + lane * 128), a1 = flag_ld(ws, F_U + (64 + lane) * 128);
          const unsigned a2 = flag_ld(ws, F_U + (128 + lane) * 128), a3 = flag_ld(ws, F_U + (192 + lane) * 128);
          if (__all(a0 == tag && a1 == tag && a2 == tag && a3 == tag)) break;
          __builtin_amdgcn_s_sleep(2);
        }
        if (lane == 0) flag_st(ws, F_E, tag);
      }
      DRAIN();
    }
    BAR();
    asm volatile("" :: "v"(xv[0]), "v"(xv[1]), "v"(xv[2]));
    bf16x8 of[10];
    {
      const unsigned vo = OFF_O + (rt * 4 + sw_) * 20480 + kk * 1024 + lane * 16;
#pragma unroll
      for (int s = 0; s < 10; s++) { int q = s + rot; q = __builtin_amdgcn_readfirstlane(q >= 10 ? q - 10 : q); bload16_sc1(of[s], vo, sws, q * 2048); }
    }
    if (w == 7) {
      for (int pc = 0; pc < 10; pc++)
        glds16_sc1(lb + L_Y + pc * 1024, OFF_YT + (ct * 80 + pc * 8 + (lane >> 3)) * 4096 + rt * 128 + (lane & 7) * 16, sws, 0);
    }
    float g;
    asm volatile("buffer_load_dword %0, %1, %2, 0 offen sc1" : "=v"(g) : "v"(OFF_G + srow * 4), "s"(sws) : "memory");
    if (r >= 16) read_wf0();
    f32x4 acc[5];
#pragma unroll
    for (int t = 0; t < 5; t++) acc[t] = (f32x4){0.f, 0.f, 0.f, 0.f};
    {
      bf16x8 wf1[5][5];
#pragma unroll
      for (int s = 0; s < 5; s++) {
        int q = 5 + s + rot; q = q >= 10 ? q - 10 : q;
#pragma unroll
        for (int t = 0; t < 5; t++) wf1[s][t] = lds_b128(wa0 + q * 10240 + t * 1024);
      }
      SBAR();
      if (w == 7) asm volatile("s_waitcnt vmcnt(16)" ::: "memory"); else asm volatile("s_waitcnt vmcnt(6)" ::: "memory");
      asm volatile("" : "+v"(of[0]), "+v"(of[1]), "+v"(of[2]), "+v"(of[3]), "+v"(of[4]) :: "memory");
      SBAR();
#pragma unroll
      for (int s = 0; s < 5; s++)
#pragma unroll
        for (int t = 0; t < 5; t++) acc[t] = mfma16(wf0[s][t], of[s], acc[t]);
      SBAR();
      if (w == 7) asm volatile("s_waitcnt vmcnt(11)" ::: "memory"); else asm volatile("s_waitcnt vmcnt(1)" ::: "memory");
      asm volatile("" : "+v"(of[5]), "+v"(of[6]), "+v"(of[7]), "+v"(of[8]), "+v"(of[9]) :: "memory");
      SBAR();
#pragma unroll
      for (int s = 0; s < 5; s++)
#pragma unroll
        for (int t = 0; t < 5; t++) acc[t] = mfma16(wf1[s][t], of[5 + s], acc[t]);
      SBAR();
    }
    const unsigned ra = lb + L_RED + (sw_ * 5) * 1024 + lane * 16;
    if (kk == 0) { *(LAS f32x4*)(size_t)(ra + 3 * 1024) = acc[3]; *(LAS f32x4*)(size_t)(ra + 4 * 1024) = acc[4]; }
    else { *(LAS f32x4*)(size_t)(ra) = acc[0]; *(LAS f32x4*)(size_t)(ra + 1024) = acc[1]; *(LAS f32x4*)(size_t)(ra + 2048) = acc[2]; }
    asm volatile("s_waitcnt vmcnt(0)" : "+v"(g) :: "memory");
    BAR();
    const float omg = 1.f - g;
    const __amdgpu_buffer_rsrc_t ro = __builtin_amdgcn_make_buffer_rsrc(out, 0, SZ, 0x00020000);
    u32x2 rv[3];
#pragma unroll
    for (int t = 0; t < 3; t++) {
      const int tt = kk ? 3 + (t < 2 ? t : 1) : t;
      const f32x4 a0 = acc[t], a1 = acc[3 + (t < 2 ? t : 1)];
      const f32x4 a = *(const LAS f32x4*)(size_t)(ra + tt * 1024) + (kk ? a1 : a0);
      const u32x2 yv = lds_tr(lb + L_Y + (16 * tt + 4 * g4 + (fr >> 2)) * 128 + (sw_ * 16 + 4 * (fr & 3)) * 2);
      const float xs[4] = {bflo(xv[t].x), bfhi(xv[t].x), bflo(xv[t].y), bfhi(xv[t].y)};
      const float ys[4] = {bflo(yv.x), bfhi(yv.x), bflo(yv.y), bfhi(yv.y)};
      float r_[4];
#pragma unroll
      for (int q = 0; q < 4; q++) r_[q] = fmaf(g, a[q], omg * (xs[q] * ys[q]));
      rv[t] = (u32x2){pk2(r_[0], r_[1]), pk2(r_[2], r_[3])};
    }
    __builtin_amdgcn_raw_buffer_store_b128(pair16(rv[0], rv[1]), ro, (srow * D + ct * 80 + 16 * ((kk ? 3 : 0) + (g4 & 1)) + 8 * (g4 >> 1)) * 2, 0, SC1);
    if (kk == 0) __builtin_amdgcn_raw_buffer_store_b64(rv[2], ro, (srow * D + ct * 80 + 32 + 4 * g4) * 2, 0, SC1);
  }
}

extern "C" void kernel_launch(void* const* d_in, const int* in_sizes, int n_in, void* d_out, int out_size, void* d_ws, size_t ws_size,
                              hipStream_t stream) {
  const u16* x = (const u16*)d_in[0]; const u16* wqkv = (const u16*)d_in[1]; const u16* wo = (const u16*)d_in[2];
  const u16* Bk = (const u16*)d_in[4]; const u16* Bv = (const u16*)d_in[5]; const u16* shk = (const u16*)d_in[6];
  const u16* hk = (const u16*)d_in[7]; const u16* Dv = (const u16*)d_in[8]; const u16* gw = (const u16*)d_in[9];
  (void)hipFuncSetAttribute((const void*)fused, hipFuncAttributeMaxDynamicSharedMemorySize, LDS_TOTAL);
  fused<<<256, 512, LDS_TOTAL, stream>>>(x, wqkv, wo, Bk, Bv, shk, hk, Dv, gw, (u16*)d_out, (char*)d_ws);
}
```
